```python
import torch

def _softmax(x, axis=-1):
    return torch.softmax(x, dim=axis)
def _gelu(x):
    return torch.nn.functional.gelu(x, approximate="tanh")
def _silu(x):
    return torch.nn.functional.silu(x)
def _sigmoid(x):
    return torch.sigmoid(x)
def _layernorm(x, axis=-1, eps=1e-5):
    m = x.mean(dim=axis, keepdim=True)
    v = x.var(dim=axis, keepdim=True, unbiased=False)
    return (x - m) / torch.sqrt(v + eps)
def _scatter_add(tgt, idx, src, axis=0):
    return tgt.clone().index_add_(axis, idx, src)
def _segment_sum(src, seg, n):
    out = torch.zeros((n,) + src.shape[1:], dtype=src.dtype)
    return out.index_add_(0, seg, src)
def _pad(x, pw):
    flat = tuple(v for pair in reversed(pw) for v in pair)
    return torch.nn.functional.pad(x, flat)

kernel_name = 'parallel_sum__ssm_conv_s5__swiglu_ffn__prompt'

def setup_inputs():

    _w = (lambda a: (lambda t: t.to(torch.bfloat16) if t.is_floating_point() else t)(torch.as_tensor(a)))
    def _A_setup():
        B, S, D = 1, 2048, 576
        P = D
        Lambda_re = -0.5 * (lambda _x: torch.exp(torch.as_tensor(_x)))((lambda *s: torch.randn(*s, dtype=torch.bfloat16) * 0.8)(P))
        Lambda_im = (lambda *s: torch.randn(*s, dtype=torch.bfloat16) * 0.8)(P) * 1.0
        B_re = (lambda *s: torch.randn(*s, dtype=torch.bfloat16) * 0.8)(P, D) * (D ** -0.5)
        B_im = (lambda *s: torch.randn(*s, dtype=torch.bfloat16) * 0.8)(P, D) * (D ** -0.5)
        C_re = (lambda *s: torch.randn(*s, dtype=torch.bfloat16) * 0.8)(D, P) * (P ** -0.5)
        C_im = (lambda *s: torch.randn(*s, dtype=torch.bfloat16) * 0.8)(D, P) * (P ** -0.5)
        log_dt = (lambda *s: torch.randn(*s, dtype=torch.bfloat16) * 0.8)(P) * 0.3 - 2.0
        Dskip = (lambda *s: torch.randn(*s, dtype=torch.bfloat16) * 0.8)(D)
        return {
            "x": _w((lambda *s: torch.randn(*s, dtype=torch.bfloat16) * 0.05)(B, S, D)),
            "Lambda_re": _w(Lambda_re),
            "Lambda_im": _w(Lambda_im),
            "B_re": _w(B_re),
            "B_im": _w(B_im),
            "C_re": _w(C_re),
            "C_im": _w(C_im),
            "log_dt": _w(log_dt),
            "Dskip": _w(Dskip),
        }
    _A_in = _A_setup()

    def _B_setup():
        B, S, D, F = 1, 2048, 576, 4608
        return {
            "x": _w((lambda *s: torch.randn(*s, dtype=torch.bfloat16) * 0.05)(B, S, D)),
            "w_gate": _w((lambda *s: torch.randn(*s, dtype=torch.bfloat16) * 0.05)(D, F)),
            "w_up": _w((lambda *s: torch.randn(*s, dtype=torch.bfloat16) * 0.05)(D, F)),
            "w_down": _w((lambda *s: torch.randn(*s, dtype=torch.bfloat16) * 0.05)(F, D)),
        }
    _B_in = _B_setup()

    return {
        "A_x": _A_in["x"],
        "A_Lambda_re": _A_in["Lambda_re"],
        "A_Lambda_im": _A_in["Lambda_im"],
        "A_B_re": _A_in["B_re"],
        "A_B_im": _A_in["B_im"],
        "A_C_re": _A_in["C_re"],
        "A_C_im": _A_in["C_im"],
        "A_log_dt": _A_in["log_dt"],
        "A_Dskip": _A_in["Dskip"],
        "B_x": _B_in["x"],
        "B_w_gate": _B_in["w_gate"],
        "B_w_up": _B_in["w_up"],
        "B_w_down": _B_in["w_down"],
    }

def reference(A_x, A_Lambda_re, A_Lambda_im, A_B_re, A_B_im, A_C_re, A_C_im, A_log_dt, A_Dskip, B_x, B_w_gate, B_w_up, B_w_down):
    def _A_ref(x, Lambda_re, Lambda_im, B_re, B_im, C_re, C_im, log_dt, Dskip):
        Bsz, L, H = x.shape
        Lam = Lambda_re + 1j * Lambda_im
        Bmat = B_re + 1j * B_im
        Cmat = C_re + 1j * C_im
        dt = torch.exp(log_dt)
        Lam_bar = torch.exp(dt * Lam)
        B_bar = ((Lam_bar - 1.0) / Lam)[:, None] * Bmat
        Bu = torch.einsum("bld,pd->blp", x + 0j, B_bar)
        h = torch.zeros_like(Bu[:, 0, :])
        hs = []
        for t in range(L):
            h = Lam_bar[None, :] * h + Bu[:, t, :]
            hs.append(h)
        hseq = torch.stack(hs, 1)
        y = 2.0 * torch.einsum("blp,dp->bld", hseq, Cmat).real + x * Dskip[None, None, :]
        return y

    def _B_ref(x, w_gate, w_up, w_down):
        g = _silu(x @ w_gate)
        u = x @ w_up
        return (g * u) @ w_down

    x = A_x
    xn = _layernorm(x, axis=-1)
    return x + _A_ref(xn, A_Lambda_re, A_Lambda_im, A_B_re, A_B_im, A_C_re, A_C_im, A_log_dt, A_Dskip) + _B_ref(xn, B_w_gate, B_w_up, B_w_down)
```

```cpp
#include <hip/hip_runtime.h>
#include <hip/hip_bf16.h>
typedef unsigned short u16;
__device__ __forceinline__ float bf2f(u16 v){ return __uint_as_float(((unsigned)v)<<16); }
__device__ __forceinline__ u16 f2bf(float f){ unsigned u=__float_as_uint(f); u += 0x7FFF + ((u>>16)&1); return (u16)(u>>16); }
constexpr int L=2048, D=576, P=576, F=4608;

__global__ void ln_k(const u16* x, float* xn){
  int row=blockIdx.x; int t=threadIdx.x;
  float v[9]; float s=0;
  for(int i=0;i<9;i++){ v[i]=bf2f(x[row*D+t+64*i]); s+=v[i]; }
  for(int o=32;o;o>>=1) s+=__shfl_xor(s,o);
  float m=s/D; float q=0;
  for(int i=0;i<9;i++){ float d=v[i]-m; q+=d*d; }
  for(int o=32;o;o>>=1) q+=__shfl_xor(q,o);
  float r=1.0f/sqrtf(q/D+1e-5f);
  for(int i=0;i<9;i++) xn[row*D+t+64*i]=bf2f(f2bf((v[i]-m)*r));
}
__global__ void bu_k(const float* xn, const u16* Bre, const u16* Bim, const u16* Lre, const u16* Lim, const u16* ldt, float* bur, float* bui){
  int p=blockIdx.x*64+threadIdx.x; int t=blockIdx.y;
  float sr=0, si=0;
  for(int d=0;d<D;d++){ float a=xn[t*D+d]; sr+=a*bf2f(Bre[p*D+d]); si+=a*bf2f(Bim[p*D+d]); }
  float lr=bf2f(Lre[p]), li=bf2f(Lim[p]), dt=expf(bf2f(ldt[p]));
  float e=expf(dt*lr), ar=e*cosf(dt*li), ai=e*sinf(dt*li);
  float nr=ar-1.f, ni=ai; float den=lr*lr+li*li;
  float cr=(nr*lr+ni*li)/den, ci=(ni*lr-nr*li)/den;
  bur[t*P+p]=cr*sr-ci*si; bui[t*P+p]=cr*si+ci*sr;
}
__global__ void scan_k(float* bur, float* bui, const u16* Lre, const u16* Lim, const u16* ldt){
  int p=blockIdx.x*64+threadIdx.x;
  float lr=bf2f(Lre[p]), li=bf2f(Lim[p]), dt=expf(bf2f(ldt[p]));
  float e=expf(dt*lr), ar=e*cosf(dt*li), ai=e*sinf(dt*li);
  float hr=0, hi=0;
  for(int t=0;t<L;t++){ float br=bur[t*P+p], bi=bui[t*P+p]; float nr=ar*hr-ai*hi+br, ni=ar*hi+ai*hr+bi; hr=nr; hi=ni; bur[t*P+p]=hr; bui[t*P+p]=hi; }
}
__global__ void h_k(const float* xn, const u16* wg, const u16* wu, float* H){
  int f=blockIdx.x*64+threadIdx.x; int t=blockIdx.y;
  float g=0,u=0;
  for(int d=0;d<D;d++){ float a=xn[t*D+d]; g+=a*bf2f(wg[d*F+f]); u+=a*bf2f(wu[d*F+f]); }
  float s=g/(1.f+expf(-g));
  H[t*F+f]=s*u;
}
__global__ void out_k(const u16* x, const float* xn, const float* H, const u16* wd, const float* hr, const float* hi, const u16* Cre, const u16* Cim, const u16* Dk, float* out){
  int d=blockIdx.x*64+threadIdx.x; int t=blockIdx.y;
  float a=0;
  for(int f=0;f<F;f++) a+=H[t*F+f]*bf2f(wd[f*D+d]);
  float y=0;
  for(int p=0;p<P;p++) y+=hr[t*P+p]*bf2f(Cre[d*P+p])-hi[t*P+p]*bf2f(Cim[d*P+p]);
  out[t*D+d]=bf2f(x[t*D+d])+2.f*y+xn[t*D+d]*bf2f(Dk[d])+a;
}
extern "C" void kernel_launch(void* const* d_in, const int* in_sizes, int n_in, void* d_out, int out_size, void* d_ws, size_t ws_size, hipStream_t stream){
  const u16* x=(const u16*)d_in[0]; const u16* Lre=(const u16*)d_in[1]; const u16* Lim=(const u16*)d_in[2];
  const u16* Bre=(const u16*)d_in[3]; const u16* Bim=(const u16*)d_in[4]; const u16* Cre=(const u16*)d_in[5]; const u16* Cim=(const u16*)d_in[6];
  const u16* ldt=(const u16*)d_in[7]; const u16* Dk=(const u16*)d_in[8];
  const u16* wg=(const u16*)d_in[10]; const u16* wu=(const u16*)d_in[11]; const u16* wd=(const u16*)d_in[12];
  float* ws=(float*)d_ws; float* xn=ws; float* bur=xn+L*D; float* bui=bur+L*P; float* H=bui+L*P;
  ln_k<<<L,64,0,stream>>>(x,xn);
  bu_k<<<dim3(P/64,L),64,0,stream>>>(xn,Bre,Bim,Lre,Lim,ldt,bur,bui);
  scan_k<<<P/64,64,0,stream>>>(bur,bui,Lre,Lim,ldt);
  h_k<<<dim3(F/64,L),64,0,stream>>>(xn,wg,wu,H);
  out_k<<<dim3(D/64,L),64,0,stream>>>(x,xn,H,wd,bur,bui,Cre,Cim,Dk,(float*)d_out);
}
```

```cpp
#include <hip/hip_runtime.h>
typedef unsigned short u16;
typedef unsigned long long u64;
#define WS_ACK 49651712u
#define WS_DBG 49659904u
#define WS_EG 49283072u
#define WS_EP 49655808u
#define WS_FLH 49356800u
#define WS_FLS 49618944u
#define WS_H 0u
#define WS_HD 23592960u
#define WS_HS 18874368u
#define WS_SIZE 49922048u
#define WS_STG 49250304u
#define WS_XE 33030144u
#define XE_DEST 84480u
#define XE_FLAG 4096u
#define XE_SRC 4224u
#define XE_TILE 337920u

struct Tab { unsigned r[32][8]; unsigned tab[5][36]; };

__global__ __launch_bounds__(256,1) void fused_kernel(const u16* x, const u16* Lre, const u16* Lim, const u16* Bre, const u16* Bim, const u16* Cre, const u16* Cim,
    const u16* ldt, const u16* Dk, const u16* wg, const u16* wu, const u16* wd, float* out, char* ws, Tab T){
  unsigned tid=threadIdx.x;
  u64 ka=(u64)__builtin_amdgcn_kernarg_segment_ptr();
  asm volatile(
R"ASM(
s_load_dwordx16 s[4:19], %[ka], 0x0
s_load_dwordx8 s[20:27], %[ka], 0x40
s_load_dwordx4 s[28:31], %[ka], 0x60
s_mov_b32 s62, %[b]
s_lshr_b32 s63, s62, 3
s_bfe_u32 s64, s62, 0x20001
s_lshr_b32 s66, s62, 5
s_lshl_b32 s67, s64, 3
s_add_u32 s67, s67, s66
s_lshl_b32 s68, s67, 5
s_add_u32 s68, s68, 112
s_load_dwordx8 s[32:39], %[ka], s68
s_cmp_ge_u32 s63, 24
s_cselect_b32 s70, 4, s64
v_lshrrev_b32 v2, 6, v0
s_nop 0
v_readfirstlane_b32 s40, v2
v_and_b32 v1, 63, v0
s_and_b32 s65, s62, 1
s_lshl_b32 s65, s65, 2
s_and_b32 s68, s63, 3
s_add_u32 s65, s65, s68
s_lshl_b32 s41, s40, 10
s_lshl_b32 s79, s40, 13
s_lshl_b32 s58, s65, 8
s_lshl_b32 s68, s40, 6
s_add_u32 s58, s58, s68
s_mul_i32 s60, s58, 1152
s_mov_b32 s46, 2359296
s_mov_b32 s47, 0x20000
s_mov_b32 s50, 5308416
s_mov_b32 s51, 0x20000
s_mov_b32 s98, 49922048
s_mov_b32 s99, 0x20000
v_and_b32 v36, 63, v1
v_lshrrev_b32 v32, 3, v36
v_and_b32 v33, 7, v36
v_lshrrev_b32 v34, 1, v32
v_xor_b32 v35, v33, v34
v_lshlrev_b32 v35, 4, v35
v_mul_u32_u24 v2, 1152, v32
v_add_u32 v2, v2, v35
v_xor_b32 v35, 64, v35
v_mul_u32_u24 v3, 1152, v32
v_add_u32 v3, v3, v35
v_and_b32 v34, 1, v34
s_and_b32 s75, s40, 1
s_lshl_b32 s76, s75, 1
v_or_b32 v34, s76, v34
v_lshlrev_b32 v34, 1, v34
v_xor_b32 v35, v33, v34
v_lshlrev_b32 v35, 4, v35
s_lshr_b32 s77, s40, 1
s_lshl_b32 s77, s77, 3
v_add_u32 v32, s77, v32
v_mul_u32_u24 v4, 9216, v32
v_add_u32 v4, v4, v35
s_waitcnt lgkmcnt(0)
s_mov_b32 s44, s4
s_and_b32 s45, s5, 0xffff
s_add_u32 m0, s79, 98304
s_add_u32 s75, s60, 0
buffer_load_dwordx4 v2, s[44:47], s75 offen lds
s_add_u32 m0, s79, 99328
s_add_u32 s75, s60, 9216
buffer_load_dwordx4 v3, s[44:47], s75 offen lds
s_bitcmp1_b32 s40, 0
s_cselect_b32 s48, s24, s22
s_cselect_b32 s49, s25, s23
s_and_b32 s49, s49, 0xffff
s_lshr_b32 s61, s32, 16
s_lshl_b32 s61, s61, 7
s_mov_b32 s96, s30
s_and_b32 s97, s31, 0xffff
s_mov_b32 s95, s58
s_lshl_b32 s58, s58, 5
v_mov_b32 v0, 0
s_and_b32 s5, s5, 0xffff
s_and_b32 s7, s7, 0xffff
s_and_b32 s9, s9, 0xffff
s_and_b32 s11, s11, 0xffff
s_and_b32 s13, s13, 0xffff
s_and_b32 s15, s15, 0xffff
s_and_b32 s17, s17, 0xffff
s_and_b32 s19, s19, 0xffff
s_and_b32 s21, s21, 0xffff
s_and_b32 s23, s23, 0xffff
s_and_b32 s25, s25, 0xffff
s_and_b32 s27, s27, 0xffff
s_and_b32 s29, s29, 0xffff
s_add_u32 m0, s79, 100352
s_add_u32 s75, s60, 18432
buffer_load_dwordx4 v2, s[44:47], s75 offen lds
s_and_b32 s31, s31, 0xffff
v_writelane_b32 v0, s95, 42
v_writelane_b32 v0, s65, 55
s_lshl_b32 s68, s67, 3
s_lshl_b32 s71, s65, 8
s_add_u32 s68, s68, s71
s_lshl_b32 s71, s40, 1
s_add_u32 s68, s68, s71
v_writelane_b32 v0, s68, 56
v_lshrrev_b32 v23, 5, v1
v_mul_u32_u24 v23, 1152, v23
v_and_b32 v27, 31, v1
v_mad_u32_u24 v23, v27, 12, v23
v_readlane_b32 s77, v0, 56
s_add_u32 m0, s79, 101376
s_add_u32 s75, s60, 27648
buffer_load_dwordx4 v3, s[44:47], s75 offen lds
s_nop 0
s_mul_i32 s77, s77, 1152
buffer_load_dwordx3 v[20:22], v23, s[44:47], s77 offen offset:0
buffer_load_dwordx3 v[24:26], v23, s[44:47], s77 offen offset:384
buffer_load_dwordx3 v[220:222], v23, s[44:47], s77 offen offset:768
s_and_b32 s94, s32, 1
v_writelane_b32 v0, s4, 7
v_writelane_b32 v0, s5, 8
v_writelane_b32 v0, s6, 58
v_writelane_b32 v0, s7, 59
v_writelane_b32 v0, s8, 60
v_writelane_b32 v0, s9, 61
v_writelane_b32 v0, s18, 62
v_writelane_b32 v0, s19, 63
v_writelane_b32 v0, s28, 9
s_add_u32 m0, s79, 102400
s_add_u32 s75, s60, 36864
buffer_load_dwordx4 v2, s[44:47], s75 offen lds
v_writelane_b32 v0, s29, 10
s_mul_i32 s68, s70, 144
s_add_u32 s68, s68, 1136
v_writelane_b32 v0, s68, 26
s_lshr_b32 s71, s64, 1
s_mul_i32 s71, s71, 9
s_and_b32 s68, s64, 1
s_mul_i32 s68, s68, 5
s_add_u32 s71, s71, s68
s_add_u32 s71, s71, s66
s_cmp_lg_u32 s94, 0
s_cselect_b32 s71, s71, 0
s_cselect_b32 s84, s10, s48
s_cselect_b32 s85, s11, s49
s_cselect_b32 s88, s12, s48
s_cselect_b32 s89, s13, s49
s_cselect_b32 s86, 663552, s50
s_mov_b32 s93, 294912
s_cselect_b32 s92, 128, s93
s_cselect_b32 s93, 0, s93
s_add_u32 m0, s79, 103424
s_add_u32 s75, s60, 46080
buffer_load_dwordx4 v3, s[44:47], s75 offen lds
s_mov_b32 s90, s86
s_mov_b32 s87, 0x20000
s_mov_b32 s91, 0x20000
v_writelane_b32 v0, s71, 54
v_writelane_b32 v0, s10, 27
v_writelane_b32 v0, s11, 28
v_writelane_b32 v0, s12, 29
v_writelane_b32 v0, s13, 30
s_and_b32 s72, s64, 1
s_add_u32 s73, s66, s72
s_sub_u32 s73, s73, 5
s_lshl_b32 s74, s73, 2
s_add_u32 s74, s74, s40
v_writelane_b32 v0, s74, 31
s_add_u32 m0, s79, 104448
s_add_u32 s75, s60, 55296
buffer_load_dwordx4 v2, s[44:47], s75 offen lds
s_mul_i32 s72, s72, 3
s_add_u32 s73, s73, s72
s_lshr_b32 s72, s64, 1
s_mul_i32 s72, s72, 7
s_add_u32 s73, s73, s72
s_lshl_b32 s73, s73, 5
s_lshl_b32 s72, s65, 2
s_add_u32 s73, s73, s72
s_add_u32 s73, s73, s40
v_writelane_b32 v0, s73, 32
s_mul_i32 s72, s64, 2304
v_writelane_b32 v0, s72, 52
s_lshl_b32 s68, s62, 2
s_add_u32 s68, s68, s40
s_add_u32 m0, s79, 105472
s_add_u32 s75, s60, 64512
buffer_load_dwordx4 v3, s[44:47], s75 offen lds
v_writelane_b32 v0, s68, 25
s_lshl_b32 s72, s68, 2
s_add_u32 s72, s72, 49651712
v_writelane_b32 v0, s72, 57
s_lshl_b32 s72, s68, 8
s_add_u32 s72, s72, 49659904
v_writelane_b32 v0, s72, 24
s_lshl_b32 s72, s58, 2
s_add_u32 s72, s72, 49356800
v_writelane_b32 v0, s72, 53
s_sub_u32 s73, s63, 24
s_lshr_b32 s74, s64, 1
s_mul_i32 s74, s74, 12
s_add_u32 s74, s74, s73
s_add_u32 m0, s41, 49152
)ASM"
R"ASM(
s_add_u32 s76, s61, 0
buffer_load_dwordx4 v4, s[48:51], s76 offen lds
s_and_b32 s68, s64, 1
s_lshl_b32 s68, s68, 2
s_add_u32 s74, s74, s68
s_cmp_ge_u32 s63, 24
s_cselect_b32 s74, s74, s63
s_cselect_b32 s54, 0, s64
s_cselect_b32 s72, 2, 1
s_cselect_b32 s56, s16, s20
s_cselect_b32 s57, s17, s21
s_cselect_b32 s59, 2048, 0
s_mov_b32 s69, 1152
s_cselect_b32 s69, 665600, s69
v_writelane_b32 v0, s69, 6
s_sub_u32 s56, s56, s59
s_subb_u32 s57, s57, 0
s_add_u32 m0, s41, 53248
s_add_u32 s76, s61, 147456
buffer_load_dwordx4 v4, s[48:51], s76 offen lds
v_writelane_b32 v0, s56, 4
v_writelane_b32 v0, s57, 5
s_min_u32 s74, s74, 23
s_mul_i32 s52, s74, 43
s_lshr_b32 s52, s52, 7
s_mul_i32 s53, s52, 3
s_sub_u32 s53, s74, s53
s_and_b32 s68, s62, 1
s_lshl_b32 s68, s68, 3
s_add_u32 s52, s52, s68
s_lshl_b32 s55, s52, 7
s_lshl_b32 s68, s40, 5
s_add_u32 s55, s55, s68
s_lshl_b32 s68, s62, 2
s_add_u32 m0, s41, 57344
s_add_u32 s76, s61, 294912
buffer_load_dwordx4 v4, s[48:51], s76 offen lds
s_add_u32 s68, s68, s40
s_cmp_eq_u32 s68, 896
s_cselect_b32 s72, 3, s72
v_writelane_b32 v0, s72, 21
v_writelane_b32 v0, s54, 16
s_sub_u32 s56, s26, 2048
s_subb_u32 s57, s27, 0
v_writelane_b32 v0, s56, 0
v_writelane_b32 v0, s57, 1
s_cmp_lt_u32 s54, 2
s_cselect_b32 s56, s14, s16
s_cselect_b32 s57, s15, s17
s_sub_u32 s56, s56, 2048
s_subb_u32 s57, s57, 0
s_add_u32 m0, s41, 61440
s_add_u32 s76, s61, 442368
buffer_load_dwordx4 v4, s[48:51], s76 offen lds
v_writelane_b32 v0, s56, 2
v_writelane_b32 v0, s57, 3
v_writelane_b32 v0, s14, 37
v_writelane_b32 v0, s15, 38
v_writelane_b32 v0, s16, 39
v_writelane_b32 v0, s17, 40
s_lshl_b32 s59, s55, 5
s_mul_i32 s56, s70, 3670016
s_add_u32 s56, s56, s59
v_writelane_b32 v0, s56, 11
s_lshr_b32 s56, s54, 1
s_mul_i32 s56, s56, 36
s_and_b32 s57, s54, 1
s_lshl_b32 s69, s57, 4
s_mov_b32 s75, 49655808
buffer_load_dword v219, off, s[96:99], s75 sc1
s_add_u32 s56, s56, s69
s_lshl_b32 s56, s56, 16
s_add_u32 s56, s56, s59
s_add_u32 s56, s56, 18874368
v_writelane_b32 v0, s56, 12
s_lshl_b32 s57, s57, 9
s_cmp_ge_u32 s63, 24
s_cselect_b32 s57, 1024, s57
s_mul_i32 s56, s53, 221184
s_add_u32 s56, s56, s57
v_writelane_b32 v0, s56, 13
s_mul_i32 s56, s52, 3
s_add_u32 s56, s56, s53
s_mul_i32 s56, s56, 337920
s_add_u32 m0, s79, 65536
s_add_u32 s75, s60, 128
buffer_load_dwordx4 v2, s[44:47], s75 offen lds
s_mul_i32 s57, s40, 21120
s_add_u32 s56, s56, s57
s_add_u32 s56, s56, 33030144
s_mul_i32 s57, s70, 4224
s_add_u32 s57, s57, s56
v_writelane_b32 v0, s57, 14
s_mul_i32 s57, s54, 84480
s_add_u32 s57, s57, s56
v_writelane_b32 v0, s57, 15
s_mul_i32 s56, s53, 384
v_writelane_b32 v0, s56, 17
s_lshl_b32 s57, s54, 5
s_add_u32 s56, s56, s57
s_cmp_ge_u32 s63, 24
s_cselect_b32 s57, 1073741824, s56
s_add_u32 m0, s79, 66560
s_add_u32 s75, s60, 9344
buffer_load_dwordx4 v3, s[44:47], s75 offen lds
v_writelane_b32 v0, s57, 19
s_mul_i32 s57, s55, 1152
s_add_u32 s56, s56, s57
v_writelane_b32 v0, s56, 18
s_lshl_b32 s56, s55, 4
s_add_u32 s56, s56, 49250304
v_writelane_b32 v0, s56, 20
s_lshr_b32 s56, s55, 6
s_lshl_b32 s57, s56, 13
s_mul_i32 s59, s70, 896
s_add_u32 s57, s57, s59
s_add_u32 s57, s57, 49356800
v_min_u32 v217, 35, v1
v_add_u32 v216, -4, v217
s_add_u32 m0, s79, 67584
s_add_u32 s75, s60, 18560
buffer_load_dwordx4 v2, s[44:47], s75 offen lds
v_cmp_gt_u32 vcc, 30, v217
s_nop 1
v_cndmask_b32 v216, v216, v217, vcc
v_lshlrev_b32 v216, 5, v216
v_add_u32 v216, s57, v216
s_and_b32 s59, s64, 1
s_lshl_b32 s59, s59, 3
s_sub_u32 s59, s59, 28
s_cmp_ge_u32 s63, 24
s_cselect_b32 s59, 16, s59
s_cselect_b32 s69, 1, 63
s_cselect_b32 s57, 30, 28
s_cselect_b32 s68, 4, 8
v_and_b32 v223, s69, v217
v_add_u32 v223, s59, v223
s_add_u32 m0, s79, 68608
s_add_u32 s75, s60, 27776
buffer_load_dwordx4 v3, s[44:47], s75 offen lds
v_lshlrev_b32 v223, 5, v223
s_lshl_b32 s56, s56, 10
s_add_u32 s56, s56, 49618944
v_add_u32 v223, s56, v223
v_subrev_u32 v217, s57, v217
v_cmp_gt_u32 vcc, s68, v217
s_nop 1
v_cndmask_b32 v216, v216, v223, vcc
s_lshl_b32 s56, s40, 8
s_add_u32 s56, s56, 153600
v_lshlrev_b32 v217, 2, v1
v_add_u32 v217, s56, v217
ds_write_b32 v217, v216
s_lshl_b32 s4, s71, 3
s_add_u32 s4, s4, s65
s_add_u32 m0, s79, 69632
s_add_u32 s75, s60, 36992
buffer_load_dwordx4 v2, s[44:47], s75 offen lds
s_mul_i32 s5, s71, 36864
s_lshr_b32 s6, s40, 1
s_mul_i32 s6, s6, 147456
s_mul_i32 s7, s64, 2304
s_add_u32 s6, s6, s7
s_bfe_u32 s83, s32, 0x80008
v_readlane_b32 s8, v0, 53
s_add_u32 s63, s6, s33
s_add_u32 s64, s6, s34
s_add_u32 s65, s6, s35
s_add_u32 s66, s6, s36
s_cmp_lg_u32 s94, 0
s_cselect_b32 s63, s5, s63
s_cmp_gt_u32 s83, 3
s_cselect_b32 s66, s66, 1073741824
s_add_u32 m0, s79, 70656
s_add_u32 s75, s60, 46208
buffer_load_dwordx4 v3, s[44:47], s75 offen lds
s_mov_b32 s67, 1073741824
s_bfe_u32 s9, s37, 0x100000
s_lshl_b32 s72, s9, 17
s_lshl_b32 s52, s9, 5
s_add_u32 s52, s52, s8
s_cmp_gt_u32 s9, 255
s_cselect_b32 s72, 1073741824, s72
s_cselect_b32 s52, 1073741824, s52
s_bfe_u32 s9, s37, 0x100010
s_lshl_b32 s69, s9, 17
s_lshl_b32 s53, s9, 5
s_add_u32 s53, s53, s8
s_cmp_gt_u32 s9, 255
s_cselect_b32 s69, 1073741824, s69
s_cselect_b32 s53, 1073741824, s53
s_add_u32 m0, s79, 71680
s_add_u32 s75, s60, 55424
buffer_load_dwordx4 v2, s[44:47], s75 offen lds
s_bfe_u32 s9, s38, 0x100000
s_lshl_b32 s73, s9, 17
s_lshl_b32 s54, s9, 5
s_add_u32 s54, s54, s8
s_cmp_gt_u32 s9, 255
s_cselect_b32 s73, 1073741824, s73
)ASM"
R"ASM(
s_cselect_b32 s54, 1073741824, s54
s_bfe_u32 s9, s38, 0x100010
s_lshl_b32 s74, s9, 17
s_lshl_b32 s55, s9, 5
s_add_u32 s55, s55, s8
s_cmp_gt_u32 s9, 255
s_cselect_b32 s74, 1073741824, s74
s_cselect_b32 s55, 1073741824, s55
s_add_u32 m0, s79, 72704
s_add_u32 s75, s60, 64640
buffer_load_dwordx4 v3, s[44:47], s75 offen lds
s_bfe_u32 s9, s39, 0x100000
s_lshl_b32 s59, s9, 17
s_lshl_b32 s56, s9, 5
s_add_u32 s56, s56, s8
s_cmp_gt_u32 s9, 255
s_cselect_b32 s59, 1073741824, s59
s_cselect_b32 s56, 1073741824, s56
s_bfe_u32 s9, s39, 0x100010
s_lshl_b32 s95, s9, 17
s_lshl_b32 s57, s9, 5
s_add_u32 s57, s57, s8
s_cmp_gt_u32 s9, 255
s_cselect_b32 s95, 1073741824, s95
s_cselect_b32 s57, 1073741824, s57
s_add_u32 m0, s41, 0
s_add_u32 s76, s61, 589824
buffer_load_dwordx4 v4, s[48:51], s76 offen lds
s_lshl_b32 s42, s4, 9
s_add_u32 s42, s42, 49283072
s_cmp_eq_u32 s40, 0
s_cselect_b32 s42, s42, 1073741824
s_lshl_b32 s4, s4, 2
s_add_u32 s4, s4, s40
s_lshl_b32 s71, s4, 14
s_add_u32 s71, s71, 23592960
s_and_b32 s77, s40, 1
s_lshl_b32 s78, s77, 1
v_lshrrev_b32 v216, 2, v1
v_and_b32 v217, 3, v1
v_xor_b32 v217, s78, v217
v_lshlrev_b32 v217, 4, v217
s_add_u32 m0, s41, 4096
s_add_u32 s76, s61, 737280
buffer_load_dwordx4 v4, s[48:51], s76 offen lds
v_mul_u32_u24 v5, 9216, v216
v_add_u32 v5, v5, v217
s_lshl_b32 s77, s77, 6
v_xor_b32 v216, s77, v2
s_mul_i32 s77, s40, 9216
v_add_u32 v216, s77, v216
s_cmp_lg_u32 s94, 0
s_cselect_b64 vcc, -1, 0
v_cndmask_b32 v5, v5, v216, vcc
s_cmp_eq_u32 s94, 0
s_cselect_b64 s[24:25], -1, 0
s_mov_b32 s23, 6912
s_add_u32 m0, s41, 8192
s_add_u32 s76, s61, 884736
buffer_load_dwordx4 v4, s[48:51], s76 offen lds
s_add_u32 m0, s41, 12288
s_add_u32 s76, s61, 1032192
buffer_load_dwordx4 v4, s[48:51], s76 offen lds
v_and_b32 v32, 31, v36
v_lshrrev_b32 v33, 5, v36
v_bfe_u32 v34, v32, 1, 3
v_lshlrev_b32 v35, 7, v32
v_add_u32 v35, s79, v35
v_add_u32 v35, 65536, v35
v_or_b32 v37, 0, v33
v_xor_b32 v37, v37, v34
v_lshl_add_u32 v6, v37, 4, v35
v_or_b32 v37, 2, v33
v_xor_b32 v37, v37, v34
v_lshl_add_u32 v7, v37, 4, v35
v_or_b32 v37, 4, v33
v_xor_b32 v37, v37, v34
v_lshl_add_u32 v8, v37, 4, v35
v_or_b32 v37, 6, v33
v_xor_b32 v37, v37, v34
v_lshl_add_u32 v9, v37, 4, v35
v_lshlrev_b32 v19, 5, v32
v_lshl_add_u32 v19, v33, 4, v19
v_add_u32 v19, s58, v19
v_lshlrev_b32 v16, 5, v33
v_add_u32 v16, 147456, v16
v_lshrrev_b32 v32, 4, v36
v_bfe_u32 v33, v36, 2, 2
v_and_b32 v34, 3, v36
v_and_b32 v35, 1, v34
v_lshlrev_b32 v35, 4, v35
v_lshrrev_b32 v37, 1, v34
v_lshl_add_u32 v35, v37, 3, v35
v_lshrrev_b32 v37, 1, v33
v_and_b32 v38, 1, v32
v_lshl_add_u32 v37, v38, 1, v37
v_lshl_add_u32 v38, v32, 10, v35
v_lshl_add_u32 v38, v33, 7, v38
v_xor_b32 v216, 0, v37
v_lshl_add_u32 v10, v216, 5, v38
v_xor_b32 v216, 1, v37
v_lshl_add_u32 v11, v216, 5, v38
v_xor_b32 v216, 2, v37
v_lshl_add_u32 v12, v216, 5, v38
v_xor_b32 v216, 3, v37
v_lshl_add_u32 v13, v216, 5, v38
v_lshrrev_b32 v37, 1, v32
v_lshlrev_b32 v37, 11, v37
v_and_b32 v38, 1, v32
v_lshl_add_u32 v37, v38, 9, v37
v_lshl_add_u32 v37, v33, 7, v37
v_lshl_add_u32 v37, v34, 3, v37
v_lshrrev_b32 v38, 1, v33
v_xor_b32 v38, s40, v38
v_lshl_add_u32 v14, v38, 5, v37
v_xor_b32 v38, 2, v38
v_lshl_add_u32 v15, v38, 5, v37
v_add_u32 v15, 1024, v15
v_lshlrev_b32 v37, 4, v32
s_lshl_b32 s75, s40, 7
s_add_u32 s75, s75, 147456
v_add_u32 v17, s75, v37
s_lshl_b32 s75, s40, 6
s_add_u32 s75, s75, 147456
v_add_u32 v18, s75, v37
v_mov_b32 v28, 0x3f803f80
v_mov_b32 v29, 0x3f803f80
v_mov_b32 v30, 0x3f803f80
v_mov_b32 v31, 0x3f803f80
s_waitcnt vmcnt(13)
s_barrier
ds_read_b128 a[0:3], v6 offset:32768
ds_read_b128 a[4:7], v6 offset:36864
ds_read_b64_tr_b16 v[160:161], v10 offset:49152
ds_read_b64_tr_b16 v[162:163], v10 offset:49664
ds_read_b64_tr_b16 v[164:165], v11 offset:49152
ds_read_b64_tr_b16 v[166:167], v11 offset:49664
ds_read_b64_tr_b16 v[168:169], v12 offset:49152
ds_read_b64_tr_b16 v[170:171], v12 offset:49664
ds_read_b64_tr_b16 v[172:173], v13 offset:49152
ds_read_b64_tr_b16 v[174:175], v13 offset:49664
ds_read_b128 a[8:11], v7 offset:32768
ds_read_b128 a[12:15], v7 offset:36864
ds_read_b128 a[16:19], v8 offset:32768
ds_read_b128 a[20:23], v8 offset:36864
ds_read_b128 a[24:27], v9 offset:32768
ds_read_b128 a[28:31], v9 offset:36864
s_waitcnt lgkmcnt(12)
v_mfma_f32_32x32x16_bf16 v[32:47], v[160:163], a[0:3], 0
ds_read_b64_tr_b16 v[176:177], v10 offset:53248
ds_read_b64_tr_b16 v[178:179], v10 offset:53760
v_mfma_f32_32x32x16_bf16 v[48:63], v[160:163], a[4:7], 0
ds_read_b64_tr_b16 v[180:181], v11 offset:53248
ds_read_b64_tr_b16 v[182:183], v11 offset:53760
s_waitcnt lgkmcnt(14)
v_mfma_f32_32x32x16_bf16 v[64:79], v[164:167], a[0:3], 0
ds_read_b64_tr_b16 v[184:185], v12 offset:53248
ds_read_b64_tr_b16 v[186:187], v12 offset:53760
v_mfma_f32_32x32x16_bf16 v[80:95], v[164:167], a[4:7], 0
ds_read_b64_tr_b16 v[188:189], v13 offset:53248
ds_read_b64_tr_b16 v[190:191], v13 offset:53760
s_waitcnt lgkmcnt(15)
v_mfma_f32_32x32x16_bf16 v[96:111], v[168:171], a[0:3], 0
ds_read_b64_tr_b16 v[192:193], v14 offset:49152
ds_read_b64_tr_b16 v[196:197], v15 offset:49152
v_mfma_f32_32x32x16_bf16 v[112:127], v[168:171], a[4:7], 0
ds_read_b64_tr_b16 v[194:195], v14 offset:53248
ds_read_b64_tr_b16 v[198:199], v15 offset:53248
s_waitcnt lgkmcnt(15)
v_mfma_f32_32x32x16_bf16 v[128:143], v[172:175], a[0:3], 0
s_waitcnt lgkmcnt(12)
s_add_u32 m0, s41, 16384
s_add_u32 s76, s61, 1179648
buffer_load_dwordx4 v4, s[48:51], s76 offen lds
v_mfma_f32_32x32x16_bf16 v[144:159], v[172:175], a[4:7], 0
s_add_u32 m0, s41, 20480
s_add_u32 s76, s61, 1327104
buffer_load_dwordx4 v4, s[48:51], s76 offen lds
s_waitcnt lgkmcnt(4)
v_mfma_f32_32x32x16_bf16 v[32:47], v[176:179], a[8:11], v[32:47]
ds_read_b64_tr_b16 v[160:161], v10 offset:57344
ds_read_b64_tr_b16 v[162:163], v10 offset:57856
v_mfma_f32_32x32x16_bf16 v[48:63], v[176:179], a[12:15], v[48:63]
ds_read_b64_tr_b16 v[164:165], v11 offset:57344
ds_read_b64_tr_b16 v[166:167], v11 offset:57856
v_mfma_f32_32x32x16_bf16 v[64:79], v[180:183], a[8:11], v[64:79]
ds_read_b64_tr_b16 v[168:169], v12 offset:57344
ds_read_b64_tr_b16 v[170:171], v12 offset:57856
v_mfma_f32_32x32x16_bf16 v[80:95], v[180:183], a[12:15], v[80:95]
ds_read_b64_tr_b16 v[172:173], v13 offset:57344
ds_read_b64_tr_b16 v[174:175], v13 offset:57856
v_mfma_f32_32x32x16_bf16 v[96:111], v[184:187], a[8:11], v[96:111]
ds_read_b64_tr_b16 v[200:201], v14 offset:57344
ds_read_b64_tr_b16 v[204:205], v15 offset:57344
v_mfma_f32_32x32x16_bf16 v[112:127], v[184:187], a[12:15], v[112:127]
s_add_u32 m0, s79, 98304
s_add_u32 s75, s60, 256
buffer_load_dwordx4 v2, s[44:47], s75 offen lds
v_mfma_f32_32x32x16_bf16 v[128:143], v[188:191], a[8:11], v[128:143]
s_add_u32 m0, s79, 99328
)ASM"
R"ASM(
s_add_u32 s75, s60, 9472
buffer_load_dwordx4 v3, s[44:47], s75 offen lds
v_mfma_f32_32x32x16_bf16 v[144:159], v[188:191], a[12:15], v[144:159]
s_add_u32 m0, s79, 100352
s_add_u32 s75, s60, 18688
buffer_load_dwordx4 v2, s[44:47], s75 offen lds
s_waitcnt lgkmcnt(2)
v_mfma_f32_32x32x16_bf16 v[32:47], v[160:163], a[16:19], v[32:47]
ds_read_b64_tr_b16 v[176:177], v10 offset:61440
ds_read_b64_tr_b16 v[178:179], v10 offset:61952
v_mfma_f32_32x32x16_bf16 v[48:63], v[160:163], a[20:23], v[48:63]
ds_read_b64_tr_b16 v[180:181], v11 offset:61440
ds_read_b64_tr_b16 v[182:183], v11 offset:61952
v_mfma_f32_32x32x16_bf16 v[64:79], v[164:167], a[16:19], v[64:79]
ds_read_b64_tr_b16 v[184:185], v12 offset:61440
ds_read_b64_tr_b16 v[186:187], v12 offset:61952
v_mfma_f32_32x32x16_bf16 v[80:95], v[164:167], a[20:23], v[80:95]
ds_read_b64_tr_b16 v[188:189], v13 offset:61440
ds_read_b64_tr_b16 v[190:191], v13 offset:61952
v_mfma_f32_16x16x32_bf16 v[208:211], v[192:195], v[28:31], 0
ds_read_b64_tr_b16 v[202:203], v14 offset:61440
v_mfma_f32_16x16x32_bf16 v[212:215], v[196:199], v[28:31], 0
ds_read_b64_tr_b16 v[206:207], v15 offset:61440
v_mfma_f32_32x32x16_bf16 v[96:111], v[168:171], a[16:19], v[96:111]
s_add_u32 m0, s79, 101376
s_add_u32 s75, s60, 27904
buffer_load_dwordx4 v3, s[44:47], s75 offen lds
v_mfma_f32_32x32x16_bf16 v[112:127], v[168:171], a[20:23], v[112:127]
s_add_u32 m0, s79, 102400
s_add_u32 s75, s60, 37120
buffer_load_dwordx4 v2, s[44:47], s75 offen lds
v_mfma_f32_32x32x16_bf16 v[128:143], v[172:175], a[16:19], v[128:143]
s_add_u32 m0, s79, 103424
s_add_u32 s75, s60, 46336
buffer_load_dwordx4 v3, s[44:47], s75 offen lds
v_mfma_f32_32x32x16_bf16 v[144:159], v[172:175], a[20:23], v[144:159]
s_add_u32 m0, s79, 104448
s_add_u32 s75, s60, 55552
buffer_load_dwordx4 v2, s[44:47], s75 offen lds
s_waitcnt lgkmcnt(2)
v_mfma_f32_32x32x16_bf16 v[32:47], v[176:179], a[24:27], v[32:47]
s_add_u32 m0, s79, 105472
s_add_u32 s75, s60, 64768
buffer_load_dwordx4 v3, s[44:47], s75 offen lds
s_add_u32 m0, s41, 24576
s_add_u32 s76, s61, 1474560
buffer_load_dwordx4 v4, s[48:51], s76 offen lds
s_add_u32 m0, s41, 28672
s_add_u32 s76, s61, 1622016
buffer_load_dwordx4 v4, s[48:51], s76 offen lds
s_add_u32 m0, s41, 32768
s_add_u32 s76, s61, 1769472
buffer_load_dwordx4 v4, s[48:51], s76 offen lds
s_add_u32 m0, s41, 36864
s_add_u32 s76, s61, 1916928
buffer_load_dwordx4 v4, s[48:51], s76 offen lds
s_add_u32 m0, s41, 40960
s_add_u32 s76, s61, 2064384
buffer_load_dwordx4 v4, s[48:51], s76 offen lds
s_add_u32 m0, s41, 45056
s_add_u32 s76, s61, 2211840
buffer_load_dwordx4 v4, s[48:51], s76 offen lds
v_mfma_f32_32x32x16_bf16 v[48:63], v[176:179], a[28:31], v[48:63]
s_waitcnt vmcnt(16) lgkmcnt(0)
s_barrier
ds_read_b128 a[32:35], v6 offset:0
ds_read_b128 a[36:39], v6 offset:4096
v_lshlrev_b32 v23, 16, v20
v_mfma_f32_32x32x16_bf16 v[64:79], v[180:183], a[24:27], v[64:79]
ds_read_b64_tr_b16 v[160:161], v10 offset:0
ds_read_b64_tr_b16 v[162:163], v10 offset:512
v_and_b32 v27, 0xffff0000, v20
v_lshlrev_b32 v223, 16, v21
v_and_b32 v216, 0xffff0000, v21
v_mfma_f32_32x32x16_bf16 v[80:95], v[180:183], a[28:31], v[80:95]
ds_read_b64_tr_b16 v[164:165], v11 offset:0
ds_read_b64_tr_b16 v[166:167], v11 offset:512
v_add_f32 v23, v23, v223
v_add_f32 v27, v27, v216
v_lshlrev_b32 v223, 16, v22
v_mfma_f32_32x32x16_bf16 v[96:111], v[184:187], a[24:27], v[96:111]
ds_read_b64_tr_b16 v[168:169], v12 offset:0
ds_read_b64_tr_b16 v[170:171], v12 offset:512
v_and_b32 v216, 0xffff0000, v22
v_add_f32 v23, v23, v223
v_add_f32 v27, v27, v216
v_mfma_f32_32x32x16_bf16 v[112:127], v[184:187], a[28:31], v[112:127]
ds_read_b64_tr_b16 v[172:173], v13 offset:0
ds_read_b64_tr_b16 v[174:175], v13 offset:512
v_lshlrev_b32 v223, 16, v24
v_and_b32 v216, 0xffff0000, v24
v_add_f32 v23, v23, v223
v_mfma_f32_32x32x16_bf16 v[128:143], v[188:191], a[24:27], v[128:143]
ds_read_b128 a[40:43], v7 offset:0
ds_read_b128 a[44:47], v7 offset:4096
v_add_f32 v27, v27, v216
v_lshlrev_b32 v223, 16, v25
v_and_b32 v216, 0xffff0000, v25
v_mfma_f32_32x32x16_bf16 v[144:159], v[188:191], a[28:31], v[144:159]
ds_read_b128 a[48:51], v8 offset:0
ds_read_b128 a[52:55], v8 offset:4096
v_add_f32 v23, v23, v223
v_add_f32 v27, v27, v216
s_waitcnt lgkmcnt(4)
v_mfma_f32_32x32x16_bf16 v[32:47], v[160:163], a[32:35], v[32:47]
ds_read_b128 a[56:59], v9 offset:0
ds_read_b128 a[60:63], v9 offset:4096
v_lshlrev_b32 v223, 16, v26
v_and_b32 v216, 0xffff0000, v26
v_add_f32 v23, v23, v223
v_mfma_f32_32x32x16_bf16 v[48:63], v[160:163], a[36:39], v[48:63]
ds_read_b64_tr_b16 v[176:177], v10 offset:4096
ds_read_b64_tr_b16 v[178:179], v10 offset:4608
v_add_f32 v27, v27, v216
v_lshlrev_b32 v223, 16, v220
v_and_b32 v216, 0xffff0000, v220
v_mfma_f32_32x32x16_bf16 v[64:79], v[164:167], a[32:35], v[64:79]
ds_read_b64_tr_b16 v[180:181], v11 offset:4096
ds_read_b64_tr_b16 v[182:183], v11 offset:4608
v_add_f32 v23, v23, v223
v_add_f32 v27, v27, v216
v_lshlrev_b32 v223, 16, v221
v_mfma_f32_32x32x16_bf16 v[80:95], v[164:167], a[36:39], v[80:95]
ds_read_b64_tr_b16 v[184:185], v12 offset:4096
ds_read_b64_tr_b16 v[186:187], v12 offset:4608
v_and_b32 v216, 0xffff0000, v221
v_add_f32 v23, v23, v223
v_add_f32 v27, v27, v216
v_mfma_f32_16x16x32_bf16 v[208:211], v[200:203], v[28:31], v[208:211]
ds_read_b64_tr_b16 v[188:189], v13 offset:4096
v_mfma_f32_16x16x32_bf16 v[212:215], v[204:207], v[28:31], v[212:215]
ds_read_b64_tr_b16 v[190:191], v13 offset:4608
v_mfma_f32_32x32x16_bf16 v[96:111], v[168:171], a[32:35], v[96:111]
s_waitcnt lgkmcnt(8)
ds_read_b64_tr_b16 v[192:193], v14 offset:0
ds_read_b64_tr_b16 v[196:197], v15 offset:0
v_lshlrev_b32 v223, 16, v222
v_and_b32 v216, 0xffff0000, v222
v_mfma_f32_32x32x16_bf16 v[112:127], v[168:171], a[36:39], v[112:127]
ds_read_b64_tr_b16 v[194:195], v14 offset:4096
ds_read_b64_tr_b16 v[198:199], v15 offset:4096
v_add_f32 v23, v23, v223
v_add_f32 v27, v27, v216
v_add_f32 v23, v23, v27
v_mfma_f32_32x32x16_bf16 v[128:143], v[172:175], a[32:35], v[128:143]
s_add_u32 m0, s41, 49152
s_add_u32 s76, s61, 2359296
buffer_load_dwordx4 v4, s[48:51], s76 offen lds
v_add_f32_dpp v23, v23, v23 row_ror:8 row_mask:0xf bank_mask:0xf
s_nop 1
v_add_f32_dpp v23, v23, v23 row_ror:4 row_mask:0xf bank_mask:0xf
v_mfma_f32_32x32x16_bf16 v[144:159], v[172:175], a[36:39], v[144:159]
s_add_u32 m0, s41, 53248
s_add_u32 s76, s61, 2506752
buffer_load_dwordx4 v4, s[48:51], s76 offen lds
v_add_f32_dpp v23, v23, v23 row_ror:2 row_mask:0xf bank_mask:0xf
s_waitcnt lgkmcnt(4)
v_mfma_f32_32x32x16_bf16 v[32:47], v[176:179], a[40:43], v[32:47]
ds_read_b64_tr_b16 v[160:161], v10 offset:8192
ds_read_b64_tr_b16 v[162:163], v10 offset:8704
v_add_f32_dpp v23, v23, v23 row_ror:1 row_mask:0xf bank_mask:0xf
v_mov_b32 v223, v23
s_nop 1
v_permlane16_swap_b32 v23, v223
v_mfma_f32_32x32x16_bf16 v[48:63], v[176:179], a[44:47], v[48:63]
ds_read_b64_tr_b16 v[164:165], v11 offset:8192
ds_read_b64_tr_b16 v[166:167], v11 offset:8704
v_add_f32 v23, v23, v223
v_mul_f32 v23, 0xbae38e39, v23
v_mov_b32 v27, 0
v_mfma_f32_32x32x16_bf16 v[64:79], v[180:183], a[40:43], v[64:79]
ds_read_b64_tr_b16 v[168:169], v12 offset:8192
ds_read_b64_tr_b16 v[170:171], v12 offset:8704
v_lshlrev_b32 v223, 16, v20
v_and_b32 v216, 0xffff0000, v20
v_add_f32 v223, v223, v23
v_mfma_f32_32x32x16_bf16 v[80:95], v[180:183], a[44:47], v[80:95]
ds_read_b64_tr_b16 v[172:173], v13 offset:8192
ds_read_b64_tr_b16 v[174:175], v13 offset:8704
v_add_f32 v216, v216, v23
v_fmac_f32 v27, v223, v223
v_fmac_f32 v27, v216, v216
v_mfma_f32_32x32x16_bf16 v[96:111], v[184:187], a[40:43], v[96:111]
ds_read_b64_tr_b16 v[200:201], v14 offset:8192
ds_read_b64_tr_b16 v[204:205], v15 offset:8192
v_lshlrev_b32 v223, 16, v21
v_and_b32 v216, 0xffff0000, v21
v_add_f32 v223, v223, v23
v_mfma_f32_32x32x16_bf16 v[112:127], v[184:187], a[44:47], v[112:127]
s_add_u32 m0, s79, 65536
s_add_u32 s75, s60, 384
buffer_load_dwordx4 v2, s[44:47], s75 offen lds
v_add_f32 v216, v216, v23
v_fmac_f32 v27, v223, v223
v_mfma_f32_32x32x16_bf16 v[128:143], v[188:191], a[40:43], v[128:143]
s_add_u32 m0, s79, 66560
s_add_u32 s75, s60, 9600
buffer_load_dwordx4 v3, s[44:47], s75 offen lds
v_fmac_f32 v27, v216, v216
v_lshlrev_b32 v223, 16, v22
)ASM"
R"ASM(
v_mfma_f32_32x32x16_bf16 v[144:159], v[188:191], a[44:47], v[144:159]
s_add_u32 m0, s79, 67584
s_add_u32 s75, s60, 18816
buffer_load_dwordx4 v2, s[44:47], s75 offen lds
v_and_b32 v216, 0xffff0000, v22
s_waitcnt lgkmcnt(2)
v_mfma_f32_32x32x16_bf16 v[32:47], v[160:163], a[48:51], v[32:47]
ds_read_b64_tr_b16 v[176:177], v10 offset:12288
ds_read_b64_tr_b16 v[178:179], v10 offset:12800
v_add_f32 v223, v223, v23
v_add_f32 v216, v216, v23
v_fmac_f32 v27, v223, v223
v_mfma_f32_32x32x16_bf16 v[48:63], v[160:163], a[52:55], v[48:63]
ds_read_b64_tr_b16 v[180:181], v11 offset:12288
ds_read_b64_tr_b16 v[182:183], v11 offset:12800
v_fmac_f32 v27, v216, v216
v_lshlrev_b32 v223, 16, v24
v_and_b32 v216, 0xffff0000, v24
v_mfma_f32_32x32x16_bf16 v[64:79], v[164:167], a[48:51], v[64:79]
ds_read_b64_tr_b16 v[184:185], v12 offset:12288
ds_read_b64_tr_b16 v[186:187], v12 offset:12800
v_add_f32 v223, v223, v23
v_add_f32 v216, v216, v23
v_fmac_f32 v27, v223, v223
v_mfma_f32_32x32x16_bf16 v[80:95], v[164:167], a[52:55], v[80:95]
ds_read_b64_tr_b16 v[188:189], v13 offset:12288
ds_read_b64_tr_b16 v[190:191], v13 offset:12800
v_fmac_f32 v27, v216, v216
v_lshlrev_b32 v223, 16, v25
v_and_b32 v216, 0xffff0000, v25
v_mfma_f32_16x16x32_bf16 v[208:211], v[192:195], v[28:31], v[208:211]
ds_read_b64_tr_b16 v[202:203], v14 offset:12288
v_mfma_f32_16x16x32_bf16 v[212:215], v[196:199], v[28:31], v[212:215]
ds_read_b64_tr_b16 v[206:207], v15 offset:12288
v_mfma_f32_32x32x16_bf16 v[96:111], v[168:171], a[48:51], v[96:111]
s_add_u32 m0, s79, 68608
s_add_u32 s75, s60, 28032
buffer_load_dwordx4 v3, s[44:47], s75 offen lds
v_add_f32 v223, v223, v23
v_add_f32 v216, v216, v23
v_mfma_f32_32x32x16_bf16 v[112:127], v[168:171], a[52:55], v[112:127]
s_add_u32 m0, s79, 69632
s_add_u32 s75, s60, 37248
buffer_load_dwordx4 v2, s[44:47], s75 offen lds
v_fmac_f32 v27, v223, v223
v_fmac_f32 v27, v216, v216
v_mfma_f32_32x32x16_bf16 v[128:143], v[172:175], a[48:51], v[128:143]
s_add_u32 m0, s79, 70656
s_add_u32 s75, s60, 46464
buffer_load_dwordx4 v3, s[44:47], s75 offen lds
v_lshlrev_b32 v223, 16, v26
v_and_b32 v216, 0xffff0000, v26
v_mfma_f32_32x32x16_bf16 v[144:159], v[172:175], a[52:55], v[144:159]
s_add_u32 m0, s79, 71680
s_add_u32 s75, s60, 55680
buffer_load_dwordx4 v2, s[44:47], s75 offen lds
v_add_f32 v223, v223, v23
s_waitcnt lgkmcnt(2)
v_mfma_f32_32x32x16_bf16 v[32:47], v[176:179], a[56:59], v[32:47]
s_add_u32 m0, s79, 72704
s_add_u32 s75, s60, 64896
buffer_load_dwordx4 v3, s[44:47], s75 offen lds
s_add_u32 m0, s41, 57344
s_add_u32 s76, s61, 2654208
buffer_load_dwordx4 v4, s[48:51], s76 offen lds
s_add_u32 m0, s41, 61440
s_add_u32 s76, s61, 2801664
buffer_load_dwordx4 v4, s[48:51], s76 offen lds
v_mfma_f32_32x32x16_bf16 v[48:63], v[176:179], a[60:63], v[48:63]
s_waitcnt vmcnt(16) lgkmcnt(0)
s_barrier
ds_read_b128 a[64:67], v6 offset:32768
ds_read_b128 a[68:71], v6 offset:36864
v_add_f32 v216, v216, v23
v_mfma_f32_32x32x16_bf16 v[64:79], v[180:183], a[56:59], v[64:79]
ds_read_b64_tr_b16 v[160:161], v10 offset:16384
ds_read_b64_tr_b16 v[162:163], v10 offset:16896
v_fmac_f32 v27, v223, v223
v_fmac_f32 v27, v216, v216
v_lshlrev_b32 v223, 16, v220
v_mfma_f32_32x32x16_bf16 v[80:95], v[180:183], a[60:63], v[80:95]
ds_read_b64_tr_b16 v[164:165], v11 offset:16384
ds_read_b64_tr_b16 v[166:167], v11 offset:16896
v_and_b32 v216, 0xffff0000, v220
v_add_f32 v223, v223, v23
v_add_f32 v216, v216, v23
v_mfma_f32_32x32x16_bf16 v[96:111], v[184:187], a[56:59], v[96:111]
ds_read_b64_tr_b16 v[168:169], v12 offset:16384
ds_read_b64_tr_b16 v[170:171], v12 offset:16896
v_fmac_f32 v27, v223, v223
v_fmac_f32 v27, v216, v216
v_lshlrev_b32 v223, 16, v221
v_mfma_f32_32x32x16_bf16 v[112:127], v[184:187], a[60:63], v[112:127]
ds_read_b64_tr_b16 v[172:173], v13 offset:16384
ds_read_b64_tr_b16 v[174:175], v13 offset:16896
v_and_b32 v216, 0xffff0000, v221
v_add_f32 v223, v223, v23
v_add_f32 v216, v216, v23
v_mfma_f32_32x32x16_bf16 v[128:143], v[188:191], a[56:59], v[128:143]
ds_read_b128 a[72:75], v7 offset:32768
ds_read_b128 a[76:79], v7 offset:36864
v_fmac_f32 v27, v223, v223
v_fmac_f32 v27, v216, v216
v_lshlrev_b32 v223, 16, v222
v_mfma_f32_32x32x16_bf16 v[144:159], v[188:191], a[60:63], v[144:159]
ds_read_b128 a[80:83], v8 offset:32768
ds_read_b128 a[84:87], v8 offset:36864
v_and_b32 v216, 0xffff0000, v222
v_add_f32 v223, v223, v23
s_waitcnt lgkmcnt(4)
v_mfma_f32_32x32x16_bf16 v[32:47], v[160:163], a[64:67], v[32:47]
ds_read_b128 a[88:91], v9 offset:32768
ds_read_b128 a[92:95], v9 offset:36864
v_add_f32 v216, v216, v23
v_fmac_f32 v27, v223, v223
v_fmac_f32 v27, v216, v216
v_mfma_f32_32x32x16_bf16 v[48:63], v[160:163], a[68:71], v[48:63]
ds_read_b64_tr_b16 v[176:177], v10 offset:20480
ds_read_b64_tr_b16 v[178:179], v10 offset:20992
v_add_f32_dpp v27, v27, v27 row_ror:8 row_mask:0xf bank_mask:0xf
s_nop 1
v_add_f32_dpp v27, v27, v27 row_ror:4 row_mask:0xf bank_mask:0xf
s_nop 1
v_add_f32_dpp v27, v27, v27 row_ror:2 row_mask:0xf bank_mask:0xf
v_mfma_f32_32x32x16_bf16 v[64:79], v[164:167], a[64:67], v[64:79]
ds_read_b64_tr_b16 v[180:181], v11 offset:20480
ds_read_b64_tr_b16 v[182:183], v11 offset:20992
v_add_f32_dpp v27, v27, v27 row_ror:1 row_mask:0xf bank_mask:0xf
v_mov_b32 v223, v27
s_nop 1
v_permlane16_swap_b32 v27, v223
v_mfma_f32_32x32x16_bf16 v[80:95], v[164:167], a[68:71], v[80:95]
ds_read_b64_tr_b16 v[184:185], v12 offset:20480
ds_read_b64_tr_b16 v[186:187], v12 offset:20992
v_add_f32 v27, v27, v223
v_mul_f32 v27, 0x3ae38e39, v27
v_add_f32 v22, 0x3727c5ac, v27
v_mfma_f32_16x16x32_bf16 v[208:211], v[200:203], v[28:31], v[208:211]
ds_read_b64_tr_b16 v[188:189], v13 offset:20480
v_mfma_f32_16x16x32_bf16 v[212:215], v[204:207], v[28:31], v[212:215]
ds_read_b64_tr_b16 v[190:191], v13 offset:20992
v_mfma_f32_32x32x16_bf16 v[96:111], v[168:171], a[64:67], v[96:111]
s_waitcnt lgkmcnt(8)
ds_read_b64_tr_b16 v[192:193], v14 offset:16384
ds_read_b64_tr_b16 v[196:197], v15 offset:16384
v_mov_b32 v20, v23
v_add_u32 v219, 1, v219
v_mfma_f32_32x32x16_bf16 v[112:127], v[168:171], a[68:71], v[112:127]
ds_read_b64_tr_b16 v[194:195], v14 offset:20480
ds_read_b64_tr_b16 v[198:199], v15 offset:20480
v_readfirstlane_b32 s43, v219
v_mov_b32 v21, v219
v_mov_b32 v23, v219
v_mfma_f32_32x32x16_bf16 v[128:143], v[172:175], a[64:67], v[128:143]
s_add_u32 m0, s41, 0
s_add_u32 s76, s61, 2949120
buffer_load_dwordx4 v4, s[48:51], s76 offen lds
v_and_b32 v223, 31, v1
v_cmp_eq_u32 vcc, 0, v223
v_mfma_f32_32x32x16_bf16 v[144:159], v[172:175], a[68:71], v[144:159]
s_add_u32 m0, s41, 4096
s_add_u32 s76, s61, 3096576
buffer_load_dwordx4 v4, s[48:51], s76 offen lds
v_lshrrev_b32 v223, 1, v1
s_waitcnt lgkmcnt(4)
v_mfma_f32_32x32x16_bf16 v[32:47], v[176:179], a[72:75], v[32:47]
ds_read_b64_tr_b16 v[160:161], v10 offset:24576
ds_read_b64_tr_b16 v[162:163], v10 offset:25088
v_mov_b32 v216, 0x7f000000
v_cndmask_b32 v223, v216, v223, vcc
v_readlane_b32 s77, v0, 56
v_mfma_f32_32x32x16_bf16 v[48:63], v[176:179], a[76:79], v[48:63]
ds_read_b64_tr_b16 v[164:165], v11 offset:24576
ds_read_b64_tr_b16 v[166:167], v11 offset:25088
s_nop 0
s_lshl_b32 s77, s77, 4
s_add_u32 s77, s77, 49250304
v_mfma_f32_32x32x16_bf16 v[64:79], v[180:183], a[72:75], v[64:79]
ds_read_b64_tr_b16 v[168:169], v12 offset:24576
ds_read_b64_tr_b16 v[170:171], v12 offset:25088
buffer_store_dwordx4 v[20:23], v223, s[96:99], s77 offen sc1
v_readlane_b32 s77, v0, 57
v_mfma_f32_32x32x16_bf16 v[80:95], v[180:183], a[76:79], v[80:95]
ds_read_b64_tr_b16 v[172:173], v13 offset:24576
ds_read_b64_tr_b16 v[174:175], v13 offset:25088
s_nop 3
s_mov_b64 exec, 1
buffer_store_dword v219, off, s[96:99], s77 sc1
s_mov_b64 exec, -1
v_mfma_f32_32x32x16_bf16 v[96:111], v[184:187], a[72:75], v[96:111]
ds_read_b64_tr_b16 v[200:201], v14 offset:24576
ds_read_b64_tr_b16 v[204:205], v15 offset:24576
v_and_b32 v223, 31, v1
v_lshlrev_b32 v223, 1, v223
v_readlane_b32 s77, v0, 54
v_mfma_f32_32x32x16_bf16 v[112:127], v[184:187], a[76:79], v[112:127]
s_add_u32 m0, s79, 98304
s_add_u32 s75, s60, 512
buffer_load_dwordx4 v2, s[44:47], s75 offen lds
s_lshl_b32 s77, s77, 6
)ASM"
R"ASM(
v_readlane_b32 s4, v0, 58
v_mfma_f32_32x32x16_bf16 v[128:143], v[188:191], a[72:75], v[128:143]
s_add_u32 m0, s79, 99328
s_add_u32 s75, s60, 9728
buffer_load_dwordx4 v3, s[44:47], s75 offen lds
v_readlane_b32 s5, v0, 59
s_mov_b32 s6, 1152
v_mfma_f32_32x32x16_bf16 v[144:159], v[188:191], a[76:79], v[144:159]
s_add_u32 m0, s79, 100352
s_add_u32 s75, s60, 18944
buffer_load_dwordx4 v2, s[44:47], s75 offen lds
s_mov_b32 s7, 0x20000
s_waitcnt lgkmcnt(2)
v_mfma_f32_32x32x16_bf16 v[32:47], v[160:163], a[80:83], v[32:47]
ds_read_b64_tr_b16 v[176:177], v10 offset:28672
ds_read_b64_tr_b16 v[178:179], v10 offset:29184
v_readlane_b32 s8, v0, 60
v_readlane_b32 s9, v0, 61
s_mov_b32 s10, 1152
v_mfma_f32_32x32x16_bf16 v[48:63], v[160:163], a[84:87], v[48:63]
ds_read_b64_tr_b16 v[180:181], v11 offset:28672
ds_read_b64_tr_b16 v[182:183], v11 offset:29184
s_mov_b32 s11, 0x20000
v_readlane_b32 s12, v0, 62
v_readlane_b32 s13, v0, 63
v_mfma_f32_32x32x16_bf16 v[64:79], v[164:167], a[80:83], v[64:79]
ds_read_b64_tr_b16 v[184:185], v12 offset:28672
ds_read_b64_tr_b16 v[186:187], v12 offset:29184
s_mov_b32 s14, 1152
s_mov_b32 s15, 0x20000
v_mfma_f32_32x32x16_bf16 v[80:95], v[164:167], a[84:87], v[80:95]
ds_read_b64_tr_b16 v[188:189], v13 offset:28672
ds_read_b64_tr_b16 v[190:191], v13 offset:29184
buffer_load_ushort v24, v223, s[4:7], s77 offen
buffer_load_ushort v25, v223, s[8:11], s77 offen
v_mfma_f32_16x16x32_bf16 v[208:211], v[192:195], v[28:31], v[208:211]
ds_read_b64_tr_b16 v[202:203], v14 offset:28672
v_mfma_f32_16x16x32_bf16 v[212:215], v[196:199], v[28:31], v[212:215]
ds_read_b64_tr_b16 v[206:207], v15 offset:28672
v_mfma_f32_32x32x16_bf16 v[96:111], v[168:171], a[80:83], v[96:111]
s_add_u32 m0, s79, 101376
s_add_u32 s75, s60, 28160
buffer_load_dwordx4 v3, s[44:47], s75 offen lds
buffer_load_ushort v26, v223, s[12:15], s77 offen
v_mfma_f32_32x32x16_bf16 v[112:127], v[168:171], a[84:87], v[112:127]
s_add_u32 m0, s79, 102400
s_add_u32 s75, s60, 37376
buffer_load_dwordx4 v2, s[44:47], s75 offen lds
v_mfma_f32_32x32x16_bf16 v[128:143], v[172:175], a[80:83], v[128:143]
s_add_u32 m0, s79, 103424
s_add_u32 s75, s60, 46592
buffer_load_dwordx4 v3, s[44:47], s75 offen lds
v_mfma_f32_32x32x16_bf16 v[144:159], v[172:175], a[84:87], v[144:159]
s_add_u32 m0, s79, 104448
s_add_u32 s75, s60, 55808
buffer_load_dwordx4 v2, s[44:47], s75 offen lds
s_waitcnt lgkmcnt(2)
v_mfma_f32_32x32x16_bf16 v[32:47], v[176:179], a[88:91], v[32:47]
s_add_u32 m0, s79, 105472
s_add_u32 s75, s60, 65024
buffer_load_dwordx4 v3, s[44:47], s75 offen lds
s_add_u32 m0, s41, 8192
s_add_u32 s76, s61, 3244032
buffer_load_dwordx4 v4, s[48:51], s76 offen lds
s_add_u32 m0, s41, 12288
s_add_u32 s76, s61, 3391488
buffer_load_dwordx4 v4, s[48:51], s76 offen lds
v_mfma_f32_32x32x16_bf16 v[48:63], v[176:179], a[92:95], v[48:63]
s_waitcnt vmcnt(19) lgkmcnt(0)
s_barrier
ds_read_b128 a[96:99], v6 offset:0
ds_read_b128 a[100:103], v6 offset:4096
v_mfma_f32_32x32x16_bf16 v[64:79], v[180:183], a[88:91], v[64:79]
ds_read_b64_tr_b16 v[160:161], v10 offset:32768
ds_read_b64_tr_b16 v[162:163], v10 offset:33280
v_mfma_f32_32x32x16_bf16 v[80:95], v[180:183], a[92:95], v[80:95]
ds_read_b64_tr_b16 v[164:165], v11 offset:32768
ds_read_b64_tr_b16 v[166:167], v11 offset:33280
v_mfma_f32_32x32x16_bf16 v[96:111], v[184:187], a[88:91], v[96:111]
ds_read_b64_tr_b16 v[168:169], v12 offset:32768
ds_read_b64_tr_b16 v[170:171], v12 offset:33280
v_mfma_f32_32x32x16_bf16 v[112:127], v[184:187], a[92:95], v[112:127]
ds_read_b64_tr_b16 v[172:173], v13 offset:32768
ds_read_b64_tr_b16 v[174:175], v13 offset:33280
v_mfma_f32_32x32x16_bf16 v[128:143], v[188:191], a[88:91], v[128:143]
ds_read_b128 a[104:107], v7 offset:0
ds_read_b128 a[108:111], v7 offset:4096
v_mfma_f32_32x32x16_bf16 v[144:159], v[188:191], a[92:95], v[144:159]
ds_read_b128 a[112:115], v8 offset:0
ds_read_b128 a[116:119], v8 offset:4096
s_waitcnt lgkmcnt(4)
v_mfma_f32_32x32x16_bf16 v[32:47], v[160:163], a[96:99], v[32:47]
ds_read_b128 a[120:123], v9 offset:0
ds_read_b128 a[124:127], v9 offset:4096
v_mfma_f32_32x32x16_bf16 v[48:63], v[160:163], a[100:103], v[48:63]
ds_read_b64_tr_b16 v[176:177], v10 offset:36864
ds_read_b64_tr_b16 v[178:179], v10 offset:37376
v_mfma_f32_32x32x16_bf16 v[64:79], v[164:167], a[96:99], v[64:79]
ds_read_b64_tr_b16 v[180:181], v11 offset:36864
ds_read_b64_tr_b16 v[182:183], v11 offset:37376
v_mfma_f32_32x32x16_bf16 v[80:95], v[164:167], a[100:103], v[80:95]
ds_read_b64_tr_b16 v[184:185], v12 offset:36864
ds_read_b64_tr_b16 v[186:187], v12 offset:37376
v_mfma_f32_16x16x32_bf16 v[208:211], v[200:203], v[28:31], v[208:211]
ds_read_b64_tr_b16 v[188:189], v13 offset:36864
v_mfma_f32_16x16x32_bf16 v[212:215], v[204:207], v[28:31], v[212:215]
ds_read_b64_tr_b16 v[190:191], v13 offset:37376
v_mfma_f32_32x32x16_bf16 v[96:111], v[168:171], a[96:99], v[96:111]
s_waitcnt lgkmcnt(8)
ds_read_b64_tr_b16 v[192:193], v14 offset:32768
ds_read_b64_tr_b16 v[196:197], v15 offset:32768
v_mfma_f32_32x32x16_bf16 v[112:127], v[168:171], a[100:103], v[112:127]
ds_read_b64_tr_b16 v[194:195], v14 offset:36864
ds_read_b64_tr_b16 v[198:199], v15 offset:36864
v_mfma_f32_32x32x16_bf16 v[128:143], v[172:175], a[96:99], v[128:143]
s_add_u32 m0, s41, 16384
s_add_u32 s76, s61, 3538944
buffer_load_dwordx4 v4, s[48:51], s76 offen lds
v_mfma_f32_32x32x16_bf16 v[144:159], v[172:175], a[100:103], v[144:159]
s_add_u32 m0, s41, 20480
s_add_u32 s76, s61, 3686400
buffer_load_dwordx4 v4, s[48:51], s76 offen lds
s_waitcnt lgkmcnt(4)
v_mfma_f32_32x32x16_bf16 v[32:47], v[176:179], a[104:107], v[32:47]
ds_read_b64_tr_b16 v[160:161], v10 offset:40960
ds_read_b64_tr_b16 v[162:163], v10 offset:41472
v_mfma_f32_32x32x16_bf16 v[48:63], v[176:179], a[108:111], v[48:63]
ds_read_b64_tr_b16 v[164:165], v11 offset:40960
ds_read_b64_tr_b16 v[166:167], v11 offset:41472
v_mfma_f32_32x32x16_bf16 v[64:79], v[180:183], a[104:107], v[64:79]
ds_read_b64_tr_b16 v[168:169], v12 offset:40960
ds_read_b64_tr_b16 v[170:171], v12 offset:41472
s_waitcnt vmcnt(11)
v_lshlrev_b32 v24, 16, v24
s_waitcnt vmcnt(10)
v_lshlrev_b32 v25, 16, v25
s_waitcnt vmcnt(8)
v_lshlrev_b32 v26, 16, v26
v_mfma_f32_32x32x16_bf16 v[80:95], v[180:183], a[108:111], v[80:95]
ds_read_b64_tr_b16 v[172:173], v13 offset:40960
ds_read_b64_tr_b16 v[174:175], v13 offset:41472
v_mul_f32 v26, 0x3fb8aa3b, v26
v_exp_f32 v26, v26
v_mfma_f32_32x32x16_bf16 v[96:111], v[184:187], a[104:107], v[96:111]
ds_read_b64_tr_b16 v[200:201], v14 offset:40960
ds_read_b64_tr_b16 v[204:205], v15 offset:40960
v_mul_f32 v27, v26, v24
v_mul_f32 v27, 0x3fb8aa3b, v27
v_mul_f32 v216, v26, v25
v_mfma_f32_32x32x16_bf16 v[112:127], v[184:187], a[108:111], v[112:127]
s_add_u32 m0, s79, 65536
s_add_u32 s75, s60, 640
buffer_load_dwordx4 v2, s[44:47], s75 offen lds
v_mul_f32 v216, 0x3e22f983, v216
v_and_b32 v217, 31, v1
v_mfma_f32_32x32x16_bf16 v[128:143], v[188:191], a[104:107], v[128:143]
s_add_u32 m0, s79, 66560
s_add_u32 s75, s60, 9856
buffer_load_dwordx4 v3, s[44:47], s75 offen lds
v_lshlrev_b32 v217, 6, v217
v_add_u32 v217, 150528, v217
v_mfma_f32_32x32x16_bf16 v[144:159], v[188:191], a[108:111], v[144:159]
s_add_u32 m0, s79, 67584
s_add_u32 s75, s60, 19072
buffer_load_dwordx4 v2, s[44:47], s75 offen lds
v_mul_f32 v222, 0x42800000, v27
s_waitcnt lgkmcnt(2)
v_mfma_f32_32x32x16_bf16 v[32:47], v[160:163], a[112:115], v[32:47]
ds_read_b64_tr_b16 v[176:177], v10 offset:45056
ds_read_b64_tr_b16 v[178:179], v10 offset:45568
v_mul_f32 v223, 0x42800000, v216
v_exp_f32 v222, v222
v_mfma_f32_32x32x16_bf16 v[48:63], v[160:163], a[116:119], v[48:63]
ds_read_b64_tr_b16 v[180:181], v11 offset:45056
ds_read_b64_tr_b16 v[182:183], v11 offset:45568
v_fract_f32 v223, v223
v_cos_f32 v20, v223
v_mfma_f32_32x32x16_bf16 v[64:79], v[164:167], a[112:115], v[64:79]
ds_read_b64_tr_b16 v[184:185], v12 offset:45056
ds_read_b64_tr_b16 v[186:187], v12 offset:45568
v_sin_f32 v21, v223
v_mul_f32 v20, v222, v20
v_mfma_f32_32x32x16_bf16 v[80:95], v[164:167], a[116:119], v[80:95]
ds_read_b64_tr_b16 v[188:189], v13 offset:45056
ds_read_b64_tr_b16 v[190:191], v13 offset:45568
v_mul_f32 v21, v222, v21
v_mul_f32 v222, 0x43800000, v27
v_mul_f32 v223, 0x43800000, v216
v_mfma_f32_16x16x32_bf16 v[208:211], v[192:195], v[28:31], v[208:211]
ds_read_b64_tr_b16 v[202:203], v14 offset:45056
v_mfma_f32_16x16x32_bf16 v[212:215], v[196:199], v[28:31], v[212:215]
ds_read_b64_tr_b16 v[206:207], v15 offset:45056
v_mfma_f32_32x32x16_bf16 v[96:111], v[168:171], a[112:115], v[96:111]
s_add_u32 m0, s79, 68608
s_add_u32 s75, s60, 28288
buffer_load_dwordx4 v3, s[44:47], s75 offen lds
v_exp_f32 v222, v222
v_mfma_f32_32x32x16_bf16 v[112:127], v[168:171], a[116:119], v[112:127]
s_add_u32 m0, s79, 69632
s_add_u32 s75, s60, 37504
)ASM"
R"ASM(
buffer_load_dwordx4 v2, s[44:47], s75 offen lds
v_fract_f32 v223, v223
v_mfma_f32_32x32x16_bf16 v[128:143], v[172:175], a[112:115], v[128:143]
s_add_u32 m0, s79, 70656
s_add_u32 s75, s60, 46720
buffer_load_dwordx4 v3, s[44:47], s75 offen lds
v_cos_f32 v22, v223
v_mfma_f32_32x32x16_bf16 v[144:159], v[172:175], a[116:119], v[144:159]
s_add_u32 m0, s79, 71680
s_add_u32 s75, s60, 55936
buffer_load_dwordx4 v2, s[44:47], s75 offen lds
s_waitcnt lgkmcnt(2)
v_mfma_f32_32x32x16_bf16 v[32:47], v[176:179], a[120:123], v[32:47]
v_readlane_b32 s20, v0, 32
v_readlane_b32 s16, v0, 27
v_readlane_b32 s17, v0, 28
s_mov_b32 s18, 663552
s_mov_b32 s19, 0x20000
v_min_u32 v160, 11, v1
v_lshlrev_b32 v160, 7, v160
s_mul_i32 s20, s20, 1536
s_add_u32 s20, s20, 0
s_mov_b32 m0, 154624
s_mov_b64 exec, s[24:25]
buffer_load_dword v160, s[16:19], s20 offen lds
s_mov_b64 exec, -1
s_add_u32 m0, s79, 72704
s_add_u32 s75, s60, 65152
buffer_load_dwordx4 v3, s[44:47], s75 offen lds
s_add_u32 m0, s41, 24576
s_add_u32 s76, s61, 3833856
buffer_load_dwordx4 v4, s[48:51], s76 offen lds
s_add_u32 m0, s41, 28672
s_add_u32 s76, s61, 3981312
buffer_load_dwordx4 v4, s[48:51], s76 offen lds
v_mfma_f32_32x32x16_bf16 v[48:63], v[176:179], a[124:127], v[48:63]
s_waitcnt vmcnt(14) lgkmcnt(0)
s_barrier
ds_read_b128 a[128:131], v6 offset:32768
ds_read_b128 a[132:135], v6 offset:36864
v_mfma_f32_32x32x16_bf16 v[64:79], v[180:183], a[120:123], v[64:79]
ds_read_b64_tr_b16 v[160:161], v10 offset:49152
ds_read_b64_tr_b16 v[162:163], v10 offset:49664
v_sin_f32 v23, v223
v_mul_f32 v22, v222, v22
v_mfma_f32_32x32x16_bf16 v[80:95], v[180:183], a[124:127], v[80:95]
ds_read_b64_tr_b16 v[164:165], v11 offset:49152
ds_read_b64_tr_b16 v[166:167], v11 offset:49664
v_mul_f32 v23, v222, v23
v_mfma_f32_32x32x16_bf16 v[96:111], v[184:187], a[120:123], v[96:111]
ds_read_b64_tr_b16 v[168:169], v12 offset:49152
ds_read_b64_tr_b16 v[170:171], v12 offset:49664
v_mfma_f32_32x32x16_bf16 v[112:127], v[184:187], a[124:127], v[112:127]
ds_read_b64_tr_b16 v[172:173], v13 offset:49152
ds_read_b64_tr_b16 v[174:175], v13 offset:49664
v_mfma_f32_32x32x16_bf16 v[128:143], v[188:191], a[120:123], v[128:143]
ds_read_b128 a[136:139], v7 offset:32768
ds_read_b128 a[140:143], v7 offset:36864
v_mfma_f32_32x32x16_bf16 v[144:159], v[188:191], a[124:127], v[144:159]
ds_read_b128 a[144:147], v8 offset:32768
ds_read_b128 a[148:151], v8 offset:36864
s_waitcnt lgkmcnt(4)
v_mfma_f32_32x32x16_bf16 v[32:47], v[160:163], a[128:131], v[32:47]
v_readlane_b32 s20, v0, 32
v_readlane_b32 s16, v0, 29
v_readlane_b32 s17, v0, 30
s_mov_b32 s18, 663552
s_mov_b32 s19, 0x20000
v_min_u32 v176, 11, v1
v_lshlrev_b32 v176, 7, v176
s_mul_i32 s20, s20, 1536
s_add_u32 s20, s20, 0
s_mov_b32 m0, 154624
s_mov_b64 exec, s[24:25]
buffer_load_dword v176, s[16:19], s20 offen lds
s_mov_b64 exec, -1
ds_read_b128 a[152:155], v9 offset:32768
ds_read_b128 a[156:159], v9 offset:36864
v_mfma_f32_32x32x16_bf16 v[48:63], v[160:163], a[132:135], v[48:63]
ds_read_b64_tr_b16 v[176:177], v10 offset:53248
ds_read_b64_tr_b16 v[178:179], v10 offset:53760
v_mfma_f32_32x32x16_bf16 v[64:79], v[164:167], a[128:131], v[64:79]
ds_read_b64_tr_b16 v[180:181], v11 offset:53248
ds_read_b64_tr_b16 v[182:183], v11 offset:53760
v_mfma_f32_32x32x16_bf16 v[80:95], v[164:167], a[132:135], v[80:95]
ds_read_b64_tr_b16 v[184:185], v12 offset:53248
ds_read_b64_tr_b16 v[186:187], v12 offset:53760
v_mfma_f32_16x16x32_bf16 v[208:211], v[200:203], v[28:31], v[208:211]
ds_read_b64_tr_b16 v[188:189], v13 offset:53248
v_mfma_f32_16x16x32_bf16 v[212:215], v[204:207], v[28:31], v[212:215]
ds_read_b64_tr_b16 v[190:191], v13 offset:53760
v_mfma_f32_32x32x16_bf16 v[96:111], v[168:171], a[128:131], v[96:111]
s_waitcnt lgkmcnt(8)
ds_read_b64_tr_b16 v[192:193], v14 offset:49152
ds_read_b64_tr_b16 v[196:197], v15 offset:49152
v_mfma_f32_32x32x16_bf16 v[112:127], v[168:171], a[132:135], v[112:127]
ds_read_b64_tr_b16 v[194:195], v14 offset:53248
ds_read_b64_tr_b16 v[198:199], v15 offset:53248
v_mfma_f32_32x32x16_bf16 v[128:143], v[172:175], a[128:131], v[128:143]
s_add_u32 m0, s41, 32768
s_add_u32 s76, s61, 4128768
buffer_load_dwordx4 v4, s[48:51], s76 offen lds
ds_write_b128 v217, v[20:23] offset:32
v_mul_f32 v222, 0x3f800000, v27
v_mfma_f32_32x32x16_bf16 v[144:159], v[172:175], a[132:135], v[144:159]
s_add_u32 m0, s41, 36864
s_add_u32 s76, s61, 4276224
buffer_load_dwordx4 v4, s[48:51], s76 offen lds
v_mul_f32 v223, 0x3f800000, v216
s_waitcnt lgkmcnt(5)
v_mfma_f32_32x32x16_bf16 v[32:47], v[176:179], a[136:139], v[32:47]
v_readlane_b32 s20, v0, 31
v_readlane_b32 s21, v0, 55
v_readlane_b32 s22, v0, 52
s_nop 1
s_lshr_b32 s20, s20, 1
s_mul_i32 s20, s20, 12
s_add_u32 s20, s20, 0
s_cmp_lt_u32 s20, 72
s_cselect_b32 s22, s22, 1073741824
s_mul_i32 s21, s21, 72
s_add_u32 s20, s20, s21
s_mul_i32 s20, s20, 9216
s_add_u32 s20, s20, s22
v_min_u32 v160, 53, v1
v_mul_u32_u24 v161, 57, v160
v_lshrrev_b32 v161, 10, v161
v_lshlrev_b32 v160, 7, v160
v_mad_u32_u24 v160, v161, s23, v160
s_mov_b32 m0, 154624
s_mov_b64 exec, s[24:25]
buffer_load_dword v160, s[48:51], s20 offen lds
s_mov_b64 exec, -1
ds_read_b64_tr_b16 v[160:161], v10 offset:57344
ds_read_b64_tr_b16 v[162:163], v10 offset:57856
v_exp_f32 v222, v222
v_fract_f32 v223, v223
v_mfma_f32_32x32x16_bf16 v[48:63], v[176:179], a[140:143], v[48:63]
ds_read_b64_tr_b16 v[164:165], v11 offset:57344
ds_read_b64_tr_b16 v[166:167], v11 offset:57856
v_cos_f32 v20, v223
v_mfma_f32_32x32x16_bf16 v[64:79], v[180:183], a[136:139], v[64:79]
ds_read_b64_tr_b16 v[168:169], v12 offset:57344
ds_read_b64_tr_b16 v[170:171], v12 offset:57856
v_sin_f32 v21, v223
v_mul_f32 v20, v222, v20
v_mfma_f32_32x32x16_bf16 v[80:95], v[180:183], a[140:143], v[80:95]
ds_read_b64_tr_b16 v[172:173], v13 offset:57344
ds_read_b64_tr_b16 v[174:175], v13 offset:57856
v_mul_f32 v21, v222, v21
v_mul_f32 v222, 0x40800000, v27
v_mul_f32 v223, 0x40800000, v216
v_mfma_f32_32x32x16_bf16 v[96:111], v[184:187], a[136:139], v[96:111]
ds_read_b64_tr_b16 v[200:201], v14 offset:57344
ds_read_b64_tr_b16 v[204:205], v15 offset:57344
v_exp_f32 v222, v222
v_fract_f32 v223, v223
v_mfma_f32_32x32x16_bf16 v[112:127], v[184:187], a[140:143], v[112:127]
s_add_u32 m0, s79, 98304
s_add_u32 s75, s60, 768
buffer_load_dwordx4 v2, s[44:47], s75 offen lds
v_cos_f32 v22, v223
v_mfma_f32_32x32x16_bf16 v[128:143], v[188:191], a[136:139], v[128:143]
s_add_u32 m0, s79, 99328
s_add_u32 s75, s60, 9984
buffer_load_dwordx4 v3, s[44:47], s75 offen lds
v_sin_f32 v23, v223
v_mfma_f32_32x32x16_bf16 v[144:159], v[188:191], a[140:143], v[144:159]
s_add_u32 m0, s79, 100352
s_add_u32 s75, s60, 19200
buffer_load_dwordx4 v2, s[44:47], s75 offen lds
v_mul_f32 v22, v222, v22
s_waitcnt lgkmcnt(2)
v_mfma_f32_32x32x16_bf16 v[32:47], v[160:163], a[144:147], v[32:47]
ds_read_b64_tr_b16 v[176:177], v10 offset:61440
ds_read_b64_tr_b16 v[178:179], v10 offset:61952
v_mul_f32 v23, v222, v23
v_mfma_f32_32x32x16_bf16 v[48:63], v[160:163], a[148:151], v[48:63]
ds_read_b64_tr_b16 v[180:181], v11 offset:61440
ds_read_b64_tr_b16 v[182:183], v11 offset:61952
v_mfma_f32_32x32x16_bf16 v[64:79], v[164:167], a[144:147], v[64:79]
ds_read_b64_tr_b16 v[184:185], v12 offset:61440
ds_read_b64_tr_b16 v[186:187], v12 offset:61952
v_mfma_f32_32x32x16_bf16 v[80:95], v[164:167], a[148:151], v[80:95]
ds_read_b64_tr_b16 v[188:189], v13 offset:61440
ds_read_b64_tr_b16 v[190:191], v13 offset:61952
v_mfma_f32_16x16x32_bf16 v[208:211], v[192:195], v[28:31], v[208:211]
ds_read_b64_tr_b16 v[202:203], v14 offset:61440
v_mfma_f32_16x16x32_bf16 v[212:215], v[196:199], v[28:31], v[212:215]
ds_read_b64_tr_b16 v[206:207], v15 offset:61440
v_mfma_f32_32x32x16_bf16 v[96:111], v[168:171], a[144:147], v[96:111]
s_add_u32 m0, s79, 101376
s_add_u32 s75, s60, 28416
buffer_load_dwordx4 v3, s[44:47], s75 offen lds
ds_write_b128 v217, v[20:23]
v_add_f32 v220, -1.0, v20
v_mfma_f32_32x32x16_bf16 v[112:127], v[168:171], a[148:151], v[112:127]
s_add_u32 m0, s79, 102400
s_add_u32 s75, s60, 37632
buffer_load_dwordx4 v2, s[44:47], s75 offen lds
)ASM"
R"ASM(
v_mov_b32 v221, v21
v_mul_f32 v26, v24, v24
v_mfma_f32_32x32x16_bf16 v[128:143], v[172:175], a[144:147], v[128:143]
s_add_u32 m0, s79, 103424
s_add_u32 s75, s60, 46848
buffer_load_dwordx4 v3, s[44:47], s75 offen lds
v_fmac_f32 v26, v25, v25
v_mfma_f32_32x32x16_bf16 v[144:159], v[172:175], a[148:151], v[144:159]
s_add_u32 m0, s79, 104448
s_add_u32 s75, s60, 56064
buffer_load_dwordx4 v2, s[44:47], s75 offen lds
s_waitcnt lgkmcnt(3)
v_mfma_f32_32x32x16_bf16 v[32:47], v[176:179], a[152:155], v[32:47]
v_readlane_b32 s20, v0, 31
v_readlane_b32 s21, v0, 55
v_readlane_b32 s22, v0, 52
s_nop 1
s_lshr_b32 s20, s20, 1
s_mul_i32 s20, s20, 12
s_add_u32 s20, s20, 3
s_cmp_lt_u32 s20, 72
s_cselect_b32 s22, s22, 1073741824
s_mul_i32 s21, s21, 72
s_add_u32 s20, s20, s21
s_mul_i32 s20, s20, 9216
s_add_u32 s20, s20, s22
v_min_u32 v160, 53, v1
v_mul_u32_u24 v161, 57, v160
v_lshrrev_b32 v161, 10, v161
v_lshlrev_b32 v160, 7, v160
v_mad_u32_u24 v160, v161, s23, v160
s_mov_b32 m0, 154624
s_mov_b64 exec, s[24:25]
buffer_load_dword v160, s[48:51], s20 offen lds
s_mov_b64 exec, -1
s_add_u32 m0, s79, 105472
s_add_u32 s75, s60, 65280
buffer_load_dwordx4 v3, s[44:47], s75 offen lds
s_add_u32 m0, s41, 40960
s_add_u32 s76, s61, 4423680
buffer_load_dwordx4 v4, s[48:51], s76 offen lds
s_add_u32 m0, s41, 45056
s_add_u32 s76, s61, 4571136
buffer_load_dwordx4 v4, s[48:51], s76 offen lds
v_mfma_f32_32x32x16_bf16 v[48:63], v[176:179], a[156:159], v[48:63]
s_waitcnt vmcnt(14) lgkmcnt(0)
s_barrier
ds_read_b128 a[160:163], v6 offset:0
ds_read_b128 a[164:167], v6 offset:4096
v_mfma_f32_32x32x16_bf16 v[64:79], v[180:183], a[152:155], v[64:79]
ds_read_b64_tr_b16 v[160:161], v10 offset:0
ds_read_b64_tr_b16 v[162:163], v10 offset:512
v_rcp_f32 v26, v26
v_mul_f32 v222, 0x40a00000, v27
v_mfma_f32_32x32x16_bf16 v[80:95], v[180:183], a[156:159], v[80:95]
ds_read_b64_tr_b16 v[164:165], v11 offset:0
ds_read_b64_tr_b16 v[166:167], v11 offset:512
v_mul_f32 v223, 0x40a00000, v216
v_exp_f32 v222, v222
v_mfma_f32_32x32x16_bf16 v[96:111], v[184:187], a[152:155], v[96:111]
ds_read_b64_tr_b16 v[168:169], v12 offset:0
ds_read_b64_tr_b16 v[170:171], v12 offset:512
v_fract_f32 v223, v223
v_cos_f32 v22, v223
v_mfma_f32_32x32x16_bf16 v[112:127], v[184:187], a[156:159], v[112:127]
ds_read_b64_tr_b16 v[172:173], v13 offset:0
ds_read_b64_tr_b16 v[174:175], v13 offset:512
v_sin_f32 v23, v223
v_mul_f32 v22, v222, v22
v_mfma_f32_32x32x16_bf16 v[128:143], v[188:191], a[152:155], v[128:143]
ds_read_b128 a[168:171], v7 offset:0
ds_read_b128 a[172:175], v7 offset:4096
v_mul_f32 v23, v222, v23
v_add_f32 v26, v26, v26
v_mul_f32 v20, v220, v24
v_mfma_f32_32x32x16_bf16 v[144:159], v[188:191], a[156:159], v[144:159]
ds_read_b128 a[176:179], v8 offset:0
ds_read_b128 a[180:183], v8 offset:4096
v_fmac_f32 v20, v221, v25
v_mul_f32 v21, v221, v24
s_waitcnt lgkmcnt(4)
v_mfma_f32_32x32x16_bf16 v[32:47], v[160:163], a[160:163], v[32:47]
ds_read_b128 a[184:187], v9 offset:0
ds_read_b128 a[188:191], v9 offset:4096
v_fma_f32 v21, -v220, v25, v21
v_mul_f32 v20, v20, v26
v_mul_f32 v21, v21, v26
v_mfma_f32_32x32x16_bf16 v[48:63], v[160:163], a[164:167], v[48:63]
ds_read_b64_tr_b16 v[176:177], v10 offset:4096
ds_read_b64_tr_b16 v[178:179], v10 offset:4608
v_mfma_f32_32x32x16_bf16 v[64:79], v[164:167], a[160:163], v[64:79]
ds_read_b64_tr_b16 v[180:181], v11 offset:4096
ds_read_b64_tr_b16 v[182:183], v11 offset:4608
v_mfma_f32_32x32x16_bf16 v[80:95], v[164:167], a[164:167], v[80:95]
ds_read_b64_tr_b16 v[184:185], v12 offset:4096
ds_read_b64_tr_b16 v[186:187], v12 offset:4608
v_mfma_f32_16x16x32_bf16 v[208:211], v[200:203], v[28:31], v[208:211]
ds_read_b64_tr_b16 v[188:189], v13 offset:4096
v_mfma_f32_16x16x32_bf16 v[212:215], v[204:207], v[28:31], v[212:215]
ds_read_b64_tr_b16 v[190:191], v13 offset:4608
v_mfma_f32_32x32x16_bf16 v[96:111], v[168:171], a[160:163], v[96:111]
s_waitcnt lgkmcnt(8)
ds_read_b64_tr_b16 v[192:193], v14 offset:0
ds_read_b64_tr_b16 v[196:197], v15 offset:0
v_mfma_f32_32x32x16_bf16 v[112:127], v[168:171], a[164:167], v[112:127]
ds_read_b64_tr_b16 v[194:195], v14 offset:4096
ds_read_b64_tr_b16 v[198:199], v15 offset:4096
v_mfma_f32_32x32x16_bf16 v[128:143], v[172:175], a[160:163], v[128:143]
s_add_u32 m0, s41, 49152
s_add_u32 s76, s61, 4718592
buffer_load_dwordx4 v4, s[48:51], s76 offen lds
ds_write_b128 v217, v[20:23] offset:16
v_mfma_f32_32x32x16_bf16 v[144:159], v[172:175], a[164:167], v[144:159]
s_add_u32 m0, s41, 53248
s_add_u32 s76, s61, 4866048
buffer_load_dwordx4 v4, s[48:51], s76 offen lds
s_waitcnt lgkmcnt(5)
v_mfma_f32_32x32x16_bf16 v[32:47], v[176:179], a[168:171], v[32:47]
v_readlane_b32 s20, v0, 31
v_readlane_b32 s21, v0, 55
v_readlane_b32 s22, v0, 52
s_nop 1
s_lshr_b32 s20, s20, 1
s_mul_i32 s20, s20, 12
s_add_u32 s20, s20, 6
s_cmp_lt_u32 s20, 72
s_cselect_b32 s22, s22, 1073741824
s_mul_i32 s21, s21, 72
s_add_u32 s20, s20, s21
s_mul_i32 s20, s20, 9216
s_add_u32 s20, s20, s22
v_min_u32 v160, 53, v1
v_mul_u32_u24 v161, 57, v160
v_lshrrev_b32 v161, 10, v161
v_lshlrev_b32 v160, 7, v160
v_mad_u32_u24 v160, v161, s23, v160
s_mov_b32 m0, 154624
s_mov_b64 exec, s[24:25]
buffer_load_dword v160, s[48:51], s20 offen lds
s_mov_b64 exec, -1
ds_read_b64_tr_b16 v[160:161], v10 offset:8192
ds_read_b64_tr_b16 v[162:163], v10 offset:8704
v_mfma_f32_32x32x16_bf16 v[48:63], v[176:179], a[172:175], v[48:63]
ds_read_b64_tr_b16 v[164:165], v11 offset:8192
ds_read_b64_tr_b16 v[166:167], v11 offset:8704
v_mfma_f32_32x32x16_bf16 v[64:79], v[180:183], a[168:171], v[64:79]
ds_read_b64_tr_b16 v[168:169], v12 offset:8192
ds_read_b64_tr_b16 v[170:171], v12 offset:8704
v_mfma_f32_32x32x16_bf16 v[80:95], v[180:183], a[172:175], v[80:95]
ds_read_b64_tr_b16 v[172:173], v13 offset:8192
ds_read_b64_tr_b16 v[174:175], v13 offset:8704
v_mfma_f32_32x32x16_bf16 v[96:111], v[184:187], a[168:171], v[96:111]
ds_read_b64_tr_b16 v[200:201], v14 offset:8192
ds_read_b64_tr_b16 v[204:205], v15 offset:8192
v_mfma_f32_32x32x16_bf16 v[112:127], v[184:187], a[172:175], v[112:127]
s_add_u32 m0, s79, 65536
s_add_u32 s75, s60, 896
buffer_load_dwordx4 v2, s[44:47], s75 offen lds
v_mfma_f32_32x32x16_bf16 v[128:143], v[188:191], a[168:171], v[128:143]
s_add_u32 m0, s79, 66560
s_add_u32 s75, s60, 10112
buffer_load_dwordx4 v3, s[44:47], s75 offen lds
v_mfma_f32_32x32x16_bf16 v[144:159], v[188:191], a[172:175], v[144:159]
s_add_u32 m0, s79, 67584
s_add_u32 s75, s60, 19328
buffer_load_dwordx4 v2, s[44:47], s75 offen lds
s_waitcnt lgkmcnt(2)
v_mfma_f32_32x32x16_bf16 v[32:47], v[160:163], a[176:179], v[32:47]
ds_read_b64_tr_b16 v[176:177], v10 offset:12288
ds_read_b64_tr_b16 v[178:179], v10 offset:12800
v_mfma_f32_32x32x16_bf16 v[48:63], v[160:163], a[180:183], v[48:63]
ds_read_b64_tr_b16 v[180:181], v11 offset:12288
ds_read_b64_tr_b16 v[182:183], v11 offset:12800
v_mfma_f32_32x32x16_bf16 v[64:79], v[164:167], a[176:179], v[64:79]
ds_read_b64_tr_b16 v[184:185], v12 offset:12288
ds_read_b64_tr_b16 v[186:187], v12 offset:12800
v_mfma_f32_32x32x16_bf16 v[80:95], v[164:167], a[180:183], v[80:95]
ds_read_b64_tr_b16 v[188:189], v13 offset:12288
ds_read_b64_tr_b16 v[190:191], v13 offset:12800
v_mfma_f32_16x16x32_bf16 v[208:211], v[192:195], v[28:31], v[208:211]
ds_read_b64_tr_b16 v[202:203], v14 offset:12288
v_mfma_f32_16x16x32_bf16 v[212:215], v[196:199], v[28:31], v[212:215]
ds_read_b64_tr_b16 v[206:207], v15 offset:12288
v_mfma_f32_32x32x16_bf16 v[96:111], v[168:171], a[176:179], v[96:111]
s_add_u32 m0, s79, 68608
s_add_u32 s75, s60, 28544
buffer_load_dwordx4 v3, s[44:47], s75 offen lds
v_mfma_f32_32x32x16_bf16 v[112:127], v[168:171], a[180:183], v[112:127]
s_add_u32 m0, s79, 69632
s_add_u32 s75, s60, 37760
buffer_load_dwordx4 v2, s[44:47], s75 offen lds
v_mfma_f32_32x32x16_bf16 v[128:143], v[172:175], a[176:179], v[128:143]
s_add_u32 m0, s79, 70656
s_add_u32 s75, s60, 46976
buffer_load_dwordx4 v3, s[44:47], s75 offen lds
v_mfma_f32_32x32x16_bf16 v[144:159], v[172:175], a[180:183], v[144:159]
s_add_u32 m0, s79, 71680
s_add_u32 s75, s60, 56192
buffer_load_dwordx4 v2, s[44:47], s75 offen lds
s_waitcnt lgkmcnt(2)
)ASM"
R"ASM(
v_mfma_f32_32x32x16_bf16 v[32:47], v[176:179], a[184:187], v[32:47]
v_readlane_b32 s20, v0, 31
v_readlane_b32 s21, v0, 55
v_readlane_b32 s22, v0, 52
s_nop 1
s_lshr_b32 s20, s20, 1
s_mul_i32 s20, s20, 12
s_add_u32 s20, s20, 9
s_cmp_lt_u32 s20, 72
s_cselect_b32 s22, s22, 1073741824
s_mul_i32 s21, s21, 72
s_add_u32 s20, s20, s21
s_mul_i32 s20, s20, 9216
s_add_u32 s20, s20, s22
v_min_u32 v160, 53, v1
v_mul_u32_u24 v161, 57, v160
v_lshrrev_b32 v161, 10, v161
v_lshlrev_b32 v160, 7, v160
v_mad_u32_u24 v160, v161, s23, v160
s_mov_b32 m0, 154624
s_mov_b64 exec, s[24:25]
buffer_load_dword v160, s[48:51], s20 offen lds
s_mov_b64 exec, -1
s_add_u32 m0, s79, 72704
s_add_u32 s75, s60, 65408
buffer_load_dwordx4 v3, s[44:47], s75 offen lds
s_add_u32 m0, s41, 57344
s_add_u32 s76, s61, 5013504
buffer_load_dwordx4 v4, s[48:51], s76 offen lds
s_add_u32 m0, s41, 61440
s_add_u32 s76, s61, 5160960
buffer_load_dwordx4 v4, s[48:51], s76 offen lds
v_mfma_f32_32x32x16_bf16 v[48:63], v[176:179], a[188:191], v[48:63]
s_waitcnt vmcnt(14) lgkmcnt(0)
s_barrier
ds_read_b128 a[192:195], v6 offset:32768
ds_read_b128 a[196:199], v6 offset:36864
v_mfma_f32_32x32x16_bf16 v[64:79], v[180:183], a[184:187], v[64:79]
ds_read_b64_tr_b16 v[160:161], v10 offset:16384
ds_read_b64_tr_b16 v[162:163], v10 offset:16896
v_mfma_f32_32x32x16_bf16 v[80:95], v[180:183], a[188:191], v[80:95]
ds_read_b64_tr_b16 v[164:165], v11 offset:16384
ds_read_b64_tr_b16 v[166:167], v11 offset:16896
v_mfma_f32_32x32x16_bf16 v[96:111], v[184:187], a[184:187], v[96:111]
ds_read_b64_tr_b16 v[168:169], v12 offset:16384
ds_read_b64_tr_b16 v[170:171], v12 offset:16896
v_mfma_f32_32x32x16_bf16 v[112:127], v[184:187], a[188:191], v[112:127]
ds_read_b64_tr_b16 v[172:173], v13 offset:16384
ds_read_b64_tr_b16 v[174:175], v13 offset:16896
v_mfma_f32_32x32x16_bf16 v[128:143], v[188:191], a[184:187], v[128:143]
ds_read_b128 a[200:203], v7 offset:32768
ds_read_b128 a[204:207], v7 offset:36864
v_mfma_f32_32x32x16_bf16 v[144:159], v[188:191], a[188:191], v[144:159]
ds_read_b128 a[208:211], v8 offset:32768
ds_read_b128 a[212:215], v8 offset:36864
s_waitcnt lgkmcnt(4)
v_mfma_f32_32x32x16_bf16 v[32:47], v[160:163], a[192:195], v[32:47]
ds_read_b128 a[216:219], v9 offset:32768
ds_read_b128 a[220:223], v9 offset:36864
v_mfma_f32_32x32x16_bf16 v[48:63], v[160:163], a[196:199], v[48:63]
ds_read_b64_tr_b16 v[176:177], v10 offset:20480
ds_read_b64_tr_b16 v[178:179], v10 offset:20992
v_mfma_f32_32x32x16_bf16 v[64:79], v[164:167], a[192:195], v[64:79]
ds_read_b64_tr_b16 v[180:181], v11 offset:20480
ds_read_b64_tr_b16 v[182:183], v11 offset:20992
v_mfma_f32_32x32x16_bf16 v[80:95], v[164:167], a[196:199], v[80:95]
ds_read_b64_tr_b16 v[184:185], v12 offset:20480
ds_read_b64_tr_b16 v[186:187], v12 offset:20992
v_mfma_f32_16x16x32_bf16 v[208:211], v[200:203], v[28:31], v[208:211]
ds_read_b64_tr_b16 v[188:189], v13 offset:20480
v_mfma_f32_16x16x32_bf16 v[212:215], v[204:207], v[28:31], v[212:215]
ds_read_b64_tr_b16 v[190:191], v13 offset:20992
v_mfma_f32_32x32x16_bf16 v[96:111], v[168:171], a[192:195], v[96:111]
s_waitcnt lgkmcnt(8)
ds_read_b64_tr_b16 v[192:193], v14 offset:16384
ds_read_b64_tr_b16 v[196:197], v15 offset:16384
v_mfma_f32_32x32x16_bf16 v[112:127], v[168:171], a[196:199], v[112:127]
ds_read_b64_tr_b16 v[194:195], v14 offset:20480
ds_read_b64_tr_b16 v[198:199], v15 offset:20480
v_mfma_f32_32x32x16_bf16 v[128:143], v[172:175], a[192:195], v[128:143]
s_add_u32 m0, s41, 131072
s_mov_b32 s62, s63
buffer_load_dwordx4 v5, s[84:87], s62 offen lds
v_mfma_f32_32x32x16_bf16 v[144:159], v[172:175], a[196:199], v[144:159]
s_add_u32 m0, s41, 135168
s_add_u32 s62, s62, s93
buffer_load_dwordx4 v5, s[88:91], s62 offen lds
s_waitcnt lgkmcnt(4)
v_mfma_f32_32x32x16_bf16 v[32:47], v[176:179], a[200:203], v[32:47]
ds_read_b64_tr_b16 v[160:161], v10 offset:24576
ds_read_b64_tr_b16 v[162:163], v10 offset:25088
v_mfma_f32_32x32x16_bf16 v[48:63], v[176:179], a[204:207], v[48:63]
ds_read_b64_tr_b16 v[164:165], v11 offset:24576
ds_read_b64_tr_b16 v[166:167], v11 offset:25088
v_mfma_f32_32x32x16_bf16 v[64:79], v[180:183], a[200:203], v[64:79]
ds_read_b64_tr_b16 v[168:169], v12 offset:24576
ds_read_b64_tr_b16 v[170:171], v12 offset:25088
v_mfma_f32_32x32x16_bf16 v[80:95], v[180:183], a[204:207], v[80:95]
ds_read_b64_tr_b16 v[172:173], v13 offset:24576
ds_read_b64_tr_b16 v[174:175], v13 offset:25088
v_mfma_f32_32x32x16_bf16 v[96:111], v[184:187], a[200:203], v[96:111]
ds_read_b64_tr_b16 v[200:201], v14 offset:24576
ds_read_b64_tr_b16 v[204:205], v15 offset:24576
v_mfma_f32_32x32x16_bf16 v[112:127], v[184:187], a[204:207], v[112:127]
s_add_u32 m0, s79, 98304
s_add_u32 s75, s60, 1024
buffer_load_dwordx4 v2, s[44:47], s75 offen lds
v_mfma_f32_32x32x16_bf16 v[128:143], v[188:191], a[200:203], v[128:143]
s_add_u32 m0, s79, 99328
s_add_u32 s75, s60, 10240
buffer_load_dwordx4 v3, s[44:47], s75 offen lds
v_mfma_f32_32x32x16_bf16 v[144:159], v[188:191], a[204:207], v[144:159]
s_add_u32 m0, s79, 100352
s_add_u32 s75, s60, 19456
buffer_load_dwordx4 v2, s[44:47], s75 offen lds
s_waitcnt lgkmcnt(2)
v_mfma_f32_32x32x16_bf16 v[32:47], v[160:163], a[208:211], v[32:47]
v_readlane_b32 s20, v0, 32
v_readlane_b32 s16, v0, 0
v_readlane_b32 s17, v0, 1
s_mov_b32 s18, 5310464
s_mov_b32 s19, 0x20000
v_min_u32 v176, 46, v1
v_lshlrev_b32 v176, 7, v176
s_mul_i32 s20, s20, 6016
s_add_u32 s20, s20, 2048
s_mov_b32 m0, 154624
s_mov_b64 exec, s[24:25]
buffer_load_dword v176, s[16:19], s20 offen lds
s_mov_b64 exec, -1
ds_read_b64_tr_b16 v[176:177], v10 offset:28672
ds_read_b64_tr_b16 v[178:179], v10 offset:29184
v_mfma_f32_32x32x16_bf16 v[48:63], v[160:163], a[212:215], v[48:63]
ds_read_b64_tr_b16 v[180:181], v11 offset:28672
ds_read_b64_tr_b16 v[182:183], v11 offset:29184
v_mfma_f32_32x32x16_bf16 v[64:79], v[164:167], a[208:211], v[64:79]
ds_read_b64_tr_b16 v[184:185], v12 offset:28672
ds_read_b64_tr_b16 v[186:187], v12 offset:29184
v_mfma_f32_32x32x16_bf16 v[80:95], v[164:167], a[212:215], v[80:95]
ds_read_b64_tr_b16 v[188:189], v13 offset:28672
ds_read_b64_tr_b16 v[190:191], v13 offset:29184
v_mfma_f32_16x16x32_bf16 v[208:211], v[192:195], v[28:31], v[208:211]
ds_read_b64_tr_b16 v[202:203], v14 offset:28672
v_mfma_f32_16x16x32_bf16 v[212:215], v[196:199], v[28:31], v[212:215]
ds_read_b64_tr_b16 v[206:207], v15 offset:28672
v_mfma_f32_32x32x16_bf16 v[96:111], v[168:171], a[208:211], v[96:111]
s_add_u32 m0, s79, 101376
s_add_u32 s75, s60, 28672
buffer_load_dwordx4 v3, s[44:47], s75 offen lds
v_mfma_f32_32x32x16_bf16 v[112:127], v[168:171], a[212:215], v[112:127]
s_add_u32 m0, s79, 102400
s_add_u32 s75, s60, 37888
buffer_load_dwordx4 v2, s[44:47], s75 offen lds
v_mfma_f32_32x32x16_bf16 v[128:143], v[172:175], a[208:211], v[128:143]
s_add_u32 m0, s79, 103424
s_add_u32 s75, s60, 47104
buffer_load_dwordx4 v3, s[44:47], s75 offen lds
v_mfma_f32_32x32x16_bf16 v[144:159], v[172:175], a[212:215], v[144:159]
s_add_u32 m0, s79, 104448
s_add_u32 s75, s60, 56320
buffer_load_dwordx4 v2, s[44:47], s75 offen lds
s_waitcnt lgkmcnt(2)
v_mfma_f32_32x32x16_bf16 v[32:47], v[176:179], a[216:219], v[32:47]
s_add_u32 m0, s79, 105472
s_add_u32 s75, s60, 65536
buffer_load_dwordx4 v3, s[44:47], s75 offen lds
v_mfma_f32_32x32x16_bf16 v[48:63], v[176:179], a[220:223], v[48:63]
s_waitcnt vmcnt(12) lgkmcnt(0)
s_barrier
ds_read_b128 a[224:227], v6 offset:0
ds_read_b128 a[228:231], v6 offset:4096
v_mfma_f32_32x32x16_bf16 v[64:79], v[180:183], a[216:219], v[64:79]
ds_read_b64_tr_b16 v[160:161], v10 offset:32768
ds_read_b64_tr_b16 v[162:163], v10 offset:33280
v_mfma_f32_32x32x16_bf16 v[80:95], v[180:183], a[220:223], v[80:95]
ds_read_b64_tr_b16 v[164:165], v11 offset:32768
ds_read_b64_tr_b16 v[166:167], v11 offset:33280
v_mfma_f32_32x32x16_bf16 v[96:111], v[184:187], a[216:219], v[96:111]
ds_read_b64_tr_b16 v[168:169], v12 offset:32768
ds_read_b64_tr_b16 v[170:171], v12 offset:33280
v_mfma_f32_32x32x16_bf16 v[112:127], v[184:187], a[220:223], v[112:127]
ds_read_b64_tr_b16 v[172:173], v13 offset:32768
ds_read_b64_tr_b16 v[174:175], v13 offset:33280
v_mfma_f32_32x32x16_bf16 v[128:143], v[188:191], a[216:219], v[128:143]
ds_read_b128 a[232:235], v7 offset:0
ds_read_b128 a[236:239], v7 offset:4096
v_mfma_f32_32x32x16_bf16 v[144:159], v[188:191], a[220:223], v[144:159]
ds_read_b128 a[240:243], v8 offset:0
ds_read_b128 a[244:247], v8 offset:4096
s_waitcnt lgkmcnt(4)
v_mfma_f32_32x32x16_bf16 v[32:47], v[160:163], a[224:227], v[32:47]
ds_read_b128 a[248:251], v9 offset:0
ds_read_b128 a[252:255], v9 offset:4096
v_mfma_f32_32x32x16_bf16 v[48:63], v[160:163], a[228:231], v[48:63]
ds_read_b64_tr_b16 v[176:177], v10 offset:36864
ds_read_b64_tr_b16 v[178:179], v10 offset:37376
v_mfma_f32_32x32x16_bf16 v[64:79], v[164:167], a[224:227], v[64:79]
ds_read_b64_tr_b16 v[180:181], v11 offset:36864
ds_read_b64_tr_b16 v[182:183], v11 offset:37376
v_mfma_f32_32x32x16_bf16 v[80:95], v[164:167], a[228:231], v[80:95]
)ASM"
R"ASM(
ds_read_b64_tr_b16 v[184:185], v12 offset:36864
ds_read_b64_tr_b16 v[186:187], v12 offset:37376
v_mfma_f32_16x16x32_bf16 v[208:211], v[200:203], v[28:31], v[208:211]
ds_read_b64_tr_b16 v[188:189], v13 offset:36864
v_mfma_f32_16x16x32_bf16 v[212:215], v[204:207], v[28:31], v[212:215]
ds_read_b64_tr_b16 v[190:191], v13 offset:37376
v_mfma_f32_32x32x16_bf16 v[96:111], v[168:171], a[224:227], v[96:111]
ds_read_b64_tr_b16 v[192:193], v14 offset:32768
ds_read_b64_tr_b16 v[196:197], v15 offset:32768
v_mfma_f32_32x32x16_bf16 v[112:127], v[168:171], a[228:231], v[112:127]
ds_read_b64_tr_b16 v[194:195], v14 offset:36864
ds_read_b64_tr_b16 v[198:199], v15 offset:36864
v_mfma_f32_32x32x16_bf16 v[128:143], v[172:175], a[224:227], v[128:143]
s_add_u32 m0, s41, 139264
s_add_u32 s62, s62, s92
buffer_load_dwordx4 v5, s[84:87], s62 offen lds
v_mfma_f32_32x32x16_bf16 v[144:159], v[172:175], a[228:231], v[144:159]
s_add_u32 m0, s41, 143360
s_add_u32 s62, s62, s93
buffer_load_dwordx4 v5, s[88:91], s62 offen lds
s_waitcnt lgkmcnt(4)
v_mfma_f32_32x32x16_bf16 v[32:47], v[176:179], a[232:235], v[32:47]
v_readlane_b32 s20, v0, 32
v_readlane_b32 s16, v0, 0
v_readlane_b32 s17, v0, 1
s_mov_b32 s18, 5310464
s_mov_b32 s19, 0x20000
v_min_u32 v160, 46, v1
v_lshlrev_b32 v160, 7, v160
s_mul_i32 s20, s20, 6016
s_add_u32 s20, s20, 2697216
s_mov_b32 m0, 154624
s_mov_b64 exec, s[24:25]
buffer_load_dword v160, s[16:19], s20 offen lds
s_mov_b64 exec, -1
ds_read_b64_tr_b16 v[160:161], v10 offset:40960
ds_read_b64_tr_b16 v[162:163], v10 offset:41472
v_and_b32 v223, 31, v1
v_lshlrev_b32 v223, 4, v223
s_lshr_b32 s77, s58, 1
v_mfma_f32_32x32x16_bf16 v[48:63], v[176:179], a[236:239], v[48:63]
ds_read_b64_tr_b16 v[164:165], v11 offset:40960
ds_read_b64_tr_b16 v[166:167], v11 offset:41472
s_add_u32 s77, s77, 49250304
buffer_load_dwordx4 v[20:23], v223, s[96:99], s77 offen sc1
v_mfma_f32_32x32x16_bf16 v[64:79], v[180:183], a[232:235], v[64:79]
ds_read_b64_tr_b16 v[168:169], v12 offset:40960
ds_read_b64_tr_b16 v[170:171], v12 offset:41472
buffer_load_dwordx4 v[24:27], v223, s[96:99], s77 offen offset:512 sc1
v_mfma_f32_32x32x16_bf16 v[80:95], v[180:183], a[236:239], v[80:95]
ds_read_b64_tr_b16 v[172:173], v13 offset:40960
ds_read_b64_tr_b16 v[174:175], v13 offset:41472
v_mfma_f32_32x32x16_bf16 v[96:111], v[184:187], a[232:235], v[96:111]
ds_read_b64_tr_b16 v[200:201], v14 offset:40960
ds_read_b64_tr_b16 v[204:205], v15 offset:40960
v_mfma_f32_32x32x16_bf16 v[112:127], v[184:187], a[236:239], v[112:127]
s_add_u32 m0, s41, 0
s_add_u32 s62, s62, s92
buffer_load_dwordx4 v5, s[84:87], s62 offen lds
v_mfma_f32_32x32x16_bf16 v[128:143], v[188:191], a[232:235], v[128:143]
s_add_u32 m0, s41, 4096
s_add_u32 s62, s62, s93
buffer_load_dwordx4 v5, s[88:91], s62 offen lds
v_mfma_f32_32x32x16_bf16 v[144:159], v[188:191], a[236:239], v[144:159]
s_waitcnt lgkmcnt(2)
v_mfma_f32_32x32x16_bf16 v[32:47], v[160:163], a[240:243], v[32:47]
ds_read_b64_tr_b16 v[176:177], v10 offset:45056
ds_read_b64_tr_b16 v[178:179], v10 offset:45568
v_mfma_f32_32x32x16_bf16 v[48:63], v[160:163], a[244:247], v[48:63]
ds_read_b64_tr_b16 v[180:181], v11 offset:45056
ds_read_b64_tr_b16 v[182:183], v11 offset:45568
v_mfma_f32_32x32x16_bf16 v[64:79], v[164:167], a[240:243], v[64:79]
ds_read_b64_tr_b16 v[184:185], v12 offset:45056
ds_read_b64_tr_b16 v[186:187], v12 offset:45568
v_mfma_f32_32x32x16_bf16 v[80:95], v[164:167], a[244:247], v[80:95]
ds_read_b64_tr_b16 v[188:189], v13 offset:45056
ds_read_b64_tr_b16 v[190:191], v13 offset:45568
v_mfma_f32_16x16x32_bf16 v[208:211], v[192:195], v[28:31], v[208:211]
ds_read_b64_tr_b16 v[202:203], v14 offset:45056
v_mfma_f32_16x16x32_bf16 v[212:215], v[196:199], v[28:31], v[212:215]
ds_read_b64_tr_b16 v[206:207], v15 offset:45056
v_mfma_f32_32x32x16_bf16 v[96:111], v[168:171], a[240:243], v[96:111]
s_add_u32 m0, s41, 8192
s_add_u32 s62, s62, s92
buffer_load_dwordx4 v5, s[84:87], s62 offen lds
v_mfma_f32_32x32x16_bf16 v[112:127], v[168:171], a[244:247], v[112:127]
v_mfma_f32_32x32x16_bf16 v[128:143], v[172:175], a[240:243], v[128:143]
v_mfma_f32_32x32x16_bf16 v[144:159], v[172:175], a[244:247], v[144:159]
s_waitcnt lgkmcnt(2)
v_mfma_f32_32x32x16_bf16 v[32:47], v[176:179], a[248:251], v[32:47]
v_readlane_b32 s20, v0, 32
v_readlane_b32 s16, v0, 37
v_readlane_b32 s17, v0, 38
s_mov_b32 s18, 663552
s_mov_b32 s19, 0x20000
v_min_u32 v160, 11, v1
v_lshlrev_b32 v160, 7, v160
s_mul_i32 s20, s20, 1536
s_add_u32 s20, s20, 0
s_mov_b32 m0, 154624
s_mov_b64 exec, s[24:25]
buffer_load_dword v160, s[16:19], s20 offen lds
s_mov_b64 exec, -1
v_mfma_f32_32x32x16_bf16 v[48:63], v[176:179], a[252:255], v[48:63]
s_waitcnt vmcnt(7) lgkmcnt(0)
s_barrier
ds_read_b128 v[224:227], v6 offset:32768
ds_read_b128 v[228:231], v6 offset:36864
v_mfma_f32_32x32x16_bf16 v[64:79], v[180:183], a[248:251], v[64:79]
ds_read_b64_tr_b16 v[160:161], v10 offset:49152
ds_read_b64_tr_b16 v[162:163], v10 offset:49664
v_mfma_f32_32x32x16_bf16 v[80:95], v[180:183], a[252:255], v[80:95]
ds_read_b64_tr_b16 v[164:165], v11 offset:49152
ds_read_b64_tr_b16 v[166:167], v11 offset:49664
v_mfma_f32_32x32x16_bf16 v[96:111], v[184:187], a[248:251], v[96:111]
ds_read_b64_tr_b16 v[168:169], v12 offset:49152
ds_read_b64_tr_b16 v[170:171], v12 offset:49664
v_mfma_f32_32x32x16_bf16 v[112:127], v[184:187], a[252:255], v[112:127]
ds_read_b64_tr_b16 v[172:173], v13 offset:49152
ds_read_b64_tr_b16 v[174:175], v13 offset:49664
v_mfma_f32_32x32x16_bf16 v[128:143], v[188:191], a[248:251], v[128:143]
ds_read_b128 v[232:235], v7 offset:32768
ds_read_b128 v[236:239], v7 offset:36864
v_mfma_f32_32x32x16_bf16 v[144:159], v[188:191], a[252:255], v[144:159]
ds_read_b128 v[240:243], v8 offset:32768
ds_read_b128 v[244:247], v8 offset:36864
s_waitcnt lgkmcnt(4)
v_mfma_f32_32x32x16_bf16 v[32:47], v[160:163], v[224:227], v[32:47]
ds_read_b128 v[248:251], v9 offset:32768
ds_read_b128 v[252:255], v9 offset:36864
v_mfma_f32_32x32x16_bf16 v[48:63], v[160:163], v[228:231], v[48:63]
ds_read_b64_tr_b16 v[176:177], v10 offset:53248
ds_read_b64_tr_b16 v[178:179], v10 offset:53760
v_mfma_f32_32x32x16_bf16 v[64:79], v[164:167], v[224:227], v[64:79]
ds_read_b64_tr_b16 v[180:181], v11 offset:53248
ds_read_b64_tr_b16 v[182:183], v11 offset:53760
v_mfma_f32_32x32x16_bf16 v[80:95], v[164:167], v[228:231], v[80:95]
ds_read_b64_tr_b16 v[184:185], v12 offset:53248
ds_read_b64_tr_b16 v[186:187], v12 offset:53760
v_mfma_f32_16x16x32_bf16 v[208:211], v[200:203], v[28:31], v[208:211]
ds_read_b64_tr_b16 v[188:189], v13 offset:53248
v_mfma_f32_16x16x32_bf16 v[212:215], v[204:207], v[28:31], v[212:215]
ds_read_b64_tr_b16 v[190:191], v13 offset:53760
v_mfma_f32_32x32x16_bf16 v[96:111], v[168:171], v[224:227], v[96:111]
ds_read_b64_tr_b16 v[192:193], v14 offset:49152
ds_read_b64_tr_b16 v[196:197], v15 offset:49152
v_mfma_f32_32x32x16_bf16 v[112:127], v[168:171], v[228:231], v[112:127]
ds_read_b64_tr_b16 v[194:195], v14 offset:53248
ds_read_b64_tr_b16 v[198:199], v15 offset:53248
v_mfma_f32_32x32x16_bf16 v[128:143], v[172:175], v[224:227], v[128:143]
ds_read_b64_tr_b16 v[200:201], v14 offset:57344
ds_read_b64_tr_b16 v[204:205], v15 offset:57344
v_mfma_f32_32x32x16_bf16 v[144:159], v[172:175], v[228:231], v[144:159]
ds_read_b64_tr_b16 v[202:203], v14 offset:61440
ds_read_b64_tr_b16 v[206:207], v15 offset:61440
s_waitcnt lgkmcnt(8)
v_mfma_f32_32x32x16_bf16 v[32:47], v[176:179], v[232:235], v[32:47]
v_readlane_b32 s20, v0, 32
v_readlane_b32 s16, v0, 39
v_readlane_b32 s17, v0, 40
s_mov_b32 s18, 663552
s_mov_b32 s19, 0x20000
v_min_u32 v160, 11, v1
v_lshlrev_b32 v160, 7, v160
s_mul_i32 s20, s20, 1536
s_add_u32 s20, s20, 0
s_mov_b32 m0, 154624
s_mov_b64 exec, s[24:25]
buffer_load_dword v160, s[16:19], s20 offen lds
s_mov_b64 exec, -1
ds_read_b64_tr_b16 v[160:161], v10 offset:57344
ds_read_b64_tr_b16 v[162:163], v10 offset:57856
v_mfma_f32_32x32x16_bf16 v[48:63], v[176:179], v[236:239], v[48:63]
ds_read_b64_tr_b16 v[164:165], v11 offset:57344
ds_read_b64_tr_b16 v[166:167], v11 offset:57856
v_mfma_f32_32x32x16_bf16 v[64:79], v[180:183], v[232:235], v[64:79]
ds_read_b64_tr_b16 v[168:169], v12 offset:57344
ds_read_b64_tr_b16 v[170:171], v12 offset:57856
v_mfma_f32_32x32x16_bf16 v[80:95], v[180:183], v[236:239], v[80:95]
ds_read_b64_tr_b16 v[172:173], v13 offset:57344
ds_read_b64_tr_b16 v[174:175], v13 offset:57856
s_waitcnt lgkmcnt(13)
v_mfma_f32_16x16x32_bf16 v[208:211], v[192:195], v[28:31], v[208:211]
s_waitcnt lgkmcnt(12)
v_mfma_f32_16x16x32_bf16 v[212:215], v[196:199], v[28:31], v[212:215]
v_mfma_f32_32x32x16_bf16 v[96:111], v[184:187], v[232:235], v[96:111]
s_add_u32 m0, s41, 12288
s_add_u32 s62, s62, s93
buffer_load_dwordx4 v5, s[88:91], s62 offen lds
v_mfma_f32_32x32x16_bf16 v[112:127], v[184:187], v[236:239], v[112:127]
s_add_u32 m0, s41, 16384
s_add_u32 s62, s62, s92
buffer_load_dwordx4 v5, s[84:87], s62 offen lds
v_mfma_f32_32x32x16_bf16 v[128:143], v[188:191], v[232:235], v[128:143]
s_add_u32 m0, s41, 20480
s_add_u32 s62, s62, s93
buffer_load_dwordx4 v5, s[88:91], s62 offen lds
v_mfma_f32_32x32x16_bf16 v[144:159], v[188:191], v[236:239], v[144:159]
s_add_u32 m0, s41, 24576
s_add_u32 s62, s62, s92
)ASM"
R"ASM(
buffer_load_dwordx4 v5, s[84:87], s62 offen lds
s_waitcnt lgkmcnt(0)
v_mfma_f32_32x32x16_bf16 v[32:47], v[160:163], v[240:243], v[32:47]
ds_read_b64_tr_b16 v[176:177], v10 offset:61440
ds_read_b64_tr_b16 v[178:179], v10 offset:61952
v_mfma_f32_32x32x16_bf16 v[48:63], v[160:163], v[244:247], v[48:63]
ds_read_b64_tr_b16 v[180:181], v11 offset:61440
ds_read_b64_tr_b16 v[182:183], v11 offset:61952
v_mfma_f32_32x32x16_bf16 v[64:79], v[164:167], v[240:243], v[64:79]
ds_read_b64_tr_b16 v[184:185], v12 offset:61440
ds_read_b64_tr_b16 v[186:187], v12 offset:61952
v_mfma_f32_32x32x16_bf16 v[80:95], v[164:167], v[244:247], v[80:95]
ds_read_b64_tr_b16 v[188:189], v13 offset:61440
ds_read_b64_tr_b16 v[190:191], v13 offset:61952
v_mfma_f32_16x16x32_bf16 v[208:211], v[200:203], v[28:31], v[208:211]
v_mfma_f32_16x16x32_bf16 v[212:215], v[204:207], v[28:31], v[212:215]
v_mfma_f32_32x32x16_bf16 v[96:111], v[168:171], v[240:243], v[96:111]
s_add_u32 m0, s41, 28672
s_add_u32 s62, s62, s93
buffer_load_dwordx4 v5, s[88:91], s62 offen lds
v_mfma_f32_32x32x16_bf16 v[112:127], v[168:171], v[244:247], v[112:127]
v_mfma_f32_32x32x16_bf16 v[128:143], v[172:175], v[240:243], v[128:143]
s_nop 0
ds_write_b128 v17, v[208:211]
ds_write_b128 v17, v[212:215] offset:64
v_mfma_f32_32x32x16_bf16 v[144:159], v[172:175], v[244:247], v[144:159]
s_waitcnt lgkmcnt(2)
v_mfma_f32_32x32x16_bf16 v[32:47], v[176:179], v[248:251], v[32:47]
v_mfma_f32_32x32x16_bf16 v[48:63], v[176:179], v[252:255], v[48:63]
v_readlane_b32 s78, v0, 26
s_nop 3
s_nop 2
s_load_dwordx16 s[4:19], %[ka], s78
s_add_u32 s78, s78, 64
s_load_dwordx16 s[20:35], %[ka], s78
s_add_u32 s78, s78, 64
s_load_dwordx4 s[36:39], %[ka], s78
v_mfma_f32_32x32x16_bf16 v[64:79], v[180:183], v[248:251], v[64:79]
v_mfma_f32_32x32x16_bf16 v[80:95], v[180:183], v[252:255], v[80:95]
v_mfma_f32_32x32x16_bf16 v[96:111], v[184:187], v[248:251], v[96:111]
v_mfma_f32_32x32x16_bf16 v[112:127], v[184:187], v[252:255], v[112:127]
v_mfma_f32_32x32x16_bf16 v[128:143], v[188:191], v[248:251], v[128:143]
s_waitcnt lgkmcnt(0)
s_barrier
v_mfma_f32_32x32x16_bf16 v[144:159], v[188:191], v[252:255], v[144:159]
s_mov_b32 s78, 20000
s_waitcnt vmcnt(8)
LSC_%=:
v_xor_b32 v220, v21, v219
v_xor_b32 v221, v23, v219
v_xor_b32 v222, v25, v219
v_or3_b32 v220, v220, v221, v222
v_xor_b32 v221, v27, v219
v_or_b32 v220, v220, v221
v_cmp_ne_u32 vcc, 0, v220
s_nop 3
s_cmp_eq_u64 vcc, 0
s_cbranch_scc1 LSD_%=
s_sub_u32 s78, s78, 1
s_cmp_eq_u32 s78, 0
s_cbranch_scc1 LSD_%=
s_sleep 2
buffer_load_dwordx4 v[20:23], v223, s[96:99], s77 offen sc1
buffer_load_dwordx4 v[24:27], v223, s[96:99], s77 offen offset:512 sc1
s_waitcnt vmcnt(0)
s_branch LSC_%=
LSD_%=:
v_rsq_f32 v21, v22
v_rsq_f32 v25, v26
v_mul_f32 v21, 0xbfb8aa3b, v21
v_mul_f32 v25, 0xbfb8aa3b, v25
ds_read_b128 v[200:203], v16 offset:0
ds_read_b128 v[204:207], v16 offset:64
ds_read_b128 v[208:211], v16 offset:16
ds_read_b128 v[212:215], v16 offset:80
s_add_u32 m0, s41, 32768
s_add_u32 s62, s62, s92
buffer_load_dwordx4 v5, s[84:87], s62 offen lds
s_waitcnt lgkmcnt(3)
v_pk_fma_f32 v[32:33], v[20:21], v[200:201], v[32:33] op_sel:[0,0,0] op_sel_hi:[0,1,1]
s_waitcnt lgkmcnt(2)
v_pk_fma_f32 v[40:41], v[20:21], v[204:205], v[40:41] op_sel:[0,0,0] op_sel_hi:[0,1,1]
v_pk_fma_f32 v[34:35], v[20:21], v[202:203], v[34:35] op_sel:[0,0,0] op_sel_hi:[0,1,1]
v_pk_fma_f32 v[42:43], v[20:21], v[206:207], v[42:43] op_sel:[0,0,0] op_sel_hi:[0,1,1]
v_pk_fma_f32 v[48:49], v[24:25], v[200:201], v[48:49] op_sel:[0,0,0] op_sel_hi:[0,1,1]
v_pk_fma_f32 v[56:57], v[24:25], v[204:205], v[56:57] op_sel:[0,0,0] op_sel_hi:[0,1,1]
v_pk_fma_f32 v[50:51], v[24:25], v[202:203], v[50:51] op_sel:[0,0,0] op_sel_hi:[0,1,1]
v_pk_fma_f32 v[58:59], v[24:25], v[206:207], v[58:59] op_sel:[0,0,0] op_sel_hi:[0,1,1]
ds_read_b128 v[200:203], v16 offset:128
ds_read_b128 v[204:207], v16 offset:192
v_pk_mul_f32 v[40:41], v[32:33], v[40:41] op_sel:[0,0] op_sel_hi:[1,1]
v_pk_mul_f32 v[32:33], v[20:21], v[32:33] op_sel:[1,0] op_sel_hi:[1,1]
v_pk_mul_f32 v[42:43], v[34:35], v[42:43] op_sel:[0,0] op_sel_hi:[1,1]
v_pk_mul_f32 v[34:35], v[20:21], v[34:35] op_sel:[1,0] op_sel_hi:[1,1]
v_pk_mul_f32 v[56:57], v[48:49], v[56:57] op_sel:[0,0] op_sel_hi:[1,1]
v_pk_mul_f32 v[48:49], v[24:25], v[48:49] op_sel:[1,0] op_sel_hi:[1,1]
v_pk_mul_f32 v[58:59], v[50:51], v[58:59] op_sel:[0,0] op_sel_hi:[1,1]
v_pk_mul_f32 v[50:51], v[24:25], v[50:51] op_sel:[1,0] op_sel_hi:[1,1]
s_add_u32 m0, s41, 36864
s_add_u32 s62, s62, s93
buffer_load_dwordx4 v5, s[88:91], s62 offen lds
v_exp_f32 v32, v32
v_exp_f32 v33, v33
v_exp_f32 v34, v34
v_exp_f32 v35, v35
v_exp_f32 v48, v48
v_exp_f32 v49, v49
v_exp_f32 v50, v50
v_exp_f32 v51, v51
v_pk_fma_f32 v[32:33], v[32:33], v[22:23], v[22:23] op_sel:[0,0,0] op_sel_hi:[1,0,0]
v_pk_fma_f32 v[34:35], v[34:35], v[22:23], v[22:23] op_sel:[0,0,0] op_sel_hi:[1,0,0]
v_pk_fma_f32 v[48:49], v[48:49], v[26:27], v[26:27] op_sel:[0,0,0] op_sel_hi:[1,0,0]
v_pk_fma_f32 v[50:51], v[50:51], v[26:27], v[26:27] op_sel:[0,0,0] op_sel_hi:[1,0,0]
s_add_u32 m0, s41, 40960
s_add_u32 s62, s62, s92
buffer_load_dwordx4 v5, s[84:87], s62 offen lds
v_rcp_f32 v32, v32
v_rcp_f32 v33, v33
v_rcp_f32 v34, v34
v_rcp_f32 v35, v35
v_rcp_f32 v48, v48
v_rcp_f32 v49, v49
v_rcp_f32 v50, v50
v_rcp_f32 v51, v51
v_pk_mul_f32 v[40:41], v[40:41], v[32:33] op_sel:[0,0] op_sel_hi:[1,1]
v_pk_mul_f32 v[42:43], v[42:43], v[34:35] op_sel:[0,0] op_sel_hi:[1,1]
v_pk_mul_f32 v[56:57], v[56:57], v[48:49] op_sel:[0,0] op_sel_hi:[1,1]
v_pk_mul_f32 v[58:59], v[58:59], v[50:51] op_sel:[0,0] op_sel_hi:[1,1]
s_add_u32 m0, s41, 45056
s_add_u32 s62, s62, s93
buffer_load_dwordx4 v5, s[88:91], s62 offen lds
v_cvt_pk_bf16_f32 v32, v40, v41
v_cvt_pk_bf16_f32 v33, v42, v43
v_cvt_pk_bf16_f32 v48, v56, v57
v_cvt_pk_bf16_f32 v49, v58, v59
s_waitcnt lgkmcnt(3)
v_pk_fma_f32 v[36:37], v[20:21], v[208:209], v[36:37] op_sel:[0,0,0] op_sel_hi:[0,1,1]
s_waitcnt lgkmcnt(2)
v_pk_fma_f32 v[44:45], v[20:21], v[212:213], v[44:45] op_sel:[0,0,0] op_sel_hi:[0,1,1]
v_pk_fma_f32 v[38:39], v[20:21], v[210:211], v[38:39] op_sel:[0,0,0] op_sel_hi:[0,1,1]
v_pk_fma_f32 v[46:47], v[20:21], v[214:215], v[46:47] op_sel:[0,0,0] op_sel_hi:[0,1,1]
v_pk_fma_f32 v[52:53], v[24:25], v[208:209], v[52:53] op_sel:[0,0,0] op_sel_hi:[0,1,1]
v_pk_fma_f32 v[60:61], v[24:25], v[212:213], v[60:61] op_sel:[0,0,0] op_sel_hi:[0,1,1]
v_pk_fma_f32 v[54:55], v[24:25], v[210:211], v[54:55] op_sel:[0,0,0] op_sel_hi:[0,1,1]
v_pk_fma_f32 v[62:63], v[24:25], v[214:215], v[62:63] op_sel:[0,0,0] op_sel_hi:[0,1,1]
ds_read_b128 v[208:211], v16 offset:144
ds_read_b128 v[212:215], v16 offset:208
v_pk_mul_f32 v[44:45], v[36:37], v[44:45] op_sel:[0,0] op_sel_hi:[1,1]
v_pk_mul_f32 v[36:37], v[20:21], v[36:37] op_sel:[1,0] op_sel_hi:[1,1]
v_pk_mul_f32 v[46:47], v[38:39], v[46:47] op_sel:[0,0] op_sel_hi:[1,1]
v_pk_mul_f32 v[38:39], v[20:21], v[38:39] op_sel:[1,0] op_sel_hi:[1,1]
v_pk_mul_f32 v[60:61], v[52:53], v[60:61] op_sel:[0,0] op_sel_hi:[1,1]
v_pk_mul_f32 v[52:53], v[24:25], v[52:53] op_sel:[1,0] op_sel_hi:[1,1]
v_pk_mul_f32 v[62:63], v[54:55], v[62:63] op_sel:[0,0] op_sel_hi:[1,1]
v_pk_mul_f32 v[54:55], v[24:25], v[54:55] op_sel:[1,0] op_sel_hi:[1,1]
s_add_u32 m0, s41, 49152
s_add_u32 s62, s62, s92
buffer_load_dwordx4 v5, s[84:87], s62 offen lds
v_exp_f32 v36, v36
v_exp_f32 v37, v37
v_exp_f32 v38, v38
v_exp_f32 v39, v39
v_exp_f32 v52, v52
v_exp_f32 v53, v53
v_exp_f32 v54, v54
v_exp_f32 v55, v55
v_pk_fma_f32 v[36:37], v[36:37], v[22:23], v[22:23] op_sel:[0,0,0] op_sel_hi:[1,0,0]
v_pk_fma_f32 v[38:39], v[38:39], v[22:23], v[22:23] op_sel:[0,0,0] op_sel_hi:[1,0,0]
v_pk_fma_f32 v[52:53], v[52:53], v[26:27], v[26:27] op_sel:[0,0,0] op_sel_hi:[1,0,0]
v_pk_fma_f32 v[54:55], v[54:55], v[26:27], v[26:27] op_sel:[0,0,0] op_sel_hi:[1,0,0]
s_add_u32 m0, s41, 53248
s_add_u32 s62, s62, s93
buffer_load_dwordx4 v5, s[88:91], s62 offen lds
v_rcp_f32 v36, v36
v_rcp_f32 v37, v37
v_rcp_f32 v38, v38
v_rcp_f32 v39, v39
v_rcp_f32 v52, v52
v_rcp_f32 v53, v53
v_rcp_f32 v54, v54
v_rcp_f32 v55, v55
v_pk_mul_f32 v[44:45], v[44:45], v[36:37] op_sel:[0,0] op_sel_hi:[1,1]
v_pk_mul_f32 v[46:47], v[46:47], v[38:39] op_sel:[0,0] op_sel_hi:[1,1]
v_pk_mul_f32 v[60:61], v[60:61], v[52:53] op_sel:[0,0] op_sel_hi:[1,1]
v_pk_mul_f32 v[62:63], v[62:63], v[54:55] op_sel:[0,0] op_sel_hi:[1,1]
s_mov_b32 s84, s48
s_mov_b32 s88, s48
s_mov_b32 s85, s49
s_mov_b32 s89, s49
s_mov_b32 s86, s50
s_mov_b32 s90, s50
s_mov_b32 s92, 294912
s_mov_b32 s93, 294912
v_and_b32 v199, 3, v1
s_and_b32 s76, s40, 1
s_lshl_b32 s76, s76, 1
v_xor_b32 v199, s76, v199
v_lshrrev_b32 v5, 2, v1
v_mul_u32_u24 v5, 9216, v5
v_lshl_add_u32 v5, v199, 4, v5
)ASM"
R"ASM(
v_cvt_pk_bf16_f32 v34, v44, v45
v_cvt_pk_bf16_f32 v35, v46, v47
v_cvt_pk_bf16_f32 v50, v60, v61
v_cvt_pk_bf16_f32 v51, v62, v63
buffer_store_dwordx4 v[32:35], v19, s[96:99], s72 offen offset:0 sc1
buffer_store_dwordx4 v[48:51], v19, s[96:99], s72 offen offset:1024 sc1
s_waitcnt lgkmcnt(3)
v_pk_fma_f32 v[64:65], v[20:21], v[200:201], v[64:65] op_sel:[0,0,0] op_sel_hi:[0,1,1]
s_waitcnt lgkmcnt(2)
v_pk_fma_f32 v[72:73], v[20:21], v[204:205], v[72:73] op_sel:[0,0,0] op_sel_hi:[0,1,1]
v_pk_fma_f32 v[66:67], v[20:21], v[202:203], v[66:67] op_sel:[0,0,0] op_sel_hi:[0,1,1]
v_pk_fma_f32 v[74:75], v[20:21], v[206:207], v[74:75] op_sel:[0,0,0] op_sel_hi:[0,1,1]
v_pk_fma_f32 v[80:81], v[24:25], v[200:201], v[80:81] op_sel:[0,0,0] op_sel_hi:[0,1,1]
v_pk_fma_f32 v[88:89], v[24:25], v[204:205], v[88:89] op_sel:[0,0,0] op_sel_hi:[0,1,1]
v_pk_fma_f32 v[82:83], v[24:25], v[202:203], v[82:83] op_sel:[0,0,0] op_sel_hi:[0,1,1]
v_pk_fma_f32 v[90:91], v[24:25], v[206:207], v[90:91] op_sel:[0,0,0] op_sel_hi:[0,1,1]
v_pk_mul_f32 v[72:73], v[64:65], v[72:73] op_sel:[0,0] op_sel_hi:[1,1]
v_pk_mul_f32 v[64:65], v[20:21], v[64:65] op_sel:[1,0] op_sel_hi:[1,1]
v_pk_mul_f32 v[74:75], v[66:67], v[74:75] op_sel:[0,0] op_sel_hi:[1,1]
v_pk_mul_f32 v[66:67], v[20:21], v[66:67] op_sel:[1,0] op_sel_hi:[1,1]
v_pk_mul_f32 v[88:89], v[80:81], v[88:89] op_sel:[0,0] op_sel_hi:[1,1]
v_pk_mul_f32 v[80:81], v[24:25], v[80:81] op_sel:[1,0] op_sel_hi:[1,1]
v_pk_mul_f32 v[90:91], v[82:83], v[90:91] op_sel:[0,0] op_sel_hi:[1,1]
v_pk_mul_f32 v[82:83], v[24:25], v[82:83] op_sel:[1,0] op_sel_hi:[1,1]
v_and_b32 v192, 63, v1
v_lshrrev_b32 v193, 4, v192
v_bfe_u32 v194, v192, 2, 2
v_and_b32 v195, 3, v192
v_and_b32 v196, 1, v195
v_lshlrev_b32 v196, 4, v196
v_lshrrev_b32 v197, 1, v195
v_lshl_add_u32 v196, v197, 3, v196
v_and_b32 v197, 1, v193
v_lshl_add_u32 v196, v197, 10, v196
v_lshrrev_b32 v198, 1, v193
v_lshl_add_u32 v196, v198, 9, v196
v_lshl_add_u32 v196, v194, 6, v196
v_lshl_add_u32 v6, v197, 5, v196
v_xor_b32 v7, 32, v6
v_add_u32 v8, 49152, v6
v_add_u32 v9, 49152, v7
v_add_u32 v10, 98304, v6
v_add_u32 v11, 98304, v7
s_and_b32 s76, s40, 1
s_lshr_b32 s77, s40, 1
s_xor_b32 s77, s77, s76
s_lshl_b32 s77, s77, 5
s_lshl_b32 s76, s76, 10
s_add_u32 s76, s76, s77
v_lshlrev_b32 v196, 8, v193
v_lshl_add_u32 v196, v194, 6, v196
v_lshl_add_u32 v196, v195, 3, v196
v_add_u32 v12, s76, v196
v_add_u32 v13, 49152, v12
v_add_u32 v14, 98304, v12
v_exp_f32 v64, v64
v_exp_f32 v65, v65
v_exp_f32 v66, v66
v_exp_f32 v67, v67
v_exp_f32 v80, v80
v_exp_f32 v81, v81
v_exp_f32 v82, v82
v_exp_f32 v83, v83
v_pk_fma_f32 v[64:65], v[64:65], v[22:23], v[22:23] op_sel:[0,0,0] op_sel_hi:[1,0,0]
v_pk_fma_f32 v[66:67], v[66:67], v[22:23], v[22:23] op_sel:[0,0,0] op_sel_hi:[1,0,0]
v_pk_fma_f32 v[80:81], v[80:81], v[26:27], v[26:27] op_sel:[0,0,0] op_sel_hi:[1,0,0]
v_pk_fma_f32 v[82:83], v[82:83], v[26:27], v[26:27] op_sel:[0,0,0] op_sel_hi:[1,0,0]
v_and_b32 v192, 63, v1
v_and_b32 v193, 31, v192
v_lshrrev_b32 v194, 5, v192
v_bfe_u32 v195, v193, 1, 3
v_lshlrev_b32 v196, 7, v193
v_add_u32 v196, 131072, v196
v_or_b32 v197, 0, v194
v_xor_b32 v197, v197, v195
v_lshl_add_u32 v2, v197, 4, v196
v_or_b32 v197, 2, v194
v_xor_b32 v197, v197, v195
v_lshl_add_u32 v3, v197, 4, v196
v_or_b32 v197, 4, v194
v_xor_b32 v197, v197, v195
v_lshl_add_u32 v4, v197, 4, v196
v_or_b32 v197, 6, v194
v_xor_b32 v197, v197, v195
v_lshl_add_u32 v15, v197, 4, v196
v_and_b32 v193, 15, v192
v_lshrrev_b32 v194, 4, v192
v_lshrrev_b32 v195, 1, v193
v_xor_b32 v194, v194, v195
v_lshlrev_b32 v196, 7, v193
v_lshl_add_u32 v196, v194, 4, v196
s_and_b32 s76, s40, 1
s_lshl_b32 s76, s76, 12
s_lshr_b32 s77, s40, 1
s_lshl_b32 s77, s77, 11
s_add_u32 s76, s76, s77
s_add_u32 s76, s76, 131072
v_add_u32 v17, s76, v196
v_xor_b32 v216, 64, v17
v_cmp_gt_u32 vcc, 32, v192
s_nop 1
v_cndmask_b32 v196, v24, v20, vcc
v_cndmask_b32 v197, v25, v21, vcc
v_mul_f32 v197, 0xbf317218, v197
s_lshl_b32 s76, s40, 9
s_add_u32 s76, s76, 148480
v_lshlrev_b32 v193, 2, v192
v_add_u32 v193, s76, v193
ds_write_b32 v193, v196
ds_write_b32 v193, v197 offset:256
v_rcp_f32 v64, v64
v_rcp_f32 v65, v65
v_rcp_f32 v66, v66
v_rcp_f32 v67, v67
v_rcp_f32 v80, v80
v_rcp_f32 v81, v81
v_rcp_f32 v82, v82
v_rcp_f32 v83, v83
v_pk_mul_f32 v[72:73], v[72:73], v[64:65] op_sel:[0,0] op_sel_hi:[1,1]
v_pk_mul_f32 v[74:75], v[74:75], v[66:67] op_sel:[0,0] op_sel_hi:[1,1]
v_pk_mul_f32 v[88:89], v[88:89], v[80:81] op_sel:[0,0] op_sel_hi:[1,1]
v_pk_mul_f32 v[90:91], v[90:91], v[82:83] op_sel:[0,0] op_sel_hi:[1,1]
v_cvt_pk_bf16_f32 v64, v72, v73
v_cvt_pk_bf16_f32 v65, v74, v75
v_cvt_pk_bf16_f32 v80, v88, v89
v_cvt_pk_bf16_f32 v81, v90, v91
s_waitcnt lgkmcnt(3)
v_pk_fma_f32 v[68:69], v[20:21], v[208:209], v[68:69] op_sel:[0,0,0] op_sel_hi:[0,1,1]
s_waitcnt lgkmcnt(2)
v_pk_fma_f32 v[76:77], v[20:21], v[212:213], v[76:77] op_sel:[0,0,0] op_sel_hi:[0,1,1]
v_pk_fma_f32 v[70:71], v[20:21], v[210:211], v[70:71] op_sel:[0,0,0] op_sel_hi:[0,1,1]
v_pk_fma_f32 v[78:79], v[20:21], v[214:215], v[78:79] op_sel:[0,0,0] op_sel_hi:[0,1,1]
v_pk_fma_f32 v[84:85], v[24:25], v[208:209], v[84:85] op_sel:[0,0,0] op_sel_hi:[0,1,1]
v_pk_fma_f32 v[92:93], v[24:25], v[212:213], v[92:93] op_sel:[0,0,0] op_sel_hi:[0,1,1]
v_pk_fma_f32 v[86:87], v[24:25], v[210:211], v[86:87] op_sel:[0,0,0] op_sel_hi:[0,1,1]
v_pk_fma_f32 v[94:95], v[24:25], v[214:215], v[94:95] op_sel:[0,0,0] op_sel_hi:[0,1,1]
v_pk_mul_f32 v[76:77], v[68:69], v[76:77] op_sel:[0,0] op_sel_hi:[1,1]
v_pk_mul_f32 v[68:69], v[20:21], v[68:69] op_sel:[1,0] op_sel_hi:[1,1]
v_pk_mul_f32 v[78:79], v[70:71], v[78:79] op_sel:[0,0] op_sel_hi:[1,1]
v_pk_mul_f32 v[70:71], v[20:21], v[70:71] op_sel:[1,0] op_sel_hi:[1,1]
v_pk_mul_f32 v[92:93], v[84:85], v[92:93] op_sel:[0,0] op_sel_hi:[1,1]
v_pk_mul_f32 v[84:85], v[24:25], v[84:85] op_sel:[1,0] op_sel_hi:[1,1]
v_pk_mul_f32 v[94:95], v[86:87], v[94:95] op_sel:[0,0] op_sel_hi:[1,1]
v_pk_mul_f32 v[86:87], v[24:25], v[86:87] op_sel:[1,0] op_sel_hi:[1,1]
v_exp_f32 v68, v68
v_exp_f32 v69, v69
v_exp_f32 v70, v70
v_exp_f32 v71, v71
v_exp_f32 v84, v84
v_exp_f32 v85, v85
v_exp_f32 v86, v86
v_exp_f32 v87, v87
v_pk_fma_f32 v[68:69], v[68:69], v[22:23], v[22:23] op_sel:[0,0,0] op_sel_hi:[1,0,0]
v_pk_fma_f32 v[70:71], v[70:71], v[22:23], v[22:23] op_sel:[0,0,0] op_sel_hi:[1,0,0]
v_pk_fma_f32 v[84:85], v[84:85], v[26:27], v[26:27] op_sel:[0,0,0] op_sel_hi:[1,0,0]
v_pk_fma_f32 v[86:87], v[86:87], v[26:27], v[26:27] op_sel:[0,0,0] op_sel_hi:[1,0,0]
v_rcp_f32 v68, v68
v_rcp_f32 v69, v69
v_rcp_f32 v70, v70
v_rcp_f32 v71, v71
v_rcp_f32 v84, v84
v_rcp_f32 v85, v85
v_rcp_f32 v86, v86
v_rcp_f32 v87, v87
v_pk_mul_f32 v[76:77], v[76:77], v[68:69] op_sel:[0,0] op_sel_hi:[1,1]
v_pk_mul_f32 v[78:79], v[78:79], v[70:71] op_sel:[0,0] op_sel_hi:[1,1]
v_pk_mul_f32 v[92:93], v[92:93], v[84:85] op_sel:[0,0] op_sel_hi:[1,1]
v_pk_mul_f32 v[94:95], v[94:95], v[86:87] op_sel:[0,0] op_sel_hi:[1,1]
v_cvt_pk_bf16_f32 v66, v76, v77
v_cvt_pk_bf16_f32 v67, v78, v79
v_cvt_pk_bf16_f32 v82, v92, v93
v_cvt_pk_bf16_f32 v83, v94, v95
s_add_u32 s75, s72, 65536
buffer_store_dwordx4 v[64:67], v19, s[96:99], s75 offen offset:0 sc1
s_add_u32 s75, s72, 65536
buffer_store_dwordx4 v[80:83], v19, s[96:99], s75 offen offset:1024 sc1
s_waitcnt vmcnt(13) lgkmcnt(0)
s_barrier
s_cmp_eq_u32 s94, 0
s_cbranch_scc1 LF0_%=
ds_read_b128 v[160:163], v2 offset:0
ds_read_b128 v[164:167], v2 offset:4096
ds_read_b128 v[168:171], v3 offset:0
ds_read_b128 v[172:175], v3 offset:4096
ds_read_b128 v[176:179], v4 offset:0
ds_read_b128 v[180:183], v4 offset:4096
s_waitcnt lgkmcnt(5)
v_mfma_f32_32x32x16_bf16 v[32:47], a[0:3], v[160:163], 0
ds_read_b128 v[184:187], v15 offset:0
ds_read_b128 v[188:191], v15 offset:4096
v_mfma_f32_32x32x16_bf16 v[48:63], a[4:7], v[160:163], 0
ds_read_b128 v[192:195], v17 offset:0
s_add_u32 m0, s41, 57344
s_mov_b32 s62, s64
buffer_load_dwordx4 v5, s[84:87], s62 offen lds
s_waitcnt lgkmcnt(7)
v_mfma_f32_32x32x16_bf16 v[64:79], a[0:3], v[164:167], 0
ds_read_b128 v[196:199], v216 offset:0
ds_read_b128 v[200:203], v16 offset:256
v_mfma_f32_32x32x16_bf16 v[80:95], a[4:7], v[164:167], 0
)ASM"
R"ASM(
ds_read_b128 v[204:207], v16 offset:320
s_waitcnt lgkmcnt(9)
v_mfma_f32_32x32x16_bf16 v[32:47], a[8:11], v[168:171], v[32:47]
ds_read_b128 v[160:163], v2 offset:8192
ds_read_b128 v[164:167], v2 offset:12288
v_mfma_f32_32x32x16_bf16 v[48:63], a[12:15], v[168:171], v[48:63]
s_add_u32 m0, s41, 61440
s_add_u32 s62, s62, s93
buffer_load_dwordx4 v5, s[88:91], s62 offen lds
s_waitcnt lgkmcnt(5)
v_mfma_f32_16x16x32_bf16 v[220:223], v[28:31], v[192:195], 0
s_waitcnt lgkmcnt(3)
v_fmac_f32 v96, v20, v200
s_waitcnt lgkmcnt(2)
v_fmac_f32 v104, v20, v204
v_mfma_f32_32x32x16_bf16 v[64:79], a[8:11], v[172:175], v[64:79]
ds_read_b128 v[192:195], v17 offset:8192
v_fmac_f32 v97, v20, v201
v_fmac_f32 v105, v20, v205
v_mul_f32 v104, v96, v104
v_mul_f32 v96, v21, v96
v_mfma_f32_32x32x16_bf16 v[80:95], a[12:15], v[172:175], v[80:95]
v_mul_f32 v105, v97, v105
v_mul_f32 v97, v21, v97
v_exp_f32 v96, v96
v_mfma_f32_32x32x16_bf16 v[32:47], a[16:19], v[176:179], v[32:47]
ds_read_b128 v[168:171], v3 offset:8192
ds_read_b128 v[172:175], v3 offset:12288
v_exp_f32 v97, v97
v_fma_f32 v96, v96, v22, v22
v_mfma_f32_32x32x16_bf16 v[48:63], a[20:23], v[176:179], v[48:63]
s_add_u32 m0, s41, 65536
s_add_u32 s62, s62, s92
buffer_load_dwordx4 v5, s[84:87], s62 offen lds
v_fma_f32 v97, v97, v22, v22
v_mfma_f32_16x16x32_bf16 v[220:223], v[28:31], v[196:199], v[220:223]
v_rcp_f32 v96, v96
v_mfma_f32_32x32x16_bf16 v[64:79], a[16:19], v[180:183], v[64:79]
v_rcp_f32 v97, v97
v_mul_f32 v104, v104, v96
v_mul_f32 v105, v105, v97
v_cvt_pk_bf16_f32 v96, v104, v105
v_mfma_f32_32x32x16_bf16 v[80:95], a[20:23], v[180:183], v[80:95]
ds_read_b128 v[208:211], v16 offset:272
ds_read_b128 v[212:215], v16 offset:336
v_fmac_f32 v98, v20, v202
v_fmac_f32 v106, v20, v206
v_fmac_f32 v99, v20, v203
v_mfma_f32_32x32x16_bf16 v[32:47], a[24:27], v[184:187], v[32:47]
ds_read_b128 v[176:179], v4 offset:8192
ds_read_b128 v[180:183], v4 offset:12288
v_fmac_f32 v107, v20, v207
v_mul_f32 v106, v98, v106
v_mul_f32 v98, v21, v98
v_mfma_f32_32x32x16_bf16 v[48:63], a[28:31], v[184:187], v[48:63]
s_add_u32 m0, s41, 69632
s_add_u32 s62, s62, s93
buffer_load_dwordx4 v5, s[88:91], s62 offen lds
v_mul_f32 v107, v99, v107
v_mul_f32 v99, v21, v99
s_waitcnt lgkmcnt(6)
v_mfma_f32_16x16x32_bf16 v[220:223], v[28:31], v[192:195], v[220:223]
ds_read_b128 v[196:199], v216 offset:8192
v_mfma_f32_32x32x16_bf16 v[64:79], a[24:27], v[188:191], v[64:79]
v_exp_f32 v98, v98
v_exp_f32 v99, v99
v_fma_f32 v98, v98, v22, v22
v_fma_f32 v99, v99, v22, v22
v_mfma_f32_32x32x16_bf16 v[80:95], a[28:31], v[188:191], v[80:95]
s_waitcnt lgkmcnt(5)
v_rcp_f32 v98, v98
v_rcp_f32 v99, v99
v_mul_f32 v106, v106, v98
v_mfma_f32_32x32x16_bf16 v[32:47], a[32:35], v[160:163], v[32:47]
ds_read_b128 v[184:187], v15 offset:8192
ds_read_b128 v[188:191], v15 offset:12288
v_mul_f32 v107, v107, v99
v_cvt_pk_bf16_f32 v97, v106, v107
v_mfma_f32_32x32x16_bf16 v[48:63], a[36:39], v[160:163], v[48:63]
s_add_u32 m0, s41, 73728
s_add_u32 s62, s62, s92
buffer_load_dwordx4 v5, s[84:87], s62 offen lds
v_mfma_f32_32x32x16_bf16 v[64:79], a[32:35], v[164:167], v[64:79]
v_subrev_u32 v17, 131072, v17
v_subrev_u32 v216, 131072, v216
ds_read_b128 v[192:195], v17 offset:0
v_fmac_f32 v112, v24, v200
v_fmac_f32 v120, v24, v204
v_mfma_f32_32x32x16_bf16 v[80:95], a[36:39], v[164:167], v[80:95]
v_fmac_f32 v113, v24, v201
v_fmac_f32 v121, v24, v205
v_subrev_u32 v2, 131072, v2
v_subrev_u32 v3, 131072, v3
v_subrev_u32 v4, 131072, v4
v_subrev_u32 v15, 131072, v15
v_mfma_f32_32x32x16_bf16 v[32:47], a[40:43], v[168:171], v[32:47]
ds_read_b128 v[160:163], v2 offset:0
ds_read_b128 v[164:167], v2 offset:4096
v_mul_f32 v120, v112, v120
v_mul_f32 v112, v25, v112
v_mul_f32 v121, v113, v121
v_mfma_f32_32x32x16_bf16 v[48:63], a[44:47], v[168:171], v[48:63]
v_mul_f32 v113, v25, v113
v_exp_f32 v112, v112
v_exp_f32 v113, v113
v_fma_f32 v112, v112, v26, v26
s_waitcnt lgkmcnt(5)
v_mfma_f32_16x16x32_bf16 v[220:223], v[28:31], v[196:199], v[220:223]
v_fma_f32 v113, v113, v26, v26
v_mfma_f32_32x32x16_bf16 v[64:79], a[40:43], v[172:175], v[64:79]
s_add_u32 m0, s41, 77824
s_add_u32 s62, s62, s93
buffer_load_dwordx4 v5, s[88:91], s62 offen lds
v_rcp_f32 v112, v112
v_mfma_f32_32x32x16_bf16 v[80:95], a[44:47], v[172:175], v[80:95]
s_waitcnt lgkmcnt(3)
v_rcp_f32 v113, v113
v_mul_f32 v120, v120, v112
v_mul_f32 v121, v121, v113
v_cvt_pk_bf16_f32 v112, v120, v121
v_mfma_f32_32x32x16_bf16 v[32:47], a[48:51], v[176:179], v[32:47]
ds_read_b128 v[168:171], v3 offset:0
ds_read_b128 v[172:175], v3 offset:4096
v_mfma_f32_32x32x16_bf16 v[48:63], a[52:55], v[176:179], v[48:63]
v_fmac_f32 v114, v24, v202
v_fmac_f32 v122, v24, v206
v_fmac_f32 v115, v24, v203
v_fmac_f32 v123, v24, v207
v_mul_f32 v122, v114, v122
v_mul_f32 v114, v25, v114
s_waitcnt lgkmcnt(4)
v_mfma_f32_16x16x32_bf16 v[220:223], v[28:31], v[192:195], v[220:223]
ds_read_b128 v[196:199], v216 offset:0
v_mfma_f32_32x32x16_bf16 v[64:79], a[48:51], v[180:183], v[64:79]
s_add_u32 m0, s41, 81920
s_add_u32 s62, s62, s92
buffer_load_dwordx4 v5, s[84:87], s62 offen lds
v_mul_f32 v123, v115, v123
v_mul_f32 v115, v25, v115
v_mfma_f32_32x32x16_bf16 v[80:95], a[52:55], v[180:183], v[80:95]
v_exp_f32 v114, v114
v_exp_f32 v115, v115
v_fma_f32 v114, v114, v26, v26
v_fma_f32 v115, v115, v26, v26
v_mfma_f32_32x32x16_bf16 v[32:47], a[56:59], v[184:187], v[32:47]
ds_read_b128 v[176:179], v4 offset:0
ds_read_b128 v[180:183], v4 offset:4096
v_rcp_f32 v114, v114
v_mfma_f32_32x32x16_bf16 v[48:63], a[60:63], v[184:187], v[48:63]
v_rcp_f32 v115, v115
v_mul_f32 v122, v122, v114
v_mul_f32 v123, v123, v115
v_cvt_pk_bf16_f32 v113, v122, v123
v_mfma_f32_32x32x16_bf16 v[64:79], a[56:59], v[188:191], v[64:79]
ds_read_b128 v[192:195], v17 offset:8192
s_add_u32 m0, s41, 86016
s_add_u32 s62, s62, s93
buffer_load_dwordx4 v5, s[88:91], s62 offen lds
v_mfma_f32_32x32x16_bf16 v[80:95], a[60:63], v[188:191], v[80:95]
s_waitcnt lgkmcnt(4)
v_mfma_f32_32x32x16_bf16 v[32:47], a[64:67], v[160:163], v[32:47]
ds_read_b128 v[184:187], v15 offset:0
ds_read_b128 v[188:191], v15 offset:4096
v_mfma_f32_32x32x16_bf16 v[48:63], a[68:71], v[160:163], v[48:63]
v_fmac_f32 v100, v20, v208
v_fmac_f32 v108, v20, v212
v_fmac_f32 v101, v20, v209
v_fmac_f32 v109, v20, v213
v_mul_f32 v108, v100, v108
v_mul_f32 v100, v21, v100
s_waitcnt lgkmcnt(5)
v_mfma_f32_16x16x32_bf16 v[220:223], v[28:31], v[196:199], v[220:223]
v_mul_f32 v109, v101, v109
v_mul_f32 v101, v21, v101
v_mfma_f32_32x32x16_bf16 v[64:79], a[64:67], v[164:167], v[64:79]
s_add_u32 m0, s41, 90112
s_add_u32 s62, s62, s92
buffer_load_dwordx4 v5, s[84:87], s62 offen lds
v_exp_f32 v100, v100
v_mfma_f32_32x32x16_bf16 v[80:95], a[68:71], v[164:167], v[80:95]
v_exp_f32 v101, v101
v_fma_f32 v100, v100, v22, v22
v_fma_f32 v101, v101, v22, v22
v_rcp_f32 v100, v100
v_mfma_f32_32x32x16_bf16 v[32:47], a[72:75], v[168:171], v[32:47]
ds_read_b128 v[160:163], v2 offset:8192
ds_read_b128 v[164:167], v2 offset:12288
v_rcp_f32 v101, v101
v_mul_f32 v108, v108, v100
v_mfma_f32_32x32x16_bf16 v[48:63], a[76:79], v[168:171], v[48:63]
v_mul_f32 v109, v109, v101
v_cvt_pk_bf16_f32 v98, v108, v109
s_waitcnt lgkmcnt(4)
v_mfma_f32_16x16x32_bf16 v[220:223], v[28:31], v[192:195], v[220:223]
ds_read_b128 v[196:199], v216 offset:8192
v_mfma_f32_32x32x16_bf16 v[64:79], a[72:75], v[172:175], v[64:79]
v_mfma_f32_32x32x16_bf16 v[80:95], a[76:79], v[172:175], v[80:95]
s_waitcnt lgkmcnt(3)
ds_read_b128 v[200:203], v16 offset:384
ds_read_b128 v[204:207], v16 offset:448
)ASM"
R"ASM(
v_fmac_f32 v102, v20, v210
v_fmac_f32 v110, v20, v214
v_mfma_f32_32x32x16_bf16 v[32:47], a[80:83], v[176:179], v[32:47]
ds_read_b128 v[168:171], v3 offset:8192
ds_read_b128 v[172:175], v3 offset:12288
v_fmac_f32 v103, v20, v211
v_fmac_f32 v111, v20, v215
v_mul_f32 v110, v102, v110
v_mfma_f32_32x32x16_bf16 v[48:63], a[84:87], v[176:179], v[48:63]
v_mul_f32 v102, v21, v102
v_mul_f32 v111, v103, v111
v_mul_f32 v103, v21, v103
v_exp_f32 v102, v102
v_mfma_f32_32x32x16_bf16 v[64:79], a[80:83], v[180:183], v[64:79]
v_exp_f32 v103, v103
v_fma_f32 v102, v102, v22, v22
v_fma_f32 v103, v103, v22, v22
v_rcp_f32 v102, v102
v_mfma_f32_32x32x16_bf16 v[80:95], a[84:87], v[180:183], v[80:95]
v_rcp_f32 v103, v103
v_mul_f32 v110, v110, v102
v_mul_f32 v111, v111, v103
v_cvt_pk_bf16_f32 v99, v110, v111
v_mfma_f32_32x32x16_bf16 v[32:47], a[88:91], v[184:187], v[32:47]
ds_read_b128 v[176:179], v4 offset:8192
ds_read_b128 v[180:183], v4 offset:12288
buffer_store_dwordx4 v[96:99], v19, s[96:99], s69 offen offset:0 sc1
v_mfma_f32_32x32x16_bf16 v[48:63], a[92:95], v[184:187], v[48:63]
s_waitcnt lgkmcnt(6)
v_mfma_f32_16x16x32_bf16 v[220:223], v[28:31], v[196:199], v[220:223]
v_mfma_f32_32x32x16_bf16 v[64:79], a[88:91], v[188:191], v[64:79]
v_mfma_f32_32x32x16_bf16 v[80:95], a[92:95], v[188:191], v[80:95]
s_waitcnt lgkmcnt(2)
v_fmac_f32 v116, v24, v208
v_fmac_f32 v124, v24, v212
v_fmac_f32 v117, v24, v209
v_fmac_f32 v125, v24, v213
v_mul_f32 v124, v116, v124
v_mfma_f32_32x32x16_bf16 v[32:47], a[96:99], v[160:163], v[32:47]
s_waitcnt vmcnt(10) lgkmcnt(0)
s_barrier
s_mov_b64 exec, 1
buffer_store_dword v219, off, s[96:99], s52 sc1
s_mov_b64 exec, -1
v_mfma_f32_32x32x16_bf16 v[48:63], a[100:103], v[160:163], v[48:63]
ds_read_b128 v[184:187], v15 offset:8192
ds_read_b128 v[188:191], v15 offset:12288
v_mul_f32 v116, v25, v116
v_mul_f32 v125, v117, v125
v_mul_f32 v117, v25, v117
v_mfma_f32_32x32x16_bf16 v[64:79], a[96:99], v[164:167], v[64:79]
ds_read_b128 v[192:195], v17 offset:16384
s_add_u32 m0, s41, 94208
s_add_u32 s62, s62, s93
buffer_load_dwordx4 v5, s[88:91], s62 offen lds
v_mfma_f32_32x32x16_bf16 v[80:95], a[100:103], v[164:167], v[80:95]
v_exp_f32 v116, v116
v_exp_f32 v117, v117
v_fma_f32 v116, v116, v26, v26
v_fma_f32 v117, v117, v26, v26
v_mfma_f32_32x32x16_bf16 v[32:47], a[104:107], v[168:171], v[32:47]
ds_read_b128 v[160:163], v2 offset:16384
ds_read_b128 v[164:167], v2 offset:20480
v_rcp_f32 v116, v116
v_mfma_f32_32x32x16_bf16 v[48:63], a[108:111], v[168:171], v[48:63]
v_rcp_f32 v117, v117
v_mul_f32 v124, v124, v116
v_mul_f32 v125, v125, v117
v_cvt_pk_bf16_f32 v114, v124, v125
v_mfma_f32_32x32x16_bf16 v[64:79], a[104:107], v[172:175], v[64:79]
ds_read_b128 v[196:199], v216 offset:16384
v_mfma_f32_32x32x16_bf16 v[80:95], a[108:111], v[172:175], v[80:95]
s_waitcnt lgkmcnt(4)
v_mfma_f32_32x32x16_bf16 v[32:47], a[112:115], v[176:179], v[32:47]
ds_read_b128 v[168:171], v3 offset:16384
ds_read_b128 v[172:175], v3 offset:20480
v_fmac_f32 v118, v24, v210
v_fmac_f32 v126, v24, v214
v_fmac_f32 v119, v24, v211
v_mfma_f32_32x32x16_bf16 v[48:63], a[116:119], v[176:179], v[48:63]
s_add_u32 m0, s41, 98304
s_add_u32 s62, s62, s92
buffer_load_dwordx4 v5, s[84:87], s62 offen lds
v_fmac_f32 v127, v24, v215
v_mul_f32 v126, v118, v126
s_waitcnt lgkmcnt(5)
v_mfma_f32_16x16x32_bf16 v[220:223], v[28:31], v[192:195], v[220:223]
v_mul_f32 v118, v25, v118
v_mul_f32 v127, v119, v127
v_mfma_f32_32x32x16_bf16 v[64:79], a[112:115], v[180:183], v[64:79]
v_mul_f32 v119, v25, v119
v_exp_f32 v118, v118
v_exp_f32 v119, v119
v_fma_f32 v118, v118, v26, v26
v_mfma_f32_32x32x16_bf16 v[80:95], a[116:119], v[180:183], v[80:95]
v_fma_f32 v119, v119, v26, v26
v_rcp_f32 v118, v118
v_rcp_f32 v119, v119
v_mul_f32 v126, v126, v118
v_mfma_f32_32x32x16_bf16 v[32:47], a[120:123], v[184:187], v[32:47]
ds_read_b128 v[176:179], v4 offset:16384
ds_read_b128 v[180:183], v4 offset:20480
v_mul_f32 v127, v127, v119
v_cvt_pk_bf16_f32 v115, v126, v127
v_mfma_f32_32x32x16_bf16 v[48:63], a[124:127], v[184:187], v[48:63]
buffer_store_dwordx4 v[112:115], v19, s[96:99], s69 offen offset:1024 sc1
s_waitcnt lgkmcnt(4)
v_mfma_f32_16x16x32_bf16 v[220:223], v[28:31], v[196:199], v[220:223]
ds_read_b128 v[192:195], v17 offset:24576
v_mfma_f32_32x32x16_bf16 v[64:79], a[120:123], v[188:191], v[64:79]
v_fmac_f32 v128, v20, v200
v_fmac_f32 v136, v20, v204
v_fmac_f32 v129, v20, v201
v_fmac_f32 v137, v20, v205
v_mul_f32 v136, v128, v136
v_mul_f32 v128, v21, v128
v_mfma_f32_32x32x16_bf16 v[80:95], a[124:127], v[188:191], v[80:95]
s_waitcnt lgkmcnt(3)
s_add_u32 m0, s41, 102400
s_add_u32 s62, s62, s93
buffer_load_dwordx4 v5, s[88:91], s62 offen lds
v_mul_f32 v137, v129, v137
v_mfma_f32_32x32x16_bf16 v[32:47], a[128:131], v[160:163], v[32:47]
ds_read_b128 v[184:187], v15 offset:16384
ds_read_b128 v[188:191], v15 offset:20480
v_mul_f32 v129, v21, v129
v_exp_f32 v128, v128
v_mfma_f32_32x32x16_bf16 v[48:63], a[132:135], v[160:163], v[48:63]
v_exp_f32 v129, v129
v_fma_f32 v128, v128, v22, v22
v_fma_f32 v129, v129, v22, v22
v_rcp_f32 v128, v128
v_mfma_f32_32x32x16_bf16 v[64:79], a[128:131], v[164:167], v[64:79]
v_rcp_f32 v129, v129
v_mul_f32 v136, v136, v128
v_mul_f32 v137, v137, v129
v_cvt_pk_bf16_f32 v128, v136, v137
v_mfma_f32_32x32x16_bf16 v[80:95], a[132:135], v[164:167], v[80:95]
v_mfma_f32_32x32x16_bf16 v[32:47], a[136:139], v[168:171], v[32:47]
ds_read_b128 v[160:163], v2 offset:24576
ds_read_b128 v[164:167], v2 offset:28672
v_mfma_f32_32x32x16_bf16 v[48:63], a[140:143], v[168:171], v[48:63]
s_waitcnt lgkmcnt(4)
v_mfma_f32_16x16x32_bf16 v[220:223], v[28:31], v[192:195], v[220:223]
ds_read_b128 v[196:199], v216 offset:24576
v_mfma_f32_32x32x16_bf16 v[64:79], a[136:139], v[172:175], v[64:79]
ds_read_b128 v[208:211], v16 offset:400
ds_read_b128 v[212:215], v16 offset:464
v_fmac_f32 v130, v20, v202
v_fmac_f32 v138, v20, v206
v_fmac_f32 v131, v20, v203
v_mfma_f32_32x32x16_bf16 v[80:95], a[140:143], v[172:175], v[80:95]
s_waitcnt lgkmcnt(5)
s_add_u32 m0, s41, 106496
s_add_u32 s62, s62, s92
buffer_load_dwordx4 v5, s[84:87], s62 offen lds
v_fmac_f32 v139, v20, v207
v_mfma_f32_32x32x16_bf16 v[32:47], a[144:147], v[176:179], v[32:47]
ds_read_b128 v[168:171], v3 offset:24576
ds_read_b128 v[172:175], v3 offset:28672
v_mul_f32 v138, v130, v138
v_mul_f32 v130, v21, v130
v_mul_f32 v139, v131, v139
v_mfma_f32_32x32x16_bf16 v[48:63], a[148:151], v[176:179], v[48:63]
v_mul_f32 v131, v21, v131
v_exp_f32 v130, v130
v_exp_f32 v131, v131
v_fma_f32 v130, v130, v22, v22
v_mfma_f32_32x32x16_bf16 v[64:79], a[144:147], v[180:183], v[64:79]
v_fma_f32 v131, v131, v22, v22
v_rcp_f32 v130, v130
v_rcp_f32 v131, v131
v_mul_f32 v138, v138, v130
v_mfma_f32_32x32x16_bf16 v[80:95], a[148:151], v[180:183], v[80:95]
v_mul_f32 v139, v139, v131
v_cvt_pk_bf16_f32 v129, v138, v139
v_mfma_f32_32x32x16_bf16 v[32:47], a[152:155], v[184:187], v[32:47]
ds_read_b128 v[176:179], v4 offset:24576
ds_read_b128 v[180:183], v4 offset:28672
v_mfma_f32_32x32x16_bf16 v[48:63], a[156:159], v[184:187], v[48:63]
s_waitcnt lgkmcnt(6)
v_mfma_f32_16x16x32_bf16 v[220:223], v[28:31], v[196:199], v[220:223]
ds_read_b128 v[192:195], v17 offset:32768
v_mfma_f32_32x32x16_bf16 v[64:79], a[152:155], v[188:191], v[64:79]
s_add_u32 m0, s41, 110592
s_add_u32 s62, s62, s93
buffer_load_dwordx4 v5, s[88:91], s62 offen lds
v_fmac_f32 v144, v24, v200
v_fmac_f32 v152, v24, v204
v_mfma_f32_32x32x16_bf16 v[80:95], a[156:159], v[188:191], v[80:95]
s_waitcnt lgkmcnt(3)
v_fmac_f32 v145, v24, v201
v_fmac_f32 v153, v24, v205
v_mul_f32 v152, v144, v152
v_mul_f32 v144, v25, v144
v_mul_f32 v153, v145, v153
v_mfma_f32_32x32x16_bf16 v[32:47], a[160:163], v[160:163], v[32:47]
ds_read_b128 v[184:187], v15 offset:24576
ds_read_b128 v[188:191], v15 offset:28672
v_mul_f32 v145, v25, v145
)ASM"
R"ASM(
v_exp_f32 v144, v144
v_mfma_f32_32x32x16_bf16 v[48:63], a[164:167], v[160:163], v[48:63]
v_exp_f32 v145, v145
v_fma_f32 v144, v144, v26, v26
v_fma_f32 v145, v145, v26, v26
v_rcp_f32 v144, v144
v_mfma_f32_32x32x16_bf16 v[64:79], a[160:163], v[164:167], v[64:79]
v_rcp_f32 v145, v145
v_mul_f32 v152, v152, v144
v_mul_f32 v153, v153, v145
v_cvt_pk_bf16_f32 v144, v152, v153
v_mfma_f32_32x32x16_bf16 v[80:95], a[164:167], v[164:167], v[80:95]
v_mfma_f32_32x32x16_bf16 v[32:47], a[168:171], v[168:171], v[32:47]
ds_read_b128 v[160:163], v2 offset:32768
ds_read_b128 v[164:167], v2 offset:36864
v_mfma_f32_32x32x16_bf16 v[48:63], a[172:175], v[168:171], v[48:63]
s_waitcnt lgkmcnt(4)
v_mfma_f32_16x16x32_bf16 v[220:223], v[28:31], v[192:195], v[220:223]
ds_read_b128 v[196:199], v216 offset:32768
v_mfma_f32_32x32x16_bf16 v[64:79], a[168:171], v[172:175], v[64:79]
s_add_u32 m0, s41, 114688
s_add_u32 s62, s62, s92
buffer_load_dwordx4 v5, s[84:87], s62 offen lds
v_fmac_f32 v146, v24, v202
v_fmac_f32 v154, v24, v206
v_mfma_f32_32x32x16_bf16 v[80:95], a[172:175], v[172:175], v[80:95]
s_waitcnt lgkmcnt(3)
v_fmac_f32 v147, v24, v203
v_fmac_f32 v155, v24, v207
v_mul_f32 v154, v146, v154
v_mul_f32 v146, v25, v146
v_mul_f32 v155, v147, v155
v_mfma_f32_32x32x16_bf16 v[32:47], a[176:179], v[176:179], v[32:47]
ds_read_b128 v[168:171], v3 offset:32768
ds_read_b128 v[172:175], v3 offset:36864
v_mul_f32 v147, v25, v147
v_exp_f32 v146, v146
v_mfma_f32_32x32x16_bf16 v[48:63], a[180:183], v[176:179], v[48:63]
v_exp_f32 v147, v147
v_fma_f32 v146, v146, v26, v26
v_fma_f32 v147, v147, v26, v26
v_rcp_f32 v146, v146
v_mfma_f32_32x32x16_bf16 v[64:79], a[176:179], v[180:183], v[64:79]
v_rcp_f32 v147, v147
v_mul_f32 v154, v154, v146
v_mul_f32 v155, v155, v147
v_cvt_pk_bf16_f32 v145, v154, v155
v_mfma_f32_32x32x16_bf16 v[80:95], a[180:183], v[180:183], v[80:95]
v_mfma_f32_32x32x16_bf16 v[32:47], a[184:187], v[184:187], v[32:47]
ds_read_b128 v[176:179], v4 offset:32768
ds_read_b128 v[180:183], v4 offset:36864
v_mfma_f32_32x32x16_bf16 v[48:63], a[188:191], v[184:187], v[48:63]
s_waitcnt lgkmcnt(4)
v_mfma_f32_16x16x32_bf16 v[220:223], v[28:31], v[196:199], v[220:223]
ds_read_b128 v[192:195], v17 offset:40960
v_mfma_f32_32x32x16_bf16 v[64:79], a[184:187], v[188:191], v[64:79]
s_add_u32 m0, s41, 118784
s_add_u32 s62, s62, s93
buffer_load_dwordx4 v5, s[88:91], s62 offen lds
v_fmac_f32 v132, v20, v208
v_fmac_f32 v140, v20, v212
v_mfma_f32_32x32x16_bf16 v[80:95], a[188:191], v[188:191], v[80:95]
s_waitcnt lgkmcnt(3)
v_fmac_f32 v133, v20, v209
v_fmac_f32 v141, v20, v213
v_mul_f32 v140, v132, v140
v_mul_f32 v132, v21, v132
v_mul_f32 v141, v133, v141
v_mfma_f32_32x32x16_bf16 v[32:47], a[192:195], v[160:163], v[32:47]
ds_read_b128 v[184:187], v15 offset:32768
ds_read_b128 v[188:191], v15 offset:36864
v_mul_f32 v133, v21, v133
v_exp_f32 v132, v132
v_mfma_f32_32x32x16_bf16 v[48:63], a[196:199], v[160:163], v[48:63]
v_exp_f32 v133, v133
v_fma_f32 v132, v132, v22, v22
v_fma_f32 v133, v133, v22, v22
v_rcp_f32 v132, v132
v_mfma_f32_32x32x16_bf16 v[64:79], a[192:195], v[164:167], v[64:79]
v_rcp_f32 v133, v133
v_mul_f32 v140, v140, v132
v_mul_f32 v141, v141, v133
v_cvt_pk_bf16_f32 v130, v140, v141
v_mfma_f32_32x32x16_bf16 v[80:95], a[196:199], v[164:167], v[80:95]
v_mfma_f32_32x32x16_bf16 v[32:47], a[200:203], v[168:171], v[32:47]
ds_read_b128 v[160:163], v2 offset:40960
ds_read_b128 v[164:167], v2 offset:45056
v_mfma_f32_32x32x16_bf16 v[48:63], a[204:207], v[168:171], v[48:63]
s_add_u32 m0, s41, 122880
s_add_u32 s62, s62, s92
buffer_load_dwordx4 v5, s[84:87], s62 offen lds
v_fmac_f32 v134, v20, v210
v_fmac_f32 v142, v20, v214
s_waitcnt lgkmcnt(4)
v_mfma_f32_16x16x32_bf16 v[220:223], v[28:31], v[192:195], v[220:223]
ds_read_b128 v[196:199], v216 offset:40960
v_mfma_f32_32x32x16_bf16 v[64:79], a[200:203], v[172:175], v[64:79]
v_fmac_f32 v135, v20, v211
v_fmac_f32 v143, v20, v215
v_mul_f32 v142, v134, v142
v_mul_f32 v134, v21, v134
v_mul_f32 v143, v135, v143
v_mul_f32 v135, v21, v135
v_mfma_f32_32x32x16_bf16 v[80:95], a[204:207], v[172:175], v[80:95]
s_waitcnt lgkmcnt(3)
v_exp_f32 v134, v134
v_exp_f32 v135, v135
v_fma_f32 v134, v134, v22, v22
v_mfma_f32_32x32x16_bf16 v[32:47], a[208:211], v[176:179], v[32:47]
ds_read_b128 v[168:171], v3 offset:40960
ds_read_b128 v[172:175], v3 offset:45056
v_fma_f32 v135, v135, v22, v22
v_rcp_f32 v134, v134
v_mfma_f32_32x32x16_bf16 v[48:63], a[212:215], v[176:179], v[48:63]
v_rcp_f32 v135, v135
v_mul_f32 v142, v142, v134
v_mul_f32 v143, v143, v135
v_cvt_pk_bf16_f32 v131, v142, v143
s_add_u32 s75, s69, 65536
v_mfma_f32_32x32x16_bf16 v[64:79], a[208:211], v[180:183], v[64:79]
buffer_store_dwordx4 v[128:131], v19, s[96:99], s75 offen offset:0 sc1
v_mfma_f32_32x32x16_bf16 v[80:95], a[212:215], v[180:183], v[80:95]
v_mfma_f32_32x32x16_bf16 v[32:47], a[216:219], v[184:187], v[32:47]
ds_read_b128 v[176:179], v4 offset:40960
ds_read_b128 v[180:183], v4 offset:45056
v_mfma_f32_32x32x16_bf16 v[48:63], a[220:223], v[184:187], v[48:63]
s_add_u32 m0, s41, 126976
s_add_u32 s62, s62, s93
buffer_load_dwordx4 v5, s[88:91], s62 offen lds
v_fmac_f32 v148, v24, v208
v_fmac_f32 v156, v24, v212
s_waitcnt lgkmcnt(4)
v_mfma_f32_16x16x32_bf16 v[220:223], v[28:31], v[196:199], v[220:223]
ds_read_b128 v[192:195], v17 offset:49152
v_mfma_f32_32x32x16_bf16 v[64:79], a[216:219], v[188:191], v[64:79]
v_fmac_f32 v149, v24, v209
v_fmac_f32 v157, v24, v213
v_mul_f32 v156, v148, v156
v_mul_f32 v148, v25, v148
v_mul_f32 v157, v149, v157
v_mul_f32 v149, v25, v149
v_mfma_f32_32x32x16_bf16 v[80:95], a[220:223], v[188:191], v[80:95]
s_waitcnt lgkmcnt(3)
v_exp_f32 v148, v148
v_exp_f32 v149, v149
v_fma_f32 v148, v148, v26, v26
v_mfma_f32_32x32x16_bf16 v[32:47], a[224:227], v[160:163], v[32:47]
ds_read_b128 v[184:187], v15 offset:40960
ds_read_b128 v[188:191], v15 offset:45056
v_fma_f32 v149, v149, v26, v26
v_rcp_f32 v148, v148
v_mfma_f32_32x32x16_bf16 v[48:63], a[228:231], v[160:163], v[48:63]
v_rcp_f32 v149, v149
v_mul_f32 v156, v156, v148
v_mul_f32 v157, v157, v149
v_cvt_pk_bf16_f32 v146, v156, v157
v_mfma_f32_32x32x16_bf16 v[64:79], a[224:227], v[164:167], v[64:79]
v_mfma_f32_32x32x16_bf16 v[80:95], a[228:231], v[164:167], v[80:95]
v_mfma_f32_32x32x16_bf16 v[32:47], a[232:235], v[168:171], v[32:47]
ds_read_b128 v[160:163], v2 offset:49152
ds_read_b128 v[164:167], v2 offset:53248
v_fmac_f32 v150, v24, v210
v_fmac_f32 v158, v24, v214
v_fmac_f32 v151, v24, v211
v_mfma_f32_32x32x16_bf16 v[48:63], a[236:239], v[168:171], v[48:63]
v_fmac_f32 v159, v24, v215
v_mul_f32 v158, v150, v158
v_mul_f32 v150, v25, v150
v_mul_f32 v159, v151, v159
v_mul_f32 v151, v25, v151
s_waitcnt lgkmcnt(4)
v_mfma_f32_16x16x32_bf16 v[220:223], v[28:31], v[192:195], v[220:223]
ds_read_b128 v[196:199], v216 offset:49152
v_mfma_f32_32x32x16_bf16 v[64:79], a[232:235], v[172:175], v[64:79]
v_exp_f32 v150, v150
v_exp_f32 v151, v151
v_fma_f32 v150, v150, v26, v26
v_fma_f32 v151, v151, v26, v26
v_mfma_f32_32x32x16_bf16 v[80:95], a[236:239], v[172:175], v[80:95]
s_waitcnt lgkmcnt(3)
v_rcp_f32 v150, v150
v_rcp_f32 v151, v151
v_mul_f32 v158, v158, v150
v_mfma_f32_32x32x16_bf16 v[32:47], a[240:243], v[176:179], v[32:47]
ds_read_b128 v[168:171], v3 offset:49152
ds_read_b128 v[172:175], v3 offset:53248
v_mul_f32 v159, v159, v151
v_cvt_pk_bf16_f32 v147, v158, v159
s_add_u32 s75, s69, 65536
v_mfma_f32_32x32x16_bf16 v[48:63], a[244:247], v[176:179], v[48:63]
buffer_store_dwordx4 v[144:147], v19, s[96:99], s75 offen offset:1024 sc1
v_mfma_f32_32x32x16_bf16 v[64:79], a[240:243], v[180:183], v[64:79]
v_mfma_f32_32x32x16_bf16 v[80:95], a[244:247], v[180:183], v[80:95]
v_mfma_f32_32x32x16_bf16 v[32:47], a[248:251], v[184:187], v[32:47]
ds_read_b128 v[176:179], v4 offset:49152
ds_read_b128 v[180:183], v4 offset:53248
v_mfma_f32_32x32x16_bf16 v[48:63], a[252:255], v[184:187], v[48:63]
s_waitcnt lgkmcnt(4)
v_mfma_f32_16x16x32_bf16 v[220:223], v[28:31], v[196:199], v[220:223]
v_mfma_f32_32x32x16_bf16 v[64:79], a[248:251], v[188:191], v[64:79]
)ASM"
R"ASM(
v_mfma_f32_32x32x16_bf16 v[80:95], a[252:255], v[188:191], v[80:95]
s_waitcnt lgkmcnt(2)
v_mfma_f32_32x32x16_bf16 v[32:47], v[224:227], v[160:163], v[32:47]
ds_read_b128 v[184:187], v15 offset:49152
ds_read_b128 v[188:191], v15 offset:53248
s_nop 1
v_and_b32 v221, 15, v1
v_lshlrev_b32 v221, 2, v221
s_lshl_b32 s76, s40, 6
v_mfma_f32_32x32x16_bf16 v[48:63], v[228:231], v[160:163], v[48:63]
v_add_u32 v221, s76, v221
v_add_u32 v221, 147456, v221
ds_write_b32 v221, v220
v_mfma_f32_32x32x16_bf16 v[64:79], v[224:227], v[164:167], v[64:79]
v_mfma_f32_32x32x16_bf16 v[80:95], v[228:231], v[164:167], v[80:95]
v_mfma_f32_32x32x16_bf16 v[32:47], v[232:235], v[168:171], v[32:47]
s_waitcnt vmcnt(13) lgkmcnt(0)
s_barrier
v_mfma_f32_32x32x16_bf16 v[48:63], v[236:239], v[168:171], v[48:63]
ds_read_b64_tr_b16 v[160:161], v8 offset:8192
ds_read_b64_tr_b16 v[162:163], v8 offset:8448
v_mfma_f32_32x32x16_bf16 v[64:79], v[232:235], v[172:175], v[64:79]
ds_read_b64_tr_b16 v[164:165], v9 offset:8192
ds_read_b64_tr_b16 v[166:167], v9 offset:8448
v_mfma_f32_32x32x16_bf16 v[80:95], v[236:239], v[172:175], v[80:95]
v_mfma_f32_32x32x16_bf16 v[32:47], v[240:243], v[176:179], v[32:47]
ds_read_b64_tr_b16 v[168:169], v8 offset:10240
ds_read_b64_tr_b16 v[170:171], v8 offset:10496
s_mov_b32 s64, s65
s_mov_b32 s65, s66
s_mov_b32 s66, s67
v_mfma_f32_32x32x16_bf16 v[48:63], v[244:247], v[176:179], v[48:63]
ds_read_b64_tr_b16 v[172:173], v9 offset:10240
ds_read_b64_tr_b16 v[174:175], v9 offset:10496
s_mov_b32 s69, s73
s_mov_b32 s73, s74
s_mov_b32 s74, s59
v_mfma_f32_32x32x16_bf16 v[64:79], v[240:243], v[180:183], v[64:79]
s_mov_b32 s59, s95
s_sub_u32 s83, s83, 1
s_mov_b32 s52, s53
s_mov_b32 s53, s54
s_mov_b32 s54, s55
s_mov_b32 s55, s56
v_mfma_f32_32x32x16_bf16 v[80:95], v[244:247], v[180:183], v[80:95]
s_mov_b32 s56, s57
v_mfma_f32_32x32x16_bf16 v[32:47], v[248:251], v[184:187], v[32:47]
ds_read_b64_tr_b16 v[176:177], v8 offset:12288
ds_read_b64_tr_b16 v[178:179], v8 offset:12544
v_mfma_f32_32x32x16_bf16 v[48:63], v[252:255], v[184:187], v[48:63]
ds_read_b64_tr_b16 v[180:181], v9 offset:12288
ds_read_b64_tr_b16 v[182:183], v9 offset:12544
v_mfma_f32_32x32x16_bf16 v[64:79], v[248:251], v[188:191], v[64:79]
v_mfma_f32_32x32x16_bf16 v[80:95], v[252:255], v[188:191], v[80:95]
s_cmp_eq_u32 s83, 0
s_waitcnt lgkmcnt(10)
v_mfma_f32_32x32x16_bf16 v[96:111], v[160:163], a[0:3], 0
ds_read_b64_tr_b16 v[184:185], v8 offset:14336
ds_read_b64_tr_b16 v[186:187], v8 offset:14592
s_add_u32 m0, s41, 131072
s_mov_b32 s62, s64
buffer_load_dwordx4 v5, s[84:87], s62 offen lds
v_and_b32 v214, 15, v1
v_mfma_f32_32x32x16_bf16 v[112:127], v[160:163], a[4:7], 0
ds_read_b64_tr_b16 v[188:189], v9 offset:14336
ds_read_b64_tr_b16 v[190:191], v9 offset:14592
v_bfe_u32 v215, v1, 4, 1
v_lshlrev_b32 v214, 2, v214
v_lshl_add_u32 v214, v215, 7, v214
v_add_u32 v214, 147456, v214
s_waitcnt lgkmcnt(12)
v_mfma_f32_32x32x16_bf16 v[128:143], v[164:167], a[0:3], 0
ds_read_b64_tr_b16 v[192:193], v13 offset:8192
ds_read_b64_tr_b16 v[194:195], v13 offset:10240
v_mfma_f32_32x32x16_bf16 v[144:159], v[164:167], a[4:7], 0
ds_read_b64_tr_b16 v[196:197], v13 offset:12288
ds_read_b64_tr_b16 v[198:199], v13 offset:14336
s_waitcnt lgkmcnt(14)
v_mfma_f32_32x32x16_bf16 v[96:111], v[168:171], a[8:11], v[96:111]
ds_read_b64_tr_b16 v[160:161], v8 offset:16384
ds_read_b64_tr_b16 v[162:163], v8 offset:16640
v_mfma_f32_32x32x16_bf16 v[112:127], v[168:171], a[12:15], v[112:127]
ds_read_b64_tr_b16 v[164:165], v9 offset:16384
ds_read_b64_tr_b16 v[166:167], v9 offset:16640
s_waitcnt lgkmcnt(6)
v_mfma_f32_16x16x32_bf16 v[220:223], v[192:195], v[28:31], 0
ds_read_b32 v2, v214
v_mfma_f32_32x32x16_bf16 v[128:143], v[172:175], a[8:11], v[128:143]
s_add_u32 m0, s41, 135168
s_add_u32 s62, s62, s93
buffer_load_dwordx4 v5, s[88:91], s62 offen lds
ds_read_b32 v3, v214 offset:64
v_and_b32 v4, 31, v1
v_lshlrev_b32 v4, 6, v4
v_mfma_f32_32x32x16_bf16 v[144:159], v[172:175], a[12:15], v[144:159]
v_add_u32 v4, 150528, v4
ds_read_b128 v[208:211], v4
ds_read_b64 v[212:213], v4 offset:16
v_bfe_u32 v19, v1, 5, 1
v_lshlrev_b32 v19, 4, v19
v_mfma_f32_32x32x16_bf16 v[96:111], v[176:179], a[16:19], v[96:111]
ds_read_b64_tr_b16 v[168:169], v8 offset:18432
ds_read_b64_tr_b16 v[170:171], v8 offset:18688
s_lshl_b32 s76, s40, 9
s_add_u32 s76, s76, 148480
v_add_u32 v19, s76, v19
v_and_b32 v16, 63, v1
v_mfma_f32_32x32x16_bf16 v[112:127], v[176:179], a[20:23], v[112:127]
ds_read_b64_tr_b16 v[172:173], v9 offset:18432
ds_read_b64_tr_b16 v[174:175], v9 offset:18688
v_cmp_gt_u32 vcc, 32, v16
v_lshlrev_b32 v16, 4, v16
s_waitcnt lgkmcnt(12)
v_mfma_f32_16x16x32_bf16 v[220:223], v[196:199], v[28:31], v[220:223]
ds_read_b64_tr_b16 v[192:193], v13 offset:16384
v_mfma_f32_32x32x16_bf16 v[128:143], v[180:183], a[16:19], v[128:143]
ds_read_b64_tr_b16 v[194:195], v13 offset:18432
s_add_u32 m0, s41, 139264
s_add_u32 s62, s62, s92
buffer_load_dwordx4 v5, s[84:87], s62 offen lds
ds_read_b128 v[200:203], v19 offset:0
v_mfma_f32_32x32x16_bf16 v[144:159], v[180:183], a[20:23], v[144:159]
ds_read_b128 v[204:207], v19 offset:256
v_mfma_f32_32x32x16_bf16 v[96:111], v[184:187], a[24:27], v[96:111]
ds_read_b64_tr_b16 v[176:177], v8 offset:20480
ds_read_b64_tr_b16 v[178:179], v8 offset:20736
v_mfma_f32_32x32x16_bf16 v[112:127], v[184:187], a[28:31], v[112:127]
ds_read_b64_tr_b16 v[180:181], v9 offset:20480
ds_read_b64_tr_b16 v[182:183], v9 offset:20736
v_mov_b32 v4, 0
v_mov_b32 v15, 0
s_waitcnt lgkmcnt(5)
v_fmac_f32 v32, v200, v2
v_fmac_f32 v64, v200, v3
v_mfma_f32_32x32x16_bf16 v[128:143], v[188:191], a[24:27], v[128:143]
ds_read_b64_tr_b16 v[196:197], v13 offset:20480
ds_read_b64_tr_b16 v[198:199], v13 offset:22528
v_fmac_f32 v33, v201, v2
v_fmac_f32 v65, v201, v3
v_fmac_f32 v34, v202, v2
v_fmac_f32 v66, v202, v3
v_mfma_f32_32x32x16_bf16 v[144:159], v[188:191], a[28:31], v[144:159]
v_fmac_f32 v35, v203, v2
v_fmac_f32 v67, v203, v3
s_waitcnt lgkmcnt(6)
v_mul_f32 v32, v204, v32
v_mul_f32 v64, v204, v64
v_mul_f32 v33, v205, v33
v_mul_f32 v65, v205, v65
v_mfma_f32_32x32x16_bf16 v[96:111], v[160:163], a[32:35], v[96:111]
ds_read_b64_tr_b16 v[184:185], v8 offset:22528
ds_read_b64_tr_b16 v[186:187], v8 offset:22784
s_add_u32 m0, s41, 143360
s_add_u32 s62, s62, s93
buffer_load_dwordx4 v5, s[88:91], s62 offen lds
v_mul_f32 v34, v206, v34
v_mfma_f32_32x32x16_bf16 v[112:127], v[160:163], a[36:39], v[112:127]
ds_read_b64_tr_b16 v[188:189], v9 offset:22528
ds_read_b64_tr_b16 v[190:191], v9 offset:22784
v_mul_f32 v66, v206, v66
v_mul_f32 v35, v207, v35
v_mul_f32 v67, v207, v67
v_mfma_f32_16x16x32_bf16 v[220:223], v[192:195], v[28:31], v[220:223]
ds_read_b128 v[200:203], v19 offset:32
v_mfma_f32_32x32x16_bf16 v[128:143], v[164:167], a[32:35], v[128:143]
ds_read_b128 v[204:207], v19 offset:288
v_fma_f32 v217, v208, v32, v33
v_fma_f32 v23, v208, v64, v65
v_fma_f32 v217, -v209, v64, v217
v_fmac_f32 v23, v209, v32
v_fma_f32 v17, v208, v217, v34
v_fma_f32 v216, v208, v23, v66
v_mfma_f32_32x32x16_bf16 v[144:159], v[164:167], a[36:39], v[144:159]
v_fma_f32 v17, -v209, v23, v17
v_fmac_f32 v216, v209, v217
v_fma_f32 v217, v208, v17, v35
v_fma_f32 v23, v208, v216, v67
v_fma_f32 v217, -v209, v216, v217
v_fmac_f32 v23, v209, v17
v_mov_b32 v27, v217
v_mfma_f32_32x32x16_bf16 v[96:111], v[168:171], a[40:43], v[96:111]
ds_read_b64_tr_b16 v[160:161], v8 offset:24576
ds_read_b64_tr_b16 v[162:163], v8 offset:24832
v_mov_b32 v215, v23
v_permlane32_swap_b32 v217, v27
s_nop 0
v_permlane32_swap_b32 v23, v215
v_fma_f32 v17, v210, v4, v217
v_mfma_f32_32x32x16_bf16 v[112:127], v[168:171], a[44:47], v[112:127]
ds_read_b64_tr_b16 v[164:165], v9 offset:24576
ds_read_b64_tr_b16 v[166:167], v9 offset:24832
s_add_u32 m0, s41, 0
s_add_u32 s62, s62, s92
buffer_load_dwordx4 v5, s[84:87], s62 offen lds
v_fma_f32 v216, v210, v15, v23
s_waitcnt lgkmcnt(10)
v_mfma_f32_16x16x32_bf16 v[220:223], v[196:199], v[28:31], v[220:223]
ds_read_b64_tr_b16 v[192:193], v13 offset:24576
v_mfma_f32_32x32x16_bf16 v[128:143], v[172:175], a[40:43], v[128:143]
ds_read_b64_tr_b16 v[194:195], v13 offset:26624
)ASM"
R"ASM(
v_fma_f32 v17, -v211, v15, v17
v_fmac_f32 v216, v211, v4
v_cndmask_b32 v217, v17, v4, vcc
v_cndmask_b32 v23, v216, v15, vcc
v_fma_f32 v4, v210, v17, v27
v_fma_f32 v15, v210, v216, v215
v_mfma_f32_32x32x16_bf16 v[144:159], v[172:175], a[44:47], v[144:159]
s_waitcnt lgkmcnt(8)
v_fma_f32 v4, -v211, v216, v4
v_fmac_f32 v15, v211, v17
v_fmac_f32 v32, v208, v217
v_fmac_f32 v64, v208, v23
v_fma_f32 v32, -v209, v23, v32
v_fmac_f32 v64, v209, v217
v_mfma_f32_32x32x16_bf16 v[96:111], v[176:179], a[48:51], v[96:111]
ds_read_b64_tr_b16 v[168:169], v8 offset:26624
ds_read_b64_tr_b16 v[170:171], v8 offset:26880
v_fmac_f32 v33, v208, v32
v_fmac_f32 v65, v208, v64
v_fma_f32 v33, -v209, v64, v33
v_fmac_f32 v65, v209, v32
v_mfma_f32_32x32x16_bf16 v[112:127], v[176:179], a[52:55], v[112:127]
ds_read_b64_tr_b16 v[172:173], v9 offset:26624
ds_read_b64_tr_b16 v[174:175], v9 offset:26880
v_fmac_f32 v34, v208, v33
v_fmac_f32 v66, v208, v65
v_fma_f32 v34, -v209, v65, v34
v_fmac_f32 v66, v209, v33
v_mfma_f32_32x32x16_bf16 v[128:143], v[180:183], a[48:51], v[128:143]
s_add_u32 m0, s41, 4096
s_add_u32 s62, s62, s93
buffer_load_dwordx4 v5, s[88:91], s62 offen lds
v_fmac_f32 v35, v208, v34
v_fmac_f32 v67, v208, v66
v_fma_f32 v35, -v209, v66, v35
v_mfma_f32_32x32x16_bf16 v[144:159], v[180:183], a[52:55], v[144:159]
v_fmac_f32 v67, v209, v34
v_mul_f32 v214, v213, v32
v_mul_f32 v32, v212, v32
v_fma_f32 v32, -v213, v64, v32
v_fma_f32 v64, v212, v64, v214
v_mul_f32 v214, v213, v33
v_mul_f32 v33, v212, v33
v_mfma_f32_32x32x16_bf16 v[96:111], v[184:187], a[56:59], v[96:111]
ds_read_b64_tr_b16 v[176:177], v8 offset:28672
ds_read_b64_tr_b16 v[178:179], v8 offset:28928
v_fma_f32 v33, -v213, v65, v33
v_fma_f32 v65, v212, v65, v214
v_mul_f32 v214, v213, v34
v_mul_f32 v34, v212, v34
v_mfma_f32_32x32x16_bf16 v[112:127], v[184:187], a[60:63], v[112:127]
ds_read_b64_tr_b16 v[180:181], v9 offset:28672
ds_read_b64_tr_b16 v[182:183], v9 offset:28928
v_fma_f32 v34, -v213, v66, v34
v_fma_f32 v66, v212, v66, v214
v_mul_f32 v214, v213, v35
v_mul_f32 v35, v212, v35
s_waitcnt lgkmcnt(8)
v_mfma_f32_16x16x32_bf16 v[220:223], v[192:195], v[28:31], v[220:223]
ds_read_b64_tr_b16 v[196:197], v13 offset:28672
v_mfma_f32_32x32x16_bf16 v[128:143], v[188:191], a[56:59], v[128:143]
ds_read_b64_tr_b16 v[198:199], v13 offset:30720
v_fma_f32 v35, -v213, v67, v35
v_fma_f32 v67, v212, v67, v214
v_cvt_pk_bf16_f32 v32, v32, v33
v_cvt_pk_bf16_f32 v33, v34, v35
v_cvt_pk_bf16_f32 v34, v64, v65
v_cvt_pk_bf16_f32 v35, v66, v67
v_mfma_f32_32x32x16_bf16 v[144:159], v[188:191], a[60:63], v[144:159]
s_waitcnt lgkmcnt(6)
buffer_store_dwordx4 v[32:35], v16, s[96:99], s71 offen offset:0
v_fmac_f32 v36, v200, v2
v_fmac_f32 v68, v200, v3
v_fmac_f32 v37, v201, v2
v_fmac_f32 v69, v201, v3
v_mfma_f32_32x32x16_bf16 v[96:111], v[160:163], a[64:67], v[96:111]
ds_read_b64_tr_b16 v[184:185], v8 offset:30720
ds_read_b64_tr_b16 v[186:187], v8 offset:30976
s_add_u32 m0, s41, 8192
s_add_u32 s62, s62, s92
buffer_load_dwordx4 v5, s[84:87], s62 offen lds
v_fmac_f32 v38, v202, v2
v_mfma_f32_32x32x16_bf16 v[112:127], v[160:163], a[68:71], v[112:127]
ds_read_b64_tr_b16 v[188:189], v9 offset:30720
ds_read_b64_tr_b16 v[190:191], v9 offset:30976
v_fmac_f32 v70, v202, v3
v_fmac_f32 v39, v203, v2
v_fmac_f32 v71, v203, v3
v_mul_f32 v36, v204, v36
v_mfma_f32_32x32x16_bf16 v[128:143], v[164:167], a[64:67], v[128:143]
ds_read_b64_tr_b16 v[192:193], v13 offset:32768
ds_read_b64_tr_b16 v[194:195], v13 offset:34816
v_mul_f32 v68, v204, v68
v_mul_f32 v37, v205, v37
v_mul_f32 v69, v205, v69
v_mul_f32 v38, v206, v38
v_mfma_f32_32x32x16_bf16 v[144:159], v[164:167], a[68:71], v[144:159]
v_mul_f32 v70, v206, v70
v_mul_f32 v39, v207, v39
v_mul_f32 v71, v207, v71
ds_read_b128 v[200:203], v19 offset:64
ds_read_b128 v[204:207], v19 offset:320
v_fma_f32 v217, v208, v36, v37
v_mfma_f32_32x32x16_bf16 v[96:111], v[168:171], a[72:75], v[96:111]
ds_read_b64_tr_b16 v[160:161], v8 offset:32768
ds_read_b64_tr_b16 v[162:163], v8 offset:33024
v_fma_f32 v23, v208, v68, v69
v_fma_f32 v217, -v209, v68, v217
v_fmac_f32 v23, v209, v36
v_fma_f32 v17, v208, v217, v38
v_mfma_f32_32x32x16_bf16 v[112:127], v[168:171], a[76:79], v[112:127]
ds_read_b64_tr_b16 v[164:165], v9 offset:32768
ds_read_b64_tr_b16 v[166:167], v9 offset:33024
v_fma_f32 v216, v208, v23, v70
v_fma_f32 v17, -v209, v23, v17
v_fmac_f32 v216, v209, v217
v_fma_f32 v217, v208, v17, v39
s_waitcnt lgkmcnt(12)
v_mfma_f32_16x16x32_bf16 v[220:223], v[196:199], v[28:31], v[220:223]
v_fma_f32 v23, v208, v216, v71
v_fma_f32 v217, -v209, v216, v217
v_mfma_f32_32x32x16_bf16 v[128:143], v[172:175], a[72:75], v[128:143]
s_add_u32 m0, s41, 12288
s_add_u32 s62, s62, s93
buffer_load_dwordx4 v5, s[88:91], s62 offen lds
v_fmac_f32 v23, v209, v17
v_mov_b32 v27, v217
v_mov_b32 v215, v23
v_mfma_f32_32x32x16_bf16 v[144:159], v[172:175], a[76:79], v[144:159]
s_waitcnt lgkmcnt(8)
v_permlane32_swap_b32 v217, v27
v_permlane32_swap_b32 v23, v215
v_fma_f32 v17, v210, v4, v217
v_fma_f32 v216, v210, v15, v23
v_fma_f32 v17, -v211, v15, v17
v_fmac_f32 v216, v211, v4
v_mfma_f32_32x32x16_bf16 v[96:111], v[176:179], a[80:83], v[96:111]
ds_read_b64_tr_b16 v[168:169], v8 offset:34816
ds_read_b64_tr_b16 v[170:171], v8 offset:35072
v_cndmask_b32 v217, v17, v4, vcc
v_cndmask_b32 v23, v216, v15, vcc
v_fma_f32 v4, v210, v17, v27
v_fma_f32 v15, v210, v216, v215
v_mfma_f32_32x32x16_bf16 v[112:127], v[176:179], a[84:87], v[112:127]
ds_read_b64_tr_b16 v[172:173], v9 offset:34816
ds_read_b64_tr_b16 v[174:175], v9 offset:35072
v_fma_f32 v4, -v211, v216, v4
v_fmac_f32 v15, v211, v17
v_fmac_f32 v36, v208, v217
v_fmac_f32 v68, v208, v23
s_waitcnt lgkmcnt(10)
v_mfma_f32_16x16x32_bf16 v[220:223], v[192:195], v[28:31], v[220:223]
ds_read_b64_tr_b16 v[196:197], v13 offset:36864
v_mfma_f32_32x32x16_bf16 v[128:143], v[180:183], a[80:83], v[128:143]
ds_read_b64_tr_b16 v[198:199], v13 offset:38912
s_add_u32 m0, s41, 16384
s_add_u32 s62, s62, s92
buffer_load_dwordx4 v5, s[84:87], s62 offen lds
v_fma_f32 v36, -v209, v23, v36
v_fmac_f32 v68, v209, v217
v_mfma_f32_32x32x16_bf16 v[144:159], v[180:183], a[84:87], v[144:159]
v_fmac_f32 v37, v208, v36
v_fmac_f32 v69, v208, v68
v_fma_f32 v37, -v209, v68, v37
v_fmac_f32 v69, v209, v36
v_fmac_f32 v38, v208, v37
v_fmac_f32 v70, v208, v69
v_fma_f32 v38, -v209, v69, v38
v_mfma_f32_32x32x16_bf16 v[96:111], v[184:187], a[88:91], v[96:111]
ds_read_b64_tr_b16 v[176:177], v8 offset:36864
ds_read_b64_tr_b16 v[178:179], v8 offset:37120
v_fmac_f32 v70, v209, v37
v_fmac_f32 v39, v208, v38
v_fmac_f32 v71, v208, v70
v_fma_f32 v39, -v209, v70, v39
v_mfma_f32_32x32x16_bf16 v[112:127], v[184:187], a[92:95], v[112:127]
ds_read_b64_tr_b16 v[180:181], v9 offset:36864
ds_read_b64_tr_b16 v[182:183], v9 offset:37120
v_fmac_f32 v71, v209, v38
v_mul_f32 v214, v213, v36
v_mul_f32 v36, v212, v36
v_fma_f32 v36, -v213, v68, v36
v_mfma_f32_32x32x16_bf16 v[128:143], v[188:191], a[88:91], v[128:143]
v_fma_f32 v68, v212, v68, v214
v_mul_f32 v214, v213, v37
v_mul_f32 v37, v212, v37
v_fma_f32 v37, -v213, v69, v37
v_fma_f32 v69, v212, v69, v214
v_mul_f32 v214, v213, v38
v_mul_f32 v38, v212, v38
v_mfma_f32_32x32x16_bf16 v[144:159], v[188:191], a[92:95], v[144:159]
s_waitcnt lgkmcnt(6)
v_fma_f32 v38, -v213, v70, v38
v_fma_f32 v70, v212, v70, v214
v_mul_f32 v214, v213, v39
v_mul_f32 v39, v212, v39
v_fma_f32 v39, -v213, v71, v39
v_fma_f32 v71, v212, v71, v214
v_mfma_f32_32x32x16_bf16 v[96:111], v[160:163], a[96:99], v[96:111]
ds_read_b64_tr_b16 v[184:185], v8 offset:38912
)ASM"
R"ASM(
ds_read_b64_tr_b16 v[186:187], v8 offset:39168
v_cvt_pk_bf16_f32 v36, v36, v37
v_cvt_pk_bf16_f32 v37, v38, v39
v_cvt_pk_bf16_f32 v38, v68, v69
v_cvt_pk_bf16_f32 v39, v70, v71
v_mfma_f32_32x32x16_bf16 v[112:127], v[160:163], a[100:103], v[112:127]
ds_read_b64_tr_b16 v[188:189], v9 offset:38912
ds_read_b64_tr_b16 v[190:191], v9 offset:39168
buffer_store_dwordx4 v[36:39], v16, s[96:99], s71 offen offset:1024
v_fmac_f32 v40, v200, v2
v_fmac_f32 v72, v200, v3
v_fmac_f32 v41, v201, v2
s_waitcnt lgkmcnt(8)
v_mfma_f32_16x16x32_bf16 v[220:223], v[196:199], v[28:31], v[220:223]
ds_read_b64_tr_b16 v[192:193], v13 offset:40960
v_mfma_f32_32x32x16_bf16 v[128:143], v[164:167], a[96:99], v[128:143]
ds_read_b64_tr_b16 v[194:195], v13 offset:43008
v_fmac_f32 v73, v201, v3
v_fmac_f32 v42, v202, v2
v_fmac_f32 v74, v202, v3
v_fmac_f32 v43, v203, v2
v_fmac_f32 v75, v203, v3
v_mul_f32 v40, v204, v40
v_mfma_f32_32x32x16_bf16 v[144:159], v[164:167], a[100:103], v[144:159]
v_mul_f32 v72, v204, v72
v_mul_f32 v41, v205, v41
v_mul_f32 v73, v205, v73
v_mul_f32 v42, v206, v42
v_mul_f32 v74, v206, v74
v_mul_f32 v43, v207, v43
v_mul_f32 v75, v207, v75
v_mfma_f32_32x32x16_bf16 v[96:111], v[168:171], a[104:107], v[96:111]
ds_read_b64_tr_b16 v[160:161], v8 offset:40960
ds_read_b64_tr_b16 v[162:163], v8 offset:41216
v_mfma_f32_32x32x16_bf16 v[112:127], v[168:171], a[108:111], v[112:127]
ds_read_b64_tr_b16 v[164:165], v9 offset:40960
ds_read_b64_tr_b16 v[166:167], v9 offset:41216
v_mfma_f32_32x32x16_bf16 v[128:143], v[172:175], a[104:107], v[128:143]
ds_read_b128 v[200:203], v19 offset:96
ds_read_b128 v[204:207], v19 offset:352
v_fma_f32 v217, v208, v40, v41
v_fma_f32 v23, v208, v72, v73
v_fma_f32 v217, -v209, v72, v217
v_fmac_f32 v23, v209, v40
v_mfma_f32_32x32x16_bf16 v[144:159], v[172:175], a[108:111], v[144:159]
s_waitcnt lgkmcnt(8)
v_fma_f32 v17, v208, v217, v42
v_fma_f32 v216, v208, v23, v74
v_fma_f32 v17, -v209, v23, v17
v_fmac_f32 v216, v209, v217
v_fma_f32 v217, v208, v17, v43
v_fma_f32 v23, v208, v216, v75
v_mfma_f32_32x32x16_bf16 v[96:111], v[176:179], a[112:115], v[96:111]
ds_read_b64_tr_b16 v[168:169], v8 offset:43008
ds_read_b64_tr_b16 v[170:171], v8 offset:43264
v_fma_f32 v217, -v209, v216, v217
v_fmac_f32 v23, v209, v17
v_mov_b32 v27, v217
v_mov_b32 v215, v23
v_mfma_f32_32x32x16_bf16 v[112:127], v[176:179], a[116:119], v[112:127]
ds_read_b64_tr_b16 v[172:173], v9 offset:43008
ds_read_b64_tr_b16 v[174:175], v9 offset:43264
v_permlane32_swap_b32 v217, v27
v_permlane32_swap_b32 v23, v215
v_fma_f32 v17, v210, v4, v217
v_fma_f32 v216, v210, v15, v23
s_waitcnt lgkmcnt(10)
v_mfma_f32_16x16x32_bf16 v[220:223], v[192:195], v[28:31], v[220:223]
v_fma_f32 v17, -v211, v15, v17
v_fmac_f32 v216, v211, v4
v_mfma_f32_32x32x16_bf16 v[128:143], v[180:183], a[112:115], v[128:143]
v_cndmask_b32 v217, v17, v4, vcc
v_cndmask_b32 v23, v216, v15, vcc
v_fma_f32 v4, v210, v17, v27
v_fma_f32 v15, v210, v216, v215
v_fma_f32 v4, -v211, v216, v4
v_fmac_f32 v15, v211, v17
v_fmac_f32 v40, v208, v217
v_mfma_f32_32x32x16_bf16 v[144:159], v[180:183], a[116:119], v[144:159]
v_fmac_f32 v72, v208, v23
v_fma_f32 v40, -v209, v23, v40
v_fmac_f32 v72, v209, v217
v_fmac_f32 v41, v208, v40
v_fmac_f32 v73, v208, v72
v_fma_f32 v41, -v209, v72, v41
v_fmac_f32 v73, v209, v40
v_mfma_f32_32x32x16_bf16 v[96:111], v[184:187], a[120:123], v[96:111]
s_waitcnt vmcnt(11) lgkmcnt(0)
s_barrier
s_mov_b64 exec, 1
buffer_store_dword v219, off, s[96:99], s52 sc1
s_mov_b64 exec, -1
ds_read_b64_tr_b16 v[176:177], v8 offset:45056
v_mfma_f32_32x32x16_bf16 v[112:127], v[184:187], a[124:127], v[112:127]
ds_read_b64_tr_b16 v[178:179], v8 offset:45312
ds_read_b64_tr_b16 v[180:181], v9 offset:45056
s_add_u32 m0, s41, 20480
s_add_u32 s62, s62, s93
buffer_load_dwordx4 v5, s[88:91], s62 offen lds
v_fmac_f32 v42, v208, v41
v_mfma_f32_32x32x16_bf16 v[128:143], v[188:191], a[120:123], v[128:143]
ds_read_b64_tr_b16 v[182:183], v9 offset:45312
ds_read_b64_tr_b16 v[196:197], v13 offset:45056
v_fmac_f32 v74, v208, v73
v_fma_f32 v42, -v209, v73, v42
v_fmac_f32 v74, v209, v41
v_fmac_f32 v43, v208, v42
v_mfma_f32_32x32x16_bf16 v[144:159], v[188:191], a[124:127], v[144:159]
ds_read_b64_tr_b16 v[198:199], v13 offset:47104
v_fmac_f32 v75, v208, v74
v_fma_f32 v43, -v209, v74, v43
v_fmac_f32 v75, v209, v42
v_mul_f32 v214, v213, v40
v_mul_f32 v40, v212, v40
v_mfma_f32_32x32x16_bf16 v[96:111], v[160:163], a[128:131], v[96:111]
ds_read_b64_tr_b16 v[184:185], v8 offset:47104
ds_read_b64_tr_b16 v[186:187], v8 offset:47360
v_fma_f32 v40, -v213, v72, v40
v_fma_f32 v72, v212, v72, v214
v_mul_f32 v214, v213, v41
v_mul_f32 v41, v212, v41
v_mfma_f32_32x32x16_bf16 v[112:127], v[160:163], a[132:135], v[112:127]
ds_read_b64_tr_b16 v[188:189], v9 offset:47104
ds_read_b64_tr_b16 v[190:191], v9 offset:47360
v_fma_f32 v41, -v213, v73, v41
v_fma_f32 v73, v212, v73, v214
v_mul_f32 v214, v213, v42
v_mul_f32 v42, v212, v42
v_mfma_f32_32x32x16_bf16 v[128:143], v[164:167], a[128:131], v[128:143]
ds_read_b64_tr_b16 v[192:193], v14 offset:0
ds_read_b64_tr_b16 v[194:195], v14 offset:2048
v_fma_f32 v42, -v213, v74, v42
v_fma_f32 v74, v212, v74, v214
v_mul_f32 v214, v213, v43
v_mul_f32 v43, v212, v43
v_mfma_f32_32x32x16_bf16 v[144:159], v[164:167], a[132:135], v[144:159]
v_fma_f32 v43, -v213, v75, v43
v_fma_f32 v75, v212, v75, v214
v_cvt_pk_bf16_f32 v40, v40, v41
v_cvt_pk_bf16_f32 v41, v42, v43
v_cvt_pk_bf16_f32 v42, v72, v73
v_cvt_pk_bf16_f32 v43, v74, v75
v_mfma_f32_32x32x16_bf16 v[96:111], v[168:171], a[136:139], v[96:111]
ds_read_b64_tr_b16 v[160:161], v10 offset:0
ds_read_b64_tr_b16 v[162:163], v10 offset:256
s_add_u32 m0, s41, 24576
s_add_u32 s62, s62, s92
buffer_load_dwordx4 v5, s[84:87], s62 offen lds
v_mfma_f32_32x32x16_bf16 v[112:127], v[168:171], a[140:143], v[112:127]
ds_read_b64_tr_b16 v[164:165], v11 offset:0
ds_read_b64_tr_b16 v[166:167], v11 offset:256
buffer_store_dwordx4 v[40:43], v16, s[96:99], s71 offen offset:2048
v_fmac_f32 v44, v200, v2
v_fmac_f32 v76, v200, v3
v_fmac_f32 v45, v201, v2
s_waitcnt lgkmcnt(10)
v_mfma_f32_16x16x32_bf16 v[220:223], v[196:199], v[28:31], v[220:223]
v_fmac_f32 v77, v201, v3
v_fmac_f32 v46, v202, v2
v_mfma_f32_32x32x16_bf16 v[128:143], v[172:175], a[136:139], v[128:143]
v_fmac_f32 v78, v202, v3
v_fmac_f32 v47, v203, v2
v_fmac_f32 v79, v203, v3
v_mul_f32 v44, v204, v44
v_mul_f32 v76, v204, v76
v_mul_f32 v45, v205, v45
v_mul_f32 v77, v205, v77
v_mfma_f32_32x32x16_bf16 v[144:159], v[172:175], a[140:143], v[144:159]
s_waitcnt lgkmcnt(6)
v_mul_f32 v46, v206, v46
v_mul_f32 v78, v206, v78
v_mul_f32 v47, v207, v47
v_mul_f32 v79, v207, v79
ds_read_b128 v[200:203], v19 offset:128
v_mfma_f32_32x32x16_bf16 v[96:111], v[176:179], a[144:147], v[96:111]
ds_read_b64_tr_b16 v[168:169], v10 offset:2048
ds_read_b64_tr_b16 v[170:171], v10 offset:2304
v_mfma_f32_32x32x16_bf16 v[112:127], v[176:179], a[148:151], v[112:127]
ds_read_b64_tr_b16 v[172:173], v11 offset:2048
ds_read_b64_tr_b16 v[174:175], v11 offset:2304
s_waitcnt lgkmcnt(9)
v_mfma_f32_16x16x32_bf16 v[220:223], v[192:195], v[28:31], v[220:223]
ds_read_b64_tr_b16 v[196:197], v14 offset:4096
v_mfma_f32_32x32x16_bf16 v[128:143], v[180:183], a[144:147], v[128:143]
ds_read_b64_tr_b16 v[198:199], v14 offset:6144
s_add_u32 m0, s41, 28672
s_add_u32 s62, s62, s93
buffer_load_dwordx4 v5, s[88:91], s62 offen lds
ds_read_b128 v[204:207], v19 offset:384
v_fma_f32 v217, v208, v44, v45
v_mfma_f32_32x32x16_bf16 v[144:159], v[180:183], a[148:151], v[144:159]
v_fma_f32 v23, v208, v76, v77
v_fma_f32 v217, -v209, v76, v217
v_fmac_f32 v23, v209, v44
v_fma_f32 v17, v208, v217, v46
v_fma_f32 v216, v208, v23, v78
v_fma_f32 v17, -v209, v23, v17
v_fmac_f32 v216, v209, v217
v_mfma_f32_32x32x16_bf16 v[96:111], v[184:187], a[152:155], v[96:111]
ds_read_b64_tr_b16 v[176:177], v10 offset:4096
)ASM"
R"ASM(
ds_read_b64_tr_b16 v[178:179], v10 offset:4352
v_fma_f32 v217, v208, v17, v47
v_fma_f32 v23, v208, v216, v79
v_fma_f32 v217, -v209, v216, v217
v_fmac_f32 v23, v209, v17
v_mfma_f32_32x32x16_bf16 v[112:127], v[184:187], a[156:159], v[112:127]
ds_read_b64_tr_b16 v[180:181], v11 offset:4096
ds_read_b64_tr_b16 v[182:183], v11 offset:4352
v_mov_b32 v27, v217
v_mov_b32 v215, v23
s_nop 0
v_permlane32_swap_b32 v217, v27
v_permlane32_swap_b32 v23, v215
v_mfma_f32_32x32x16_bf16 v[128:143], v[188:191], a[152:155], v[128:143]
v_fma_f32 v17, v210, v4, v217
v_fma_f32 v216, v210, v15, v23
v_fma_f32 v17, -v211, v15, v17
v_fmac_f32 v216, v211, v4
v_cndmask_b32 v217, v17, v4, vcc
v_cndmask_b32 v23, v216, v15, vcc
v_fma_f32 v4, v210, v17, v27
v_mfma_f32_32x32x16_bf16 v[144:159], v[188:191], a[156:159], v[144:159]
s_waitcnt lgkmcnt(7)
v_fma_f32 v15, v210, v216, v215
v_fma_f32 v4, -v211, v216, v4
v_fmac_f32 v15, v211, v17
v_fmac_f32 v44, v208, v217
v_fmac_f32 v76, v208, v23
v_fma_f32 v44, -v209, v23, v44
v_mfma_f32_32x32x16_bf16 v[96:111], v[160:163], a[160:163], v[96:111]
ds_read_b64_tr_b16 v[184:185], v10 offset:6144
ds_read_b64_tr_b16 v[186:187], v10 offset:6400
s_add_u32 m0, s41, 32768
s_add_u32 s62, s62, s92
buffer_load_dwordx4 v5, s[84:87], s62 offen lds
v_fmac_f32 v76, v209, v217
v_mfma_f32_32x32x16_bf16 v[112:127], v[160:163], a[164:167], v[112:127]
ds_read_b64_tr_b16 v[188:189], v11 offset:6144
ds_read_b64_tr_b16 v[190:191], v11 offset:6400
v_fmac_f32 v45, v208, v44
v_fmac_f32 v77, v208, v76
v_fma_f32 v45, -v209, v76, v45
v_fmac_f32 v77, v209, v44
s_waitcnt lgkmcnt(9)
v_mfma_f32_16x16x32_bf16 v[220:223], v[196:199], v[28:31], v[220:223]
ds_read_b64_tr_b16 v[192:193], v14 offset:8192
v_mfma_f32_32x32x16_bf16 v[128:143], v[164:167], a[160:163], v[128:143]
ds_read_b64_tr_b16 v[194:195], v14 offset:10240
v_fmac_f32 v46, v208, v45
v_fmac_f32 v78, v208, v77
v_fma_f32 v46, -v209, v77, v46
v_fmac_f32 v78, v209, v45
v_fmac_f32 v47, v208, v46
v_fmac_f32 v79, v208, v78
v_mfma_f32_32x32x16_bf16 v[144:159], v[164:167], a[164:167], v[144:159]
v_fma_f32 v47, -v209, v78, v47
v_fmac_f32 v79, v209, v46
v_mul_f32 v214, v213, v44
v_mul_f32 v44, v212, v44
v_fma_f32 v44, -v213, v76, v44
v_fma_f32 v76, v212, v76, v214
v_mul_f32 v214, v213, v45
v_mfma_f32_32x32x16_bf16 v[96:111], v[168:171], a[168:171], v[96:111]
ds_read_b64_tr_b16 v[160:161], v10 offset:8192
ds_read_b64_tr_b16 v[162:163], v10 offset:8448
v_mul_f32 v45, v212, v45
v_fma_f32 v45, -v213, v77, v45
v_fma_f32 v77, v212, v77, v214
v_mul_f32 v214, v213, v46
v_mfma_f32_32x32x16_bf16 v[112:127], v[168:171], a[172:175], v[112:127]
ds_read_b64_tr_b16 v[164:165], v11 offset:8192
ds_read_b64_tr_b16 v[166:167], v11 offset:8448
v_mul_f32 v46, v212, v46
v_fma_f32 v46, -v213, v78, v46
v_fma_f32 v78, v212, v78, v214
v_mul_f32 v214, v213, v47
v_mfma_f32_32x32x16_bf16 v[128:143], v[172:175], a[168:171], v[128:143]
s_add_u32 m0, s41, 36864
s_add_u32 s62, s62, s93
buffer_load_dwordx4 v5, s[88:91], s62 offen lds
v_mul_f32 v47, v212, v47
v_fma_f32 v47, -v213, v79, v47
v_fma_f32 v79, v212, v79, v214
v_mfma_f32_32x32x16_bf16 v[144:159], v[172:175], a[172:175], v[144:159]
s_waitcnt lgkmcnt(6)
v_cvt_pk_bf16_f32 v44, v44, v45
v_cvt_pk_bf16_f32 v45, v46, v47
v_cvt_pk_bf16_f32 v46, v76, v77
v_cvt_pk_bf16_f32 v47, v78, v79
buffer_store_dwordx4 v[44:47], v16, s[96:99], s71 offen offset:3072
v_mfma_f32_32x32x16_bf16 v[96:111], v[176:179], a[176:179], v[96:111]
ds_read_b64_tr_b16 v[168:169], v10 offset:10240
ds_read_b64_tr_b16 v[170:171], v10 offset:10496
v_fmac_f32 v48, v200, v2
v_fmac_f32 v80, v200, v3
v_fmac_f32 v49, v201, v2
v_fmac_f32 v81, v201, v3
v_mfma_f32_32x32x16_bf16 v[112:127], v[176:179], a[180:183], v[112:127]
ds_read_b64_tr_b16 v[172:173], v11 offset:10240
ds_read_b64_tr_b16 v[174:175], v11 offset:10496
v_fmac_f32 v50, v202, v2
v_fmac_f32 v82, v202, v3
v_fmac_f32 v51, v203, v2
v_fmac_f32 v83, v203, v3
s_waitcnt lgkmcnt(8)
v_mfma_f32_16x16x32_bf16 v[220:223], v[192:195], v[28:31], v[220:223]
ds_read_b64_tr_b16 v[196:197], v14 offset:12288
v_mfma_f32_32x32x16_bf16 v[128:143], v[180:183], a[176:179], v[128:143]
ds_read_b64_tr_b16 v[198:199], v14 offset:14336
v_mul_f32 v48, v204, v48
v_mul_f32 v80, v204, v80
v_mul_f32 v49, v205, v49
v_mul_f32 v81, v205, v81
v_mul_f32 v50, v206, v50
v_mul_f32 v82, v206, v82
v_mfma_f32_32x32x16_bf16 v[144:159], v[180:183], a[180:183], v[144:159]
v_mul_f32 v51, v207, v51
v_mul_f32 v83, v207, v83
ds_read_b128 v[200:203], v19 offset:160
ds_read_b128 v[204:207], v19 offset:416
v_fma_f32 v217, v208, v48, v49
v_fma_f32 v23, v208, v80, v81
v_mfma_f32_32x32x16_bf16 v[96:111], v[184:187], a[184:187], v[96:111]
ds_read_b64_tr_b16 v[176:177], v10 offset:12288
ds_read_b64_tr_b16 v[178:179], v10 offset:12544
s_add_u32 m0, s41, 40960
s_add_u32 s62, s62, s92
buffer_load_dwordx4 v5, s[84:87], s62 offen lds
v_fma_f32 v217, -v209, v80, v217
v_mfma_f32_32x32x16_bf16 v[112:127], v[184:187], a[188:191], v[112:127]
ds_read_b64_tr_b16 v[180:181], v11 offset:12288
ds_read_b64_tr_b16 v[182:183], v11 offset:12544
v_fmac_f32 v23, v209, v48
v_fma_f32 v17, v208, v217, v50
v_fma_f32 v216, v208, v23, v82
v_fma_f32 v17, -v209, v23, v17
v_mfma_f32_32x32x16_bf16 v[128:143], v[188:191], a[184:187], v[128:143]
ds_read_b64_tr_b16 v[192:193], v14 offset:16384
ds_read_b64_tr_b16 v[194:195], v14 offset:18432
v_fmac_f32 v216, v209, v217
v_fma_f32 v217, v208, v17, v51
v_fma_f32 v23, v208, v216, v83
v_fma_f32 v217, -v209, v216, v217
v_mfma_f32_32x32x16_bf16 v[144:159], v[188:191], a[188:191], v[144:159]
s_waitcnt lgkmcnt(10)
v_fmac_f32 v23, v209, v17
v_mov_b32 v27, v217
v_mov_b32 v215, v23
s_nop 0
v_permlane32_swap_b32 v217, v27
v_permlane32_swap_b32 v23, v215
v_fma_f32 v17, v210, v4, v217
v_mfma_f32_32x32x16_bf16 v[96:111], v[160:163], a[192:195], v[96:111]
ds_read_b64_tr_b16 v[184:185], v10 offset:14336
ds_read_b64_tr_b16 v[186:187], v10 offset:14592
v_fma_f32 v216, v210, v15, v23
v_fma_f32 v17, -v211, v15, v17
v_fmac_f32 v216, v211, v4
v_cndmask_b32 v217, v17, v4, vcc
v_mfma_f32_32x32x16_bf16 v[112:127], v[160:163], a[196:199], v[112:127]
ds_read_b64_tr_b16 v[188:189], v11 offset:14336
ds_read_b64_tr_b16 v[190:191], v11 offset:14592
v_cndmask_b32 v23, v216, v15, vcc
v_fma_f32 v4, v210, v17, v27
v_fma_f32 v15, v210, v216, v215
v_fma_f32 v4, -v211, v216, v4
s_waitcnt lgkmcnt(12)
v_mfma_f32_16x16x32_bf16 v[220:223], v[196:199], v[28:31], v[220:223]
v_fmac_f32 v15, v211, v17
v_fmac_f32 v48, v208, v217
v_mfma_f32_32x32x16_bf16 v[128:143], v[164:167], a[192:195], v[128:143]
v_fmac_f32 v80, v208, v23
v_fma_f32 v48, -v209, v23, v48
v_fmac_f32 v80, v209, v217
v_fmac_f32 v49, v208, v48
v_fmac_f32 v81, v208, v80
v_fma_f32 v49, -v209, v80, v49
v_fmac_f32 v81, v209, v48
v_mfma_f32_32x32x16_bf16 v[144:159], v[164:167], a[196:199], v[144:159]
s_add_u32 m0, s41, 45056
s_add_u32 s62, s62, s93
buffer_load_dwordx4 v5, s[88:91], s62 offen lds
v_fmac_f32 v50, v208, v49
v_fmac_f32 v82, v208, v81
v_fma_f32 v50, -v209, v81, v50
v_mfma_f32_32x32x16_bf16 v[96:111], v[168:171], a[200:203], v[96:111]
ds_read_b64_tr_b16 v[160:161], v10 offset:16384
ds_read_b64_tr_b16 v[162:163], v10 offset:16640
v_fmac_f32 v82, v209, v49
v_fmac_f32 v51, v208, v50
v_fmac_f32 v83, v208, v82
v_fma_f32 v51, -v209, v82, v51
v_mfma_f32_32x32x16_bf16 v[112:127], v[168:171], a[204:207], v[112:127]
ds_read_b64_tr_b16 v[164:165], v11 offset:16384
ds_read_b64_tr_b16 v[166:167], v11 offset:16640
v_fmac_f32 v83, v209, v50
v_mul_f32 v214, v213, v48
v_mul_f32 v48, v212, v48
v_fma_f32 v48, -v213, v80, v48
s_waitcnt lgkmcnt(8)
)ASM"
R"ASM(
v_mfma_f32_16x16x32_bf16 v[220:223], v[192:195], v[28:31], v[220:223]
ds_read_b64_tr_b16 v[196:197], v14 offset:20480
v_mfma_f32_32x32x16_bf16 v[128:143], v[172:175], a[200:203], v[128:143]
ds_read_b64_tr_b16 v[198:199], v14 offset:22528
v_fma_f32 v80, v212, v80, v214
v_mul_f32 v214, v213, v49
v_mul_f32 v49, v212, v49
v_fma_f32 v49, -v213, v81, v49
v_fma_f32 v81, v212, v81, v214
v_mul_f32 v214, v213, v50
v_mfma_f32_32x32x16_bf16 v[144:159], v[172:175], a[204:207], v[144:159]
s_waitcnt lgkmcnt(6)
v_mul_f32 v50, v212, v50
v_fma_f32 v50, -v213, v82, v50
v_fma_f32 v82, v212, v82, v214
v_mul_f32 v214, v213, v51
v_mul_f32 v51, v212, v51
v_fma_f32 v51, -v213, v83, v51
v_mfma_f32_32x32x16_bf16 v[96:111], v[176:179], a[208:211], v[96:111]
ds_read_b64_tr_b16 v[168:169], v10 offset:18432
ds_read_b64_tr_b16 v[170:171], v10 offset:18688
v_fma_f32 v83, v212, v83, v214
v_cvt_pk_bf16_f32 v48, v48, v49
v_cvt_pk_bf16_f32 v49, v50, v51
v_cvt_pk_bf16_f32 v50, v80, v81
v_mfma_f32_32x32x16_bf16 v[112:127], v[176:179], a[212:215], v[112:127]
ds_read_b64_tr_b16 v[172:173], v11 offset:18432
ds_read_b64_tr_b16 v[174:175], v11 offset:18688
s_add_u32 m0, s41, 49152
s_add_u32 s62, s62, s92
buffer_load_dwordx4 v5, s[84:87], s62 offen lds
v_cvt_pk_bf16_f32 v51, v82, v83
v_mfma_f32_32x32x16_bf16 v[128:143], v[180:183], a[208:211], v[128:143]
s_add_u32 s71, s71, 4096
buffer_store_dwordx4 v[48:51], v16, s[96:99], s71 offen offset:0
v_fmac_f32 v52, v200, v2
v_fmac_f32 v84, v200, v3
v_fmac_f32 v53, v201, v2
v_fmac_f32 v85, v201, v3
v_mfma_f32_32x32x16_bf16 v[144:159], v[180:183], a[212:215], v[144:159]
v_fmac_f32 v54, v202, v2
v_fmac_f32 v86, v202, v3
v_fmac_f32 v55, v203, v2
v_fmac_f32 v87, v203, v3
v_mul_f32 v52, v204, v52
v_mul_f32 v84, v204, v84
v_mul_f32 v53, v205, v53
v_mfma_f32_32x32x16_bf16 v[96:111], v[184:187], a[216:219], v[96:111]
ds_read_b64_tr_b16 v[176:177], v10 offset:20480
ds_read_b64_tr_b16 v[178:179], v10 offset:20736
v_mul_f32 v85, v205, v85
v_mul_f32 v54, v206, v54
v_mul_f32 v86, v206, v86
v_mul_f32 v55, v207, v55
v_mfma_f32_32x32x16_bf16 v[112:127], v[184:187], a[220:223], v[112:127]
ds_read_b64_tr_b16 v[180:181], v11 offset:20480
ds_read_b64_tr_b16 v[182:183], v11 offset:20736
v_mul_f32 v87, v207, v87
s_waitcnt lgkmcnt(8)
v_mfma_f32_16x16x32_bf16 v[220:223], v[196:199], v[28:31], v[220:223]
ds_read_b64_tr_b16 v[192:193], v14 offset:24576
v_mfma_f32_32x32x16_bf16 v[128:143], v[188:191], a[216:219], v[128:143]
ds_read_b64_tr_b16 v[194:195], v14 offset:26624
ds_read_b128 v[200:203], v19 offset:192
v_mfma_f32_32x32x16_bf16 v[144:159], v[188:191], a[220:223], v[144:159]
s_waitcnt lgkmcnt(7)
s_add_u32 m0, s41, 53248
s_add_u32 s62, s62, s93
buffer_load_dwordx4 v5, s[88:91], s62 offen lds
ds_read_b128 v[204:207], v19 offset:448
v_fma_f32 v217, v208, v52, v53
v_mfma_f32_32x32x16_bf16 v[96:111], v[160:163], a[224:227], v[96:111]
ds_read_b64_tr_b16 v[184:185], v10 offset:22528
ds_read_b64_tr_b16 v[186:187], v10 offset:22784
v_fma_f32 v23, v208, v84, v85
v_fma_f32 v217, -v209, v84, v217
v_fmac_f32 v23, v209, v52
v_fma_f32 v17, v208, v217, v54
v_mfma_f32_32x32x16_bf16 v[112:127], v[160:163], a[228:231], v[112:127]
ds_read_b64_tr_b16 v[188:189], v11 offset:22528
ds_read_b64_tr_b16 v[190:191], v11 offset:22784
v_fma_f32 v216, v208, v23, v86
v_fma_f32 v17, -v209, v23, v17
v_fmac_f32 v216, v209, v217
v_fma_f32 v217, v208, v17, v55
v_mfma_f32_32x32x16_bf16 v[128:143], v[164:167], a[224:227], v[128:143]
v_fma_f32 v23, v208, v216, v87
v_fma_f32 v217, -v209, v216, v217
v_fmac_f32 v23, v209, v17
v_mov_b32 v27, v217
v_mov_b32 v215, v23
s_nop 0
v_permlane32_swap_b32 v217, v27
v_permlane32_swap_b32 v23, v215
v_mfma_f32_32x32x16_bf16 v[144:159], v[164:167], a[228:231], v[144:159]
v_fma_f32 v17, v210, v4, v217
v_fma_f32 v216, v210, v15, v23
v_fma_f32 v17, -v211, v15, v17
v_fmac_f32 v216, v211, v4
v_cndmask_b32 v217, v17, v4, vcc
v_cndmask_b32 v23, v216, v15, vcc
v_fma_f32 v4, v210, v17, v27
v_mfma_f32_32x32x16_bf16 v[96:111], v[168:171], a[232:235], v[96:111]
ds_read_b64_tr_b16 v[160:161], v10 offset:24576
ds_read_b64_tr_b16 v[162:163], v10 offset:24832
v_fma_f32 v15, v210, v216, v215
v_fma_f32 v4, -v211, v216, v4
v_fmac_f32 v15, v211, v17
v_fmac_f32 v52, v208, v217
v_mfma_f32_32x32x16_bf16 v[112:127], v[168:171], a[236:239], v[112:127]
ds_read_b64_tr_b16 v[164:165], v11 offset:24576
ds_read_b64_tr_b16 v[166:167], v11 offset:24832
v_fmac_f32 v84, v208, v23
v_fma_f32 v52, -v209, v23, v52
v_fmac_f32 v84, v209, v217
v_fmac_f32 v53, v208, v52
s_waitcnt lgkmcnt(10)
v_mfma_f32_16x16x32_bf16 v[220:223], v[192:195], v[28:31], v[220:223]
ds_read_b64_tr_b16 v[196:197], v14 offset:28672
v_mfma_f32_32x32x16_bf16 v[128:143], v[172:175], a[232:235], v[128:143]
ds_read_b64_tr_b16 v[198:199], v14 offset:30720
v_fmac_f32 v85, v208, v84
v_fma_f32 v53, -v209, v84, v53
v_fmac_f32 v85, v209, v52
v_fmac_f32 v54, v208, v53
v_fmac_f32 v86, v208, v85
v_fma_f32 v54, -v209, v85, v54
v_mfma_f32_32x32x16_bf16 v[144:159], v[172:175], a[236:239], v[144:159]
s_waitcnt lgkmcnt(6)
v_fmac_f32 v86, v209, v53
v_fmac_f32 v55, v208, v54
v_fmac_f32 v87, v208, v86
v_fma_f32 v55, -v209, v86, v55
v_fmac_f32 v87, v209, v54
v_mul_f32 v214, v213, v52
v_mfma_f32_32x32x16_bf16 v[96:111], v[176:179], a[240:243], v[96:111]
ds_read_b64_tr_b16 v[168:169], v10 offset:26624
ds_read_b64_tr_b16 v[170:171], v10 offset:26880
v_mul_f32 v52, v212, v52
v_fma_f32 v52, -v213, v84, v52
v_fma_f32 v84, v212, v84, v214
v_mul_f32 v214, v213, v53
v_mfma_f32_32x32x16_bf16 v[112:127], v[176:179], a[244:247], v[112:127]
ds_read_b64_tr_b16 v[172:173], v11 offset:26624
ds_read_b64_tr_b16 v[174:175], v11 offset:26880
v_mul_f32 v53, v212, v53
v_fma_f32 v53, -v213, v85, v53
v_fma_f32 v85, v212, v85, v214
v_mul_f32 v214, v213, v54
v_mfma_f32_32x32x16_bf16 v[128:143], v[180:183], a[240:243], v[128:143]
v_mul_f32 v54, v212, v54
v_fma_f32 v54, -v213, v86, v54
v_fma_f32 v86, v212, v86, v214
v_mul_f32 v214, v213, v55
v_mul_f32 v55, v212, v55
v_fma_f32 v55, -v213, v87, v55
v_fma_f32 v87, v212, v87, v214
v_mfma_f32_32x32x16_bf16 v[144:159], v[180:183], a[244:247], v[144:159]
v_cvt_pk_bf16_f32 v52, v52, v53
v_cvt_pk_bf16_f32 v53, v54, v55
v_cvt_pk_bf16_f32 v54, v84, v85
v_cvt_pk_bf16_f32 v55, v86, v87
buffer_store_dwordx4 v[52:55], v16, s[96:99], s71 offen offset:1024
v_fmac_f32 v56, v200, v2
v_mfma_f32_32x32x16_bf16 v[96:111], v[184:187], a[248:251], v[96:111]
ds_read_b64_tr_b16 v[176:177], v10 offset:28672
ds_read_b64_tr_b16 v[178:179], v10 offset:28928
v_fmac_f32 v88, v200, v3
v_fmac_f32 v57, v201, v2
v_fmac_f32 v89, v201, v3
v_fmac_f32 v58, v202, v2
v_mfma_f32_32x32x16_bf16 v[112:127], v[184:187], a[252:255], v[112:127]
ds_read_b64_tr_b16 v[180:181], v11 offset:28672
ds_read_b64_tr_b16 v[182:183], v11 offset:28928
v_fmac_f32 v90, v202, v3
v_fmac_f32 v59, v203, v2
v_fmac_f32 v91, v203, v3
v_mul_f32 v56, v204, v56
s_waitcnt lgkmcnt(8)
v_mfma_f32_16x16x32_bf16 v[220:223], v[196:199], v[28:31], v[220:223]
v_mul_f32 v88, v204, v88
v_mul_f32 v57, v205, v57
v_mfma_f32_32x32x16_bf16 v[128:143], v[188:191], a[248:251], v[128:143]
v_mul_f32 v89, v205, v89
v_mul_f32 v58, v206, v58
v_mul_f32 v90, v206, v90
v_mul_f32 v59, v207, v59
v_mul_f32 v91, v207, v91
ds_read_b128 v[200:203], v19 offset:224
v_mfma_f32_32x32x16_bf16 v[144:159], v[188:191], a[252:255], v[144:159]
s_waitcnt lgkmcnt(5)
ds_read_b128 v[204:207], v19 offset:480
v_fma_f32 v217, v208, v56, v57
v_fma_f32 v23, v208, v88, v89
v_fma_f32 v217, -v209, v88, v217
v_fmac_f32 v23, v209, v56
v_fma_f32 v17, v208, v217, v58
v_mfma_f32_32x32x16_bf16 v[96:111], v[160:163], v[224:227], v[96:111]
ds_read_b64_tr_b16 v[184:185], v10 offset:30720
ds_read_b64_tr_b16 v[186:187], v10 offset:30976
)ASM"
R"ASM(
v_fma_f32 v216, v208, v23, v90
v_fma_f32 v17, -v209, v23, v17
v_fmac_f32 v216, v209, v217
v_fma_f32 v217, v208, v17, v59
v_mfma_f32_32x32x16_bf16 v[112:127], v[160:163], v[228:231], v[112:127]
ds_read_b64_tr_b16 v[188:189], v11 offset:30720
ds_read_b64_tr_b16 v[190:191], v11 offset:30976
v_fma_f32 v23, v208, v216, v91
v_fma_f32 v217, -v209, v216, v217
v_fmac_f32 v23, v209, v17
v_mov_b32 v27, v217
v_mfma_f32_32x32x16_bf16 v[128:143], v[164:167], v[224:227], v[128:143]
ds_write_b128 v18, v[220:223] offset:256
v_mov_b32 v215, v23
v_permlane32_swap_b32 v217, v27
s_nop 0
v_permlane32_swap_b32 v23, v215
v_fma_f32 v17, v210, v4, v217
v_fma_f32 v216, v210, v15, v23
v_fma_f32 v17, -v211, v15, v17
v_mfma_f32_32x32x16_bf16 v[144:159], v[164:167], v[228:231], v[144:159]
v_fmac_f32 v216, v211, v4
v_cndmask_b32 v217, v17, v4, vcc
v_cndmask_b32 v23, v216, v15, vcc
v_fma_f32 v4, v210, v17, v27
v_fma_f32 v15, v210, v216, v215
v_fma_f32 v4, -v211, v216, v4
v_fmac_f32 v15, v211, v17
v_mfma_f32_32x32x16_bf16 v[96:111], v[168:171], v[232:235], v[96:111]
s_waitcnt vmcnt(14) lgkmcnt(0)
s_barrier
ds_read_b64_tr_b16 v[160:161], v10 offset:32768
v_mfma_f32_32x32x16_bf16 v[112:127], v[168:171], v[236:239], v[112:127]
ds_read_b64_tr_b16 v[162:163], v10 offset:33024
ds_read_b64_tr_b16 v[164:165], v11 offset:32768
v_fmac_f32 v56, v208, v217
v_fmac_f32 v88, v208, v23
v_fma_f32 v56, -v209, v23, v56
v_fmac_f32 v88, v209, v217
v_mfma_f32_32x32x16_bf16 v[128:143], v[172:175], v[232:235], v[128:143]
ds_read_b64_tr_b16 v[166:167], v11 offset:33024
v_fmac_f32 v57, v208, v56
v_fmac_f32 v89, v208, v88
v_fma_f32 v57, -v209, v88, v57
v_fmac_f32 v89, v209, v56
v_fmac_f32 v58, v208, v57
v_fmac_f32 v90, v208, v89
v_mfma_f32_32x32x16_bf16 v[144:159], v[172:175], v[236:239], v[144:159]
v_fma_f32 v58, -v209, v89, v58
v_fmac_f32 v90, v209, v57
v_fmac_f32 v59, v208, v58
v_fmac_f32 v91, v208, v90
v_fma_f32 v59, -v209, v90, v59
v_fmac_f32 v91, v209, v58
v_mfma_f32_32x32x16_bf16 v[96:111], v[176:179], v[240:243], v[96:111]
ds_read_b64_tr_b16 v[168:169], v10 offset:34816
ds_read_b64_tr_b16 v[170:171], v10 offset:35072
v_mul_f32 v214, v213, v56
v_mul_f32 v56, v212, v56
v_fma_f32 v56, -v213, v88, v56
v_fma_f32 v88, v212, v88, v214
v_mfma_f32_32x32x16_bf16 v[112:127], v[176:179], v[244:247], v[112:127]
ds_read_b64_tr_b16 v[172:173], v11 offset:34816
ds_read_b64_tr_b16 v[174:175], v11 offset:35072
v_mul_f32 v214, v213, v57
v_mul_f32 v57, v212, v57
v_fma_f32 v57, -v213, v89, v57
v_fma_f32 v89, v212, v89, v214
v_mfma_f32_32x32x16_bf16 v[128:143], v[180:183], v[240:243], v[128:143]
v_mul_f32 v214, v213, v58
v_mul_f32 v58, v212, v58
v_fma_f32 v58, -v213, v90, v58
v_fma_f32 v90, v212, v90, v214
v_mul_f32 v214, v213, v59
v_mul_f32 v59, v212, v59
v_fma_f32 v59, -v213, v91, v59
v_mfma_f32_32x32x16_bf16 v[144:159], v[180:183], v[244:247], v[144:159]
v_fma_f32 v91, v212, v91, v214
v_cvt_pk_bf16_f32 v56, v56, v57
v_cvt_pk_bf16_f32 v57, v58, v59
v_cvt_pk_bf16_f32 v58, v88, v89
v_cvt_pk_bf16_f32 v59, v90, v91
buffer_store_dwordx4 v[56:59], v16, s[96:99], s71 offen offset:2048
v_mfma_f32_32x32x16_bf16 v[96:111], v[184:187], v[248:251], v[96:111]
ds_read_b64_tr_b16 v[176:177], v10 offset:36864
ds_read_b64_tr_b16 v[178:179], v10 offset:37120
v_fmac_f32 v60, v200, v2
v_fmac_f32 v92, v200, v3
v_fmac_f32 v61, v201, v2
v_fmac_f32 v93, v201, v3
v_fmac_f32 v62, v202, v2
v_fmac_f32 v94, v202, v3
v_fmac_f32 v63, v203, v2
v_fmac_f32 v95, v203, v3
v_mul_f32 v60, v204, v60
v_mul_f32 v92, v204, v92
v_mul_f32 v61, v205, v61
v_mul_f32 v93, v205, v93
v_mul_f32 v62, v206, v62
v_mul_f32 v94, v206, v94
v_mul_f32 v63, v207, v63
v_mul_f32 v95, v207, v95
v_fma_f32 v217, v208, v60, v61
v_fma_f32 v23, v208, v92, v93
v_fma_f32 v217, -v209, v92, v217
v_fmac_f32 v23, v209, v60
v_fma_f32 v17, v208, v217, v62
v_fma_f32 v216, v208, v23, v94
v_fma_f32 v17, -v209, v23, v17
v_fmac_f32 v216, v209, v217
v_fma_f32 v217, v208, v17, v63
v_fma_f32 v23, v208, v216, v95
v_fma_f32 v217, -v209, v216, v217
v_fmac_f32 v23, v209, v17
v_mov_b32 v27, v217
v_mov_b32 v215, v23
s_nop 0
v_permlane32_swap_b32 v217, v27
v_permlane32_swap_b32 v23, v215
v_fma_f32 v17, v210, v4, v217
v_fma_f32 v216, v210, v15, v23
v_fma_f32 v17, -v211, v15, v17
v_fmac_f32 v216, v211, v4
v_cndmask_b32 v217, v17, v4, vcc
v_cndmask_b32 v23, v216, v15, vcc
v_fma_f32 v4, v210, v17, v27
v_fma_f32 v15, v210, v216, v215
v_fma_f32 v4, -v211, v216, v4
v_fmac_f32 v15, v211, v17
v_fmac_f32 v60, v208, v217
v_fmac_f32 v92, v208, v23
v_fma_f32 v60, -v209, v23, v60
v_fmac_f32 v92, v209, v217
v_fmac_f32 v61, v208, v60
v_fmac_f32 v93, v208, v92
v_fma_f32 v61, -v209, v92, v61
v_fmac_f32 v93, v209, v60
v_fmac_f32 v62, v208, v61
v_fmac_f32 v94, v208, v93
v_fma_f32 v62, -v209, v93, v62
v_fmac_f32 v94, v209, v61
v_fmac_f32 v63, v208, v62
v_fmac_f32 v95, v208, v94
v_fma_f32 v63, -v209, v94, v63
v_fmac_f32 v95, v209, v62
v_mul_f32 v214, v213, v60
v_mul_f32 v60, v212, v60
v_fma_f32 v60, -v213, v92, v60
v_fma_f32 v92, v212, v92, v214
v_mul_f32 v214, v213, v61
v_mul_f32 v61, v212, v61
v_fma_f32 v61, -v213, v93, v61
v_fma_f32 v93, v212, v93, v214
v_mul_f32 v214, v213, v62
v_mul_f32 v62, v212, v62
v_fma_f32 v62, -v213, v94, v62
v_fma_f32 v94, v212, v94, v214
v_mul_f32 v214, v213, v63
v_mul_f32 v63, v212, v63
v_fma_f32 v63, -v213, v95, v63
v_fma_f32 v95, v212, v95, v214
v_cvt_pk_bf16_f32 v60, v60, v61
v_cvt_pk_bf16_f32 v61, v62, v63
v_cvt_pk_bf16_f32 v62, v92, v93
v_cvt_pk_bf16_f32 v63, v94, v95
buffer_store_dwordx4 v[60:63], v16, s[96:99], s71 offen offset:3072
v_mfma_f32_32x32x16_bf16 v[112:127], v[184:187], v[252:255], v[112:127]
ds_read_b64_tr_b16 v[180:181], v11 offset:36864
ds_read_b64_tr_b16 v[182:183], v11 offset:37120
v_mov_b32 v200, v4
v_mov_b32 v201, v15
v_and_b32 v16, 31, v1
v_lshlrev_b32 v16, 3, v16
v_mfma_f32_32x32x16_bf16 v[128:143], v[188:191], v[248:251], v[128:143]
s_lshl_b32 s76, s40, 8
s_add_u32 s76, s76, 152576
v_add_u32 v19, s76, v16
ds_write_b64 v19, v[200:201]
v_mfma_f32_32x32x16_bf16 v[144:159], v[188:191], v[252:255], v[144:159]
s_mov_b32 s64, s65
s_mov_b32 s65, s66
s_mov_b32 s66, s67
s_mov_b32 s69, s73
s_mov_b32 s73, s74
s_mov_b32 s74, s59
s_mov_b32 s59, s95
s_sub_u32 s83, s83, 1
s_mov_b32 s52, s53
s_mov_b32 s53, s54
s_mov_b32 s54, s55
s_mov_b32 s55, s56
s_mov_b32 s56, s57
s_cmp_eq_u32 s83, 1
s_waitcnt lgkmcnt(0)
s_barrier
v_add_u32 v16, 152576, v16
v_lshlrev_b32 v19, 3, v16
v_and_b32 v19, 31, v1
v_lshlrev_b32 v19, 6, v19
v_add_u32 v19, 150528, v19
)ASM"
R"ASM(
ds_read_b64 v[200:201], v16 offset:0
ds_read_b64 v[202:203], v16 offset:256
ds_read_b64 v[204:205], v16 offset:512
ds_read_b64 v[206:207], v16 offset:768
ds_read_b64 v[208:209], v19 offset:32
s_waitcnt lgkmcnt(0)
v_fma_f32 v212, v208, v200, v202
v_fma_f32 v213, v208, v201, v203
v_fma_f32 v212, -v209, v201, v212
v_fmac_f32 v213, v209, v200
v_fma_f32 v214, v208, v212, v204
v_fma_f32 v215, v208, v213, v205
v_fma_f32 v214, -v209, v213, v214
v_fmac_f32 v215, v209, v212
v_fma_f32 v200, v208, v214, v206
v_fma_f32 v202, v208, v215, v207
v_fma_f32 v200, -v209, v215, v200
v_fmac_f32 v202, v209, v214
v_mov_b32 v201, v219
v_mov_b32 v203, v219
v_and_b32 v19, 31, v1
v_lshlrev_b32 v19, 4, v19
buffer_store_dwordx4 v[200:203], v19, s[96:99], s42 offen sc1
v_bfe_u32 v16, v1, 5, 1
v_and_b32 v19, 31, v1
v_lshlrev_b32 v19, 5, v19
v_lshl_add_u32 v19, v16, 4, v19
v_add_u32 v19, s58, v19
v_lshlrev_b32 v16, 5, v16
v_add_u32 v16, 147456, v16
s_waitcnt lgkmcnt(0)
s_branch LLAST_%=
LF0_%=:
ds_read_b64_tr_b16 v[160:161], v10 offset:32768
ds_read_b64_tr_b16 v[162:163], v10 offset:33024
ds_read_b64_tr_b16 v[164:165], v11 offset:32768
ds_read_b64_tr_b16 v[166:167], v11 offset:33024
ds_read_b64_tr_b16 v[168:169], v10 offset:34816
ds_read_b64_tr_b16 v[170:171], v10 offset:35072
ds_read_b64_tr_b16 v[172:173], v11 offset:34816
ds_read_b64_tr_b16 v[174:175], v11 offset:35072
ds_read_b64_tr_b16 v[176:177], v10 offset:36864
ds_read_b64_tr_b16 v[178:179], v10 offset:37120
ds_read_b64_tr_b16 v[180:181], v11 offset:36864
ds_read_b64_tr_b16 v[182:183], v11 offset:37120
LA_%=:
s_waitcnt lgkmcnt(10)
v_mfma_f32_32x32x16_bf16 v[32:47], v[160:163], a[0:3], 0
ds_read_b64_tr_b16 v[184:185], v10 offset:38912
ds_read_b64_tr_b16 v[186:187], v10 offset:39168
v_mfma_f32_32x32x16_bf16 v[48:63], v[160:163], a[4:7], 0
ds_read_b64_tr_b16 v[188:189], v11 offset:38912
ds_read_b64_tr_b16 v[190:191], v11 offset:39168
s_waitcnt lgkmcnt(12)
v_mfma_f32_32x32x16_bf16 v[64:79], v[164:167], a[0:3], 0
ds_read_b64_tr_b16 v[192:193], v14 offset:32768
ds_read_b64_tr_b16 v[194:195], v14 offset:34816
v_mfma_f32_32x32x16_bf16 v[80:95], v[164:167], a[4:7], 0
ds_read_b64_tr_b16 v[196:197], v14 offset:36864
ds_read_b64_tr_b16 v[198:199], v14 offset:38912
s_waitcnt lgkmcnt(14)
v_mfma_f32_32x32x16_bf16 v[32:47], v[168:171], a[8:11], v[32:47]
ds_read_b64_tr_b16 v[160:161], v10 offset:40960
ds_read_b64_tr_b16 v[162:163], v10 offset:41216
v_mfma_f32_32x32x16_bf16 v[48:63], v[168:171], a[12:15], v[48:63]
ds_read_b64_tr_b16 v[164:165], v11 offset:40960
ds_read_b64_tr_b16 v[166:167], v11 offset:41216
s_waitcnt lgkmcnt(6)
v_mfma_f32_16x16x32_bf16 v[220:223], v[192:195], v[28:31], 0
ds_read_b128 v[200:203], v16 offset:256
v_mfma_f32_32x32x16_bf16 v[64:79], v[172:175], a[8:11], v[64:79]
s_add_u32 m0, s41, 57344
s_mov_b32 s62, s64
buffer_load_dwordx4 v5, s[84:87], s62 offen lds
ds_read_b128 v[204:207], v16 offset:320
v_mfma_f32_32x32x16_bf16 v[80:95], v[172:175], a[12:15], v[80:95]
v_mfma_f32_32x32x16_bf16 v[32:47], v[176:179], a[16:19], v[32:47]
ds_read_b64_tr_b16 v[168:169], v10 offset:43008
ds_read_b64_tr_b16 v[170:171], v10 offset:43264
v_mfma_f32_32x32x16_bf16 v[48:63], v[176:179], a[20:23], v[48:63]
ds_read_b64_tr_b16 v[172:173], v11 offset:43008
ds_read_b64_tr_b16 v[174:175], v11 offset:43264
s_waitcnt lgkmcnt(5)
v_fmac_f32 v96, v20, v200
s_waitcnt lgkmcnt(4)
v_fmac_f32 v104, v20, v204
v_fmac_f32 v97, v20, v201
v_mfma_f32_16x16x32_bf16 v[220:223], v[196:199], v[28:31], v[220:223]
ds_read_b64_tr_b16 v[192:193], v14 offset:40960
v_mfma_f32_32x32x16_bf16 v[64:79], v[180:183], a[16:19], v[64:79]
ds_read_b64_tr_b16 v[194:195], v14 offset:43008
s_add_u32 m0, s41, 61440
s_add_u32 s62, s62, s93
buffer_load_dwordx4 v5, s[88:91], s62 offen lds
v_fmac_f32 v105, v20, v205
v_mfma_f32_32x32x16_bf16 v[80:95], v[180:183], a[20:23], v[80:95]
v_mul_f32 v104, v96, v104
v_mul_f32 v96, v21, v96
v_mul_f32 v105, v97, v105
v_mul_f32 v97, v21, v97
v_exp_f32 v96, v96
v_mfma_f32_32x32x16_bf16 v[32:47], v[184:187], a[24:27], v[32:47]
ds_read_b64_tr_b16 v[176:177], v10 offset:45056
ds_read_b64_tr_b16 v[178:179], v10 offset:45312
v_exp_f32 v97, v97
v_fma_f32 v96, v96, v22, v22
v_mfma_f32_32x32x16_bf16 v[48:63], v[184:187], a[28:31], v[48:63]
ds_read_b64_tr_b16 v[180:181], v11 offset:45056
ds_read_b64_tr_b16 v[182:183], v11 offset:45312
v_fma_f32 v97, v97, v22, v22
v_rcp_f32 v96, v96
v_mfma_f32_32x32x16_bf16 v[64:79], v[188:191], a[24:27], v[64:79]
ds_read_b64_tr_b16 v[196:197], v14 offset:45056
ds_read_b64_tr_b16 v[198:199], v14 offset:47104
v_rcp_f32 v97, v97
v_mul_f32 v104, v104, v96
v_mfma_f32_32x32x16_bf16 v[80:95], v[188:191], a[28:31], v[80:95]
s_waitcnt lgkmcnt(8)
s_add_u32 m0, s41, 65536
s_add_u32 s62, s62, s92
buffer_load_dwordx4 v5, s[84:87], s62 offen lds
v_mul_f32 v105, v105, v97
v_mfma_f32_32x32x16_bf16 v[32:47], v[160:163], a[32:35], v[32:47]
ds_read_b64_tr_b16 v[184:185], v10 offset:47104
ds_read_b64_tr_b16 v[186:187], v10 offset:47360
v_cvt_pk_bf16_f32 v96, v104, v105
v_mfma_f32_32x32x16_bf16 v[48:63], v[160:163], a[36:39], v[48:63]
ds_read_b64_tr_b16 v[188:189], v11 offset:47104
ds_read_b64_tr_b16 v[190:191], v11 offset:47360
s_waitcnt lgkmcnt(10)
v_mfma_f32_16x16x32_bf16 v[220:223], v[192:195], v[28:31], v[220:223]
ds_read_b128 v[208:211], v16 offset:272
v_mfma_f32_32x32x16_bf16 v[64:79], v[164:167], a[32:35], v[64:79]
s_add_u32 m0, s41, 69632
s_add_u32 s62, s62, s93
buffer_load_dwordx4 v5, s[88:91], s62 offen lds
ds_read_b128 v[212:215], v16 offset:336
v_fmac_f32 v98, v20, v202
v_mfma_f32_32x32x16_bf16 v[80:95], v[164:167], a[36:39], v[80:95]
v_fmac_f32 v106, v20, v206
v_fmac_f32 v99, v20, v203
v_fmac_f32 v107, v20, v207
v_mul_f32 v106, v98, v106
v_mul_f32 v98, v21, v98
v_mul_f32 v107, v99, v107
v_mfma_f32_32x32x16_bf16 v[32:47], v[168:171], a[40:43], v[32:47]
ds_read_b64_tr_b16 v[160:161], v6 offset:0
ds_read_b64_tr_b16 v[162:163], v6 offset:256
v_mul_f32 v99, v21, v99
v_exp_f32 v98, v98
v_mfma_f32_32x32x16_bf16 v[48:63], v[168:171], a[44:47], v[48:63]
ds_read_b64_tr_b16 v[164:165], v7 offset:0
ds_read_b64_tr_b16 v[166:167], v7 offset:256
v_exp_f32 v99, v99
v_fma_f32 v98, v98, v22, v22
s_waitcnt lgkmcnt(10)
v_mfma_f32_16x16x32_bf16 v[220:223], v[196:199], v[28:31], v[220:223]
ds_read_b64_tr_b16 v[192:193], v12 offset:0
v_mfma_f32_32x32x16_bf16 v[64:79], v[172:175], a[40:43], v[64:79]
ds_read_b64_tr_b16 v[194:195], v12 offset:2048
s_add_u32 m0, s41, 73728
s_add_u32 s62, s62, s92
buffer_load_dwordx4 v5, s[84:87], s62 offen lds
v_fma_f32 v99, v99, v22, v22
v_mfma_f32_32x32x16_bf16 v[80:95], v[172:175], a[44:47], v[80:95]
s_waitcnt lgkmcnt(8)
v_rcp_f32 v98, v98
v_rcp_f32 v99, v99
v_mul_f32 v106, v106, v98
v_mfma_f32_32x32x16_bf16 v[32:47], v[176:179], a[48:51], v[32:47]
ds_read_b64_tr_b16 v[168:169], v6 offset:2048
ds_read_b64_tr_b16 v[170:171], v6 offset:2304
v_mul_f32 v107, v107, v99
v_cvt_pk_bf16_f32 v97, v106, v107
v_fmac_f32 v112, v24, v200
v_mfma_f32_32x32x16_bf16 v[48:63], v[176:179], a[52:55], v[48:63]
ds_read_b64_tr_b16 v[172:173], v7 offset:2048
ds_read_b64_tr_b16 v[174:175], v7 offset:2304
v_fmac_f32 v120, v24, v204
v_fmac_f32 v113, v24, v201
v_fmac_f32 v121, v24, v205
v_mfma_f32_32x32x16_bf16 v[64:79], v[180:183], a[48:51], v[64:79]
s_add_u32 m0, s41, 77824
s_add_u32 s62, s62, s93
buffer_load_dwordx4 v5, s[88:91], s62 offen lds
v_mul_f32 v120, v112, v120
v_mul_f32 v112, v25, v112
v_mfma_f32_32x32x16_bf16 v[80:95], v[180:183], a[52:55], v[80:95]
v_mul_f32 v121, v113, v121
v_mul_f32 v113, v25, v113
v_exp_f32 v112, v112
v_exp_f32 v113, v113
v_mfma_f32_32x32x16_bf16 v[32:47], v[184:187], a[56:59], v[32:47]
ds_read_b64_tr_b16 v[176:177], v6 offset:4096
ds_read_b64_tr_b16 v[178:179], v6 offset:4352
v_fma_f32 v112, v112, v26, v26
v_fma_f32 v113, v113, v26, v26
v_mfma_f32_32x32x16_bf16 v[48:63], v[184:187], a[60:63], v[48:63]
ds_read_b64_tr_b16 v[180:181], v7 offset:4096
ds_read_b64_tr_b16 v[182:183], v7 offset:4352
)ASM"
R"ASM(
v_rcp_f32 v112, v112
s_waitcnt lgkmcnt(8)
v_mfma_f32_16x16x32_bf16 v[220:223], v[192:195], v[28:31], v[220:223]
ds_read_b64_tr_b16 v[196:197], v12 offset:4096
v_mfma_f32_32x32x16_bf16 v[64:79], v[188:191], a[56:59], v[64:79]
ds_read_b64_tr_b16 v[198:199], v12 offset:6144
v_rcp_f32 v113, v113
v_mul_f32 v120, v120, v112
v_mul_f32 v121, v121, v113
v_mfma_f32_32x32x16_bf16 v[80:95], v[188:191], a[60:63], v[80:95]
s_waitcnt lgkmcnt(6)
v_cvt_pk_bf16_f32 v112, v120, v121
v_fmac_f32 v114, v24, v202
v_fmac_f32 v122, v24, v206
v_fmac_f32 v115, v24, v203
v_fmac_f32 v123, v24, v207
v_mfma_f32_32x32x16_bf16 v[32:47], v[160:163], a[64:67], v[32:47]
ds_read_b64_tr_b16 v[184:185], v6 offset:6144
ds_read_b64_tr_b16 v[186:187], v6 offset:6400
v_mul_f32 v122, v114, v122
v_mul_f32 v114, v25, v114
v_mul_f32 v123, v115, v123
v_mfma_f32_32x32x16_bf16 v[48:63], v[160:163], a[68:71], v[48:63]
ds_read_b64_tr_b16 v[188:189], v7 offset:6144
ds_read_b64_tr_b16 v[190:191], v7 offset:6400
v_mul_f32 v115, v25, v115
v_exp_f32 v114, v114
v_mfma_f32_32x32x16_bf16 v[64:79], v[164:167], a[64:67], v[64:79]
ds_read_b64_tr_b16 v[192:193], v12 offset:8192
ds_read_b64_tr_b16 v[194:195], v12 offset:10240
v_exp_f32 v115, v115
v_fma_f32 v114, v114, v26, v26
v_mfma_f32_32x32x16_bf16 v[80:95], v[164:167], a[68:71], v[80:95]
s_add_u32 m0, s41, 81920
s_add_u32 s62, s62, s92
buffer_load_dwordx4 v5, s[84:87], s62 offen lds
v_fma_f32 v115, v115, v26, v26
v_mfma_f32_32x32x16_bf16 v[32:47], v[168:171], a[72:75], v[32:47]
ds_read_b64_tr_b16 v[160:161], v6 offset:8192
ds_read_b64_tr_b16 v[162:163], v6 offset:8448
v_rcp_f32 v114, v114
v_mfma_f32_32x32x16_bf16 v[48:63], v[168:171], a[76:79], v[48:63]
ds_read_b64_tr_b16 v[164:165], v7 offset:8192
ds_read_b64_tr_b16 v[166:167], v7 offset:8448
v_rcp_f32 v115, v115
v_mul_f32 v122, v122, v114
s_waitcnt lgkmcnt(10)
v_mfma_f32_16x16x32_bf16 v[220:223], v[196:199], v[28:31], v[220:223]
v_mul_f32 v123, v123, v115
v_cvt_pk_bf16_f32 v113, v122, v123
v_mfma_f32_32x32x16_bf16 v[64:79], v[172:175], a[72:75], v[64:79]
s_add_u32 m0, s41, 86016
s_add_u32 s62, s62, s93
buffer_load_dwordx4 v5, s[88:91], s62 offen lds
v_fmac_f32 v100, v20, v208
v_fmac_f32 v108, v20, v212
v_mfma_f32_32x32x16_bf16 v[80:95], v[172:175], a[76:79], v[80:95]
s_waitcnt lgkmcnt(6)
v_fmac_f32 v101, v20, v209
v_fmac_f32 v109, v20, v213
v_mul_f32 v108, v100, v108
v_mul_f32 v100, v21, v100
v_mul_f32 v109, v101, v109
v_mfma_f32_32x32x16_bf16 v[32:47], v[176:179], a[80:83], v[32:47]
ds_read_b64_tr_b16 v[168:169], v6 offset:10240
ds_read_b64_tr_b16 v[170:171], v6 offset:10496
v_mul_f32 v101, v21, v101
v_exp_f32 v100, v100
v_mfma_f32_32x32x16_bf16 v[48:63], v[176:179], a[84:87], v[48:63]
ds_read_b64_tr_b16 v[172:173], v7 offset:10240
ds_read_b64_tr_b16 v[174:175], v7 offset:10496
v_exp_f32 v101, v101
v_fma_f32 v100, v100, v22, v22
s_waitcnt lgkmcnt(8)
v_mfma_f32_16x16x32_bf16 v[220:223], v[192:195], v[28:31], v[220:223]
ds_read_b64_tr_b16 v[196:197], v12 offset:12288
v_mfma_f32_32x32x16_bf16 v[64:79], v[180:183], a[80:83], v[64:79]
ds_read_b64_tr_b16 v[198:199], v12 offset:14336
s_add_u32 m0, s41, 90112
s_add_u32 s62, s62, s92
buffer_load_dwordx4 v5, s[84:87], s62 offen lds
v_fma_f32 v101, v101, v22, v22
v_mfma_f32_32x32x16_bf16 v[80:95], v[180:183], a[84:87], v[80:95]
v_rcp_f32 v100, v100
v_rcp_f32 v101, v101
v_mul_f32 v108, v108, v100
v_mul_f32 v109, v109, v101
v_mfma_f32_32x32x16_bf16 v[32:47], v[184:187], a[88:91], v[32:47]
ds_read_b64_tr_b16 v[176:177], v6 offset:12288
ds_read_b64_tr_b16 v[178:179], v6 offset:12544
v_cvt_pk_bf16_f32 v98, v108, v109
v_mfma_f32_32x32x16_bf16 v[48:63], v[184:187], a[92:95], v[48:63]
ds_read_b64_tr_b16 v[180:181], v7 offset:12288
ds_read_b64_tr_b16 v[182:183], v7 offset:12544
v_mfma_f32_32x32x16_bf16 v[64:79], v[188:191], a[88:91], v[64:79]
ds_read_b128 v[200:203], v16 offset:384
ds_read_b128 v[204:207], v16 offset:448
v_fmac_f32 v102, v20, v210
v_fmac_f32 v110, v20, v214
v_fmac_f32 v103, v20, v211
v_mfma_f32_32x32x16_bf16 v[80:95], v[188:191], a[92:95], v[80:95]
s_waitcnt lgkmcnt(8)
v_fmac_f32 v111, v20, v215
v_mul_f32 v110, v102, v110
v_mul_f32 v102, v21, v102
v_mul_f32 v111, v103, v111
v_mul_f32 v103, v21, v103
v_mfma_f32_32x32x16_bf16 v[32:47], v[160:163], a[96:99], v[32:47]
ds_read_b64_tr_b16 v[184:185], v6 offset:14336
ds_read_b64_tr_b16 v[186:187], v6 offset:14592
v_exp_f32 v102, v102
v_mfma_f32_32x32x16_bf16 v[48:63], v[160:163], a[100:103], v[48:63]
ds_read_b64_tr_b16 v[188:189], v7 offset:14336
ds_read_b64_tr_b16 v[190:191], v7 offset:14592
v_exp_f32 v103, v103
v_fma_f32 v102, v102, v22, v22
s_waitcnt lgkmcnt(10)
v_mfma_f32_16x16x32_bf16 v[220:223], v[196:199], v[28:31], v[220:223]
ds_read_b64_tr_b16 v[192:193], v12 offset:16384
v_mfma_f32_32x32x16_bf16 v[64:79], v[164:167], a[96:99], v[64:79]
ds_read_b64_tr_b16 v[194:195], v12 offset:18432
v_fma_f32 v103, v103, v22, v22
v_rcp_f32 v102, v102
v_mfma_f32_32x32x16_bf16 v[80:95], v[164:167], a[100:103], v[80:95]
v_rcp_f32 v103, v103
v_mul_f32 v110, v110, v102
v_mul_f32 v111, v111, v103
v_cvt_pk_bf16_f32 v99, v110, v111
v_mfma_f32_32x32x16_bf16 v[32:47], v[168:171], a[104:107], v[32:47]
ds_read_b64_tr_b16 v[160:161], v6 offset:16384
ds_read_b64_tr_b16 v[162:163], v6 offset:16640
buffer_store_dwordx4 v[96:99], v19, s[96:99], s69 offen offset:0 sc1
v_fmac_f32 v116, v24, v208
v_mfma_f32_32x32x16_bf16 v[48:63], v[168:171], a[108:111], v[48:63]
ds_read_b64_tr_b16 v[164:165], v7 offset:16384
ds_read_b64_tr_b16 v[166:167], v7 offset:16640
v_fmac_f32 v124, v24, v212
v_fmac_f32 v117, v24, v209
v_fmac_f32 v125, v24, v213
v_mfma_f32_32x32x16_bf16 v[64:79], v[172:175], a[104:107], v[64:79]
v_mul_f32 v124, v116, v124
v_mul_f32 v116, v25, v116
v_mul_f32 v125, v117, v125
v_mul_f32 v117, v25, v117
v_exp_f32 v116, v116
v_mfma_f32_32x32x16_bf16 v[80:95], v[172:175], a[108:111], v[80:95]
s_waitcnt lgkmcnt(6)
v_exp_f32 v117, v117
v_fma_f32 v116, v116, v26, v26
v_fma_f32 v117, v117, v26, v26
v_mfma_f32_32x32x16_bf16 v[32:47], v[176:179], a[112:115], v[32:47]
ds_read_b64_tr_b16 v[168:169], v6 offset:18432
ds_read_b64_tr_b16 v[170:171], v6 offset:18688
v_rcp_f32 v116, v116
v_mfma_f32_32x32x16_bf16 v[48:63], v[176:179], a[116:119], v[48:63]
ds_read_b64_tr_b16 v[172:173], v7 offset:18432
ds_read_b64_tr_b16 v[174:175], v7 offset:18688
v_rcp_f32 v117, v117
v_mul_f32 v124, v124, v116
s_waitcnt lgkmcnt(8)
v_mfma_f32_16x16x32_bf16 v[220:223], v[192:195], v[28:31], v[220:223]
v_mul_f32 v125, v125, v117
v_cvt_pk_bf16_f32 v114, v124, v125
v_mfma_f32_32x32x16_bf16 v[64:79], v[180:183], a[112:115], v[64:79]
v_fmac_f32 v118, v24, v210
v_fmac_f32 v126, v24, v214
v_fmac_f32 v119, v24, v211
v_fmac_f32 v127, v24, v215
v_mul_f32 v126, v118, v126
v_mul_f32 v118, v25, v118
v_mfma_f32_32x32x16_bf16 v[80:95], v[180:183], a[116:119], v[80:95]
v_mul_f32 v127, v119, v127
v_mul_f32 v119, v25, v119
v_exp_f32 v118, v118
v_exp_f32 v119, v119
v_mfma_f32_32x32x16_bf16 v[32:47], v[184:187], a[120:123], v[32:47]
s_waitcnt vmcnt(10) lgkmcnt(0)
s_barrier
s_mov_b64 exec, 1
buffer_store_dword v219, off, s[96:99], s52 sc1
s_mov_b64 exec, -1
v_mfma_f32_32x32x16_bf16 v[48:63], v[184:187], a[124:127], v[48:63]
ds_read_b64_tr_b16 v[176:177], v6 offset:20480
ds_read_b64_tr_b16 v[178:179], v6 offset:20736
v_fma_f32 v118, v118, v26, v26
v_fma_f32 v119, v119, v26, v26
v_mfma_f32_32x32x16_bf16 v[64:79], v[188:191], a[120:123], v[64:79]
ds_read_b64_tr_b16 v[180:181], v7 offset:20480
ds_read_b64_tr_b16 v[182:183], v7 offset:20736
v_rcp_f32 v118, v118
v_mfma_f32_32x32x16_bf16 v[80:95], v[188:191], a[124:127], v[80:95]
ds_read_b64_tr_b16 v[196:197], v12 offset:20480
ds_read_b64_tr_b16 v[198:199], v12 offset:22528
v_rcp_f32 v119, v119
v_mfma_f32_32x32x16_bf16 v[32:47], v[160:163], a[128:131], v[32:47]
ds_read_b64_tr_b16 v[184:185], v6 offset:22528
ds_read_b64_tr_b16 v[186:187], v6 offset:22784
v_mul_f32 v126, v126, v118
v_mul_f32 v127, v127, v119
v_cvt_pk_bf16_f32 v115, v126, v127
)ASM"
R"ASM(
v_mfma_f32_32x32x16_bf16 v[48:63], v[160:163], a[132:135], v[48:63]
ds_read_b64_tr_b16 v[188:189], v7 offset:22528
ds_read_b64_tr_b16 v[190:191], v7 offset:22784
buffer_store_dwordx4 v[112:115], v19, s[96:99], s69 offen offset:1024 sc1
v_fmac_f32 v128, v20, v200
v_mfma_f32_32x32x16_bf16 v[64:79], v[164:167], a[128:131], v[64:79]
ds_read_b64_tr_b16 v[192:193], v12 offset:24576
ds_read_b64_tr_b16 v[194:195], v12 offset:26624
v_fmac_f32 v136, v20, v204
v_fmac_f32 v129, v20, v201
v_fmac_f32 v137, v20, v205
v_mfma_f32_32x32x16_bf16 v[80:95], v[164:167], a[132:135], v[80:95]
s_add_u32 m0, s41, 94208
s_add_u32 s62, s62, s93
buffer_load_dwordx4 v5, s[88:91], s62 offen lds
v_mul_f32 v136, v128, v136
v_mul_f32 v128, v21, v128
v_mfma_f32_32x32x16_bf16 v[32:47], v[168:171], a[136:139], v[32:47]
ds_read_b64_tr_b16 v[160:161], v6 offset:24576
ds_read_b64_tr_b16 v[162:163], v6 offset:24832
v_mul_f32 v137, v129, v137
v_mul_f32 v129, v21, v129
v_mfma_f32_32x32x16_bf16 v[48:63], v[168:171], a[140:143], v[48:63]
ds_read_b64_tr_b16 v[164:165], v7 offset:24576
ds_read_b64_tr_b16 v[166:167], v7 offset:24832
v_exp_f32 v128, v128
s_waitcnt lgkmcnt(10)
v_mfma_f32_16x16x32_bf16 v[220:223], v[196:199], v[28:31], v[220:223]
v_exp_f32 v129, v129
v_mfma_f32_32x32x16_bf16 v[64:79], v[172:175], a[136:139], v[64:79]
s_add_u32 m0, s41, 98304
s_add_u32 s62, s62, s92
buffer_load_dwordx4 v5, s[84:87], s62 offen lds
v_fma_f32 v128, v128, v22, v22
v_fma_f32 v129, v129, v22, v22
v_mfma_f32_32x32x16_bf16 v[80:95], v[172:175], a[140:143], v[80:95]
s_waitcnt lgkmcnt(6)
v_rcp_f32 v128, v128
v_rcp_f32 v129, v129
v_mul_f32 v136, v136, v128
v_mfma_f32_32x32x16_bf16 v[32:47], v[176:179], a[144:147], v[32:47]
ds_read_b64_tr_b16 v[168:169], v6 offset:26624
ds_read_b64_tr_b16 v[170:171], v6 offset:26880
v_mul_f32 v137, v137, v129
v_cvt_pk_bf16_f32 v128, v136, v137
v_mfma_f32_32x32x16_bf16 v[48:63], v[176:179], a[148:151], v[48:63]
ds_read_b64_tr_b16 v[172:173], v7 offset:26624
ds_read_b64_tr_b16 v[174:175], v7 offset:26880
s_waitcnt lgkmcnt(8)
v_mfma_f32_16x16x32_bf16 v[220:223], v[192:195], v[28:31], v[220:223]
ds_read_b64_tr_b16 v[196:197], v12 offset:28672
v_mfma_f32_32x32x16_bf16 v[64:79], v[180:183], a[144:147], v[64:79]
ds_read_b64_tr_b16 v[198:199], v12 offset:30720
s_add_u32 m0, s41, 102400
s_add_u32 s62, s62, s93
buffer_load_dwordx4 v5, s[88:91], s62 offen lds
v_mfma_f32_32x32x16_bf16 v[80:95], v[180:183], a[148:151], v[80:95]
ds_read_b128 v[208:211], v16 offset:400
ds_read_b128 v[212:215], v16 offset:464
v_fmac_f32 v130, v20, v202
v_fmac_f32 v138, v20, v206
v_fmac_f32 v131, v20, v203
v_mfma_f32_32x32x16_bf16 v[32:47], v[184:187], a[152:155], v[32:47]
ds_read_b64_tr_b16 v[176:177], v6 offset:28672
ds_read_b64_tr_b16 v[178:179], v6 offset:28928
v_fmac_f32 v139, v20, v207
v_mul_f32 v138, v130, v138
v_mul_f32 v130, v21, v130
v_mfma_f32_32x32x16_bf16 v[48:63], v[184:187], a[156:159], v[48:63]
ds_read_b64_tr_b16 v[180:181], v7 offset:28672
ds_read_b64_tr_b16 v[182:183], v7 offset:28928
v_mul_f32 v139, v131, v139
v_mul_f32 v131, v21, v131
v_mfma_f32_32x32x16_bf16 v[64:79], v[188:191], a[152:155], v[64:79]
v_exp_f32 v130, v130
v_exp_f32 v131, v131
v_fma_f32 v130, v130, v22, v22
v_fma_f32 v131, v131, v22, v22
v_mfma_f32_32x32x16_bf16 v[80:95], v[188:191], a[156:159], v[80:95]
s_waitcnt lgkmcnt(8)
v_rcp_f32 v130, v130
v_rcp_f32 v131, v131
v_mul_f32 v138, v138, v130
v_mfma_f32_32x32x16_bf16 v[32:47], v[160:163], a[160:163], v[32:47]
ds_read_b64_tr_b16 v[184:185], v6 offset:30720
ds_read_b64_tr_b16 v[186:187], v6 offset:30976
v_mul_f32 v139, v139, v131
v_cvt_pk_bf16_f32 v129, v138, v139
v_fmac_f32 v144, v24, v200
v_mfma_f32_32x32x16_bf16 v[48:63], v[160:163], a[164:167], v[48:63]
ds_read_b64_tr_b16 v[188:189], v7 offset:30720
ds_read_b64_tr_b16 v[190:191], v7 offset:30976
v_fmac_f32 v152, v24, v204
v_fmac_f32 v145, v24, v201
v_fmac_f32 v153, v24, v205
s_waitcnt lgkmcnt(10)
v_mfma_f32_16x16x32_bf16 v[220:223], v[196:199], v[28:31], v[220:223]
ds_read_b64_tr_b16 v[192:193], v12 offset:32768
v_mfma_f32_32x32x16_bf16 v[64:79], v[164:167], a[160:163], v[64:79]
ds_read_b64_tr_b16 v[194:195], v12 offset:34816
s_add_u32 m0, s41, 106496
s_add_u32 s62, s62, s92
buffer_load_dwordx4 v5, s[84:87], s62 offen lds
v_mul_f32 v152, v144, v152
v_mfma_f32_32x32x16_bf16 v[80:95], v[164:167], a[164:167], v[80:95]
v_mul_f32 v144, v25, v144
v_mul_f32 v153, v145, v153
v_mul_f32 v145, v25, v145
v_exp_f32 v144, v144
v_mfma_f32_32x32x16_bf16 v[32:47], v[168:171], a[168:171], v[32:47]
ds_read_b64_tr_b16 v[160:161], v6 offset:32768
ds_read_b64_tr_b16 v[162:163], v6 offset:33024
v_exp_f32 v145, v145
v_fma_f32 v144, v144, v26, v26
v_mfma_f32_32x32x16_bf16 v[48:63], v[168:171], a[172:175], v[48:63]
ds_read_b64_tr_b16 v[164:165], v7 offset:32768
ds_read_b64_tr_b16 v[166:167], v7 offset:33024
v_fma_f32 v145, v145, v26, v26
v_rcp_f32 v144, v144
v_mfma_f32_32x32x16_bf16 v[64:79], v[172:175], a[168:171], v[64:79]
s_add_u32 m0, s41, 110592
s_add_u32 s62, s62, s93
buffer_load_dwordx4 v5, s[88:91], s62 offen lds
v_rcp_f32 v145, v145
v_mfma_f32_32x32x16_bf16 v[80:95], v[172:175], a[172:175], v[80:95]
s_waitcnt lgkmcnt(6)
v_mul_f32 v152, v152, v144
v_mul_f32 v153, v153, v145
v_cvt_pk_bf16_f32 v144, v152, v153
v_fmac_f32 v146, v24, v202
v_fmac_f32 v154, v24, v206
v_mfma_f32_32x32x16_bf16 v[32:47], v[176:179], a[176:179], v[32:47]
ds_read_b64_tr_b16 v[168:169], v6 offset:34816
ds_read_b64_tr_b16 v[170:171], v6 offset:35072
v_fmac_f32 v147, v24, v203
v_fmac_f32 v155, v24, v207
v_mul_f32 v154, v146, v154
v_mfma_f32_32x32x16_bf16 v[48:63], v[176:179], a[180:183], v[48:63]
ds_read_b64_tr_b16 v[172:173], v7 offset:34816
ds_read_b64_tr_b16 v[174:175], v7 offset:35072
v_mul_f32 v146, v25, v146
v_mul_f32 v155, v147, v155
v_mul_f32 v147, v25, v147
s_waitcnt lgkmcnt(8)
v_mfma_f32_16x16x32_bf16 v[220:223], v[192:195], v[28:31], v[220:223]
ds_read_b64_tr_b16 v[196:197], v12 offset:36864
v_mfma_f32_32x32x16_bf16 v[64:79], v[180:183], a[176:179], v[64:79]
ds_read_b64_tr_b16 v[198:199], v12 offset:38912
v_exp_f32 v146, v146
v_exp_f32 v147, v147
v_mfma_f32_32x32x16_bf16 v[80:95], v[180:183], a[180:183], v[80:95]
v_fma_f32 v146, v146, v26, v26
v_fma_f32 v147, v147, v26, v26
v_rcp_f32 v146, v146
v_rcp_f32 v147, v147
v_mfma_f32_32x32x16_bf16 v[32:47], v[184:187], a[184:187], v[32:47]
ds_read_b64_tr_b16 v[176:177], v6 offset:36864
ds_read_b64_tr_b16 v[178:179], v6 offset:37120
v_mul_f32 v154, v154, v146
v_mul_f32 v155, v155, v147
v_cvt_pk_bf16_f32 v145, v154, v155
v_mfma_f32_32x32x16_bf16 v[48:63], v[184:187], a[188:191], v[48:63]
ds_read_b64_tr_b16 v[180:181], v7 offset:36864
ds_read_b64_tr_b16 v[182:183], v7 offset:37120
v_mfma_f32_32x32x16_bf16 v[64:79], v[188:191], a[184:187], v[64:79]
ds_read_b64_tr_b16 v[192:193], v12 offset:40960
ds_read_b64_tr_b16 v[194:195], v12 offset:43008
v_fmac_f32 v132, v20, v208
v_fmac_f32 v140, v20, v212
v_fmac_f32 v133, v20, v209
v_mfma_f32_32x32x16_bf16 v[80:95], v[188:191], a[188:191], v[80:95]
s_waitcnt lgkmcnt(8)
s_add_u32 m0, s41, 114688
s_add_u32 s62, s62, s92
buffer_load_dwordx4 v5, s[84:87], s62 offen lds
v_fmac_f32 v141, v20, v213
v_mfma_f32_32x32x16_bf16 v[32:47], v[160:163], a[192:195], v[32:47]
ds_read_b64_tr_b16 v[184:185], v6 offset:38912
ds_read_b64_tr_b16 v[186:187], v6 offset:39168
v_mul_f32 v140, v132, v140
v_mul_f32 v132, v21, v132
v_mul_f32 v141, v133, v141
v_mfma_f32_32x32x16_bf16 v[48:63], v[160:163], a[196:199], v[48:63]
ds_read_b64_tr_b16 v[188:189], v7 offset:38912
ds_read_b64_tr_b16 v[190:191], v7 offset:39168
v_mul_f32 v133, v21, v133
v_exp_f32 v132, v132
s_waitcnt lgkmcnt(10)
v_mfma_f32_16x16x32_bf16 v[220:223], v[196:199], v[28:31], v[220:223]
v_exp_f32 v133, v133
v_mfma_f32_32x32x16_bf16 v[64:79], v[164:167], a[192:195], v[64:79]
v_fma_f32 v132, v132, v22, v22
v_fma_f32 v133, v133, v22, v22
v_rcp_f32 v132, v132
v_rcp_f32 v133, v133
v_mfma_f32_32x32x16_bf16 v[80:95], v[164:167], a[196:199], v[80:95]
s_add_u32 m0, s41, 118784
s_add_u32 s62, s62, s93
buffer_load_dwordx4 v5, s[88:91], s62 offen lds
v_mul_f32 v140, v140, v132
)ASM"
R"ASM(
v_mul_f32 v141, v141, v133
v_mfma_f32_32x32x16_bf16 v[32:47], v[168:171], a[200:203], v[32:47]
ds_read_b64_tr_b16 v[160:161], v6 offset:40960
ds_read_b64_tr_b16 v[162:163], v6 offset:41216
v_cvt_pk_bf16_f32 v130, v140, v141
v_mfma_f32_32x32x16_bf16 v[48:63], v[168:171], a[204:207], v[48:63]
ds_read_b64_tr_b16 v[164:165], v7 offset:40960
ds_read_b64_tr_b16 v[166:167], v7 offset:41216
v_fmac_f32 v134, v20, v210
v_fmac_f32 v142, v20, v214
v_fmac_f32 v135, v20, v211
s_waitcnt lgkmcnt(8)
v_mfma_f32_16x16x32_bf16 v[220:223], v[192:195], v[28:31], v[220:223]
ds_read_b64_tr_b16 v[196:197], v12 offset:45056
v_mfma_f32_32x32x16_bf16 v[64:79], v[172:175], a[200:203], v[64:79]
ds_read_b64_tr_b16 v[198:199], v12 offset:47104
v_fmac_f32 v143, v20, v215
v_mul_f32 v142, v134, v142
v_mul_f32 v134, v21, v134
v_mul_f32 v143, v135, v143
v_mfma_f32_32x32x16_bf16 v[80:95], v[172:175], a[204:207], v[80:95]
s_waitcnt lgkmcnt(6)
v_mul_f32 v135, v21, v135
v_exp_f32 v134, v134
v_exp_f32 v135, v135
v_mfma_f32_32x32x16_bf16 v[32:47], v[176:179], a[208:211], v[32:47]
ds_read_b64_tr_b16 v[168:169], v6 offset:43008
ds_read_b64_tr_b16 v[170:171], v6 offset:43264
v_fma_f32 v134, v134, v22, v22
v_fma_f32 v135, v135, v22, v22
v_mfma_f32_32x32x16_bf16 v[48:63], v[176:179], a[212:215], v[48:63]
ds_read_b64_tr_b16 v[172:173], v7 offset:43008
ds_read_b64_tr_b16 v[174:175], v7 offset:43264
v_rcp_f32 v134, v134
v_mfma_f32_32x32x16_bf16 v[64:79], v[180:183], a[208:211], v[64:79]
s_add_u32 m0, s41, 122880
s_add_u32 s62, s62, s92
buffer_load_dwordx4 v5, s[84:87], s62 offen lds
v_rcp_f32 v135, v135
v_mfma_f32_32x32x16_bf16 v[80:95], v[180:183], a[212:215], v[80:95]
v_mul_f32 v142, v142, v134
v_mul_f32 v143, v143, v135
v_cvt_pk_bf16_f32 v131, v142, v143
s_add_u32 s75, s69, 65536
buffer_store_dwordx4 v[128:131], v19, s[96:99], s75 offen offset:0 sc1
v_mfma_f32_32x32x16_bf16 v[32:47], v[184:187], a[216:219], v[32:47]
ds_read_b64_tr_b16 v[176:177], v6 offset:45056
ds_read_b64_tr_b16 v[178:179], v6 offset:45312
v_mfma_f32_32x32x16_bf16 v[48:63], v[184:187], a[220:223], v[48:63]
ds_read_b64_tr_b16 v[180:181], v7 offset:45056
ds_read_b64_tr_b16 v[182:183], v7 offset:45312
v_fmac_f32 v148, v24, v208
v_fmac_f32 v156, v24, v212
v_fmac_f32 v149, v24, v209
s_waitcnt lgkmcnt(8)
v_mfma_f32_16x16x32_bf16 v[220:223], v[196:199], v[28:31], v[220:223]
ds_read_b64_tr_b16 v[192:193], v13 offset:0
v_mfma_f32_32x32x16_bf16 v[64:79], v[188:191], a[216:219], v[64:79]
ds_read_b64_tr_b16 v[194:195], v13 offset:2048
v_fmac_f32 v157, v24, v213
v_mul_f32 v156, v148, v156
v_mul_f32 v148, v25, v148
v_mul_f32 v157, v149, v157
v_mfma_f32_32x32x16_bf16 v[80:95], v[188:191], a[220:223], v[80:95]
s_waitcnt lgkmcnt(6)
s_add_u32 m0, s41, 126976
s_add_u32 s62, s62, s93
buffer_load_dwordx4 v5, s[88:91], s62 offen lds
v_mul_f32 v149, v25, v149
v_mfma_f32_32x32x16_bf16 v[32:47], v[160:163], a[224:227], v[32:47]
ds_read_b64_tr_b16 v[184:185], v6 offset:47104
ds_read_b64_tr_b16 v[186:187], v6 offset:47360
v_exp_f32 v148, v148
v_mfma_f32_32x32x16_bf16 v[48:63], v[160:163], a[228:231], v[48:63]
ds_read_b64_tr_b16 v[188:189], v7 offset:47104
ds_read_b64_tr_b16 v[190:191], v7 offset:47360
v_exp_f32 v149, v149
v_fma_f32 v148, v148, v26, v26
v_mfma_f32_32x32x16_bf16 v[64:79], v[164:167], a[224:227], v[64:79]
v_fma_f32 v149, v149, v26, v26
v_rcp_f32 v148, v148
v_rcp_f32 v149, v149
v_mul_f32 v156, v156, v148
v_mfma_f32_32x32x16_bf16 v[80:95], v[164:167], a[228:231], v[80:95]
v_mul_f32 v157, v157, v149
v_cvt_pk_bf16_f32 v146, v156, v157
v_mfma_f32_32x32x16_bf16 v[32:47], v[168:171], a[232:235], v[32:47]
ds_read_b64_tr_b16 v[160:161], v8 offset:0
ds_read_b64_tr_b16 v[162:163], v8 offset:256
v_fmac_f32 v150, v24, v210
v_fmac_f32 v158, v24, v214
v_fmac_f32 v151, v24, v211
v_mfma_f32_32x32x16_bf16 v[48:63], v[168:171], a[236:239], v[48:63]
ds_read_b64_tr_b16 v[164:165], v9 offset:0
ds_read_b64_tr_b16 v[166:167], v9 offset:256
v_fmac_f32 v159, v24, v215
v_mul_f32 v158, v150, v158
v_mul_f32 v150, v25, v150
s_waitcnt lgkmcnt(8)
v_mfma_f32_16x16x32_bf16 v[220:223], v[192:195], v[28:31], v[220:223]
ds_read_b64_tr_b16 v[196:197], v13 offset:4096
v_mfma_f32_32x32x16_bf16 v[64:79], v[172:175], a[232:235], v[64:79]
ds_read_b64_tr_b16 v[198:199], v13 offset:6144
v_mul_f32 v159, v151, v159
v_mul_f32 v151, v25, v151
v_exp_f32 v150, v150
v_mfma_f32_32x32x16_bf16 v[80:95], v[172:175], a[236:239], v[80:95]
s_waitcnt lgkmcnt(6)
v_exp_f32 v151, v151
v_fma_f32 v150, v150, v26, v26
v_fma_f32 v151, v151, v26, v26
v_mfma_f32_32x32x16_bf16 v[32:47], v[176:179], a[240:243], v[32:47]
ds_read_b64_tr_b16 v[168:169], v8 offset:2048
ds_read_b64_tr_b16 v[170:171], v8 offset:2304
v_rcp_f32 v150, v150
v_mfma_f32_32x32x16_bf16 v[48:63], v[176:179], a[244:247], v[48:63]
ds_read_b64_tr_b16 v[172:173], v9 offset:2048
ds_read_b64_tr_b16 v[174:175], v9 offset:2304
v_rcp_f32 v151, v151
v_mul_f32 v158, v158, v150
v_mfma_f32_32x32x16_bf16 v[64:79], v[180:183], a[240:243], v[64:79]
v_mul_f32 v159, v159, v151
v_cvt_pk_bf16_f32 v147, v158, v159
s_add_u32 s75, s69, 65536
buffer_store_dwordx4 v[144:147], v19, s[96:99], s75 offen offset:1024 sc1
v_mfma_f32_32x32x16_bf16 v[80:95], v[180:183], a[244:247], v[80:95]
v_mfma_f32_32x32x16_bf16 v[32:47], v[184:187], a[248:251], v[32:47]
ds_read_b64_tr_b16 v[176:177], v8 offset:4096
ds_read_b64_tr_b16 v[178:179], v8 offset:4352
v_mfma_f32_32x32x16_bf16 v[48:63], v[184:187], a[252:255], v[48:63]
ds_read_b64_tr_b16 v[180:181], v9 offset:4096
ds_read_b64_tr_b16 v[182:183], v9 offset:4352
s_waitcnt lgkmcnt(8)
v_mfma_f32_16x16x32_bf16 v[220:223], v[196:199], v[28:31], v[220:223]
v_mfma_f32_32x32x16_bf16 v[64:79], v[188:191], a[248:251], v[64:79]
v_mfma_f32_32x32x16_bf16 v[80:95], v[188:191], a[252:255], v[80:95]
s_waitcnt lgkmcnt(4)
v_mfma_f32_32x32x16_bf16 v[32:47], v[160:163], v[224:227], v[32:47]
ds_read_b64_tr_b16 v[184:185], v8 offset:6144
ds_read_b64_tr_b16 v[186:187], v8 offset:6400
v_mfma_f32_32x32x16_bf16 v[48:63], v[160:163], v[228:231], v[48:63]
ds_read_b64_tr_b16 v[188:189], v9 offset:6144
ds_read_b64_tr_b16 v[190:191], v9 offset:6400
v_mfma_f32_32x32x16_bf16 v[64:79], v[164:167], v[224:227], v[64:79]
ds_write_b128 v18, v[220:223] offset:0
v_mfma_f32_32x32x16_bf16 v[80:95], v[164:167], v[228:231], v[80:95]
v_mfma_f32_32x32x16_bf16 v[32:47], v[168:171], v[232:235], v[32:47]
s_waitcnt vmcnt(13) lgkmcnt(0)
s_barrier
v_mfma_f32_32x32x16_bf16 v[48:63], v[168:171], v[236:239], v[48:63]
ds_read_b64_tr_b16 v[160:161], v8 offset:8192
ds_read_b64_tr_b16 v[162:163], v8 offset:8448
v_mfma_f32_32x32x16_bf16 v[64:79], v[172:175], v[232:235], v[64:79]
ds_read_b64_tr_b16 v[164:165], v9 offset:8192
ds_read_b64_tr_b16 v[166:167], v9 offset:8448
v_mfma_f32_32x32x16_bf16 v[80:95], v[172:175], v[236:239], v[80:95]
v_mfma_f32_32x32x16_bf16 v[32:47], v[176:179], v[240:243], v[32:47]
ds_read_b64_tr_b16 v[168:169], v8 offset:10240
ds_read_b64_tr_b16 v[170:171], v8 offset:10496
s_mov_b32 s64, s65
s_mov_b32 s65, s66
s_mov_b32 s66, s67
v_mfma_f32_32x32x16_bf16 v[48:63], v[176:179], v[244:247], v[48:63]
ds_read_b64_tr_b16 v[172:173], v9 offset:10240
ds_read_b64_tr_b16 v[174:175], v9 offset:10496
s_mov_b32 s69, s73
s_mov_b32 s73, s74
s_mov_b32 s74, s59
v_mfma_f32_32x32x16_bf16 v[64:79], v[180:183], v[240:243], v[64:79]
s_mov_b32 s59, s95
s_sub_u32 s83, s83, 1
s_mov_b32 s52, s53
s_mov_b32 s53, s54
s_mov_b32 s54, s55
s_mov_b32 s55, s56
v_mfma_f32_32x32x16_bf16 v[80:95], v[180:183], v[244:247], v[80:95]
s_mov_b32 s56, s57
v_mfma_f32_32x32x16_bf16 v[32:47], v[184:187], v[248:251], v[32:47]
ds_read_b64_tr_b16 v[176:177], v8 offset:12288
ds_read_b64_tr_b16 v[178:179], v8 offset:12544
v_mfma_f32_32x32x16_bf16 v[48:63], v[184:187], v[252:255], v[48:63]
ds_read_b64_tr_b16 v[180:181], v9 offset:12288
ds_read_b64_tr_b16 v[182:183], v9 offset:12544
v_mfma_f32_32x32x16_bf16 v[64:79], v[188:191], v[248:251], v[64:79]
v_mfma_f32_32x32x16_bf16 v[80:95], v[188:191], v[252:255], v[80:95]
s_cmp_eq_u32 s83, 0
s_waitcnt lgkmcnt(10)
v_mfma_f32_32x32x16_bf16 v[96:111], v[160:163], a[0:3], 0
ds_read_b64_tr_b16 v[184:185], v8 offset:14336
ds_read_b64_tr_b16 v[186:187], v8 offset:14592
v_mfma_f32_32x32x16_bf16 v[112:127], v[160:163], a[4:7], 0
ds_read_b64_tr_b16 v[188:189], v9 offset:14336
ds_read_b64_tr_b16 v[190:191], v9 offset:14592
s_waitcnt lgkmcnt(12)
v_mfma_f32_32x32x16_bf16 v[128:143], v[164:167], a[0:3], 0
ds_read_b64_tr_b16 v[192:193], v13 offset:8192
ds_read_b64_tr_b16 v[194:195], v13 offset:10240
v_mfma_f32_32x32x16_bf16 v[144:159], v[164:167], a[4:7], 0
ds_read_b64_tr_b16 v[196:197], v13 offset:12288
ds_read_b64_tr_b16 v[198:199], v13 offset:14336
)ASM"
R"ASM(
s_waitcnt lgkmcnt(14)
v_mfma_f32_32x32x16_bf16 v[96:111], v[168:171], a[8:11], v[96:111]
ds_read_b64_tr_b16 v[160:161], v8 offset:16384
ds_read_b64_tr_b16 v[162:163], v8 offset:16640
v_mfma_f32_32x32x16_bf16 v[112:127], v[168:171], a[12:15], v[112:127]
ds_read_b64_tr_b16 v[164:165], v9 offset:16384
ds_read_b64_tr_b16 v[166:167], v9 offset:16640
s_waitcnt lgkmcnt(6)
v_mfma_f32_16x16x32_bf16 v[220:223], v[192:195], v[28:31], 0
ds_read_b128 v[200:203], v16 offset:0
v_mfma_f32_32x32x16_bf16 v[128:143], v[172:175], a[8:11], v[128:143]
s_add_u32 m0, s41, 131072
s_mov_b32 s62, s64
buffer_load_dwordx4 v5, s[84:87], s62 offen lds
ds_read_b128 v[204:207], v16 offset:64
v_mfma_f32_32x32x16_bf16 v[144:159], v[172:175], a[12:15], v[144:159]
v_mfma_f32_32x32x16_bf16 v[96:111], v[176:179], a[16:19], v[96:111]
ds_read_b64_tr_b16 v[168:169], v8 offset:18432
ds_read_b64_tr_b16 v[170:171], v8 offset:18688
v_mfma_f32_32x32x16_bf16 v[112:127], v[176:179], a[20:23], v[112:127]
ds_read_b64_tr_b16 v[172:173], v9 offset:18432
ds_read_b64_tr_b16 v[174:175], v9 offset:18688
s_waitcnt lgkmcnt(5)
v_fmac_f32 v32, v20, v200
s_waitcnt lgkmcnt(4)
v_fmac_f32 v40, v20, v204
v_fmac_f32 v33, v20, v201
v_mfma_f32_16x16x32_bf16 v[220:223], v[196:199], v[28:31], v[220:223]
ds_read_b64_tr_b16 v[192:193], v13 offset:16384
v_mfma_f32_32x32x16_bf16 v[128:143], v[180:183], a[16:19], v[128:143]
ds_read_b64_tr_b16 v[194:195], v13 offset:18432
s_add_u32 m0, s41, 135168
s_add_u32 s62, s62, s93
buffer_load_dwordx4 v5, s[88:91], s62 offen lds
v_fmac_f32 v41, v20, v205
v_mfma_f32_32x32x16_bf16 v[144:159], v[180:183], a[20:23], v[144:159]
v_mul_f32 v40, v32, v40
v_mul_f32 v32, v21, v32
v_mul_f32 v41, v33, v41
v_mul_f32 v33, v21, v33
v_exp_f32 v32, v32
v_mfma_f32_32x32x16_bf16 v[96:111], v[184:187], a[24:27], v[96:111]
ds_read_b64_tr_b16 v[176:177], v8 offset:20480
ds_read_b64_tr_b16 v[178:179], v8 offset:20736
v_exp_f32 v33, v33
v_fma_f32 v32, v32, v22, v22
v_mfma_f32_32x32x16_bf16 v[112:127], v[184:187], a[28:31], v[112:127]
ds_read_b64_tr_b16 v[180:181], v9 offset:20480
ds_read_b64_tr_b16 v[182:183], v9 offset:20736
v_fma_f32 v33, v33, v22, v22
v_rcp_f32 v32, v32
v_mfma_f32_32x32x16_bf16 v[128:143], v[188:191], a[24:27], v[128:143]
ds_read_b64_tr_b16 v[196:197], v13 offset:20480
ds_read_b64_tr_b16 v[198:199], v13 offset:22528
v_rcp_f32 v33, v33
v_mul_f32 v40, v40, v32
v_mfma_f32_32x32x16_bf16 v[144:159], v[188:191], a[28:31], v[144:159]
s_waitcnt lgkmcnt(8)
s_add_u32 m0, s41, 139264
s_add_u32 s62, s62, s92
buffer_load_dwordx4 v5, s[84:87], s62 offen lds
v_mul_f32 v41, v41, v33
v_mfma_f32_32x32x16_bf16 v[96:111], v[160:163], a[32:35], v[96:111]
ds_read_b64_tr_b16 v[184:185], v8 offset:22528
ds_read_b64_tr_b16 v[186:187], v8 offset:22784
v_cvt_pk_bf16_f32 v32, v40, v41
v_mfma_f32_32x32x16_bf16 v[112:127], v[160:163], a[36:39], v[112:127]
ds_read_b64_tr_b16 v[188:189], v9 offset:22528
ds_read_b64_tr_b16 v[190:191], v9 offset:22784
s_waitcnt lgkmcnt(10)
v_mfma_f32_16x16x32_bf16 v[220:223], v[192:195], v[28:31], v[220:223]
ds_read_b128 v[208:211], v16 offset:16
v_mfma_f32_32x32x16_bf16 v[128:143], v[164:167], a[32:35], v[128:143]
s_add_u32 m0, s41, 143360
s_add_u32 s62, s62, s93
buffer_load_dwordx4 v5, s[88:91], s62 offen lds
ds_read_b128 v[212:215], v16 offset:80
v_fmac_f32 v34, v20, v202
v_mfma_f32_32x32x16_bf16 v[144:159], v[164:167], a[36:39], v[144:159]
v_fmac_f32 v42, v20, v206
v_fmac_f32 v35, v20, v203
v_fmac_f32 v43, v20, v207
v_mul_f32 v42, v34, v42
v_mul_f32 v34, v21, v34
v_mul_f32 v43, v35, v43
v_mfma_f32_32x32x16_bf16 v[96:111], v[168:171], a[40:43], v[96:111]
ds_read_b64_tr_b16 v[160:161], v8 offset:24576
ds_read_b64_tr_b16 v[162:163], v8 offset:24832
v_mul_f32 v35, v21, v35
v_exp_f32 v34, v34
v_mfma_f32_32x32x16_bf16 v[112:127], v[168:171], a[44:47], v[112:127]
ds_read_b64_tr_b16 v[164:165], v9 offset:24576
ds_read_b64_tr_b16 v[166:167], v9 offset:24832
v_exp_f32 v35, v35
v_fma_f32 v34, v34, v22, v22
s_waitcnt lgkmcnt(10)
v_mfma_f32_16x16x32_bf16 v[220:223], v[196:199], v[28:31], v[220:223]
ds_read_b64_tr_b16 v[192:193], v13 offset:24576
v_mfma_f32_32x32x16_bf16 v[128:143], v[172:175], a[40:43], v[128:143]
ds_read_b64_tr_b16 v[194:195], v13 offset:26624
s_add_u32 m0, s41, 0
s_add_u32 s62, s62, s92
buffer_load_dwordx4 v5, s[84:87], s62 offen lds
v_fma_f32 v35, v35, v22, v22
v_mfma_f32_32x32x16_bf16 v[144:159], v[172:175], a[44:47], v[144:159]
s_waitcnt lgkmcnt(8)
v_rcp_f32 v34, v34
v_rcp_f32 v35, v35
v_mul_f32 v42, v42, v34
v_mfma_f32_32x32x16_bf16 v[96:111], v[176:179], a[48:51], v[96:111]
ds_read_b64_tr_b16 v[168:169], v8 offset:26624
ds_read_b64_tr_b16 v[170:171], v8 offset:26880
v_mul_f32 v43, v43, v35
v_cvt_pk_bf16_f32 v33, v42, v43
v_fmac_f32 v48, v24, v200
v_mfma_f32_32x32x16_bf16 v[112:127], v[176:179], a[52:55], v[112:127]
ds_read_b64_tr_b16 v[172:173], v9 offset:26624
ds_read_b64_tr_b16 v[174:175], v9 offset:26880
v_fmac_f32 v56, v24, v204
v_fmac_f32 v49, v24, v201
v_fmac_f32 v57, v24, v205
v_mfma_f32_32x32x16_bf16 v[128:143], v[180:183], a[48:51], v[128:143]
s_add_u32 m0, s41, 4096
s_add_u32 s62, s62, s93
buffer_load_dwordx4 v5, s[88:91], s62 offen lds
v_mul_f32 v56, v48, v56
v_mul_f32 v48, v25, v48
v_mfma_f32_32x32x16_bf16 v[144:159], v[180:183], a[52:55], v[144:159]
v_mul_f32 v57, v49, v57
v_mul_f32 v49, v25, v49
v_exp_f32 v48, v48
v_exp_f32 v49, v49
v_mfma_f32_32x32x16_bf16 v[96:111], v[184:187], a[56:59], v[96:111]
ds_read_b64_tr_b16 v[176:177], v8 offset:28672
ds_read_b64_tr_b16 v[178:179], v8 offset:28928
v_fma_f32 v48, v48, v26, v26
v_fma_f32 v49, v49, v26, v26
v_mfma_f32_32x32x16_bf16 v[112:127], v[184:187], a[60:63], v[112:127]
ds_read_b64_tr_b16 v[180:181], v9 offset:28672
ds_read_b64_tr_b16 v[182:183], v9 offset:28928
v_rcp_f32 v48, v48
s_waitcnt lgkmcnt(8)
v_mfma_f32_16x16x32_bf16 v[220:223], v[192:195], v[28:31], v[220:223]
ds_read_b64_tr_b16 v[196:197], v13 offset:28672
v_mfma_f32_32x32x16_bf16 v[128:143], v[188:191], a[56:59], v[128:143]
ds_read_b64_tr_b16 v[198:199], v13 offset:30720
v_rcp_f32 v49, v49
v_mul_f32 v56, v56, v48
v_mul_f32 v57, v57, v49
v_mfma_f32_32x32x16_bf16 v[144:159], v[188:191], a[60:63], v[144:159]
s_waitcnt lgkmcnt(6)
v_cvt_pk_bf16_f32 v48, v56, v57
v_fmac_f32 v50, v24, v202
v_fmac_f32 v58, v24, v206
v_fmac_f32 v51, v24, v203
v_fmac_f32 v59, v24, v207
v_mfma_f32_32x32x16_bf16 v[96:111], v[160:163], a[64:67], v[96:111]
ds_read_b64_tr_b16 v[184:185], v8 offset:30720
ds_read_b64_tr_b16 v[186:187], v8 offset:30976
v_mul_f32 v58, v50, v58
v_mul_f32 v50, v25, v50
v_mul_f32 v59, v51, v59
v_mfma_f32_32x32x16_bf16 v[112:127], v[160:163], a[68:71], v[112:127]
ds_read_b64_tr_b16 v[188:189], v9 offset:30720
ds_read_b64_tr_b16 v[190:191], v9 offset:30976
v_mul_f32 v51, v25, v51
v_exp_f32 v50, v50
v_mfma_f32_32x32x16_bf16 v[128:143], v[164:167], a[64:67], v[128:143]
ds_read_b64_tr_b16 v[192:193], v13 offset:32768
ds_read_b64_tr_b16 v[194:195], v13 offset:34816
v_exp_f32 v51, v51
v_fma_f32 v50, v50, v26, v26
v_mfma_f32_32x32x16_bf16 v[144:159], v[164:167], a[68:71], v[144:159]
s_add_u32 m0, s41, 8192
s_add_u32 s62, s62, s92
buffer_load_dwordx4 v5, s[84:87], s62 offen lds
v_fma_f32 v51, v51, v26, v26
v_mfma_f32_32x32x16_bf16 v[96:111], v[168:171], a[72:75], v[96:111]
ds_read_b64_tr_b16 v[160:161], v8 offset:32768
ds_read_b64_tr_b16 v[162:163], v8 offset:33024
v_rcp_f32 v50, v50
v_mfma_f32_32x32x16_bf16 v[112:127], v[168:171], a[76:79], v[112:127]
ds_read_b64_tr_b16 v[164:165], v9 offset:32768
ds_read_b64_tr_b16 v[166:167], v9 offset:33024
v_rcp_f32 v51, v51
v_mul_f32 v58, v58, v50
s_waitcnt lgkmcnt(10)
v_mfma_f32_16x16x32_bf16 v[220:223], v[196:199], v[28:31], v[220:223]
v_mul_f32 v59, v59, v51
v_cvt_pk_bf16_f32 v49, v58, v59
v_mfma_f32_32x32x16_bf16 v[128:143], v[172:175], a[72:75], v[128:143]
s_add_u32 m0, s41, 12288
s_add_u32 s62, s62, s93
buffer_load_dwordx4 v5, s[88:91], s62 offen lds
v_fmac_f32 v36, v20, v208
v_fmac_f32 v44, v20, v212
v_mfma_f32_32x32x16_bf16 v[144:159], v[172:175], a[76:79], v[144:159]
s_waitcnt lgkmcnt(6)
v_fmac_f32 v37, v20, v209
v_fmac_f32 v45, v20, v213
)ASM"
R"ASM(
v_mul_f32 v44, v36, v44
v_mul_f32 v36, v21, v36
v_mul_f32 v45, v37, v45
v_mfma_f32_32x32x16_bf16 v[96:111], v[176:179], a[80:83], v[96:111]
ds_read_b64_tr_b16 v[168:169], v8 offset:34816
ds_read_b64_tr_b16 v[170:171], v8 offset:35072
v_mul_f32 v37, v21, v37
v_exp_f32 v36, v36
v_mfma_f32_32x32x16_bf16 v[112:127], v[176:179], a[84:87], v[112:127]
ds_read_b64_tr_b16 v[172:173], v9 offset:34816
ds_read_b64_tr_b16 v[174:175], v9 offset:35072
v_exp_f32 v37, v37
v_fma_f32 v36, v36, v22, v22
s_waitcnt lgkmcnt(8)
v_mfma_f32_16x16x32_bf16 v[220:223], v[192:195], v[28:31], v[220:223]
ds_read_b64_tr_b16 v[196:197], v13 offset:36864
v_mfma_f32_32x32x16_bf16 v[128:143], v[180:183], a[80:83], v[128:143]
ds_read_b64_tr_b16 v[198:199], v13 offset:38912
s_add_u32 m0, s41, 16384
s_add_u32 s62, s62, s92
buffer_load_dwordx4 v5, s[84:87], s62 offen lds
v_fma_f32 v37, v37, v22, v22
v_mfma_f32_32x32x16_bf16 v[144:159], v[180:183], a[84:87], v[144:159]
v_rcp_f32 v36, v36
v_rcp_f32 v37, v37
v_mul_f32 v44, v44, v36
v_mul_f32 v45, v45, v37
v_mfma_f32_32x32x16_bf16 v[96:111], v[184:187], a[88:91], v[96:111]
ds_read_b64_tr_b16 v[176:177], v8 offset:36864
ds_read_b64_tr_b16 v[178:179], v8 offset:37120
v_cvt_pk_bf16_f32 v34, v44, v45
v_mfma_f32_32x32x16_bf16 v[112:127], v[184:187], a[92:95], v[112:127]
ds_read_b64_tr_b16 v[180:181], v9 offset:36864
ds_read_b64_tr_b16 v[182:183], v9 offset:37120
v_mfma_f32_32x32x16_bf16 v[128:143], v[188:191], a[88:91], v[128:143]
ds_read_b128 v[200:203], v16 offset:128
ds_read_b128 v[204:207], v16 offset:192
v_fmac_f32 v38, v20, v210
v_fmac_f32 v46, v20, v214
v_fmac_f32 v39, v20, v211
v_mfma_f32_32x32x16_bf16 v[144:159], v[188:191], a[92:95], v[144:159]
s_waitcnt lgkmcnt(8)
v_fmac_f32 v47, v20, v215
v_mul_f32 v46, v38, v46
v_mul_f32 v38, v21, v38
v_mul_f32 v47, v39, v47
v_mul_f32 v39, v21, v39
v_mfma_f32_32x32x16_bf16 v[96:111], v[160:163], a[96:99], v[96:111]
ds_read_b64_tr_b16 v[184:185], v8 offset:38912
ds_read_b64_tr_b16 v[186:187], v8 offset:39168
v_exp_f32 v38, v38
v_mfma_f32_32x32x16_bf16 v[112:127], v[160:163], a[100:103], v[112:127]
ds_read_b64_tr_b16 v[188:189], v9 offset:38912
ds_read_b64_tr_b16 v[190:191], v9 offset:39168
v_exp_f32 v39, v39
v_fma_f32 v38, v38, v22, v22
s_waitcnt lgkmcnt(10)
v_mfma_f32_16x16x32_bf16 v[220:223], v[196:199], v[28:31], v[220:223]
ds_read_b64_tr_b16 v[192:193], v13 offset:40960
v_mfma_f32_32x32x16_bf16 v[128:143], v[164:167], a[96:99], v[128:143]
ds_read_b64_tr_b16 v[194:195], v13 offset:43008
v_fma_f32 v39, v39, v22, v22
v_rcp_f32 v38, v38
v_mfma_f32_32x32x16_bf16 v[144:159], v[164:167], a[100:103], v[144:159]
v_rcp_f32 v39, v39
v_mul_f32 v46, v46, v38
v_mul_f32 v47, v47, v39
v_cvt_pk_bf16_f32 v35, v46, v47
v_mfma_f32_32x32x16_bf16 v[96:111], v[168:171], a[104:107], v[96:111]
ds_read_b64_tr_b16 v[160:161], v8 offset:40960
ds_read_b64_tr_b16 v[162:163], v8 offset:41216
buffer_store_dwordx4 v[32:35], v19, s[96:99], s69 offen offset:0 sc1
v_fmac_f32 v52, v24, v208
v_mfma_f32_32x32x16_bf16 v[112:127], v[168:171], a[108:111], v[112:127]
ds_read_b64_tr_b16 v[164:165], v9 offset:40960
ds_read_b64_tr_b16 v[166:167], v9 offset:41216
v_fmac_f32 v60, v24, v212
v_fmac_f32 v53, v24, v209
v_fmac_f32 v61, v24, v213
v_mfma_f32_32x32x16_bf16 v[128:143], v[172:175], a[104:107], v[128:143]
v_mul_f32 v60, v52, v60
v_mul_f32 v52, v25, v52
v_mul_f32 v61, v53, v61
v_mul_f32 v53, v25, v53
v_exp_f32 v52, v52
v_mfma_f32_32x32x16_bf16 v[144:159], v[172:175], a[108:111], v[144:159]
s_waitcnt lgkmcnt(6)
v_exp_f32 v53, v53
v_fma_f32 v52, v52, v26, v26
v_fma_f32 v53, v53, v26, v26
v_mfma_f32_32x32x16_bf16 v[96:111], v[176:179], a[112:115], v[96:111]
ds_read_b64_tr_b16 v[168:169], v8 offset:43008
ds_read_b64_tr_b16 v[170:171], v8 offset:43264
v_rcp_f32 v52, v52
v_mfma_f32_32x32x16_bf16 v[112:127], v[176:179], a[116:119], v[112:127]
ds_read_b64_tr_b16 v[172:173], v9 offset:43008
ds_read_b64_tr_b16 v[174:175], v9 offset:43264
v_rcp_f32 v53, v53
v_mul_f32 v60, v60, v52
s_waitcnt lgkmcnt(8)
v_mfma_f32_16x16x32_bf16 v[220:223], v[192:195], v[28:31], v[220:223]
v_mul_f32 v61, v61, v53
v_cvt_pk_bf16_f32 v50, v60, v61
v_mfma_f32_32x32x16_bf16 v[128:143], v[180:183], a[112:115], v[128:143]
v_fmac_f32 v54, v24, v210
v_fmac_f32 v62, v24, v214
v_fmac_f32 v55, v24, v211
v_fmac_f32 v63, v24, v215
v_mul_f32 v62, v54, v62
v_mul_f32 v54, v25, v54
v_mfma_f32_32x32x16_bf16 v[144:159], v[180:183], a[116:119], v[144:159]
v_mul_f32 v63, v55, v63
v_mul_f32 v55, v25, v55
v_exp_f32 v54, v54
v_exp_f32 v55, v55
v_mfma_f32_32x32x16_bf16 v[96:111], v[184:187], a[120:123], v[96:111]
s_waitcnt vmcnt(10) lgkmcnt(0)
s_barrier
s_mov_b64 exec, 1
buffer_store_dword v219, off, s[96:99], s52 sc1
s_mov_b64 exec, -1
v_mfma_f32_32x32x16_bf16 v[112:127], v[184:187], a[124:127], v[112:127]
ds_read_b64_tr_b16 v[176:177], v8 offset:45056
ds_read_b64_tr_b16 v[178:179], v8 offset:45312
v_fma_f32 v54, v54, v26, v26
v_fma_f32 v55, v55, v26, v26
v_mfma_f32_32x32x16_bf16 v[128:143], v[188:191], a[120:123], v[128:143]
ds_read_b64_tr_b16 v[180:181], v9 offset:45056
ds_read_b64_tr_b16 v[182:183], v9 offset:45312
v_rcp_f32 v54, v54
v_mfma_f32_32x32x16_bf16 v[144:159], v[188:191], a[124:127], v[144:159]
ds_read_b64_tr_b16 v[196:197], v13 offset:45056
ds_read_b64_tr_b16 v[198:199], v13 offset:47104
v_rcp_f32 v55, v55
v_mfma_f32_32x32x16_bf16 v[96:111], v[160:163], a[128:131], v[96:111]
ds_read_b64_tr_b16 v[184:185], v8 offset:47104
ds_read_b64_tr_b16 v[186:187], v8 offset:47360
v_mul_f32 v62, v62, v54
v_mul_f32 v63, v63, v55
v_cvt_pk_bf16_f32 v51, v62, v63
v_mfma_f32_32x32x16_bf16 v[112:127], v[160:163], a[132:135], v[112:127]
ds_read_b64_tr_b16 v[188:189], v9 offset:47104
ds_read_b64_tr_b16 v[190:191], v9 offset:47360
buffer_store_dwordx4 v[48:51], v19, s[96:99], s69 offen offset:1024 sc1
v_fmac_f32 v64, v20, v200
v_mfma_f32_32x32x16_bf16 v[128:143], v[164:167], a[128:131], v[128:143]
ds_read_b64_tr_b16 v[192:193], v14 offset:0
ds_read_b64_tr_b16 v[194:195], v14 offset:2048
v_fmac_f32 v72, v20, v204
v_fmac_f32 v65, v20, v201
v_fmac_f32 v73, v20, v205
v_mfma_f32_32x32x16_bf16 v[144:159], v[164:167], a[132:135], v[144:159]
s_add_u32 m0, s41, 20480
s_add_u32 s62, s62, s93
buffer_load_dwordx4 v5, s[88:91], s62 offen lds
v_mul_f32 v72, v64, v72
v_mul_f32 v64, v21, v64
v_mfma_f32_32x32x16_bf16 v[96:111], v[168:171], a[136:139], v[96:111]
ds_read_b64_tr_b16 v[160:161], v10 offset:0
ds_read_b64_tr_b16 v[162:163], v10 offset:256
v_mul_f32 v73, v65, v73
v_mul_f32 v65, v21, v65
v_mfma_f32_32x32x16_bf16 v[112:127], v[168:171], a[140:143], v[112:127]
ds_read_b64_tr_b16 v[164:165], v11 offset:0
ds_read_b64_tr_b16 v[166:167], v11 offset:256
v_exp_f32 v64, v64
s_waitcnt lgkmcnt(10)
v_mfma_f32_16x16x32_bf16 v[220:223], v[196:199], v[28:31], v[220:223]
v_exp_f32 v65, v65
v_mfma_f32_32x32x16_bf16 v[128:143], v[172:175], a[136:139], v[128:143]
s_add_u32 m0, s41, 24576
s_add_u32 s62, s62, s92
buffer_load_dwordx4 v5, s[84:87], s62 offen lds
v_fma_f32 v64, v64, v22, v22
v_fma_f32 v65, v65, v22, v22
v_mfma_f32_32x32x16_bf16 v[144:159], v[172:175], a[140:143], v[144:159]
s_waitcnt lgkmcnt(6)
v_rcp_f32 v64, v64
v_rcp_f32 v65, v65
v_mul_f32 v72, v72, v64
v_mfma_f32_32x32x16_bf16 v[96:111], v[176:179], a[144:147], v[96:111]
ds_read_b64_tr_b16 v[168:169], v10 offset:2048
ds_read_b64_tr_b16 v[170:171], v10 offset:2304
v_mul_f32 v73, v73, v65
v_cvt_pk_bf16_f32 v64, v72, v73
v_mfma_f32_32x32x16_bf16 v[112:127], v[176:179], a[148:151], v[112:127]
ds_read_b64_tr_b16 v[172:173], v11 offset:2048
ds_read_b64_tr_b16 v[174:175], v11 offset:2304
s_waitcnt lgkmcnt(8)
v_mfma_f32_16x16x32_bf16 v[220:223], v[192:195], v[28:31], v[220:223]
ds_read_b64_tr_b16 v[196:197], v14 offset:4096
v_mfma_f32_32x32x16_bf16 v[128:143], v[180:183], a[144:147], v[128:143]
ds_read_b64_tr_b16 v[198:199], v14 offset:6144
s_add_u32 m0, s41, 28672
s_add_u32 s62, s62, s93
buffer_load_dwordx4 v5, s[88:91], s62 offen lds
v_mfma_f32_32x32x16_bf16 v[144:159], v[180:183], a[148:151], v[144:159]
ds_read_b128 v[208:211], v16 offset:144
ds_read_b128 v[212:215], v16 offset:208
v_fmac_f32 v66, v20, v202
)ASM"
R"ASM(
v_fmac_f32 v74, v20, v206
v_fmac_f32 v67, v20, v203
v_mfma_f32_32x32x16_bf16 v[96:111], v[184:187], a[152:155], v[96:111]
ds_read_b64_tr_b16 v[176:177], v10 offset:4096
ds_read_b64_tr_b16 v[178:179], v10 offset:4352
v_fmac_f32 v75, v20, v207
v_mul_f32 v74, v66, v74
v_mul_f32 v66, v21, v66
v_mfma_f32_32x32x16_bf16 v[112:127], v[184:187], a[156:159], v[112:127]
ds_read_b64_tr_b16 v[180:181], v11 offset:4096
ds_read_b64_tr_b16 v[182:183], v11 offset:4352
v_mul_f32 v75, v67, v75
v_mul_f32 v67, v21, v67
v_mfma_f32_32x32x16_bf16 v[128:143], v[188:191], a[152:155], v[128:143]
v_exp_f32 v66, v66
v_exp_f32 v67, v67
v_fma_f32 v66, v66, v22, v22
v_fma_f32 v67, v67, v22, v22
v_mfma_f32_32x32x16_bf16 v[144:159], v[188:191], a[156:159], v[144:159]
s_waitcnt lgkmcnt(8)
v_rcp_f32 v66, v66
v_rcp_f32 v67, v67
v_mul_f32 v74, v74, v66
v_mfma_f32_32x32x16_bf16 v[96:111], v[160:163], a[160:163], v[96:111]
ds_read_b64_tr_b16 v[184:185], v10 offset:6144
ds_read_b64_tr_b16 v[186:187], v10 offset:6400
v_mul_f32 v75, v75, v67
v_cvt_pk_bf16_f32 v65, v74, v75
v_fmac_f32 v80, v24, v200
v_mfma_f32_32x32x16_bf16 v[112:127], v[160:163], a[164:167], v[112:127]
ds_read_b64_tr_b16 v[188:189], v11 offset:6144
ds_read_b64_tr_b16 v[190:191], v11 offset:6400
v_fmac_f32 v88, v24, v204
v_fmac_f32 v81, v24, v201
v_fmac_f32 v89, v24, v205
s_waitcnt lgkmcnt(10)
v_mfma_f32_16x16x32_bf16 v[220:223], v[196:199], v[28:31], v[220:223]
ds_read_b64_tr_b16 v[192:193], v14 offset:8192
v_mfma_f32_32x32x16_bf16 v[128:143], v[164:167], a[160:163], v[128:143]
ds_read_b64_tr_b16 v[194:195], v14 offset:10240
s_add_u32 m0, s41, 32768
s_add_u32 s62, s62, s92
buffer_load_dwordx4 v5, s[84:87], s62 offen lds
v_mul_f32 v88, v80, v88
v_mfma_f32_32x32x16_bf16 v[144:159], v[164:167], a[164:167], v[144:159]
v_mul_f32 v80, v25, v80
v_mul_f32 v89, v81, v89
v_mul_f32 v81, v25, v81
v_exp_f32 v80, v80
v_mfma_f32_32x32x16_bf16 v[96:111], v[168:171], a[168:171], v[96:111]
ds_read_b64_tr_b16 v[160:161], v10 offset:8192
ds_read_b64_tr_b16 v[162:163], v10 offset:8448
v_exp_f32 v81, v81
v_fma_f32 v80, v80, v26, v26
v_mfma_f32_32x32x16_bf16 v[112:127], v[168:171], a[172:175], v[112:127]
ds_read_b64_tr_b16 v[164:165], v11 offset:8192
ds_read_b64_tr_b16 v[166:167], v11 offset:8448
v_fma_f32 v81, v81, v26, v26
v_rcp_f32 v80, v80
v_mfma_f32_32x32x16_bf16 v[128:143], v[172:175], a[168:171], v[128:143]
s_add_u32 m0, s41, 36864
s_add_u32 s62, s62, s93
buffer_load_dwordx4 v5, s[88:91], s62 offen lds
v_rcp_f32 v81, v81
v_mfma_f32_32x32x16_bf16 v[144:159], v[172:175], a[172:175], v[144:159]
s_waitcnt lgkmcnt(6)
v_mul_f32 v88, v88, v80
v_mul_f32 v89, v89, v81
v_cvt_pk_bf16_f32 v80, v88, v89
v_fmac_f32 v82, v24, v202
v_fmac_f32 v90, v24, v206
v_mfma_f32_32x32x16_bf16 v[96:111], v[176:179], a[176:179], v[96:111]
ds_read_b64_tr_b16 v[168:169], v10 offset:10240
ds_read_b64_tr_b16 v[170:171], v10 offset:10496
v_fmac_f32 v83, v24, v203
v_fmac_f32 v91, v24, v207
v_mul_f32 v90, v82, v90
v_mfma_f32_32x32x16_bf16 v[112:127], v[176:179], a[180:183], v[112:127]
ds_read_b64_tr_b16 v[172:173], v11 offset:10240
ds_read_b64_tr_b16 v[174:175], v11 offset:10496
v_mul_f32 v82, v25, v82
v_mul_f32 v91, v83, v91
v_mul_f32 v83, v25, v83
s_waitcnt lgkmcnt(8)
v_mfma_f32_16x16x32_bf16 v[220:223], v[192:195], v[28:31], v[220:223]
ds_read_b64_tr_b16 v[196:197], v14 offset:12288
v_mfma_f32_32x32x16_bf16 v[128:143], v[180:183], a[176:179], v[128:143]
ds_read_b64_tr_b16 v[198:199], v14 offset:14336
v_exp_f32 v82, v82
v_exp_f32 v83, v83
v_mfma_f32_32x32x16_bf16 v[144:159], v[180:183], a[180:183], v[144:159]
v_fma_f32 v82, v82, v26, v26
v_fma_f32 v83, v83, v26, v26
v_rcp_f32 v82, v82
v_rcp_f32 v83, v83
v_mfma_f32_32x32x16_bf16 v[96:111], v[184:187], a[184:187], v[96:111]
ds_read_b64_tr_b16 v[176:177], v10 offset:12288
ds_read_b64_tr_b16 v[178:179], v10 offset:12544
v_mul_f32 v90, v90, v82
v_mul_f32 v91, v91, v83
v_cvt_pk_bf16_f32 v81, v90, v91
v_mfma_f32_32x32x16_bf16 v[112:127], v[184:187], a[188:191], v[112:127]
ds_read_b64_tr_b16 v[180:181], v11 offset:12288
ds_read_b64_tr_b16 v[182:183], v11 offset:12544
v_mfma_f32_32x32x16_bf16 v[128:143], v[188:191], a[184:187], v[128:143]
ds_read_b64_tr_b16 v[192:193], v14 offset:16384
ds_read_b64_tr_b16 v[194:195], v14 offset:18432
v_fmac_f32 v68, v20, v208
v_fmac_f32 v76, v20, v212
v_fmac_f32 v69, v20, v209
v_mfma_f32_32x32x16_bf16 v[144:159], v[188:191], a[188:191], v[144:159]
s_waitcnt lgkmcnt(8)
s_add_u32 m0, s41, 40960
s_add_u32 s62, s62, s92
buffer_load_dwordx4 v5, s[84:87], s62 offen lds
v_fmac_f32 v77, v20, v213
v_mfma_f32_32x32x16_bf16 v[96:111], v[160:163], a[192:195], v[96:111]
ds_read_b64_tr_b16 v[184:185], v10 offset:14336
ds_read_b64_tr_b16 v[186:187], v10 offset:14592
v_mul_f32 v76, v68, v76
v_mul_f32 v68, v21, v68
v_mul_f32 v77, v69, v77
v_mfma_f32_32x32x16_bf16 v[112:127], v[160:163], a[196:199], v[112:127]
ds_read_b64_tr_b16 v[188:189], v11 offset:14336
ds_read_b64_tr_b16 v[190:191], v11 offset:14592
v_mul_f32 v69, v21, v69
v_exp_f32 v68, v68
s_waitcnt lgkmcnt(10)
v_mfma_f32_16x16x32_bf16 v[220:223], v[196:199], v[28:31], v[220:223]
v_exp_f32 v69, v69
v_mfma_f32_32x32x16_bf16 v[128:143], v[164:167], a[192:195], v[128:143]
v_fma_f32 v68, v68, v22, v22
v_fma_f32 v69, v69, v22, v22
v_rcp_f32 v68, v68
v_rcp_f32 v69, v69
v_mfma_f32_32x32x16_bf16 v[144:159], v[164:167], a[196:199], v[144:159]
s_add_u32 m0, s41, 45056
s_add_u32 s62, s62, s93
buffer_load_dwordx4 v5, s[88:91], s62 offen lds
v_mul_f32 v76, v76, v68
v_mul_f32 v77, v77, v69
v_mfma_f32_32x32x16_bf16 v[96:111], v[168:171], a[200:203], v[96:111]
ds_read_b64_tr_b16 v[160:161], v10 offset:16384
ds_read_b64_tr_b16 v[162:163], v10 offset:16640
v_cvt_pk_bf16_f32 v66, v76, v77
v_mfma_f32_32x32x16_bf16 v[112:127], v[168:171], a[204:207], v[112:127]
ds_read_b64_tr_b16 v[164:165], v11 offset:16384
ds_read_b64_tr_b16 v[166:167], v11 offset:16640
v_fmac_f32 v70, v20, v210
v_fmac_f32 v78, v20, v214
v_fmac_f32 v71, v20, v211
s_waitcnt lgkmcnt(8)
v_mfma_f32_16x16x32_bf16 v[220:223], v[192:195], v[28:31], v[220:223]
ds_read_b64_tr_b16 v[196:197], v14 offset:20480
v_mfma_f32_32x32x16_bf16 v[128:143], v[172:175], a[200:203], v[128:143]
ds_read_b64_tr_b16 v[198:199], v14 offset:22528
v_fmac_f32 v79, v20, v215
v_mul_f32 v78, v70, v78
v_mul_f32 v70, v21, v70
v_mul_f32 v79, v71, v79
v_mfma_f32_32x32x16_bf16 v[144:159], v[172:175], a[204:207], v[144:159]
s_waitcnt lgkmcnt(6)
v_mul_f32 v71, v21, v71
v_exp_f32 v70, v70
v_exp_f32 v71, v71
v_mfma_f32_32x32x16_bf16 v[96:111], v[176:179], a[208:211], v[96:111]
ds_read_b64_tr_b16 v[168:169], v10 offset:18432
ds_read_b64_tr_b16 v[170:171], v10 offset:18688
v_fma_f32 v70, v70, v22, v22
v_fma_f32 v71, v71, v22, v22
v_mfma_f32_32x32x16_bf16 v[112:127], v[176:179], a[212:215], v[112:127]
ds_read_b64_tr_b16 v[172:173], v11 offset:18432
ds_read_b64_tr_b16 v[174:175], v11 offset:18688
v_rcp_f32 v70, v70
v_mfma_f32_32x32x16_bf16 v[128:143], v[180:183], a[208:211], v[128:143]
s_add_u32 m0, s41, 49152
s_add_u32 s62, s62, s92
buffer_load_dwordx4 v5, s[84:87], s62 offen lds
v_rcp_f32 v71, v71
v_mfma_f32_32x32x16_bf16 v[144:159], v[180:183], a[212:215], v[144:159]
v_mul_f32 v78, v78, v70
v_mul_f32 v79, v79, v71
v_cvt_pk_bf16_f32 v67, v78, v79
s_add_u32 s75, s69, 65536
buffer_store_dwordx4 v[64:67], v19, s[96:99], s75 offen offset:0 sc1
v_mfma_f32_32x32x16_bf16 v[96:111], v[184:187], a[216:219], v[96:111]
ds_read_b64_tr_b16 v[176:177], v10 offset:20480
ds_read_b64_tr_b16 v[178:179], v10 offset:20736
v_mfma_f32_32x32x16_bf16 v[112:127], v[184:187], a[220:223], v[112:127]
ds_read_b64_tr_b16 v[180:181], v11 offset:20480
ds_read_b64_tr_b16 v[182:183], v11 offset:20736
v_fmac_f32 v84, v24, v208
v_fmac_f32 v92, v24, v212
v_fmac_f32 v85, v24, v209
s_waitcnt lgkmcnt(8)
v_mfma_f32_16x16x32_bf16 v[220:223], v[196:199], v[28:31], v[220:223]
ds_read_b64_tr_b16 v[192:193], v14 offset:24576
v_mfma_f32_32x32x16_bf16 v[128:143], v[188:191], a[216:219], v[128:143]
ds_read_b64_tr_b16 v[194:195], v14 offset:26624
v_fmac_f32 v93, v24, v213
)ASM"
R"ASM(
v_mul_f32 v92, v84, v92
v_mul_f32 v84, v25, v84
v_mul_f32 v93, v85, v93
v_mfma_f32_32x32x16_bf16 v[144:159], v[188:191], a[220:223], v[144:159]
s_waitcnt lgkmcnt(6)
s_add_u32 m0, s41, 53248
s_add_u32 s62, s62, s93
buffer_load_dwordx4 v5, s[88:91], s62 offen lds
v_mul_f32 v85, v25, v85
v_mfma_f32_32x32x16_bf16 v[96:111], v[160:163], a[224:227], v[96:111]
ds_read_b64_tr_b16 v[184:185], v10 offset:22528
ds_read_b64_tr_b16 v[186:187], v10 offset:22784
v_exp_f32 v84, v84
v_mfma_f32_32x32x16_bf16 v[112:127], v[160:163], a[228:231], v[112:127]
ds_read_b64_tr_b16 v[188:189], v11 offset:22528
ds_read_b64_tr_b16 v[190:191], v11 offset:22784
v_exp_f32 v85, v85
v_fma_f32 v84, v84, v26, v26
v_mfma_f32_32x32x16_bf16 v[128:143], v[164:167], a[224:227], v[128:143]
v_fma_f32 v85, v85, v26, v26
v_rcp_f32 v84, v84
v_rcp_f32 v85, v85
v_mul_f32 v92, v92, v84
v_mfma_f32_32x32x16_bf16 v[144:159], v[164:167], a[228:231], v[144:159]
v_mul_f32 v93, v93, v85
v_cvt_pk_bf16_f32 v82, v92, v93
v_mfma_f32_32x32x16_bf16 v[96:111], v[168:171], a[232:235], v[96:111]
ds_read_b64_tr_b16 v[160:161], v10 offset:24576
ds_read_b64_tr_b16 v[162:163], v10 offset:24832
v_fmac_f32 v86, v24, v210
v_fmac_f32 v94, v24, v214
v_fmac_f32 v87, v24, v211
v_mfma_f32_32x32x16_bf16 v[112:127], v[168:171], a[236:239], v[112:127]
ds_read_b64_tr_b16 v[164:165], v11 offset:24576
ds_read_b64_tr_b16 v[166:167], v11 offset:24832
v_fmac_f32 v95, v24, v215
v_mul_f32 v94, v86, v94
v_mul_f32 v86, v25, v86
s_waitcnt lgkmcnt(8)
v_mfma_f32_16x16x32_bf16 v[220:223], v[192:195], v[28:31], v[220:223]
ds_read_b64_tr_b16 v[196:197], v14 offset:28672
v_mfma_f32_32x32x16_bf16 v[128:143], v[172:175], a[232:235], v[128:143]
ds_read_b64_tr_b16 v[198:199], v14 offset:30720
v_mul_f32 v95, v87, v95
v_mul_f32 v87, v25, v87
v_exp_f32 v86, v86
v_mfma_f32_32x32x16_bf16 v[144:159], v[172:175], a[236:239], v[144:159]
s_waitcnt lgkmcnt(6)
v_exp_f32 v87, v87
v_fma_f32 v86, v86, v26, v26
v_fma_f32 v87, v87, v26, v26
v_mfma_f32_32x32x16_bf16 v[96:111], v[176:179], a[240:243], v[96:111]
ds_read_b64_tr_b16 v[168:169], v10 offset:26624
ds_read_b64_tr_b16 v[170:171], v10 offset:26880
v_rcp_f32 v86, v86
v_mfma_f32_32x32x16_bf16 v[112:127], v[176:179], a[244:247], v[112:127]
ds_read_b64_tr_b16 v[172:173], v11 offset:26624
ds_read_b64_tr_b16 v[174:175], v11 offset:26880
v_rcp_f32 v87, v87
v_mul_f32 v94, v94, v86
v_mfma_f32_32x32x16_bf16 v[128:143], v[180:183], a[240:243], v[128:143]
v_mul_f32 v95, v95, v87
v_cvt_pk_bf16_f32 v83, v94, v95
s_add_u32 s75, s69, 65536
buffer_store_dwordx4 v[80:83], v19, s[96:99], s75 offen offset:1024 sc1
v_mfma_f32_32x32x16_bf16 v[144:159], v[180:183], a[244:247], v[144:159]
v_mfma_f32_32x32x16_bf16 v[96:111], v[184:187], a[248:251], v[96:111]
ds_read_b64_tr_b16 v[176:177], v10 offset:28672
ds_read_b64_tr_b16 v[178:179], v10 offset:28928
v_mfma_f32_32x32x16_bf16 v[112:127], v[184:187], a[252:255], v[112:127]
ds_read_b64_tr_b16 v[180:181], v11 offset:28672
ds_read_b64_tr_b16 v[182:183], v11 offset:28928
s_waitcnt lgkmcnt(8)
v_mfma_f32_16x16x32_bf16 v[220:223], v[196:199], v[28:31], v[220:223]
v_mfma_f32_32x32x16_bf16 v[128:143], v[188:191], a[248:251], v[128:143]
v_mfma_f32_32x32x16_bf16 v[144:159], v[188:191], a[252:255], v[144:159]
s_waitcnt lgkmcnt(4)
v_mfma_f32_32x32x16_bf16 v[96:111], v[160:163], v[224:227], v[96:111]
ds_read_b64_tr_b16 v[184:185], v10 offset:30720
ds_read_b64_tr_b16 v[186:187], v10 offset:30976
v_mfma_f32_32x32x16_bf16 v[112:127], v[160:163], v[228:231], v[112:127]
ds_read_b64_tr_b16 v[188:189], v11 offset:30720
ds_read_b64_tr_b16 v[190:191], v11 offset:30976
v_mfma_f32_32x32x16_bf16 v[128:143], v[164:167], v[224:227], v[128:143]
ds_write_b128 v18, v[220:223] offset:256
v_mfma_f32_32x32x16_bf16 v[144:159], v[164:167], v[228:231], v[144:159]
v_mfma_f32_32x32x16_bf16 v[96:111], v[168:171], v[232:235], v[96:111]
s_waitcnt vmcnt(13) lgkmcnt(0)
s_barrier
v_mfma_f32_32x32x16_bf16 v[112:127], v[168:171], v[236:239], v[112:127]
ds_read_b64_tr_b16 v[160:161], v10 offset:32768
ds_read_b64_tr_b16 v[162:163], v10 offset:33024
v_mfma_f32_32x32x16_bf16 v[128:143], v[172:175], v[232:235], v[128:143]
ds_read_b64_tr_b16 v[164:165], v11 offset:32768
ds_read_b64_tr_b16 v[166:167], v11 offset:33024
v_mfma_f32_32x32x16_bf16 v[144:159], v[172:175], v[236:239], v[144:159]
v_mfma_f32_32x32x16_bf16 v[96:111], v[176:179], v[240:243], v[96:111]
ds_read_b64_tr_b16 v[168:169], v10 offset:34816
ds_read_b64_tr_b16 v[170:171], v10 offset:35072
s_mov_b32 s64, s65
s_mov_b32 s65, s66
s_mov_b32 s66, s67
v_mfma_f32_32x32x16_bf16 v[112:127], v[176:179], v[244:247], v[112:127]
ds_read_b64_tr_b16 v[172:173], v11 offset:34816
ds_read_b64_tr_b16 v[174:175], v11 offset:35072
s_mov_b32 s69, s73
s_mov_b32 s73, s74
s_mov_b32 s74, s59
v_mfma_f32_32x32x16_bf16 v[128:143], v[180:183], v[240:243], v[128:143]
s_mov_b32 s59, s95
s_sub_u32 s83, s83, 1
s_mov_b32 s52, s53
s_mov_b32 s53, s54
s_mov_b32 s54, s55
s_mov_b32 s55, s56
v_mfma_f32_32x32x16_bf16 v[144:159], v[180:183], v[244:247], v[144:159]
s_mov_b32 s56, s57
v_mfma_f32_32x32x16_bf16 v[96:111], v[184:187], v[248:251], v[96:111]
ds_read_b64_tr_b16 v[176:177], v10 offset:36864
ds_read_b64_tr_b16 v[178:179], v10 offset:37120
v_mfma_f32_32x32x16_bf16 v[112:127], v[184:187], v[252:255], v[112:127]
ds_read_b64_tr_b16 v[180:181], v11 offset:36864
ds_read_b64_tr_b16 v[182:183], v11 offset:37120
v_mfma_f32_32x32x16_bf16 v[128:143], v[188:191], v[248:251], v[128:143]
v_mfma_f32_32x32x16_bf16 v[144:159], v[188:191], v[252:255], v[144:159]
s_cmp_eq_u32 s83, 1
s_cbranch_scc1 LLAST_%=
s_cmp_eq_u32 s83, 0
s_cbranch_scc0 LA_%=
s_waitcnt lgkmcnt(0)
s_nop 6
s_waitcnt vmcnt(0)
s_mov_b64 exec, 1
buffer_store_dword v219, off, s[96:99], s52 sc1
s_mov_b64 exec, -1
ds_read_b128 v[200:203], v16 offset:256
ds_read_b128 v[204:207], v16 offset:320
ds_read_b128 v[208:211], v16 offset:272
ds_read_b128 v[212:215], v16 offset:336
s_waitcnt lgkmcnt(3)
v_pk_fma_f32 v[96:97], v[20:21], v[200:201], v[96:97] op_sel:[0,0,0] op_sel_hi:[0,1,1]
s_waitcnt lgkmcnt(2)
v_pk_fma_f32 v[104:105], v[20:21], v[204:205], v[104:105] op_sel:[0,0,0] op_sel_hi:[0,1,1]
v_pk_fma_f32 v[98:99], v[20:21], v[202:203], v[98:99] op_sel:[0,0,0] op_sel_hi:[0,1,1]
v_pk_fma_f32 v[106:107], v[20:21], v[206:207], v[106:107] op_sel:[0,0,0] op_sel_hi:[0,1,1]
v_pk_fma_f32 v[112:113], v[24:25], v[200:201], v[112:113] op_sel:[0,0,0] op_sel_hi:[0,1,1]
v_pk_fma_f32 v[120:121], v[24:25], v[204:205], v[120:121] op_sel:[0,0,0] op_sel_hi:[0,1,1]
v_pk_fma_f32 v[114:115], v[24:25], v[202:203], v[114:115] op_sel:[0,0,0] op_sel_hi:[0,1,1]
v_pk_fma_f32 v[122:123], v[24:25], v[206:207], v[122:123] op_sel:[0,0,0] op_sel_hi:[0,1,1]
ds_read_b128 v[200:203], v16 offset:384
ds_read_b128 v[204:207], v16 offset:448
v_pk_mul_f32 v[104:105], v[96:97], v[104:105] op_sel:[0,0] op_sel_hi:[1,1]
v_pk_mul_f32 v[96:97], v[20:21], v[96:97] op_sel:[1,0] op_sel_hi:[1,1]
v_pk_mul_f32 v[106:107], v[98:99], v[106:107] op_sel:[0,0] op_sel_hi:[1,1]
v_pk_mul_f32 v[98:99], v[20:21], v[98:99] op_sel:[1,0] op_sel_hi:[1,1]
v_pk_mul_f32 v[120:121], v[112:113], v[120:121] op_sel:[0,0] op_sel_hi:[1,1]
v_pk_mul_f32 v[112:113], v[24:25], v[112:113] op_sel:[1,0] op_sel_hi:[1,1]
v_pk_mul_f32 v[122:123], v[114:115], v[122:123] op_sel:[0,0] op_sel_hi:[1,1]
v_pk_mul_f32 v[114:115], v[24:25], v[114:115] op_sel:[1,0] op_sel_hi:[1,1]
v_exp_f32 v96, v96
v_exp_f32 v97, v97
v_exp_f32 v98, v98
v_exp_f32 v99, v99
v_exp_f32 v112, v112
v_exp_f32 v113, v113
v_exp_f32 v114, v114
v_exp_f32 v115, v115
v_pk_fma_f32 v[96:97], v[96:97], v[22:23], v[22:23] op_sel:[0,0,0] op_sel_hi:[1,0,0]
v_pk_fma_f32 v[98:99], v[98:99], v[22:23], v[22:23] op_sel:[0,0,0] op_sel_hi:[1,0,0]
v_pk_fma_f32 v[112:113], v[112:113], v[26:27], v[26:27] op_sel:[0,0,0] op_sel_hi:[1,0,0]
v_pk_fma_f32 v[114:115], v[114:115], v[26:27], v[26:27] op_sel:[0,0,0] op_sel_hi:[1,0,0]
v_rcp_f32 v96, v96
v_rcp_f32 v97, v97
v_rcp_f32 v98, v98
v_rcp_f32 v99, v99
v_rcp_f32 v112, v112
v_rcp_f32 v113, v113
v_rcp_f32 v114, v114
v_rcp_f32 v115, v115
v_pk_mul_f32 v[104:105], v[104:105], v[96:97] op_sel:[0,0] op_sel_hi:[1,1]
v_pk_mul_f32 v[106:107], v[106:107], v[98:99] op_sel:[0,0] op_sel_hi:[1,1]
v_pk_mul_f32 v[120:121], v[120:121], v[112:113] op_sel:[0,0] op_sel_hi:[1,1]
v_pk_mul_f32 v[122:123], v[122:123], v[114:115] op_sel:[0,0] op_sel_hi:[1,1]
v_cvt_pk_bf16_f32 v96, v104, v105
v_cvt_pk_bf16_f32 v97, v106, v107
v_cvt_pk_bf16_f32 v112, v120, v121
v_cvt_pk_bf16_f32 v113, v122, v123
s_waitcnt lgkmcnt(3)
v_pk_fma_f32 v[100:101], v[20:21], v[208:209], v[100:101] op_sel:[0,0,0] op_sel_hi:[0,1,1]
s_waitcnt lgkmcnt(2)
v_pk_fma_f32 v[108:109], v[20:21], v[212:213], v[108:109] op_sel:[0,0,0] op_sel_hi:[0,1,1]
v_pk_fma_f32 v[102:103], v[20:21], v[210:211], v[102:103] op_sel:[0,0,0] op_sel_hi:[0,1,1]
v_pk_fma_f32 v[110:111], v[20:21], v[214:215], v[110:111] op_sel:[0,0,0] op_sel_hi:[0,1,1]
v_pk_fma_f32 v[116:117], v[24:25], v[208:209], v[116:117] op_sel:[0,0,0] op_sel_hi:[0,1,1]
v_pk_fma_f32 v[124:125], v[24:25], v[212:213], v[124:125] op_sel:[0,0,0] op_sel_hi:[0,1,1]
v_pk_fma_f32 v[118:119], v[24:25], v[210:211], v[118:119] op_sel:[0,0,0] op_sel_hi:[0,1,1]
v_pk_fma_f32 v[126:127], v[24:25], v[214:215], v[126:127] op_sel:[0,0,0] op_sel_hi:[0,1,1]
ds_read_b128 v[208:211], v16 offset:400
ds_read_b128 v[212:215], v16 offset:464
v_pk_mul_f32 v[108:109], v[100:101], v[108:109] op_sel:[0,0] op_sel_hi:[1,1]
)ASM"
R"ASM(
v_pk_mul_f32 v[100:101], v[20:21], v[100:101] op_sel:[1,0] op_sel_hi:[1,1]
v_pk_mul_f32 v[110:111], v[102:103], v[110:111] op_sel:[0,0] op_sel_hi:[1,1]
v_pk_mul_f32 v[102:103], v[20:21], v[102:103] op_sel:[1,0] op_sel_hi:[1,1]
v_pk_mul_f32 v[124:125], v[116:117], v[124:125] op_sel:[0,0] op_sel_hi:[1,1]
v_pk_mul_f32 v[116:117], v[24:25], v[116:117] op_sel:[1,0] op_sel_hi:[1,1]
v_pk_mul_f32 v[126:127], v[118:119], v[126:127] op_sel:[0,0] op_sel_hi:[1,1]
v_pk_mul_f32 v[118:119], v[24:25], v[118:119] op_sel:[1,0] op_sel_hi:[1,1]
v_exp_f32 v100, v100
v_exp_f32 v101, v101
v_exp_f32 v102, v102
v_exp_f32 v103, v103
v_exp_f32 v116, v116
v_exp_f32 v117, v117
v_exp_f32 v118, v118
v_exp_f32 v119, v119
v_pk_fma_f32 v[100:101], v[100:101], v[22:23], v[22:23] op_sel:[0,0,0] op_sel_hi:[1,0,0]
v_pk_fma_f32 v[102:103], v[102:103], v[22:23], v[22:23] op_sel:[0,0,0] op_sel_hi:[1,0,0]
v_pk_fma_f32 v[116:117], v[116:117], v[26:27], v[26:27] op_sel:[0,0,0] op_sel_hi:[1,0,0]
v_pk_fma_f32 v[118:119], v[118:119], v[26:27], v[26:27] op_sel:[0,0,0] op_sel_hi:[1,0,0]
v_rcp_f32 v100, v100
v_rcp_f32 v101, v101
v_rcp_f32 v102, v102
v_rcp_f32 v103, v103
v_rcp_f32 v116, v116
v_rcp_f32 v117, v117
v_rcp_f32 v118, v118
v_rcp_f32 v119, v119
v_pk_mul_f32 v[108:109], v[108:109], v[100:101] op_sel:[0,0] op_sel_hi:[1,1]
v_pk_mul_f32 v[110:111], v[110:111], v[102:103] op_sel:[0,0] op_sel_hi:[1,1]
v_pk_mul_f32 v[124:125], v[124:125], v[116:117] op_sel:[0,0] op_sel_hi:[1,1]
v_pk_mul_f32 v[126:127], v[126:127], v[118:119] op_sel:[0,0] op_sel_hi:[1,1]
v_cvt_pk_bf16_f32 v98, v108, v109
v_cvt_pk_bf16_f32 v99, v110, v111
v_cvt_pk_bf16_f32 v114, v124, v125
v_cvt_pk_bf16_f32 v115, v126, v127
buffer_store_dwordx4 v[96:99], v19, s[96:99], s69 offen offset:0 sc1
buffer_store_dwordx4 v[112:115], v19, s[96:99], s69 offen offset:1024 sc1
s_waitcnt lgkmcnt(3)
v_pk_fma_f32 v[128:129], v[20:21], v[200:201], v[128:129] op_sel:[0,0,0] op_sel_hi:[0,1,1]
s_waitcnt lgkmcnt(2)
v_pk_fma_f32 v[136:137], v[20:21], v[204:205], v[136:137] op_sel:[0,0,0] op_sel_hi:[0,1,1]
v_pk_fma_f32 v[130:131], v[20:21], v[202:203], v[130:131] op_sel:[0,0,0] op_sel_hi:[0,1,1]
v_pk_fma_f32 v[138:139], v[20:21], v[206:207], v[138:139] op_sel:[0,0,0] op_sel_hi:[0,1,1]
v_pk_fma_f32 v[144:145], v[24:25], v[200:201], v[144:145] op_sel:[0,0,0] op_sel_hi:[0,1,1]
v_pk_fma_f32 v[152:153], v[24:25], v[204:205], v[152:153] op_sel:[0,0,0] op_sel_hi:[0,1,1]
v_pk_fma_f32 v[146:147], v[24:25], v[202:203], v[146:147] op_sel:[0,0,0] op_sel_hi:[0,1,1]
v_pk_fma_f32 v[154:155], v[24:25], v[206:207], v[154:155] op_sel:[0,0,0] op_sel_hi:[0,1,1]
v_pk_mul_f32 v[136:137], v[128:129], v[136:137] op_sel:[0,0] op_sel_hi:[1,1]
v_pk_mul_f32 v[128:129], v[20:21], v[128:129] op_sel:[1,0] op_sel_hi:[1,1]
v_pk_mul_f32 v[138:139], v[130:131], v[138:139] op_sel:[0,0] op_sel_hi:[1,1]
v_pk_mul_f32 v[130:131], v[20:21], v[130:131] op_sel:[1,0] op_sel_hi:[1,1]
v_pk_mul_f32 v[152:153], v[144:145], v[152:153] op_sel:[0,0] op_sel_hi:[1,1]
v_pk_mul_f32 v[144:145], v[24:25], v[144:145] op_sel:[1,0] op_sel_hi:[1,1]
v_pk_mul_f32 v[154:155], v[146:147], v[154:155] op_sel:[0,0] op_sel_hi:[1,1]
v_pk_mul_f32 v[146:147], v[24:25], v[146:147] op_sel:[1,0] op_sel_hi:[1,1]
v_exp_f32 v128, v128
v_exp_f32 v129, v129
v_exp_f32 v130, v130
v_exp_f32 v131, v131
v_exp_f32 v144, v144
v_exp_f32 v145, v145
v_exp_f32 v146, v146
v_exp_f32 v147, v147
v_pk_fma_f32 v[128:129], v[128:129], v[22:23], v[22:23] op_sel:[0,0,0] op_sel_hi:[1,0,0]
v_pk_fma_f32 v[130:131], v[130:131], v[22:23], v[22:23] op_sel:[0,0,0] op_sel_hi:[1,0,0]
v_pk_fma_f32 v[144:145], v[144:145], v[26:27], v[26:27] op_sel:[0,0,0] op_sel_hi:[1,0,0]
v_pk_fma_f32 v[146:147], v[146:147], v[26:27], v[26:27] op_sel:[0,0,0] op_sel_hi:[1,0,0]
v_rcp_f32 v128, v128
v_rcp_f32 v129, v129
v_rcp_f32 v130, v130
v_rcp_f32 v131, v131
v_rcp_f32 v144, v144
v_rcp_f32 v145, v145
v_rcp_f32 v146, v146
v_rcp_f32 v147, v147
v_pk_mul_f32 v[136:137], v[136:137], v[128:129] op_sel:[0,0] op_sel_hi:[1,1]
v_pk_mul_f32 v[138:139], v[138:139], v[130:131] op_sel:[0,0] op_sel_hi:[1,1]
v_pk_mul_f32 v[152:153], v[152:153], v[144:145] op_sel:[0,0] op_sel_hi:[1,1]
v_pk_mul_f32 v[154:155], v[154:155], v[146:147] op_sel:[0,0] op_sel_hi:[1,1]
v_cvt_pk_bf16_f32 v128, v136, v137
v_cvt_pk_bf16_f32 v129, v138, v139
v_cvt_pk_bf16_f32 v144, v152, v153
v_cvt_pk_bf16_f32 v145, v154, v155
s_waitcnt lgkmcnt(1)
v_pk_fma_f32 v[132:133], v[20:21], v[208:209], v[132:133] op_sel:[0,0,0] op_sel_hi:[0,1,1]
s_waitcnt lgkmcnt(0)
v_pk_fma_f32 v[140:141], v[20:21], v[212:213], v[140:141] op_sel:[0,0,0] op_sel_hi:[0,1,1]
v_pk_fma_f32 v[134:135], v[20:21], v[210:211], v[134:135] op_sel:[0,0,0] op_sel_hi:[0,1,1]
v_pk_fma_f32 v[142:143], v[20:21], v[214:215], v[142:143] op_sel:[0,0,0] op_sel_hi:[0,1,1]
v_pk_fma_f32 v[148:149], v[24:25], v[208:209], v[148:149] op_sel:[0,0,0] op_sel_hi:[0,1,1]
v_pk_fma_f32 v[156:157], v[24:25], v[212:213], v[156:157] op_sel:[0,0,0] op_sel_hi:[0,1,1]
v_pk_fma_f32 v[150:151], v[24:25], v[210:211], v[150:151] op_sel:[0,0,0] op_sel_hi:[0,1,1]
v_pk_fma_f32 v[158:159], v[24:25], v[214:215], v[158:159] op_sel:[0,0,0] op_sel_hi:[0,1,1]
v_pk_mul_f32 v[140:141], v[132:133], v[140:141] op_sel:[0,0] op_sel_hi:[1,1]
v_pk_mul_f32 v[132:133], v[20:21], v[132:133] op_sel:[1,0] op_sel_hi:[1,1]
v_pk_mul_f32 v[142:143], v[134:135], v[142:143] op_sel:[0,0] op_sel_hi:[1,1]
v_pk_mul_f32 v[134:135], v[20:21], v[134:135] op_sel:[1,0] op_sel_hi:[1,1]
v_pk_mul_f32 v[156:157], v[148:149], v[156:157] op_sel:[0,0] op_sel_hi:[1,1]
v_pk_mul_f32 v[148:149], v[24:25], v[148:149] op_sel:[1,0] op_sel_hi:[1,1]
v_pk_mul_f32 v[158:159], v[150:151], v[158:159] op_sel:[0,0] op_sel_hi:[1,1]
v_pk_mul_f32 v[150:151], v[24:25], v[150:151] op_sel:[1,0] op_sel_hi:[1,1]
v_exp_f32 v132, v132
v_exp_f32 v133, v133
v_exp_f32 v134, v134
v_exp_f32 v135, v135
v_exp_f32 v148, v148
v_exp_f32 v149, v149
v_exp_f32 v150, v150
v_exp_f32 v151, v151
v_pk_fma_f32 v[132:133], v[132:133], v[22:23], v[22:23] op_sel:[0,0,0] op_sel_hi:[1,0,0]
v_pk_fma_f32 v[134:135], v[134:135], v[22:23], v[22:23] op_sel:[0,0,0] op_sel_hi:[1,0,0]
v_pk_fma_f32 v[148:149], v[148:149], v[26:27], v[26:27] op_sel:[0,0,0] op_sel_hi:[1,0,0]
v_pk_fma_f32 v[150:151], v[150:151], v[26:27], v[26:27] op_sel:[0,0,0] op_sel_hi:[1,0,0]
v_rcp_f32 v132, v132
v_rcp_f32 v133, v133
v_rcp_f32 v134, v134
v_rcp_f32 v135, v135
v_rcp_f32 v148, v148
v_rcp_f32 v149, v149
v_rcp_f32 v150, v150
v_rcp_f32 v151, v151
v_pk_mul_f32 v[140:141], v[140:141], v[132:133] op_sel:[0,0] op_sel_hi:[1,1]
v_pk_mul_f32 v[142:143], v[142:143], v[134:135] op_sel:[0,0] op_sel_hi:[1,1]
v_pk_mul_f32 v[156:157], v[156:157], v[148:149] op_sel:[0,0] op_sel_hi:[1,1]
v_pk_mul_f32 v[158:159], v[158:159], v[150:151] op_sel:[0,0] op_sel_hi:[1,1]
v_cvt_pk_bf16_f32 v130, v140, v141
v_cvt_pk_bf16_f32 v131, v142, v143
v_cvt_pk_bf16_f32 v146, v156, v157
v_cvt_pk_bf16_f32 v147, v158, v159
s_add_u32 s75, s69, 65536
buffer_store_dwordx4 v[128:131], v19, s[96:99], s75 offen offset:0 sc1
s_add_u32 s75, s69, 65536
buffer_store_dwordx4 v[144:147], v19, s[96:99], s75 offen offset:1024 sc1
s_waitcnt vmcnt(0)
s_mov_b64 exec, 1
buffer_store_dword v219, off, s[96:99], s53 sc1
s_mov_b64 exec, -1
v_readlane_b32 s76, v0, 21
s_nop 3
s_cmp_eq_u32 s76, 3
s_cbranch_scc0 LBE_%=
v_lshlrev_b32 v2, 4, v1
s_mov_b32 s77, 49651712
s_mov_b32 s78, 20000
LBL_%=:
buffer_load_dwordx4 v[32:35], v2, s[96:99], s77 offen offset:0 sc1
buffer_load_dwordx4 v[36:39], v2, s[96:99], s77 offen offset:1024 sc1
buffer_load_dwordx4 v[40:43], v2, s[96:99], s77 offen offset:2048 sc1
buffer_load_dwordx4 v[44:47], v2, s[96:99], s77 offen offset:3072 sc1
v_mov_b32 v3, 0
s_waitcnt vmcnt(3)
v_xor_b32 v4, v32, v219
v_or_b32 v3, v3, v4
v_xor_b32 v4, v33, v219
v_or_b32 v3, v3, v4
v_xor_b32 v4, v34, v219
v_or_b32 v3, v3, v4
v_xor_b32 v4, v35, v219
v_or_b32 v3, v3, v4
s_waitcnt vmcnt(2)
v_xor_b32 v4, v36, v219
v_or_b32 v3, v3, v4
v_xor_b32 v4, v37, v219
v_or_b32 v3, v3, v4
v_xor_b32 v4, v38, v219
v_or_b32 v3, v3, v4
v_xor_b32 v4, v39, v219
v_or_b32 v3, v3, v4
s_waitcnt vmcnt(1)
v_xor_b32 v4, v40, v219
v_or_b32 v3, v3, v4
v_xor_b32 v4, v41, v219
v_or_b32 v3, v3, v4
v_xor_b32 v4, v42, v219
v_or_b32 v3, v3, v4
v_xor_b32 v4, v43, v219
v_or_b32 v3, v3, v4
s_waitcnt vmcnt(0)
v_xor_b32 v4, v44, v219
v_or_b32 v3, v3, v4
v_xor_b32 v4, v45, v219
v_or_b32 v3, v3, v4
v_xor_b32 v4, v46, v219
v_or_b32 v3, v3, v4
v_xor_b32 v4, v47, v219
v_or_b32 v3, v3, v4
v_cmp_ne_u32 vcc, 0, v3
s_sub_u32 s78, s78, 1
s_cmp_eq_u32 s78, 0
s_cbranch_scc1 LBM_%=
s_nop 0
s_cmp_lg_u64 vcc, 0
s_cbranch_scc1 LBL_%=
LBM_%=:
s_mov_b32 s77, 49655808
s_mov_b64 exec, 1
buffer_store_dword v219, off, s[96:99], s77 sc1
s_mov_b64 exec, -1
LBE_%=:
s_branch LEND_%=
)ASM"
R"ASM(
LLAST_%=:
s_waitcnt lgkmcnt(10)
v_mfma_f32_32x32x16_bf16 v[32:47], v[160:163], a[0:3], 0
ds_read_b64_tr_b16 v[184:185], v10 offset:38912
ds_read_b64_tr_b16 v[186:187], v10 offset:39168
s_mov_b32 s72, s52
s_mov_b32 s79, s53
s_mov_b32 s41, s54
s_mov_b32 s42, s94
v_mfma_f32_32x32x16_bf16 v[48:63], v[160:163], a[4:7], 0
ds_read_b64_tr_b16 v[188:189], v11 offset:38912
ds_read_b64_tr_b16 v[190:191], v11 offset:39168
s_mov_b32 s83, s73
s_lshl_b32 s76, s40, 8
s_add_u32 s76, s76, 153600
v_lshlrev_b32 v216, 2, v1
s_waitcnt lgkmcnt(12)
v_mfma_f32_32x32x16_bf16 v[64:79], v[164:167], a[0:3], 0
v_add_u32 v216, s76, v216
ds_read_b64_tr_b16 v[192:193], v14 offset:32768
ds_read_b64_tr_b16 v[194:195], v14 offset:34816
v_mfma_f32_32x32x16_bf16 v[80:95], v[164:167], a[4:7], 0
ds_read_b64_tr_b16 v[196:197], v14 offset:36864
ds_read_b64_tr_b16 v[198:199], v14 offset:38912
s_waitcnt lgkmcnt(14)
v_mfma_f32_32x32x16_bf16 v[32:47], v[168:171], a[8:11], v[32:47]
ds_read_b64_tr_b16 v[160:161], v10 offset:40960
ds_read_b64_tr_b16 v[162:163], v10 offset:41216
v_mfma_f32_32x32x16_bf16 v[48:63], v[168:171], a[12:15], v[48:63]
ds_read_b64_tr_b16 v[164:165], v11 offset:40960
ds_read_b64_tr_b16 v[166:167], v11 offset:41216
s_waitcnt lgkmcnt(6)
v_mfma_f32_16x16x32_bf16 v[220:223], v[192:195], v[28:31], 0
ds_read_b32 v15, v216
v_mfma_f32_32x32x16_bf16 v[64:79], v[172:175], a[8:11], v[64:79]
v_readlane_b32 s44, v0, 0
v_readlane_b32 s45, v0, 1
v_readlane_b32 s60, v0, 11
v_readlane_b32 s90, v0, 17
v_readlane_b32 s68, v0, 16
s_mov_b32 s47, 0x20000
v_mfma_f32_32x32x16_bf16 v[80:95], v[172:175], a[12:15], v[80:95]
s_mov_b32 s46, 5310464
v_and_b32 v216, 15, v1
v_lshrrev_b32 v217, 4, v1
v_lshlrev_b32 v2, 5, v216
v_and_b32 v23, 1, v217
v_mfma_f32_32x32x16_bf16 v[32:47], v[176:179], a[16:19], v[32:47]
ds_read_b64_tr_b16 v[168:169], v10 offset:43008
ds_read_b64_tr_b16 v[170:171], v10 offset:43264
v_lshl_add_u32 v2, v23, 4, v2
v_lshrrev_b32 v23, 1, v217
v_lshl_add_u32 v2, v23, 16, v2
s_lshl_b32 s61, s40, 11
v_mfma_f32_32x32x16_bf16 v[48:63], v[176:179], a[20:23], v[48:63]
ds_read_b64_tr_b16 v[172:173], v11 offset:43008
ds_read_b64_tr_b16 v[174:175], v11 offset:43264
v_lshrrev_b32 v216, 3, v1
v_and_b32 v23, 7, v1
v_bfe_u32 v217, v216, 1, 1
s_and_b32 s76, s40, 1
s_waitcnt lgkmcnt(9)
v_mfma_f32_16x16x32_bf16 v[220:223], v[196:199], v[28:31], v[220:223]
ds_read_b64_tr_b16 v[192:193], v14 offset:40960
v_mfma_f32_32x32x16_bf16 v[64:79], v[180:183], a[16:19], v[64:79]
s_lshl_b32 s76, s76, 1
v_or_b32 v217, s76, v217
v_lshlrev_b32 v217, 1, v217
v_xor_b32 v217, v217, v23
s_lshl_b32 s76, s40, 3
ds_read_b64_tr_b16 v[194:195], v14 offset:43008
v_mfma_f32_32x32x16_bf16 v[80:95], v[180:183], a[20:23], v[80:95]
v_add_u32 v216, s76, v216
v_mul_u32_u24 v216, 1152, v216
v_lshl_add_u32 v216, v217, 4, v216
v_add_u32 v216, s90, v216
v_add_u32 v3, 2048, v216
v_add_u32 v4, 1152, v216
v_mfma_f32_32x32x16_bf16 v[32:47], v[184:187], a[24:27], v[32:47]
ds_read_b64_tr_b16 v[176:177], v10 offset:45056
ds_read_b64_tr_b16 v[178:179], v10 offset:45312
v_add_u32 v5, 256, v3
v_add_u32 v27, 128, v4
s_waitcnt lgkmcnt(8)
buffer_load_dword v17, v15, s[96:99], 0 offen sc1
v_mfma_f32_32x32x16_bf16 v[48:63], v[184:187], a[28:31], v[48:63]
ds_read_b64_tr_b16 v[180:181], v11 offset:45056
ds_read_b64_tr_b16 v[182:183], v11 offset:45312
s_add_u32 m0, s61, 57344
v_mfma_f32_32x32x16_bf16 v[64:79], v[188:191], a[24:27], v[64:79]
ds_read_b64_tr_b16 v[196:197], v14 offset:45056
ds_read_b64_tr_b16 v[198:199], v14 offset:47104
v_mfma_f32_32x32x16_bf16 v[80:95], v[188:191], a[28:31], v[80:95]
s_waitcnt lgkmcnt(8)
buffer_load_dwordx4 v3, s[44:47], s4 offen offset:0 lds
ds_read_b128 v[200:203], v16 offset:256
ds_read_b128 v[204:207], v16 offset:320
v_mfma_f32_32x32x16_bf16 v[32:47], v[160:163], a[32:35], v[32:47]
ds_read_b64_tr_b16 v[184:185], v10 offset:47104
ds_read_b64_tr_b16 v[186:187], v10 offset:47360
v_mfma_f32_32x32x16_bf16 v[48:63], v[160:163], a[36:39], v[48:63]
ds_read_b64_tr_b16 v[188:189], v11 offset:47104
ds_read_b64_tr_b16 v[190:191], v11 offset:47360
s_waitcnt lgkmcnt(12)
v_mfma_f32_16x16x32_bf16 v[220:223], v[192:195], v[28:31], v[220:223]
buffer_load_dwordx4 v4, s[44:47], s4 offen offset:1024 lds
v_mfma_f32_32x32x16_bf16 v[64:79], v[164:167], a[32:35], v[64:79]
s_add_u32 m0, s61, 65536
s_waitcnt lgkmcnt(5)
v_fmac_f32 v96, v20, v200
s_waitcnt lgkmcnt(4)
v_fmac_f32 v104, v20, v204
v_fmac_f32 v97, v20, v201
v_fmac_f32 v105, v20, v205
v_mul_f32 v104, v96, v104
v_mfma_f32_32x32x16_bf16 v[80:95], v[164:167], a[36:39], v[80:95]
v_mul_f32 v96, v21, v96
v_mul_f32 v105, v97, v105
v_mul_f32 v97, v21, v97
v_exp_f32 v96, v96
v_mfma_f32_32x32x16_bf16 v[32:47], v[168:171], a[40:43], v[32:47]
ds_read_b64_tr_b16 v[160:161], v6 offset:0
ds_read_b64_tr_b16 v[162:163], v6 offset:256
buffer_load_dwordx4 v3, s[44:47], s5 offen offset:0 lds
v_exp_f32 v97, v97
v_mfma_f32_32x32x16_bf16 v[48:63], v[168:171], a[44:47], v[48:63]
ds_read_b64_tr_b16 v[164:165], v7 offset:0
ds_read_b64_tr_b16 v[166:167], v7 offset:256
v_fma_f32 v96, v96, v22, v22
v_fma_f32 v97, v97, v22, v22
v_rcp_f32 v96, v96
v_mfma_f32_16x16x32_bf16 v[220:223], v[196:199], v[28:31], v[220:223]
ds_read_b64_tr_b16 v[192:193], v12 offset:0
v_mfma_f32_32x32x16_bf16 v[64:79], v[172:175], a[40:43], v[64:79]
buffer_load_dwordx4 v4, s[44:47], s5 offen offset:1024 lds
ds_read_b64_tr_b16 v[194:195], v12 offset:2048
v_rcp_f32 v97, v97
v_mul_f32 v104, v104, v96
v_mfma_f32_32x32x16_bf16 v[80:95], v[172:175], a[44:47], v[80:95]
s_waitcnt lgkmcnt(6)
v_mul_f32 v105, v105, v97
v_cvt_pk_bf16_f32 v96, v104, v105
ds_read_b128 v[208:211], v16 offset:272
ds_read_b128 v[212:215], v16 offset:336
v_fmac_f32 v98, v20, v202
v_mfma_f32_32x32x16_bf16 v[32:47], v[176:179], a[48:51], v[32:47]
ds_read_b64_tr_b16 v[168:169], v6 offset:2048
ds_read_b64_tr_b16 v[170:171], v6 offset:2304
s_add_u32 m0, s61, 73728
v_fmac_f32 v106, v20, v206
v_fmac_f32 v99, v20, v203
v_fmac_f32 v107, v20, v207
v_mfma_f32_32x32x16_bf16 v[48:63], v[176:179], a[52:55], v[48:63]
ds_read_b64_tr_b16 v[172:173], v7 offset:2048
ds_read_b64_tr_b16 v[174:175], v7 offset:2304
buffer_load_dwordx4 v5, s[44:47], s4 offen offset:0 lds
v_mul_f32 v106, v98, v106
v_mul_f32 v98, v21, v98
v_mfma_f32_32x32x16_bf16 v[64:79], v[180:183], a[48:51], v[64:79]
v_mul_f32 v107, v99, v107
v_mul_f32 v99, v21, v99
v_exp_f32 v98, v98
v_exp_f32 v99, v99
v_mfma_f32_32x32x16_bf16 v[80:95], v[180:183], a[52:55], v[80:95]
v_fma_f32 v98, v98, v22, v22
v_fma_f32 v99, v99, v22, v22
v_rcp_f32 v98, v98
v_rcp_f32 v99, v99
v_mfma_f32_32x32x16_bf16 v[32:47], v[184:187], a[56:59], v[32:47]
ds_read_b64_tr_b16 v[176:177], v6 offset:4096
ds_read_b64_tr_b16 v[178:179], v6 offset:4352
buffer_load_dwordx4 v27, s[44:47], s5 offen offset:1024 lds
v_mul_f32 v106, v106, v98
v_mul_f32 v107, v107, v99
v_mfma_f32_32x32x16_bf16 v[48:63], v[184:187], a[60:63], v[48:63]
ds_read_b64_tr_b16 v[180:181], v7 offset:4096
ds_read_b64_tr_b16 v[182:183], v7 offset:4352
v_cvt_pk_bf16_f32 v97, v106, v107
v_fmac_f32 v112, v24, v200
v_fmac_f32 v120, v24, v204
v_fmac_f32 v113, v24, v201
s_waitcnt lgkmcnt(10)
v_mfma_f32_16x16x32_bf16 v[220:223], v[192:195], v[28:31], v[220:223]
ds_read_b64_tr_b16 v[196:197], v12 offset:4096
v_mfma_f32_32x32x16_bf16 v[64:79], v[188:191], a[56:59], v[64:79]
s_add_u32 m0, s61, 81920
s_nop 0
buffer_load_dwordx4 v3, s[44:47], s6 offen offset:0 lds
ds_read_b64_tr_b16 v[198:199], v12 offset:6144
v_fmac_f32 v121, v24, v205
v_mul_f32 v120, v112, v120
v_mfma_f32_32x32x16_bf16 v[80:95], v[188:191], a[60:63], v[80:95]
s_waitcnt lgkmcnt(6)
v_mul_f32 v112, v25, v112
v_mul_f32 v121, v113, v121
v_mul_f32 v113, v25, v113
v_exp_f32 v112, v112
v_mfma_f32_32x32x16_bf16 v[32:47], v[160:163], a[64:67], v[32:47]
ds_read_b64_tr_b16 v[184:185], v6 offset:6144
ds_read_b64_tr_b16 v[186:187], v6 offset:6400
)ASM"
R"ASM(
s_mov_b32 s65, 20000
s_waitcnt vmcnt(7)
LPC1_%=:
v_cmp_ne_u32 s[92:93], s43, v17
s_nop 3
s_and_b32 s92, s92, 0x3ff
s_and_b32 s93, s93, 0x0
s_cmp_eq_u64 s[92:93], 0
s_cbranch_scc1 LPD1_%=
s_sub_u32 s65, s65, 1
s_cmp_eq_u32 s65, 0
s_cbranch_scc1 LPD1_%=
s_sleep 2
buffer_load_dword v17, v15, s[96:99], 0 offen sc1
s_waitcnt vmcnt(0)
s_branch LPC1_%=
LPD1_%=:
v_mfma_f32_32x32x16_bf16 v[48:63], v[160:163], a[68:71], v[48:63]
ds_read_b64_tr_b16 v[188:189], v7 offset:6144
ds_read_b64_tr_b16 v[190:191], v7 offset:6400
buffer_load_dwordx4 v4, s[44:47], s6 offen offset:1024 lds
v_exp_f32 v113, v113
v_mfma_f32_32x32x16_bf16 v[64:79], v[164:167], a[64:67], v[64:79]
ds_read_b64_tr_b16 v[192:193], v12 offset:8192
ds_read_b64_tr_b16 v[194:195], v12 offset:10240
v_fma_f32 v112, v112, v26, v26
v_fma_f32 v113, v113, v26, v26
v_rcp_f32 v112, v112
v_mfma_f32_32x32x16_bf16 v[80:95], v[164:167], a[68:71], v[80:95]
v_rcp_f32 v113, v113
v_mul_f32 v120, v120, v112
v_mul_f32 v121, v121, v113
v_cvt_pk_bf16_f32 v112, v120, v121
v_fmac_f32 v114, v24, v202
v_mfma_f32_32x32x16_bf16 v[32:47], v[168:171], a[72:75], v[32:47]
ds_read_b64_tr_b16 v[160:161], v6 offset:8192
ds_read_b64_tr_b16 v[162:163], v6 offset:8448
v_fmac_f32 v122, v24, v206
v_fmac_f32 v115, v24, v203
v_fmac_f32 v123, v24, v207
v_mul_f32 v122, v114, v122
v_mfma_f32_32x32x16_bf16 v[48:63], v[168:171], a[76:79], v[48:63]
ds_read_b64_tr_b16 v[164:165], v7 offset:8192
ds_read_b64_tr_b16 v[166:167], v7 offset:8448
s_add_u32 m0, s61, 90112
s_nop 0
buffer_load_dwordx4 v3, s[44:47], s7 offen offset:0 lds
v_mul_f32 v114, v25, v114
s_waitcnt lgkmcnt(10)
v_mfma_f32_16x16x32_bf16 v[220:223], v[196:199], v[28:31], v[220:223]
v_mul_f32 v123, v115, v123
v_mul_f32 v115, v25, v115
v_mfma_f32_32x32x16_bf16 v[64:79], v[172:175], a[72:75], v[64:79]
v_exp_f32 v114, v114
v_exp_f32 v115, v115
v_fma_f32 v114, v114, v26, v26
v_fma_f32 v115, v115, v26, v26
v_mfma_f32_32x32x16_bf16 v[80:95], v[172:175], a[76:79], v[80:95]
s_waitcnt lgkmcnt(6)
buffer_load_dwordx4 v4, s[44:47], s7 offen offset:1024 lds
v_rcp_f32 v114, v114
v_rcp_f32 v115, v115
v_mfma_f32_32x32x16_bf16 v[32:47], v[176:179], a[80:83], v[32:47]
ds_read_b64_tr_b16 v[168:169], v6 offset:10240
ds_read_b64_tr_b16 v[170:171], v6 offset:10496
v_mul_f32 v122, v122, v114
v_mul_f32 v123, v123, v115
v_cvt_pk_bf16_f32 v113, v122, v123
v_fmac_f32 v100, v20, v208
v_mfma_f32_32x32x16_bf16 v[48:63], v[176:179], a[84:87], v[48:63]
ds_read_b64_tr_b16 v[172:173], v7 offset:10240
ds_read_b64_tr_b16 v[174:175], v7 offset:10496
v_fmac_f32 v108, v20, v212
v_fmac_f32 v101, v20, v209
v_fmac_f32 v109, v20, v213
v_mul_f32 v108, v100, v108
s_waitcnt lgkmcnt(8)
v_mfma_f32_16x16x32_bf16 v[220:223], v[192:195], v[28:31], v[220:223]
ds_read_b64_tr_b16 v[196:197], v12 offset:12288
v_mfma_f32_32x32x16_bf16 v[64:79], v[180:183], a[80:83], v[64:79]
s_add_u32 s62, s60, 0
buffer_load_dwordx4 a[0:3], v2, s[96:99], s62 offen sc1
ds_read_b64_tr_b16 v[198:199], v12 offset:14336
v_mul_f32 v100, v21, v100
v_mul_f32 v109, v101, v109
v_mfma_f32_32x32x16_bf16 v[80:95], v[180:183], a[84:87], v[80:95]
v_mul_f32 v101, v21, v101
v_exp_f32 v100, v100
v_exp_f32 v101, v101
v_fma_f32 v100, v100, v22, v22
v_mfma_f32_32x32x16_bf16 v[32:47], v[184:187], a[88:91], v[32:47]
ds_read_b64_tr_b16 v[176:177], v6 offset:12288
ds_read_b64_tr_b16 v[178:179], v6 offset:12544
v_fma_f32 v101, v101, v22, v22
v_rcp_f32 v100, v100
v_mfma_f32_32x32x16_bf16 v[48:63], v[184:187], a[92:95], v[48:63]
ds_read_b64_tr_b16 v[180:181], v7 offset:12288
ds_read_b64_tr_b16 v[182:183], v7 offset:12544
buffer_load_dwordx4 a[4:7], v2, s[96:99], s62 offen offset:512 sc1
v_rcp_f32 v101, v101
v_mfma_f32_32x32x16_bf16 v[64:79], v[188:191], a[88:91], v[64:79]
v_mul_f32 v108, v108, v100
v_mul_f32 v109, v109, v101
v_cvt_pk_bf16_f32 v98, v108, v109
ds_read_b128 v[200:203], v16 offset:384
ds_read_b128 v[204:207], v16 offset:448
v_fmac_f32 v102, v20, v210
v_mfma_f32_32x32x16_bf16 v[80:95], v[188:191], a[92:95], v[80:95]
s_waitcnt lgkmcnt(8)
v_fmac_f32 v110, v20, v214
v_fmac_f32 v103, v20, v211
v_fmac_f32 v111, v20, v215
v_mul_f32 v110, v102, v110
v_mul_f32 v102, v21, v102
v_mfma_f32_32x32x16_bf16 v[32:47], v[160:163], a[96:99], v[32:47]
ds_read_b64_tr_b16 v[184:185], v6 offset:14336
ds_read_b64_tr_b16 v[186:187], v6 offset:14592
v_mul_f32 v111, v103, v111
v_mul_f32 v103, v21, v103
v_exp_f32 v102, v102
v_mfma_f32_32x32x16_bf16 v[48:63], v[160:163], a[100:103], v[48:63]
ds_read_b64_tr_b16 v[188:189], v7 offset:14336
ds_read_b64_tr_b16 v[190:191], v7 offset:14592
s_add_u32 m0, s61, 98304
s_nop 0
buffer_load_dwordx4 v5, s[44:47], s6 offen offset:0 lds
s_waitcnt lgkmcnt(10)
v_mfma_f32_16x16x32_bf16 v[220:223], v[196:199], v[28:31], v[220:223]
ds_read_b64_tr_b16 v[192:193], v12 offset:16384
v_mfma_f32_32x32x16_bf16 v[64:79], v[164:167], a[96:99], v[64:79]
ds_read_b64_tr_b16 v[194:195], v12 offset:18432
v_exp_f32 v103, v103
v_fma_f32 v102, v102, v22, v22
v_fma_f32 v103, v103, v22, v22
v_mfma_f32_32x32x16_bf16 v[80:95], v[164:167], a[100:103], v[80:95]
buffer_load_dwordx4 v27, s[44:47], s7 offen offset:1024 lds
v_rcp_f32 v102, v102
v_rcp_f32 v103, v103
v_mul_f32 v110, v110, v102
v_mfma_f32_32x32x16_bf16 v[32:47], v[168:171], a[104:107], v[32:47]
ds_read_b64_tr_b16 v[160:161], v6 offset:16384
ds_read_b64_tr_b16 v[162:163], v6 offset:16640
v_mul_f32 v111, v111, v103
v_cvt_pk_bf16_f32 v99, v110, v111
buffer_store_dwordx4 v[96:99], v19, s[96:99], s69 offen offset:0 sc1
v_mfma_f32_32x32x16_bf16 v[48:63], v[168:171], a[108:111], v[48:63]
ds_read_b64_tr_b16 v[164:165], v7 offset:16384
ds_read_b64_tr_b16 v[166:167], v7 offset:16640
v_fmac_f32 v116, v24, v208
v_fmac_f32 v124, v24, v212
v_fmac_f32 v117, v24, v209
v_fmac_f32 v125, v24, v213
v_mfma_f32_32x32x16_bf16 v[64:79], v[172:175], a[104:107], v[64:79]
v_mul_f32 v124, v116, v124
v_mul_f32 v116, v25, v116
v_mul_f32 v125, v117, v125
v_mul_f32 v117, v25, v117
v_exp_f32 v116, v116
v_mfma_f32_32x32x16_bf16 v[80:95], v[172:175], a[108:111], v[80:95]
s_waitcnt lgkmcnt(6)
s_add_u32 s62, s60, 131072
buffer_load_dwordx4 a[8:11], v2, s[96:99], s62 offen sc1
v_exp_f32 v117, v117
v_fma_f32 v116, v116, v26, v26
v_mfma_f32_32x32x16_bf16 v[32:47], v[176:179], a[112:115], v[32:47]
ds_read_b64_tr_b16 v[168:169], v6 offset:18432
ds_read_b64_tr_b16 v[170:171], v6 offset:18688
v_fma_f32 v117, v117, v26, v26
v_rcp_f32 v116, v116
v_mfma_f32_32x32x16_bf16 v[48:63], v[176:179], a[116:119], v[48:63]
ds_read_b64_tr_b16 v[172:173], v7 offset:18432
ds_read_b64_tr_b16 v[174:175], v7 offset:18688
v_rcp_f32 v117, v117
v_mul_f32 v124, v124, v116
v_mul_f32 v125, v125, v117
s_waitcnt lgkmcnt(8)
v_mfma_f32_16x16x32_bf16 v[220:223], v[192:195], v[28:31], v[220:223]
buffer_load_dwordx4 a[12:15], v2, s[96:99], s62 offen offset:512 sc1
v_mfma_f32_32x32x16_bf16 v[64:79], v[180:183], a[112:115], v[64:79]
v_cvt_pk_bf16_f32 v114, v124, v125
v_fmac_f32 v118, v24, v210
v_fmac_f32 v126, v24, v214
v_fmac_f32 v119, v24, v211
v_fmac_f32 v127, v24, v215
v_mul_f32 v126, v118, v126
v_mfma_f32_32x32x16_bf16 v[80:95], v[180:183], a[116:119], v[80:95]
v_mul_f32 v118, v25, v118
v_mul_f32 v127, v119, v127
v_mul_f32 v119, v25, v119
v_exp_f32 v118, v118
v_mfma_f32_32x32x16_bf16 v[32:47], v[184:187], a[120:123], v[32:47]
s_waitcnt vmcnt(17) lgkmcnt(0)
s_barrier
s_mov_b64 exec, 1
buffer_store_dword v219, off, s[96:99], s72 sc1
s_mov_b64 exec, -1
v_mfma_f32_32x32x16_bf16 v[48:63], v[184:187], a[124:127], v[48:63]
ds_read_b64_tr_b16 v[176:177], v6 offset:20480
ds_read_b64_tr_b16 v[178:179], v6 offset:20736
s_add_u32 m0, s61, 106496
)ASM"
R"ASM(
s_nop 0
buffer_load_dwordx4 v3, s[44:47], s8 offen offset:0 lds
v_mfma_f32_32x32x16_bf16 v[64:79], v[188:191], a[120:123], v[64:79]
ds_read_b64_tr_b16 v[180:181], v7 offset:20480
ds_read_b64_tr_b16 v[182:183], v7 offset:20736
v_exp_f32 v119, v119
v_fma_f32 v118, v118, v26, v26
v_fma_f32 v119, v119, v26, v26
v_mfma_f32_32x32x16_bf16 v[80:95], v[188:191], a[124:127], v[80:95]
ds_read_b64_tr_b16 v[196:197], v12 offset:20480
ds_read_b64_tr_b16 v[198:199], v12 offset:22528
v_rcp_f32 v118, v118
v_mfma_f32_32x32x16_bf16 v[32:47], v[160:163], a[128:131], v[32:47]
ds_read_b64_tr_b16 v[184:185], v6 offset:22528
ds_read_b64_tr_b16 v[186:187], v6 offset:22784
buffer_load_dwordx4 v4, s[44:47], s8 offen offset:1024 lds
v_rcp_f32 v119, v119
v_mfma_f32_32x32x16_bf16 v[48:63], v[160:163], a[132:135], v[48:63]
ds_read_b64_tr_b16 v[188:189], v7 offset:22528
ds_read_b64_tr_b16 v[190:191], v7 offset:22784
v_mul_f32 v126, v126, v118
v_mul_f32 v127, v127, v119
v_cvt_pk_bf16_f32 v115, v126, v127
v_mfma_f32_32x32x16_bf16 v[64:79], v[164:167], a[128:131], v[64:79]
ds_read_b64_tr_b16 v[192:193], v12 offset:24576
ds_read_b64_tr_b16 v[194:195], v12 offset:26624
buffer_store_dwordx4 v[112:115], v19, s[96:99], s69 offen offset:1024 sc1
v_fmac_f32 v128, v20, v200
v_fmac_f32 v136, v20, v204
v_mfma_f32_32x32x16_bf16 v[80:95], v[164:167], a[132:135], v[80:95]
v_fmac_f32 v129, v20, v201
v_fmac_f32 v137, v20, v205
v_mul_f32 v136, v128, v136
v_mul_f32 v128, v21, v128
v_mul_f32 v137, v129, v137
v_mul_f32 v129, v21, v129
v_mfma_f32_32x32x16_bf16 v[32:47], v[168:171], a[136:139], v[32:47]
ds_read_b64_tr_b16 v[160:161], v6 offset:24576
ds_read_b64_tr_b16 v[162:163], v6 offset:24832
s_add_u32 s62, s60, 262144
buffer_load_dwordx4 a[16:19], v2, s[96:99], s62 offen sc1
v_mfma_f32_32x32x16_bf16 v[48:63], v[168:171], a[140:143], v[48:63]
ds_read_b64_tr_b16 v[164:165], v7 offset:24576
ds_read_b64_tr_b16 v[166:167], v7 offset:24832
v_exp_f32 v128, v128
v_exp_f32 v129, v129
s_waitcnt lgkmcnt(10)
v_mfma_f32_16x16x32_bf16 v[220:223], v[196:199], v[28:31], v[220:223]
v_fma_f32 v128, v128, v22, v22
v_fma_f32 v129, v129, v22, v22
v_mfma_f32_32x32x16_bf16 v[64:79], v[172:175], a[136:139], v[64:79]
buffer_load_dwordx4 a[20:23], v2, s[96:99], s62 offen offset:512 sc1
v_rcp_f32 v128, v128
v_rcp_f32 v129, v129
v_mul_f32 v136, v136, v128
v_mfma_f32_32x32x16_bf16 v[80:95], v[172:175], a[140:143], v[80:95]
s_waitcnt lgkmcnt(6)
v_mul_f32 v137, v137, v129
v_cvt_pk_bf16_f32 v128, v136, v137
ds_read_b128 v[208:211], v16 offset:400
ds_read_b128 v[212:215], v16 offset:464
v_fmac_f32 v130, v20, v202
v_mfma_f32_32x32x16_bf16 v[32:47], v[176:179], a[144:147], v[32:47]
ds_read_b64_tr_b16 v[168:169], v6 offset:26624
ds_read_b64_tr_b16 v[170:171], v6 offset:26880
v_fmac_f32 v138, v20, v206
v_fmac_f32 v131, v20, v203
v_fmac_f32 v139, v20, v207
v_mul_f32 v138, v130, v138
v_mfma_f32_32x32x16_bf16 v[48:63], v[176:179], a[148:151], v[48:63]
ds_read_b64_tr_b16 v[172:173], v7 offset:26624
ds_read_b64_tr_b16 v[174:175], v7 offset:26880
v_mul_f32 v130, v21, v130
v_mul_f32 v139, v131, v139
v_mul_f32 v131, v21, v131
s_waitcnt lgkmcnt(10)
v_mfma_f32_16x16x32_bf16 v[220:223], v[192:195], v[28:31], v[220:223]
ds_read_b64_tr_b16 v[196:197], v12 offset:28672
v_mfma_f32_32x32x16_bf16 v[64:79], v[180:183], a[144:147], v[64:79]
s_add_u32 m0, s61, 114688
s_nop 0
buffer_load_dwordx4 v3, s[44:47], s9 offen offset:0 lds
ds_read_b64_tr_b16 v[198:199], v12 offset:30720
v_exp_f32 v130, v130
v_mfma_f32_32x32x16_bf16 v[80:95], v[180:183], a[148:151], v[80:95]
v_exp_f32 v131, v131
v_fma_f32 v130, v130, v22, v22
v_fma_f32 v131, v131, v22, v22
v_rcp_f32 v130, v130
v_mfma_f32_32x32x16_bf16 v[32:47], v[184:187], a[152:155], v[32:47]
ds_read_b64_tr_b16 v[176:177], v6 offset:28672
ds_read_b64_tr_b16 v[178:179], v6 offset:28928
v_rcp_f32 v131, v131
v_mul_f32 v138, v138, v130
v_mul_f32 v139, v139, v131
v_mfma_f32_32x32x16_bf16 v[48:63], v[184:187], a[156:159], v[48:63]
ds_read_b64_tr_b16 v[180:181], v7 offset:28672
ds_read_b64_tr_b16 v[182:183], v7 offset:28928
buffer_load_dwordx4 v4, s[44:47], s9 offen offset:1024 lds
v_cvt_pk_bf16_f32 v129, v138, v139
v_fmac_f32 v144, v24, v200
v_mfma_f32_32x32x16_bf16 v[64:79], v[188:191], a[152:155], v[64:79]
v_fmac_f32 v152, v24, v204
v_fmac_f32 v145, v24, v201
v_fmac_f32 v153, v24, v205
v_mul_f32 v152, v144, v152
v_mul_f32 v144, v25, v144
v_mul_f32 v153, v145, v153
v_mfma_f32_32x32x16_bf16 v[80:95], v[188:191], a[156:159], v[80:95]
s_waitcnt lgkmcnt(6)
s_add_u32 s62, s60, 393216
v_mul_f32 v145, v25, v145
v_exp_f32 v144, v144
s_cmp_lg_u32 s42, 0
s_cselect_b64 s[58:59], -1, 0
v_mfma_f32_32x32x16_bf16 v[32:47], v[160:163], a[160:163], v[32:47]
ds_read_b64_tr_b16 v[184:185], v6 offset:30720
ds_read_b64_tr_b16 v[186:187], v6 offset:30976
buffer_load_dwordx4 a[24:27], v2, s[96:99], s62 offen sc1
v_exp_f32 v145, v145
v_mfma_f32_32x32x16_bf16 v[48:63], v[160:163], a[164:167], v[48:63]
ds_read_b64_tr_b16 v[188:189], v7 offset:30720
ds_read_b64_tr_b16 v[190:191], v7 offset:30976
v_fma_f32 v144, v144, v26, v26
v_fma_f32 v145, v145, v26, v26
v_rcp_f32 v144, v144
s_waitcnt lgkmcnt(8)
v_mfma_f32_16x16x32_bf16 v[220:223], v[196:199], v[28:31], v[220:223]
ds_read_b64_tr_b16 v[192:193], v12 offset:32768
v_mfma_f32_32x32x16_bf16 v[64:79], v[164:167], a[160:163], v[64:79]
buffer_load_dwordx4 a[28:31], v2, s[96:99], s62 offen offset:512 sc1
ds_read_b64_tr_b16 v[194:195], v12 offset:34816
v_rcp_f32 v145, v145
v_mul_f32 v152, v152, v144
v_mfma_f32_32x32x16_bf16 v[80:95], v[164:167], a[164:167], v[80:95]
v_mul_f32 v153, v153, v145
v_cvt_pk_bf16_f32 v144, v152, v153
v_fmac_f32 v146, v24, v202
v_fmac_f32 v154, v24, v206
v_fmac_f32 v147, v24, v203
v_fmac_f32 v155, v24, v207
v_mfma_f32_32x32x16_bf16 v[32:47], v[168:171], a[168:171], v[32:47]
ds_read_b64_tr_b16 v[160:161], v6 offset:32768
ds_read_b64_tr_b16 v[162:163], v6 offset:33024
v_mul_f32 v154, v146, v154
v_mul_f32 v146, v25, v146
v_mul_f32 v155, v147, v155
v_mul_f32 v147, v25, v147
v_mfma_f32_32x32x16_bf16 v[48:63], v[168:171], a[172:175], v[48:63]
ds_read_b64_tr_b16 v[164:165], v7 offset:32768
ds_read_b64_tr_b16 v[166:167], v7 offset:33024
s_add_u32 m0, s61, 122880
s_nop 0
buffer_load_dwordx4 v5, s[44:47], s8 offen offset:0 lds
v_lshlrev_b32 v216, 4, v1
v_mfma_f32_32x32x16_bf16 v[64:79], v[172:175], a[168:171], v[64:79]
v_exp_f32 v146, v146
v_exp_f32 v147, v147
v_fma_f32 v146, v146, v26, v26
v_fma_f32 v147, v147, v26, v26
v_mfma_f32_32x32x16_bf16 v[80:95], v[172:175], a[172:175], v[80:95]
s_waitcnt lgkmcnt(6)
v_rcp_f32 v146, v146
v_rcp_f32 v147, v147
v_mul_f32 v154, v154, v146
v_mfma_f32_32x32x16_bf16 v[32:47], v[176:179], a[176:179], v[32:47]
ds_read_b64_tr_b16 v[168:169], v6 offset:34816
ds_read_b64_tr_b16 v[170:171], v6 offset:35072
buffer_load_dwordx4 v27, s[44:47], s9 offen offset:1024 lds
v_mul_f32 v155, v155, v147
v_cvt_pk_bf16_f32 v145, v154, v155
v_mfma_f32_32x32x16_bf16 v[48:63], v[176:179], a[180:183], v[48:63]
ds_read_b64_tr_b16 v[172:173], v7 offset:34816
ds_read_b64_tr_b16 v[174:175], v7 offset:35072
v_fmac_f32 v132, v20, v208
v_fmac_f32 v140, v20, v212
v_fmac_f32 v133, v20, v209
v_fmac_f32 v141, v20, v213
s_waitcnt lgkmcnt(8)
v_mfma_f32_16x16x32_bf16 v[220:223], v[192:195], v[28:31], v[220:223]
ds_read_b64_tr_b16 v[196:197], v12 offset:36864
v_mfma_f32_32x32x16_bf16 v[64:79], v[180:183], a[176:179], v[64:79]
ds_read_b64_tr_b16 v[198:199], v12 offset:38912
v_mul_f32 v140, v132, v140
v_mul_f32 v132, v21, v132
v_mul_f32 v141, v133, v141
v_mul_f32 v133, v21, v133
s_sub_u32 s71, s71, 4096
v_mfma_f32_32x32x16_bf16 v[80:95], v[180:183], a[180:183], v[80:95]
s_add_u32 s62, s60, 524288
buffer_load_dwordx4 a[32:35], v2, s[96:99], s62 offen sc1
v_exp_f32 v132, v132
v_exp_f32 v133, v133
v_mfma_f32_32x32x16_bf16 v[32:47], v[184:187], a[184:187], v[32:47]
ds_read_b64_tr_b16 v[176:177], v6 offset:36864
ds_read_b64_tr_b16 v[178:179], v6 offset:37120
v_fma_f32 v132, v132, v22, v22
v_fma_f32 v133, v133, v22, v22
v_rcp_f32 v132, v132
v_readlane_b32 s77, v0, 54
)ASM"
R"ASM(
v_readlane_b32 s78, v0, 55
v_lshrrev_b32 v217, 5, v1
s_lshl_b32 s76, s40, 1
v_add_u32 v217, s76, v217
v_cmp_gt_u32 vcc, s78, v217
v_sub_u32 v217, 7, v217
v_lshlrev_b32 v217, 9, v217
v_and_b32 v23, 31, v1
v_lshl_add_u32 v217, v23, 4, v217
v_mov_b32 v23, 0x7f000000
v_cndmask_b32 v217, v23, v217, vcc
s_lshl_b32 s77, s77, 3
s_add_u32 s77, s77, s78
s_lshl_b32 s77, s77, 9
s_add_u32 s77, s77, 49278976
s_mov_b32 s78, m0
s_lshl_b32 s76, s40, 10
s_add_u32 m0, s76, 155648
s_mov_b64 exec, s[58:59]
buffer_load_dwordx4 v217, s[96:99], s77 offen sc1 lds
s_mov_b64 exec, -1
s_mov_b32 m0, s78
v_mfma_f32_32x32x16_bf16 v[48:63], v[184:187], a[188:191], v[48:63]
ds_read_b64_tr_b16 v[180:181], v7 offset:36864
ds_read_b64_tr_b16 v[182:183], v7 offset:37120
v_rcp_f32 v133, v133
v_mul_f32 v140, v140, v132
v_mul_f32 v141, v141, v133
v_mfma_f32_32x32x16_bf16 v[64:79], v[188:191], a[184:187], v[64:79]
buffer_load_dwordx4 a[36:39], v2, s[96:99], s62 offen offset:512 sc1
ds_read_b64_tr_b16 v[192:193], v12 offset:40960
ds_read_b64_tr_b16 v[194:195], v12 offset:43008
v_cvt_pk_bf16_f32 v130, v140, v141
v_fmac_f32 v134, v20, v210
v_mfma_f32_32x32x16_bf16 v[80:95], v[188:191], a[188:191], v[80:95]
s_waitcnt lgkmcnt(8)
v_fmac_f32 v142, v20, v214
v_fmac_f32 v135, v20, v211
v_fmac_f32 v143, v20, v215
v_mul_f32 v142, v134, v142
v_mul_f32 v134, v21, v134
s_mov_b64 exec, s[58:59]
buffer_load_dwordx4 v[96:99], v216, s[96:99], s71 offen offset:0
s_mov_b64 exec, -1
v_mfma_f32_32x32x16_bf16 v[32:47], v[160:163], a[192:195], v[32:47]
ds_read_b64_tr_b16 v[184:185], v6 offset:38912
ds_read_b64_tr_b16 v[186:187], v6 offset:39168
v_mul_f32 v143, v135, v143
v_mul_f32 v135, v21, v135
v_exp_f32 v134, v134
v_mfma_f32_32x32x16_bf16 v[48:63], v[160:163], a[196:199], v[48:63]
ds_read_b64_tr_b16 v[188:189], v7 offset:38912
ds_read_b64_tr_b16 v[190:191], v7 offset:39168
v_exp_f32 v135, v135
v_fma_f32 v134, v134, v22, v22
v_fma_f32 v135, v135, v22, v22
s_waitcnt lgkmcnt(10)
v_mfma_f32_16x16x32_bf16 v[220:223], v[196:199], v[28:31], v[220:223]
s_add_u32 m0, s61, 131072
s_mov_b64 exec, s[58:59]
buffer_load_dwordx4 v[100:103], v216, s[96:99], s71 offen offset:1024
s_mov_b64 exec, -1
v_mfma_f32_32x32x16_bf16 v[64:79], v[164:167], a[192:195], v[64:79]
buffer_load_dwordx4 v3, s[44:47], s10 offen offset:0 lds
v_rcp_f32 v134, v134
v_rcp_f32 v135, v135
v_mul_f32 v142, v142, v134
v_mfma_f32_32x32x16_bf16 v[80:95], v[164:167], a[196:199], v[80:95]
v_mul_f32 v143, v143, v135
v_cvt_pk_bf16_f32 v131, v142, v143
s_add_u32 s75, s69, 65536
buffer_store_dwordx4 v[128:131], v19, s[96:99], s75 offen offset:0 sc1
v_fmac_f32 v148, v24, v208
v_fmac_f32 v156, v24, v212
v_mfma_f32_32x32x16_bf16 v[32:47], v[168:171], a[200:203], v[32:47]
ds_read_b64_tr_b16 v[160:161], v6 offset:40960
ds_read_b64_tr_b16 v[162:163], v6 offset:41216
v_fmac_f32 v149, v24, v209
v_fmac_f32 v157, v24, v213
v_mul_f32 v156, v148, v156
v_mul_f32 v148, v25, v148
v_mfma_f32_32x32x16_bf16 v[48:63], v[168:171], a[204:207], v[48:63]
ds_read_b64_tr_b16 v[164:165], v7 offset:40960
ds_read_b64_tr_b16 v[166:167], v7 offset:41216
buffer_load_dwordx4 v4, s[44:47], s10 offen offset:1024 lds
v_mul_f32 v157, v149, v157
v_mul_f32 v149, v25, v149
s_waitcnt lgkmcnt(8)
v_mfma_f32_16x16x32_bf16 v[220:223], v[192:195], v[28:31], v[220:223]
ds_read_b64_tr_b16 v[196:197], v12 offset:45056
s_mov_b64 exec, s[58:59]
buffer_load_dwordx4 v[104:107], v216, s[96:99], s71 offen offset:2048
s_mov_b64 exec, -1
v_mfma_f32_32x32x16_bf16 v[64:79], v[172:175], a[200:203], v[64:79]
ds_read_b64_tr_b16 v[198:199], v12 offset:47104
v_exp_f32 v148, v148
v_exp_f32 v149, v149
v_fma_f32 v148, v148, v26, v26
s_mov_b64 exec, s[58:59]
buffer_load_dwordx4 v[108:111], v216, s[96:99], s71 offen offset:3072
s_mov_b64 exec, -1
s_add_u32 s71, s71, 4096
v_mfma_f32_32x32x16_bf16 v[80:95], v[172:175], a[204:207], v[80:95]
s_waitcnt lgkmcnt(6)
s_add_u32 s62, s60, 655360
buffer_load_dwordx4 a[40:43], v2, s[96:99], s62 offen sc1
v_fma_f32 v149, v149, v26, v26
v_rcp_f32 v148, v148
v_mfma_f32_32x32x16_bf16 v[32:47], v[176:179], a[208:211], v[32:47]
ds_read_b64_tr_b16 v[168:169], v6 offset:43008
ds_read_b64_tr_b16 v[170:171], v6 offset:43264
v_rcp_f32 v149, v149
v_mul_f32 v156, v156, v148
v_mul_f32 v157, v157, v149
v_mfma_f32_32x32x16_bf16 v[48:63], v[176:179], a[212:215], v[48:63]
ds_read_b64_tr_b16 v[172:173], v7 offset:43008
ds_read_b64_tr_b16 v[174:175], v7 offset:43264
v_cvt_pk_bf16_f32 v146, v156, v157
v_fmac_f32 v150, v24, v210
v_fmac_f32 v158, v24, v214
v_fmac_f32 v151, v24, v211
v_mfma_f32_32x32x16_bf16 v[64:79], v[180:183], a[208:211], v[64:79]
buffer_load_dwordx4 a[44:47], v2, s[96:99], s62 offen offset:512 sc1
v_fmac_f32 v159, v24, v215
v_mul_f32 v158, v150, v158
v_mul_f32 v150, v25, v150
v_mul_f32 v159, v151, v159
v_mul_f32 v151, v25, v151
v_mfma_f32_32x32x16_bf16 v[80:95], v[180:183], a[212:215], v[80:95]
v_exp_f32 v150, v150
v_exp_f32 v151, v151
v_fma_f32 v150, v150, v26, v26
v_fma_f32 v151, v151, v26, v26
s_mov_b64 exec, s[58:59]
buffer_load_dwordx4 v[112:115], v216, s[96:99], s71 offen offset:0
s_mov_b64 exec, -1
v_mfma_f32_32x32x16_bf16 v[32:47], v[184:187], a[216:219], v[32:47]
ds_read_b64_tr_b16 v[176:177], v6 offset:45056
ds_read_b64_tr_b16 v[178:179], v6 offset:45312
v_rcp_f32 v150, v150
v_rcp_f32 v151, v151
v_mfma_f32_32x32x16_bf16 v[48:63], v[184:187], a[220:223], v[48:63]
ds_read_b64_tr_b16 v[180:181], v7 offset:45056
ds_read_b64_tr_b16 v[182:183], v7 offset:45312
v_mul_f32 v158, v158, v150
v_mul_f32 v159, v159, v151
v_cvt_pk_bf16_f32 v147, v158, v159
s_add_u32 s75, s69, 65536
s_waitcnt lgkmcnt(8)
v_mfma_f32_16x16x32_bf16 v[220:223], v[196:199], v[28:31], v[220:223]
ds_read_b64_tr_b16 v[192:193], v13 offset:0
v_mfma_f32_32x32x16_bf16 v[64:79], v[188:191], a[216:219], v[64:79]
s_add_u32 m0, s61, 139264
s_nop 0
buffer_load_dwordx4 v3, s[44:47], s11 offen offset:0 lds
ds_read_b64_tr_b16 v[194:195], v13 offset:2048
buffer_store_dwordx4 v[144:147], v19, s[96:99], s75 offen offset:1024 sc1
v_mfma_f32_32x32x16_bf16 v[80:95], v[188:191], a[220:223], v[80:95]
s_waitcnt lgkmcnt(6)
s_mov_b64 exec, s[58:59]
buffer_load_dwordx4 v[116:119], v216, s[96:99], s71 offen offset:1024
s_mov_b64 exec, -1
v_mfma_f32_32x32x16_bf16 v[32:47], v[160:163], a[224:227], v[32:47]
ds_read_b64_tr_b16 v[184:185], v6 offset:47104
ds_read_b64_tr_b16 v[186:187], v6 offset:47360
s_mov_b64 exec, s[58:59]
buffer_load_dwordx4 v[120:123], v216, s[96:99], s71 offen offset:2048
s_mov_b64 exec, -1
v_mfma_f32_32x32x16_bf16 v[48:63], v[160:163], a[228:231], v[48:63]
ds_read_b64_tr_b16 v[188:189], v7 offset:47104
ds_read_b64_tr_b16 v[190:191], v7 offset:47360
buffer_load_dwordx4 v4, s[44:47], s11 offen offset:1024 lds
v_mfma_f32_32x32x16_bf16 v[64:79], v[164:167], a[224:227], v[64:79]
v_mfma_f32_32x32x16_bf16 v[80:95], v[164:167], a[228:231], v[80:95]
v_mfma_f32_32x32x16_bf16 v[32:47], v[168:171], a[232:235], v[32:47]
ds_read_b64_tr_b16 v[160:161], v8 offset:0
ds_read_b64_tr_b16 v[162:163], v8 offset:256
s_add_u32 s62, s60, 786432
buffer_load_dwordx4 a[48:51], v2, s[96:99], s62 offen sc1
buffer_load_dword v17, v15, s[96:99], 0 offen sc1
v_mfma_f32_32x32x16_bf16 v[48:63], v[168:171], a[236:239], v[48:63]
ds_read_b64_tr_b16 v[164:165], v9 offset:0
ds_read_b64_tr_b16 v[166:167], v9 offset:256
s_waitcnt lgkmcnt(8)
v_mfma_f32_16x16x32_bf16 v[220:223], v[192:195], v[28:31], v[220:223]
ds_read_b64_tr_b16 v[196:197], v13 offset:4096
v_mfma_f32_32x32x16_bf16 v[64:79], v[172:175], a[232:235], v[64:79]
buffer_load_dwordx4 a[52:55], v2, s[96:99], s62 offen offset:512 sc1
ds_read_b64_tr_b16 v[198:199], v13 offset:6144
v_mfma_f32_32x32x16_bf16 v[80:95], v[172:175], a[236:239], v[80:95]
s_waitcnt lgkmcnt(6)
s_mov_b64 exec, s[58:59]
buffer_load_dwordx4 v[124:127], v216, s[96:99], s71 offen offset:3072
s_mov_b64 exec, -1
v_mfma_f32_32x32x16_bf16 v[32:47], v[176:179], a[240:243], v[32:47]
ds_read_b64_tr_b16 v[168:169], v8 offset:2048
ds_read_b64_tr_b16 v[170:171], v8 offset:2304
v_mfma_f32_32x32x16_bf16 v[48:63], v[176:179], a[244:247], v[48:63]
ds_read_b64_tr_b16 v[172:173], v9 offset:2048
ds_read_b64_tr_b16 v[174:175], v9 offset:2304
)ASM"
R"ASM(
v_mfma_f32_32x32x16_bf16 v[64:79], v[180:183], a[240:243], v[64:79]
s_add_u32 s62, s60, 917504
buffer_load_dwordx4 a[56:59], v2, s[96:99], s62 offen sc1
v_mfma_f32_32x32x16_bf16 v[80:95], v[180:183], a[244:247], v[80:95]
v_mfma_f32_32x32x16_bf16 v[32:47], v[184:187], a[248:251], v[32:47]
ds_read_b64_tr_b16 v[176:177], v8 offset:4096
ds_read_b64_tr_b16 v[178:179], v8 offset:4352
v_mfma_f32_32x32x16_bf16 v[48:63], v[184:187], a[252:255], v[48:63]
ds_read_b64_tr_b16 v[180:181], v9 offset:4096
ds_read_b64_tr_b16 v[182:183], v9 offset:4352
buffer_load_dwordx4 a[60:63], v2, s[96:99], s62 offen offset:512 sc1
s_waitcnt lgkmcnt(8)
v_mfma_f32_16x16x32_bf16 v[220:223], v[196:199], v[28:31], v[220:223]
v_mfma_f32_32x32x16_bf16 v[64:79], v[188:191], a[248:251], v[64:79]
v_mfma_f32_32x32x16_bf16 v[80:95], v[188:191], a[252:255], v[80:95]
s_waitcnt lgkmcnt(4)
v_mfma_f32_32x32x16_bf16 v[32:47], v[160:163], v[224:227], v[32:47]
ds_read_b64_tr_b16 v[184:185], v8 offset:6144
ds_read_b64_tr_b16 v[186:187], v8 offset:6400
s_add_u32 s62, s60, 1048576
buffer_load_dwordx4 a[64:67], v2, s[96:99], s62 offen sc1
v_mfma_f32_32x32x16_bf16 v[48:63], v[160:163], v[228:231], v[48:63]
ds_read_b64_tr_b16 v[188:189], v9 offset:6144
ds_read_b64_tr_b16 v[190:191], v9 offset:6400
v_mfma_f32_32x32x16_bf16 v[64:79], v[164:167], v[224:227], v[64:79]
ds_write_b128 v18, v[220:223] offset:0
v_mfma_f32_32x32x16_bf16 v[80:95], v[164:167], v[228:231], v[80:95]
buffer_load_dwordx4 a[68:71], v2, s[96:99], s62 offen offset:512 sc1
v_mfma_f32_32x32x16_bf16 v[32:47], v[168:171], v[232:235], v[32:47]
s_waitcnt lgkmcnt(0)
s_barrier
v_mfma_f32_32x32x16_bf16 v[48:63], v[168:171], v[236:239], v[48:63]
v_lshrrev_b32 v23, 4, v1
v_bfe_u32 v221, v1, 2, 2
v_and_b32 v217, 3, v1
v_lshlrev_b32 v216, 11, v23
v_lshl_add_u32 v216, v221, 7, v216
v_lshl_add_u32 v216, v217, 3, v216
v_mfma_f32_32x32x16_bf16 v[64:79], v[172:175], v[232:235], v[64:79]
v_lshrrev_b32 v217, 1, v221
v_and_b32 v23, 1, v23
v_lshl_add_u32 v217, v23, 1, v217
s_add_u32 s76, s68, 0
s_and_b32 s76, s76, 3
v_xor_b32 v23, s76, v217
v_mfma_f32_32x32x16_bf16 v[80:95], v[172:175], v[236:239], v[80:95]
s_add_u32 s62, s60, 1179648
buffer_load_dwordx4 a[72:75], v2, s[96:99], s62 offen sc1
v_lshl_add_u32 v192, v23, 5, v216
v_add_u32 v196, 49152, v192
v_add_u32 v28, 98304, v192
v_mfma_f32_32x32x16_bf16 v[32:47], v[176:179], v[240:243], v[32:47]
s_add_u32 s76, s68, 1
s_and_b32 s76, s76, 3
v_xor_b32 v23, s76, v217
v_lshl_add_u32 v193, v23, 5, v216
v_add_u32 v197, 49152, v193
v_add_u32 v29, 98304, v193
v_mfma_f32_32x32x16_bf16 v[48:63], v[176:179], v[244:247], v[48:63]
s_add_u32 s76, s68, 2
s_and_b32 s76, s76, 3
v_xor_b32 v23, s76, v217
v_lshl_add_u32 v194, v23, 5, v216
v_add_u32 v198, 49152, v194
v_add_u32 v30, 98304, v194
v_mfma_f32_32x32x16_bf16 v[64:79], v[180:183], v[240:243], v[64:79]
buffer_load_dwordx4 a[76:79], v2, s[96:99], s62 offen offset:512 sc1
s_add_u32 s76, s68, 3
s_and_b32 s76, s76, 3
v_xor_b32 v23, s76, v217
v_lshl_add_u32 v195, v23, 5, v216
v_add_u32 v199, 49152, v195
v_mfma_f32_32x32x16_bf16 v[80:95], v[180:183], v[244:247], v[80:95]
v_add_u32 v31, 98304, v195
v_mfma_f32_32x32x16_bf16 v[32:47], v[184:187], v[248:251], v[32:47]
v_mfma_f32_32x32x16_bf16 v[48:63], v[184:187], v[252:255], v[48:63]
v_mfma_f32_32x32x16_bf16 v[64:79], v[188:191], v[248:251], v[64:79]
v_mfma_f32_32x32x16_bf16 v[80:95], v[188:191], v[252:255], v[80:95]
ds_read_b128 v[200:203], v16 offset:0
ds_read_b128 v[204:207], v16 offset:64
ds_read_b128 v[208:211], v16 offset:16
ds_read_b128 v[212:215], v16 offset:80
s_mov_b32 s65, 20000
s_waitcnt vmcnt(7)
LSC2_%=:
v_cmp_ne_u32 s[92:93], s43, v17
s_nop 3
s_and_b32 s92, s92, 0x3c00
s_and_b32 s93, s93, 0x0
s_cmp_eq_u64 s[92:93], 0
s_cbranch_scc1 LSD2_%=
s_sub_u32 s65, s65, 1
s_cmp_eq_u32 s65, 0
s_cbranch_scc1 LSD2_%=
s_sleep 2
buffer_load_dword v17, v15, s[96:99], 0 offen sc1
s_waitcnt vmcnt(0)
s_branch LSC2_%=
LSD2_%=:
s_waitcnt lgkmcnt(3)
v_pk_fma_f32 v[32:33], v[20:21], v[200:201], v[32:33] op_sel:[0,0,0] op_sel_hi:[0,1,1]
s_waitcnt lgkmcnt(2)
v_pk_fma_f32 v[40:41], v[20:21], v[204:205], v[40:41] op_sel:[0,0,0] op_sel_hi:[0,1,1]
v_pk_fma_f32 v[34:35], v[20:21], v[202:203], v[34:35] op_sel:[0,0,0] op_sel_hi:[0,1,1]
v_pk_fma_f32 v[42:43], v[20:21], v[206:207], v[42:43] op_sel:[0,0,0] op_sel_hi:[0,1,1]
v_pk_fma_f32 v[48:49], v[24:25], v[200:201], v[48:49] op_sel:[0,0,0] op_sel_hi:[0,1,1]
v_pk_fma_f32 v[56:57], v[24:25], v[204:205], v[56:57] op_sel:[0,0,0] op_sel_hi:[0,1,1]
v_pk_fma_f32 v[50:51], v[24:25], v[202:203], v[50:51] op_sel:[0,0,0] op_sel_hi:[0,1,1]
v_pk_fma_f32 v[58:59], v[24:25], v[206:207], v[58:59] op_sel:[0,0,0] op_sel_hi:[0,1,1]
ds_read_b128 v[200:203], v16 offset:128
ds_read_b128 v[204:207], v16 offset:192
v_pk_mul_f32 v[40:41], v[32:33], v[40:41] op_sel:[0,0] op_sel_hi:[1,1]
v_pk_mul_f32 v[32:33], v[20:21], v[32:33] op_sel:[1,0] op_sel_hi:[1,1]
v_pk_mul_f32 v[42:43], v[34:35], v[42:43] op_sel:[0,0] op_sel_hi:[1,1]
v_pk_mul_f32 v[34:35], v[20:21], v[34:35] op_sel:[1,0] op_sel_hi:[1,1]
v_pk_mul_f32 v[56:57], v[48:49], v[56:57] op_sel:[0,0] op_sel_hi:[1,1]
v_pk_mul_f32 v[48:49], v[24:25], v[48:49] op_sel:[1,0] op_sel_hi:[1,1]
v_pk_mul_f32 v[58:59], v[50:51], v[58:59] op_sel:[0,0] op_sel_hi:[1,1]
v_pk_mul_f32 v[50:51], v[24:25], v[50:51] op_sel:[1,0] op_sel_hi:[1,1]
s_add_u32 s62, s60, 1310720
buffer_load_dwordx4 a[80:83], v2, s[96:99], s62 offen sc1
v_exp_f32 v32, v32
v_exp_f32 v33, v33
v_exp_f32 v34, v34
v_exp_f32 v35, v35
v_exp_f32 v48, v48
v_exp_f32 v49, v49
v_exp_f32 v50, v50
v_exp_f32 v51, v51
v_pk_fma_f32 v[32:33], v[32:33], v[22:23], v[22:23] op_sel:[0,0,0] op_sel_hi:[1,0,0]
v_pk_fma_f32 v[34:35], v[34:35], v[22:23], v[22:23] op_sel:[0,0,0] op_sel_hi:[1,0,0]
v_pk_fma_f32 v[48:49], v[48:49], v[26:27], v[26:27] op_sel:[0,0,0] op_sel_hi:[1,0,0]
v_pk_fma_f32 v[50:51], v[50:51], v[26:27], v[26:27] op_sel:[0,0,0] op_sel_hi:[1,0,0]
buffer_load_dwordx4 a[84:87], v2, s[96:99], s62 offen offset:512 sc1
v_rcp_f32 v32, v32
v_rcp_f32 v33, v33
v_rcp_f32 v34, v34
v_rcp_f32 v35, v35
v_rcp_f32 v48, v48
v_rcp_f32 v49, v49
v_rcp_f32 v50, v50
v_rcp_f32 v51, v51
v_pk_mul_f32 v[40:41], v[40:41], v[32:33] op_sel:[0,0] op_sel_hi:[1,1]
v_pk_mul_f32 v[42:43], v[42:43], v[34:35] op_sel:[0,0] op_sel_hi:[1,1]
v_pk_mul_f32 v[56:57], v[56:57], v[48:49] op_sel:[0,0] op_sel_hi:[1,1]
v_pk_mul_f32 v[58:59], v[58:59], v[50:51] op_sel:[0,0] op_sel_hi:[1,1]
s_add_u32 s62, s60, 1441792
buffer_load_dwordx4 a[88:91], v2, s[96:99], s62 offen sc1
v_cvt_pk_bf16_f32 v32, v40, v41
v_cvt_pk_bf16_f32 v33, v42, v43
v_cvt_pk_bf16_f32 v48, v56, v57
v_cvt_pk_bf16_f32 v49, v58, v59
s_waitcnt lgkmcnt(3)
v_pk_fma_f32 v[36:37], v[20:21], v[208:209], v[36:37] op_sel:[0,0,0] op_sel_hi:[0,1,1]
s_waitcnt lgkmcnt(2)
v_pk_fma_f32 v[44:45], v[20:21], v[212:213], v[44:45] op_sel:[0,0,0] op_sel_hi:[0,1,1]
v_pk_fma_f32 v[38:39], v[20:21], v[210:211], v[38:39] op_sel:[0,0,0] op_sel_hi:[0,1,1]
v_pk_fma_f32 v[46:47], v[20:21], v[214:215], v[46:47] op_sel:[0,0,0] op_sel_hi:[0,1,1]
v_pk_fma_f32 v[52:53], v[24:25], v[208:209], v[52:53] op_sel:[0,0,0] op_sel_hi:[0,1,1]
v_pk_fma_f32 v[60:61], v[24:25], v[212:213], v[60:61] op_sel:[0,0,0] op_sel_hi:[0,1,1]
v_pk_fma_f32 v[54:55], v[24:25], v[210:211], v[54:55] op_sel:[0,0,0] op_sel_hi:[0,1,1]
v_pk_fma_f32 v[62:63], v[24:25], v[214:215], v[62:63] op_sel:[0,0,0] op_sel_hi:[0,1,1]
ds_read_b128 v[208:211], v16 offset:144
ds_read_b128 v[212:215], v16 offset:208
v_pk_mul_f32 v[44:45], v[36:37], v[44:45] op_sel:[0,0] op_sel_hi:[1,1]
v_pk_mul_f32 v[36:37], v[20:21], v[36:37] op_sel:[1,0] op_sel_hi:[1,1]
v_pk_mul_f32 v[46:47], v[38:39], v[46:47] op_sel:[0,0] op_sel_hi:[1,1]
v_pk_mul_f32 v[38:39], v[20:21], v[38:39] op_sel:[1,0] op_sel_hi:[1,1]
v_pk_mul_f32 v[60:61], v[52:53], v[60:61] op_sel:[0,0] op_sel_hi:[1,1]
v_pk_mul_f32 v[52:53], v[24:25], v[52:53] op_sel:[1,0] op_sel_hi:[1,1]
v_pk_mul_f32 v[62:63], v[54:55], v[62:63] op_sel:[0,0] op_sel_hi:[1,1]
v_pk_mul_f32 v[54:55], v[24:25], v[54:55] op_sel:[1,0] op_sel_hi:[1,1]
buffer_load_dwordx4 a[92:95], v2, s[96:99], s62 offen offset:512 sc1
v_exp_f32 v36, v36
v_exp_f32 v37, v37
v_exp_f32 v38, v38
v_exp_f32 v39, v39
v_exp_f32 v52, v52
v_exp_f32 v53, v53
v_exp_f32 v54, v54
v_exp_f32 v55, v55
v_pk_fma_f32 v[36:37], v[36:37], v[22:23], v[22:23] op_sel:[0,0,0] op_sel_hi:[1,0,0]
v_pk_fma_f32 v[38:39], v[38:39], v[22:23], v[22:23] op_sel:[0,0,0] op_sel_hi:[1,0,0]
v_pk_fma_f32 v[52:53], v[52:53], v[26:27], v[26:27] op_sel:[0,0,0] op_sel_hi:[1,0,0]
v_pk_fma_f32 v[54:55], v[54:55], v[26:27], v[26:27] op_sel:[0,0,0] op_sel_hi:[1,0,0]
s_add_u32 s62, s60, 1572864
buffer_load_dwordx4 a[96:99], v2, s[96:99], s62 offen sc1
v_rcp_f32 v36, v36
v_rcp_f32 v37, v37
v_rcp_f32 v38, v38
v_rcp_f32 v39, v39
v_rcp_f32 v52, v52
v_rcp_f32 v53, v53
v_rcp_f32 v54, v54
v_rcp_f32 v55, v55
v_pk_mul_f32 v[44:45], v[44:45], v[36:37] op_sel:[0,0] op_sel_hi:[1,1]
v_pk_mul_f32 v[46:47], v[46:47], v[38:39] op_sel:[0,0] op_sel_hi:[1,1]
v_pk_mul_f32 v[60:61], v[60:61], v[52:53] op_sel:[0,0] op_sel_hi:[1,1]
v_pk_mul_f32 v[62:63], v[62:63], v[54:55] op_sel:[0,0] op_sel_hi:[1,1]
buffer_load_dwordx4 a[100:103], v2, s[96:99], s62 offen offset:512 sc1
)ASM"
R"ASM(
v_cvt_pk_bf16_f32 v34, v44, v45
v_cvt_pk_bf16_f32 v35, v46, v47
v_cvt_pk_bf16_f32 v50, v60, v61
v_cvt_pk_bf16_f32 v51, v62, v63
s_waitcnt lgkmcnt(3)
v_pk_fma_f32 v[64:65], v[20:21], v[200:201], v[64:65] op_sel:[0,0,0] op_sel_hi:[0,1,1]
s_waitcnt lgkmcnt(2)
v_pk_fma_f32 v[72:73], v[20:21], v[204:205], v[72:73] op_sel:[0,0,0] op_sel_hi:[0,1,1]
v_pk_fma_f32 v[66:67], v[20:21], v[202:203], v[66:67] op_sel:[0,0,0] op_sel_hi:[0,1,1]
v_pk_fma_f32 v[74:75], v[20:21], v[206:207], v[74:75] op_sel:[0,0,0] op_sel_hi:[0,1,1]
v_pk_fma_f32 v[80:81], v[24:25], v[200:201], v[80:81] op_sel:[0,0,0] op_sel_hi:[0,1,1]
v_pk_fma_f32 v[88:89], v[24:25], v[204:205], v[88:89] op_sel:[0,0,0] op_sel_hi:[0,1,1]
v_pk_fma_f32 v[82:83], v[24:25], v[202:203], v[82:83] op_sel:[0,0,0] op_sel_hi:[0,1,1]
v_pk_fma_f32 v[90:91], v[24:25], v[206:207], v[90:91] op_sel:[0,0,0] op_sel_hi:[0,1,1]
v_pk_mul_f32 v[72:73], v[64:65], v[72:73] op_sel:[0,0] op_sel_hi:[1,1]
v_pk_mul_f32 v[64:65], v[20:21], v[64:65] op_sel:[1,0] op_sel_hi:[1,1]
v_pk_mul_f32 v[74:75], v[66:67], v[74:75] op_sel:[0,0] op_sel_hi:[1,1]
v_pk_mul_f32 v[66:67], v[20:21], v[66:67] op_sel:[1,0] op_sel_hi:[1,1]
v_pk_mul_f32 v[88:89], v[80:81], v[88:89] op_sel:[0,0] op_sel_hi:[1,1]
v_pk_mul_f32 v[80:81], v[24:25], v[80:81] op_sel:[1,0] op_sel_hi:[1,1]
v_pk_mul_f32 v[90:91], v[82:83], v[90:91] op_sel:[0,0] op_sel_hi:[1,1]
v_pk_mul_f32 v[82:83], v[24:25], v[82:83] op_sel:[1,0] op_sel_hi:[1,1]
s_add_u32 s62, s60, 1703936
buffer_load_dwordx4 a[104:107], v2, s[96:99], s62 offen sc1
v_exp_f32 v64, v64
v_exp_f32 v65, v65
v_exp_f32 v66, v66
v_exp_f32 v67, v67
v_exp_f32 v80, v80
v_exp_f32 v81, v81
v_exp_f32 v82, v82
v_exp_f32 v83, v83
v_pk_fma_f32 v[64:65], v[64:65], v[22:23], v[22:23] op_sel:[0,0,0] op_sel_hi:[1,0,0]
v_pk_fma_f32 v[66:67], v[66:67], v[22:23], v[22:23] op_sel:[0,0,0] op_sel_hi:[1,0,0]
v_pk_fma_f32 v[80:81], v[80:81], v[26:27], v[26:27] op_sel:[0,0,0] op_sel_hi:[1,0,0]
v_pk_fma_f32 v[82:83], v[82:83], v[26:27], v[26:27] op_sel:[0,0,0] op_sel_hi:[1,0,0]
buffer_load_dwordx4 a[108:111], v2, s[96:99], s62 offen offset:512 sc1
v_rcp_f32 v64, v64
v_rcp_f32 v65, v65
v_rcp_f32 v66, v66
v_rcp_f32 v67, v67
v_rcp_f32 v80, v80
v_rcp_f32 v81, v81
v_rcp_f32 v82, v82
v_rcp_f32 v83, v83
v_pk_mul_f32 v[72:73], v[72:73], v[64:65] op_sel:[0,0] op_sel_hi:[1,1]
v_pk_mul_f32 v[74:75], v[74:75], v[66:67] op_sel:[0,0] op_sel_hi:[1,1]
v_pk_mul_f32 v[88:89], v[88:89], v[80:81] op_sel:[0,0] op_sel_hi:[1,1]
v_pk_mul_f32 v[90:91], v[90:91], v[82:83] op_sel:[0,0] op_sel_hi:[1,1]
s_add_u32 m0, s61, 0
s_nop 0
buffer_load_dwordx4 v5, s[44:47], s10 offen offset:0 lds
v_cvt_pk_bf16_f32 v64, v72, v73
v_cvt_pk_bf16_f32 v65, v74, v75
v_cvt_pk_bf16_f32 v80, v88, v89
v_cvt_pk_bf16_f32 v81, v90, v91
s_waitcnt lgkmcnt(1)
v_pk_fma_f32 v[68:69], v[20:21], v[208:209], v[68:69] op_sel:[0,0,0] op_sel_hi:[0,1,1]
s_waitcnt lgkmcnt(0)
v_pk_fma_f32 v[76:77], v[20:21], v[212:213], v[76:77] op_sel:[0,0,0] op_sel_hi:[0,1,1]
v_pk_fma_f32 v[70:71], v[20:21], v[210:211], v[70:71] op_sel:[0,0,0] op_sel_hi:[0,1,1]
v_pk_fma_f32 v[78:79], v[20:21], v[214:215], v[78:79] op_sel:[0,0,0] op_sel_hi:[0,1,1]
v_pk_fma_f32 v[84:85], v[24:25], v[208:209], v[84:85] op_sel:[0,0,0] op_sel_hi:[0,1,1]
v_pk_fma_f32 v[92:93], v[24:25], v[212:213], v[92:93] op_sel:[0,0,0] op_sel_hi:[0,1,1]
v_pk_fma_f32 v[86:87], v[24:25], v[210:211], v[86:87] op_sel:[0,0,0] op_sel_hi:[0,1,1]
v_pk_fma_f32 v[94:95], v[24:25], v[214:215], v[94:95] op_sel:[0,0,0] op_sel_hi:[0,1,1]
v_pk_mul_f32 v[76:77], v[68:69], v[76:77] op_sel:[0,0] op_sel_hi:[1,1]
v_pk_mul_f32 v[68:69], v[20:21], v[68:69] op_sel:[1,0] op_sel_hi:[1,1]
v_pk_mul_f32 v[78:79], v[70:71], v[78:79] op_sel:[0,0] op_sel_hi:[1,1]
v_pk_mul_f32 v[70:71], v[20:21], v[70:71] op_sel:[1,0] op_sel_hi:[1,1]
v_pk_mul_f32 v[92:93], v[84:85], v[92:93] op_sel:[0,0] op_sel_hi:[1,1]
v_pk_mul_f32 v[84:85], v[24:25], v[84:85] op_sel:[1,0] op_sel_hi:[1,1]
v_pk_mul_f32 v[94:95], v[86:87], v[94:95] op_sel:[0,0] op_sel_hi:[1,1]
v_pk_mul_f32 v[86:87], v[24:25], v[86:87] op_sel:[1,0] op_sel_hi:[1,1]
buffer_load_dwordx4 v27, s[44:47], s11 offen offset:1024 lds
v_exp_f32 v68, v68
v_exp_f32 v69, v69
v_exp_f32 v70, v70
v_exp_f32 v71, v71
v_exp_f32 v84, v84
v_exp_f32 v85, v85
v_exp_f32 v86, v86
v_exp_f32 v87, v87
v_pk_fma_f32 v[68:69], v[68:69], v[22:23], v[22:23] op_sel:[0,0,0] op_sel_hi:[1,0,0]
v_pk_fma_f32 v[70:71], v[70:71], v[22:23], v[22:23] op_sel:[0,0,0] op_sel_hi:[1,0,0]
v_pk_fma_f32 v[84:85], v[84:85], v[26:27], v[26:27] op_sel:[0,0,0] op_sel_hi:[1,0,0]
v_pk_fma_f32 v[86:87], v[86:87], v[26:27], v[26:27] op_sel:[0,0,0] op_sel_hi:[1,0,0]
s_add_u32 m0, s61, 8192
s_nop 0
buffer_load_dwordx4 v3, s[44:47], s12 offen offset:0 lds
v_rcp_f32 v68, v68
v_rcp_f32 v69, v69
v_rcp_f32 v70, v70
v_rcp_f32 v71, v71
v_rcp_f32 v84, v84
v_rcp_f32 v85, v85
v_rcp_f32 v86, v86
v_rcp_f32 v87, v87
v_pk_mul_f32 v[76:77], v[76:77], v[68:69] op_sel:[0,0] op_sel_hi:[1,1]
v_pk_mul_f32 v[78:79], v[78:79], v[70:71] op_sel:[0,0] op_sel_hi:[1,1]
v_pk_mul_f32 v[92:93], v[92:93], v[84:85] op_sel:[0,0] op_sel_hi:[1,1]
v_pk_mul_f32 v[94:95], v[94:95], v[86:87] op_sel:[0,0] op_sel_hi:[1,1]
buffer_load_dwordx4 v4, s[44:47], s12 offen offset:1024 lds
v_cvt_pk_bf16_f32 v66, v76, v77
v_cvt_pk_bf16_f32 v67, v78, v79
v_cvt_pk_bf16_f32 v82, v92, v93
v_cvt_pk_bf16_f32 v83, v94, v95
s_add_u32 m0, s61, 16384
s_nop 0
buffer_load_dwordx4 v3, s[44:47], s13 offen offset:0 lds
buffer_load_dwordx4 v4, s[44:47], s13 offen offset:1024 lds
s_waitcnt vmcnt(20)
s_mov_b64 exec, 1
buffer_store_dword v219, off, s[96:99], s79 sc1
s_mov_b64 exec, -1
buffer_store_dwordx4 v[32:35], v19, s[96:99], s83 offen offset:0 sc1
buffer_store_dwordx4 v[48:51], v19, s[96:99], s83 offen offset:1024 sc1
s_add_u32 s75, s83, 65536
buffer_store_dwordx4 v[64:67], v19, s[96:99], s75 offen offset:0 sc1
s_add_u32 s75, s83, 65536
buffer_store_dwordx4 v[80:83], v19, s[96:99], s75 offen offset:1024 sc1
v_readlane_b32 s48, v0, 2
v_readlane_b32 s49, v0, 3
v_readlane_b32 s84, v0, 4
v_readlane_b32 s85, v0, 5
v_readlane_b32 s86, v0, 6
v_readlane_b32 s70, v0, 12
v_readlane_b32 s95, v0, 13
s_mov_b32 s51, 0x20000
s_mov_b32 s87, 0x20000
s_mov_b32 s50, 665600
v_readlane_b32 s52, v0, 7
v_readlane_b32 s53, v0, 8
v_readlane_b32 s56, v0, 9
v_readlane_b32 s57, v0, 10
v_readlane_b32 s66, v0, 14
v_readlane_b32 s67, v0, 15
s_mov_b32 s55, 0x20000
s_mov_b32 s59, 0x20000
s_mov_b32 s54, 2359296
s_mov_b32 s58, 4718592
v_and_b32 v13, 15, v1
v_lshrrev_b32 v14, 4, v1
v_mul_u32_u24 v8, 1152, v13
v_lshl_add_u32 v8, v14, 3, v8
v_lshlrev_b32 v14, 3, v14
v_lshlrev_b32 v13, 4, v13
v_readlane_b32 s88, v0, 18
v_readlane_b32 s89, v0, 19
v_readlane_b32 s90, v0, 20
s_nop 1
s_nop 0
s_add_u32 s91, s88, 18432
s_waitcnt lgkmcnt(0)
s_barrier
s_cmp_eq_u32 s42, 0
s_cbranch_scc0 LSF_%=
buffer_load_dwordx2 v[32:33], v8, s[52:55], s88 offen offset:0
buffer_load_dwordx2 v[34:35], v8, s[52:55], s91 offen offset:0
buffer_load_dwordx2 v[44:45], v14, s[84:87], s89 offen offset:0
buffer_load_dwordx2 v[36:37], v8, s[52:55], s88 offen offset:128
buffer_load_dwordx2 v[38:39], v8, s[52:55], s91 offen offset:128
buffer_load_dwordx2 v[46:47], v14, s[84:87], s89 offen offset:128
buffer_load_dwordx2 v[40:41], v8, s[52:55], s88 offen offset:256
buffer_load_dwordx2 v[42:43], v8, s[52:55], s91 offen offset:256
buffer_load_dwordx2 v[48:49], v14, s[84:87], s89 offen offset:256
buffer_load_dwordx4 v[50:53], v13, s[96:99], s90 offen offset:0 sc1
buffer_load_dwordx4 v[54:57], v13, s[96:99], s90 offen offset:256 sc1
s_branch LNS_%=
LSF_%=:
v_readlane_b32 s76, v0, 54
v_readlane_b32 s77, v0, 55
v_and_b32 v176, 31, v1
v_lshlrev_b32 v177, 3, v176
v_add_u32 v177, 152576, v177
v_lshlrev_b32 v176, 6, v176
v_add_u32 v176, 150528, v176
ds_read_b128 v[160:163], v176 offset:0
ds_read_b128 v[164:167], v176 offset:16
ds_read_b128 v[168:171], v176 offset:32
v_and_b32 v217, 31, v1
v_lshlrev_b32 v217, 4, v217
v_add_u32 v172, 155648, v217
ds_read_b128 v[224:227], v172 offset:0
ds_read_b128 v[228:231], v172 offset:512
ds_read_b128 v[232:235], v172 offset:1024
ds_read_b128 v[236:239], v172 offset:1536
ds_read_b128 v[240:243], v172 offset:2048
ds_read_b128 v[244:247], v172 offset:2560
ds_read_b128 v[248:251], v172 offset:3072
s_lshl_b32 s76, s76, 3
s_add_u32 s76, s76, s77
s_lshl_b32 s76, s76, 9
s_add_u32 s76, s76, 49283072
s_mov_b32 s73, s76
buffer_load_dwordx2 v[32:33], v8, s[52:55], s88 offen offset:0
buffer_load_dwordx2 v[34:35], v8, s[52:55], s91 offen offset:0
buffer_load_dwordx2 v[44:45], v14, s[84:87], s89 offen offset:0
s_mov_b32 s78, 20000
s_waitcnt lgkmcnt(0)
)ASM"
R"ASM(
s_branch LEK_%=
LEG_%=:
s_sleep 2
s_sub_u32 s72, s73, 512
s_cmp_gt_u32 s77, 0
s_cselect_b32 s72, s72, 1073741824
buffer_load_dwordx4 v[224:227], v217, s[96:99], s72 offen sc1
s_sub_u32 s72, s73, 1024
s_cmp_gt_u32 s77, 1
s_cselect_b32 s72, s72, 1073741824
buffer_load_dwordx4 v[228:231], v217, s[96:99], s72 offen sc1
s_sub_u32 s72, s73, 1536
s_cmp_gt_u32 s77, 2
s_cselect_b32 s72, s72, 1073741824
buffer_load_dwordx4 v[232:235], v217, s[96:99], s72 offen sc1
s_sub_u32 s72, s73, 2048
s_cmp_gt_u32 s77, 3
s_cselect_b32 s72, s72, 1073741824
buffer_load_dwordx4 v[236:239], v217, s[96:99], s72 offen sc1
s_sub_u32 s72, s73, 2560
s_cmp_gt_u32 s77, 4
s_cselect_b32 s72, s72, 1073741824
buffer_load_dwordx4 v[240:243], v217, s[96:99], s72 offen sc1
s_sub_u32 s72, s73, 3072
s_cmp_gt_u32 s77, 5
s_cselect_b32 s72, s72, 1073741824
buffer_load_dwordx4 v[244:247], v217, s[96:99], s72 offen sc1
s_sub_u32 s72, s73, 3584
s_cmp_gt_u32 s77, 6
s_cselect_b32 s72, s72, 1073741824
buffer_load_dwordx4 v[248:251], v217, s[96:99], s72 offen sc1
s_waitcnt vmcnt(0)
LEK_%=:
v_mov_b32 v174, 0
s_cmp_gt_u32 s77, 0
s_cselect_b32 s76, s43, 0
v_xor_b32 v175, s76, v225
v_xor_b32 v172, s76, v227
v_or3_b32 v174, v174, v175, v172
s_cmp_gt_u32 s77, 1
s_cselect_b32 s76, s43, 0
v_xor_b32 v175, s76, v229
v_xor_b32 v172, s76, v231
v_or3_b32 v174, v174, v175, v172
s_cmp_gt_u32 s77, 2
s_cselect_b32 s76, s43, 0
v_xor_b32 v175, s76, v233
v_xor_b32 v172, s76, v235
v_or3_b32 v174, v174, v175, v172
s_cmp_gt_u32 s77, 3
s_cselect_b32 s76, s43, 0
v_xor_b32 v175, s76, v237
v_xor_b32 v172, s76, v239
v_or3_b32 v174, v174, v175, v172
s_cmp_gt_u32 s77, 4
s_cselect_b32 s76, s43, 0
v_xor_b32 v175, s76, v241
v_xor_b32 v172, s76, v243
v_or3_b32 v174, v174, v175, v172
s_cmp_gt_u32 s77, 5
s_cselect_b32 s76, s43, 0
v_xor_b32 v175, s76, v245
v_xor_b32 v172, s76, v247
v_or3_b32 v174, v174, v175, v172
s_cmp_gt_u32 s77, 6
s_cselect_b32 s76, s43, 0
v_xor_b32 v175, s76, v249
v_xor_b32 v172, s76, v251
v_or3_b32 v174, v174, v175, v172
v_cmp_ne_u32 vcc, 0, v174
s_sub_u32 s78, s78, 1
s_cmp_eq_u32 s78, 0
s_cbranch_scc1 LEH_%=
s_nop 0
s_cmp_lg_u64 vcc, 0
s_cbranch_scc1 LEG_%=
LEH_%=:
v_fmac_f32 v244, v170, v248
v_fmac_f32 v246, v170, v250
v_fma_f32 v244, -v171, v250, v244
v_fmac_f32 v246, v171, v248
v_fmac_f32 v240, v170, v244
v_fmac_f32 v242, v170, v246
v_fma_f32 v240, -v171, v246, v240
v_fmac_f32 v242, v171, v244
v_fmac_f32 v236, v170, v240
v_fmac_f32 v238, v170, v242
v_fma_f32 v236, -v171, v242, v236
v_fmac_f32 v238, v171, v240
v_fmac_f32 v232, v170, v236
v_fmac_f32 v234, v170, v238
v_fma_f32 v232, -v171, v238, v232
v_fmac_f32 v234, v171, v236
v_fmac_f32 v228, v170, v232
v_fmac_f32 v230, v170, v234
v_fma_f32 v228, -v171, v234, v228
v_fmac_f32 v230, v171, v232
v_fmac_f32 v224, v170, v228
v_fmac_f32 v226, v170, v230
v_fma_f32 v224, -v171, v230, v224
v_fmac_f32 v226, v171, v228
ds_read_b64 v[228:229], v177 offset:0
ds_read_b64 v[230:231], v177 offset:256
ds_read_b64 v[232:233], v177 offset:512
s_waitcnt lgkmcnt(0)
s_cmp_gt_u32 s40, 0
s_cbranch_scc0 LEI_%=
v_fmac_f32 v228, v168, v224
v_fmac_f32 v229, v168, v226
v_fma_f32 v228, -v169, v226, v228
v_fmac_f32 v229, v169, v224
v_mov_b32 v224, v228
v_mov_b32 v226, v229
s_cmp_gt_u32 s40, 1
s_cbranch_scc0 LEI_%=
v_fmac_f32 v230, v168, v224
v_fmac_f32 v231, v168, v226
v_fma_f32 v230, -v169, v226, v230
v_fmac_f32 v231, v169, v224
v_mov_b32 v224, v230
v_mov_b32 v226, v231
s_cmp_gt_u32 s40, 2
s_cbranch_scc0 LEI_%=
v_fmac_f32 v232, v168, v224
v_fmac_f32 v233, v168, v226
v_fma_f32 v232, -v169, v226, v232
v_fmac_f32 v233, v169, v224
v_mov_b32 v224, v232
v_mov_b32 v226, v233
LEI_%=:
v_cmp_gt_u32 vcc, 32, v1
s_nop 1
v_cndmask_b32 v236, v166, v160, vcc
v_cndmask_b32 v237, v167, v161, vcc
v_mul_f32 v174, v164, v224
v_mul_f32 v175, v164, v226
v_fma_f32 v174, -v165, v226, v174
v_fmac_f32 v175, v165, v224
v_mul_f32 v172, v236, v174
v_mul_f32 v173, v236, v175
v_fma_f32 v172, -v237, v175, v172
v_fmac_f32 v173, v237, v174
s_lshl_b32 s76, s40, 13
s_add_u32 s76, s76, 24576
v_and_b32 v241, 31, v1
v_lshrrev_b32 v242, 5, v1
v_lshlrev_b32 v238, 5, v241
v_lshl_add_u32 v238, v242, 3, v238
v_add_u32 v238, s76, v238
v_lshrrev_b32 v241, 4, v1
v_bfe_u32 v242, v1, 2, 2
v_lshlrev_b32 v239, 8, v241
v_lshl_add_u32 v239, v242, 5, v239
v_and_b32 v242, 3, v1
v_lshl_add_u32 v239, v242, 3, v239
v_add_u32 v239, s76, v239
v_and_b32 v242, 15, v1
v_lshlrev_b32 v240, 5, v242
v_and_b32 v242, 1, v241
v_lshl_add_u32 v240, v242, 4, v240
v_lshrrev_b32 v242, 1, v241
v_lshl_add_u32 v240, v242, 16, v240
v_readlane_b32 s76, v0, 54
v_readlane_b32 s77, v0, 42
s_nop 2
s_lshl_b32 s76, s76, 17
s_lshl_b32 s77, s77, 5
s_add_u32 s76, s76, s77
s_add_u32 s76, s76, 18874368
s_add_u32 s77, s76, 2359296
v_lshlrev_b32 v128, 16, v96
v_lshlrev_b32 v129, 16, v98
v_and_b32 v130, 0xffff0000, v96
v_and_b32 v131, 0xffff0000, v98
v_lshlrev_b32 v132, 16, v97
v_lshlrev_b32 v133, 16, v99
v_and_b32 v134, 0xffff0000, v97
v_and_b32 v135, 0xffff0000, v99
v_pk_add_f32 v[128:129], v[128:129], v[172:173]
v_pk_mul_f32 v[174:175], v[160:161], v[172:173] op_sel:[0,0] op_sel_hi:[0,1]
v_pk_fma_f32 v[174:175], v[160:161], v[172:173], v[174:175] op_sel:[1,1,0] op_sel_hi:[1,0,1] neg_lo:[1,0,0]
v_pk_add_f32 v[130:131], v[130:131], v[174:175]
v_pk_mul_f32 v[172:173], v[160:161], v[174:175] op_sel:[0,0] op_sel_hi:[0,1]
v_pk_fma_f32 v[172:173], v[160:161], v[174:175], v[172:173] op_sel:[1,1,0] op_sel_hi:[1,0,1] neg_lo:[1,0,0]
v_pk_add_f32 v[132:133], v[132:133], v[172:173]
v_pk_mul_f32 v[174:175], v[160:161], v[172:173] op_sel:[0,0] op_sel_hi:[0,1]
v_pk_fma_f32 v[174:175], v[160:161], v[172:173], v[174:175] op_sel:[1,1,0] op_sel_hi:[1,0,1] neg_lo:[1,0,0]
v_pk_add_f32 v[134:135], v[134:135], v[174:175]
v_pk_mul_f32 v[172:173], v[166:167], v[174:175] op_sel:[0,0] op_sel_hi:[0,1]
v_pk_fma_f32 v[172:173], v[166:167], v[174:175], v[172:173] op_sel:[1,1,0] op_sel_hi:[1,0,1] neg_lo:[1,0,0]
v_cvt_pk_bf16_f32 v96, v128, v130
v_cvt_pk_bf16_f32 v97, v132, v134
v_cvt_pk_bf16_f32 v98, -v129, -v131
v_cvt_pk_bf16_f32 v99, -v133, -v135
buffer_load_dwordx2 v[36:37], v8, s[52:55], s88 offen offset:128
ds_write_b64 v238, v[96:97] offset:0
ds_write_b64 v238, v[98:99] offset:1024
v_lshlrev_b32 v136, 16, v100
v_lshlrev_b32 v137, 16, v102
v_and_b32 v138, 0xffff0000, v100
)ASM"
R"ASM(
v_and_b32 v139, 0xffff0000, v102
v_lshlrev_b32 v140, 16, v101
v_lshlrev_b32 v141, 16, v103
v_and_b32 v142, 0xffff0000, v101
v_and_b32 v143, 0xffff0000, v103
v_pk_add_f32 v[136:137], v[136:137], v[172:173]
v_pk_mul_f32 v[174:175], v[160:161], v[172:173] op_sel:[0,0] op_sel_hi:[0,1]
v_pk_fma_f32 v[174:175], v[160:161], v[172:173], v[174:175] op_sel:[1,1,0] op_sel_hi:[1,0,1] neg_lo:[1,0,0]
v_pk_add_f32 v[138:139], v[138:139], v[174:175]
v_pk_mul_f32 v[172:173], v[160:161], v[174:175] op_sel:[0,0] op_sel_hi:[0,1]
v_pk_fma_f32 v[172:173], v[160:161], v[174:175], v[172:173] op_sel:[1,1,0] op_sel_hi:[1,0,1] neg_lo:[1,0,0]
v_pk_add_f32 v[140:141], v[140:141], v[172:173]
v_pk_mul_f32 v[174:175], v[160:161], v[172:173] op_sel:[0,0] op_sel_hi:[0,1]
v_pk_fma_f32 v[174:175], v[160:161], v[172:173], v[174:175] op_sel:[1,1,0] op_sel_hi:[1,0,1] neg_lo:[1,0,0]
v_pk_add_f32 v[142:143], v[142:143], v[174:175]
v_pk_mul_f32 v[172:173], v[166:167], v[174:175] op_sel:[0,0] op_sel_hi:[0,1]
v_pk_fma_f32 v[172:173], v[166:167], v[174:175], v[172:173] op_sel:[1,1,0] op_sel_hi:[1,0,1] neg_lo:[1,0,0]
v_cvt_pk_bf16_f32 v100, v136, v138
v_cvt_pk_bf16_f32 v101, v140, v142
v_cvt_pk_bf16_f32 v102, -v137, -v139
v_cvt_pk_bf16_f32 v103, -v141, -v143
buffer_load_dwordx2 v[38:39], v8, s[52:55], s91 offen offset:128
ds_write_b64 v238, v[100:101] offset:16
ds_write_b64 v238, v[102:103] offset:1040
ds_read_b64_tr_b16 v[96:97], v239 offset:0
ds_read_b64_tr_b16 v[98:99], v239 offset:128
ds_read_b64_tr_b16 v[100:101], v239 offset:1024
ds_read_b64_tr_b16 v[102:103], v239 offset:1152
v_lshlrev_b32 v128, 16, v104
v_lshlrev_b32 v129, 16, v106
v_and_b32 v130, 0xffff0000, v104
v_and_b32 v131, 0xffff0000, v106
v_lshlrev_b32 v132, 16, v105
v_lshlrev_b32 v133, 16, v107
v_and_b32 v134, 0xffff0000, v105
v_and_b32 v135, 0xffff0000, v107
v_pk_add_f32 v[128:129], v[128:129], v[172:173]
v_pk_mul_f32 v[174:175], v[160:161], v[172:173] op_sel:[0,0] op_sel_hi:[0,1]
v_pk_fma_f32 v[174:175], v[160:161], v[172:173], v[174:175] op_sel:[1,1,0] op_sel_hi:[1,0,1] neg_lo:[1,0,0]
v_pk_add_f32 v[130:131], v[130:131], v[174:175]
v_pk_mul_f32 v[172:173], v[160:161], v[174:175] op_sel:[0,0] op_sel_hi:[0,1]
v_pk_fma_f32 v[172:173], v[160:161], v[174:175], v[172:173] op_sel:[1,1,0] op_sel_hi:[1,0,1] neg_lo:[1,0,0]
v_pk_add_f32 v[132:133], v[132:133], v[172:173]
v_pk_mul_f32 v[174:175], v[160:161], v[172:173] op_sel:[0,0] op_sel_hi:[0,1]
v_pk_fma_f32 v[174:175], v[160:161], v[172:173], v[174:175] op_sel:[1,1,0] op_sel_hi:[1,0,1] neg_lo:[1,0,0]
v_pk_add_f32 v[134:135], v[134:135], v[174:175]
v_pk_mul_f32 v[172:173], v[166:167], v[174:175] op_sel:[0,0] op_sel_hi:[0,1]
v_pk_fma_f32 v[172:173], v[166:167], v[174:175], v[172:173] op_sel:[1,1,0] op_sel_hi:[1,0,1] neg_lo:[1,0,0]
v_cvt_pk_bf16_f32 v104, v128, v130
v_cvt_pk_bf16_f32 v105, v132, v134
v_cvt_pk_bf16_f32 v106, -v129, -v131
v_cvt_pk_bf16_f32 v107, -v133, -v135
buffer_load_dwordx2 v[46:47], v14, s[84:87], s89 offen offset:128
ds_write_b64 v238, v[104:105] offset:2048
ds_write_b64 v238, v[106:107] offset:3072
s_waitcnt lgkmcnt(4)
buffer_store_dwordx4 v[96:99], v240, s[96:99], s76 offen offset:0 sc1
s_waitcnt lgkmcnt(2)
buffer_store_dwordx4 v[100:103], v240, s[96:99], s77 offen offset:0 sc1
v_lshlrev_b32 v136, 16, v108
v_lshlrev_b32 v137, 16, v110
v_and_b32 v138, 0xffff0000, v108
v_and_b32 v139, 0xffff0000, v110
v_lshlrev_b32 v140, 16, v109
v_lshlrev_b32 v141, 16, v111
v_and_b32 v142, 0xffff0000, v109
v_and_b32 v143, 0xffff0000, v111
v_pk_add_f32 v[136:137], v[136:137], v[172:173]
v_pk_mul_f32 v[174:175], v[160:161], v[172:173] op_sel:[0,0] op_sel_hi:[0,1]
v_pk_fma_f32 v[174:175], v[160:161], v[172:173], v[174:175] op_sel:[1,1,0] op_sel_hi:[1,0,1] neg_lo:[1,0,0]
v_pk_add_f32 v[138:139], v[138:139], v[174:175]
v_pk_mul_f32 v[172:173], v[160:161], v[174:175] op_sel:[0,0] op_sel_hi:[0,1]
v_pk_fma_f32 v[172:173], v[160:161], v[174:175], v[172:173] op_sel:[1,1,0] op_sel_hi:[1,0,1] neg_lo:[1,0,0]
v_pk_add_f32 v[140:141], v[140:141], v[172:173]
v_pk_mul_f32 v[174:175], v[160:161], v[172:173] op_sel:[0,0] op_sel_hi:[0,1]
v_pk_fma_f32 v[174:175], v[160:161], v[172:173], v[174:175] op_sel:[1,1,0] op_sel_hi:[1,0,1] neg_lo:[1,0,0]
v_pk_add_f32 v[142:143], v[142:143], v[174:175]
v_pk_mul_f32 v[172:173], v[166:167], v[174:175] op_sel:[0,0] op_sel_hi:[0,1]
v_pk_fma_f32 v[172:173], v[166:167], v[174:175], v[172:173] op_sel:[1,1,0] op_sel_hi:[1,0,1] neg_lo:[1,0,0]
v_cvt_pk_bf16_f32 v108, v136, v138
v_cvt_pk_bf16_f32 v109, v140, v142
v_cvt_pk_bf16_f32 v110, -v137, -v139
v_cvt_pk_bf16_f32 v111, -v141, -v143
buffer_load_dwordx2 v[40:41], v8, s[52:55], s88 offen offset:256
ds_write_b64 v238, v[108:109] offset:2064
ds_write_b64 v238, v[110:111] offset:3088
ds_read_b64_tr_b16 v[104:105], v239 offset:2048
ds_read_b64_tr_b16 v[106:107], v239 offset:2176
ds_read_b64_tr_b16 v[108:109], v239 offset:3072
ds_read_b64_tr_b16 v[110:111], v239 offset:3200
v_lshlrev_b32 v128, 16, v112
v_lshlrev_b32 v129, 16, v114
v_and_b32 v130, 0xffff0000, v112
v_and_b32 v131, 0xffff0000, v114
v_lshlrev_b32 v132, 16, v113
v_lshlrev_b32 v133, 16, v115
v_and_b32 v134, 0xffff0000, v113
v_and_b32 v135, 0xffff0000, v115
v_pk_add_f32 v[128:129], v[128:129], v[172:173]
v_pk_mul_f32 v[174:175], v[160:161], v[172:173] op_sel:[0,0] op_sel_hi:[0,1]
v_pk_fma_f32 v[174:175], v[160:161], v[172:173], v[174:175] op_sel:[1,1,0] op_sel_hi:[1,0,1] neg_lo:[1,0,0]
v_pk_add_f32 v[130:131], v[130:131], v[174:175]
v_pk_mul_f32 v[172:173], v[160:161], v[174:175] op_sel:[0,0] op_sel_hi:[0,1]
v_pk_fma_f32 v[172:173], v[160:161], v[174:175], v[172:173] op_sel:[1,1,0] op_sel_hi:[1,0,1] neg_lo:[1,0,0]
v_pk_add_f32 v[132:133], v[132:133], v[172:173]
v_pk_mul_f32 v[174:175], v[160:161], v[172:173] op_sel:[0,0] op_sel_hi:[0,1]
v_pk_fma_f32 v[174:175], v[160:161], v[172:173], v[174:175] op_sel:[1,1,0] op_sel_hi:[1,0,1] neg_lo:[1,0,0]
v_pk_add_f32 v[134:135], v[134:135], v[174:175]
v_pk_mul_f32 v[172:173], v[166:167], v[174:175] op_sel:[0,0] op_sel_hi:[0,1]
v_pk_fma_f32 v[172:173], v[166:167], v[174:175], v[172:173] op_sel:[1,1,0] op_sel_hi:[1,0,1] neg_lo:[1,0,0]
v_cvt_pk_bf16_f32 v112, v128, v130
v_cvt_pk_bf16_f32 v113, v132, v134
v_cvt_pk_bf16_f32 v114, -v129, -v131
v_cvt_pk_bf16_f32 v115, -v133, -v135
buffer_load_dwordx2 v[42:43], v8, s[52:55], s91 offen offset:256
ds_write_b64 v238, v[112:113] offset:4096
ds_write_b64 v238, v[114:115] offset:5120
s_waitcnt lgkmcnt(4)
buffer_store_dwordx4 v[104:107], v240, s[96:99], s76 offen offset:512 sc1
s_waitcnt lgkmcnt(2)
buffer_store_dwordx4 v[108:111], v240, s[96:99], s77 offen offset:512 sc1
v_lshlrev_b32 v136, 16, v116
v_lshlrev_b32 v137, 16, v118
v_and_b32 v138, 0xffff0000, v116
v_and_b32 v139, 0xffff0000, v118
v_lshlrev_b32 v140, 16, v117
v_lshlrev_b32 v141, 16, v119
v_and_b32 v142, 0xffff0000, v117
v_and_b32 v143, 0xffff0000, v119
v_pk_add_f32 v[136:137], v[136:137], v[172:173]
v_pk_mul_f32 v[174:175], v[160:161], v[172:173] op_sel:[0,0] op_sel_hi:[0,1]
v_pk_fma_f32 v[174:175], v[160:161], v[172:173], v[174:175] op_sel:[1,1,0] op_sel_hi:[1,0,1] neg_lo:[1,0,0]
v_pk_add_f32 v[138:139], v[138:139], v[174:175]
v_pk_mul_f32 v[172:173], v[160:161], v[174:175] op_sel:[0,0] op_sel_hi:[0,1]
v_pk_fma_f32 v[172:173], v[160:161], v[174:175], v[172:173] op_sel:[1,1,0] op_sel_hi:[1,0,1] neg_lo:[1,0,0]
v_pk_add_f32 v[140:141], v[140:141], v[172:173]
v_pk_mul_f32 v[174:175], v[160:161], v[172:173] op_sel:[0,0] op_sel_hi:[0,1]
v_pk_fma_f32 v[174:175], v[160:161], v[172:173], v[174:175] op_sel:[1,1,0] op_sel_hi:[1,0,1] neg_lo:[1,0,0]
v_pk_add_f32 v[142:143], v[142:143], v[174:175]
v_pk_mul_f32 v[172:173], v[166:167], v[174:175] op_sel:[0,0] op_sel_hi:[0,1]
v_pk_fma_f32 v[172:173], v[166:167], v[174:175], v[172:173] op_sel:[1,1,0] op_sel_hi:[1,0,1] neg_lo:[1,0,0]
v_cvt_pk_bf16_f32 v116, v136, v138
v_cvt_pk_bf16_f32 v117, v140, v142
v_cvt_pk_bf16_f32 v118, -v137, -v139
v_cvt_pk_bf16_f32 v119, -v141, -v143
buffer_load_dwordx2 v[48:49], v14, s[84:87], s89 offen offset:256
ds_write_b64 v238, v[116:117] offset:4112
ds_write_b64 v238, v[118:119] offset:5136
ds_read_b64_tr_b16 v[112:113], v239 offset:4096
ds_read_b64_tr_b16 v[114:115], v239 offset:4224
ds_read_b64_tr_b16 v[116:117], v239 offset:5120
ds_read_b64_tr_b16 v[118:119], v239 offset:5248
v_lshlrev_b32 v128, 16, v120
v_lshlrev_b32 v129, 16, v122
v_and_b32 v130, 0xffff0000, v120
v_and_b32 v131, 0xffff0000, v122
v_lshlrev_b32 v132, 16, v121
v_lshlrev_b32 v133, 16, v123
v_and_b32 v134, 0xffff0000, v121
v_and_b32 v135, 0xffff0000, v123
v_pk_add_f32 v[128:129], v[128:129], v[172:173]
v_pk_mul_f32 v[174:175], v[160:161], v[172:173] op_sel:[0,0] op_sel_hi:[0,1]
v_pk_fma_f32 v[174:175], v[160:161], v[172:173], v[174:175] op_sel:[1,1,0] op_sel_hi:[1,0,1] neg_lo:[1,0,0]
v_pk_add_f32 v[130:131], v[130:131], v[174:175]
v_pk_mul_f32 v[172:173], v[160:161], v[174:175] op_sel:[0,0] op_sel_hi:[0,1]
v_pk_fma_f32 v[172:173], v[160:161], v[174:175], v[172:173] op_sel:[1,1,0] op_sel_hi:[1,0,1] neg_lo:[1,0,0]
v_pk_add_f32 v[132:133], v[132:133], v[172:173]
v_pk_mul_f32 v[174:175], v[160:161], v[172:173] op_sel:[0,0] op_sel_hi:[0,1]
v_pk_fma_f32 v[174:175], v[160:161], v[172:173], v[174:175] op_sel:[1,1,0] op_sel_hi:[1,0,1] neg_lo:[1,0,0]
v_pk_add_f32 v[134:135], v[134:135], v[174:175]
v_pk_mul_f32 v[172:173], v[166:167], v[174:175] op_sel:[0,0] op_sel_hi:[0,1]
v_pk_fma_f32 v[172:173], v[166:167], v[174:175], v[172:173] op_sel:[1,1,0] op_sel_hi:[1,0,1] neg_lo:[1,0,0]
v_cvt_pk_bf16_f32 v120, v128, v130
v_cvt_pk_bf16_f32 v121, v132, v134
v_cvt_pk_bf16_f32 v122, -v129, -v131
v_cvt_pk_bf16_f32 v123, -v133, -v135
buffer_load_dwordx4 v[50:53], v13, s[96:99], s90 offen offset:0 sc1
ds_write_b64 v238, v[120:121] offset:6144
ds_write_b64 v238, v[122:123] offset:7168
s_waitcnt lgkmcnt(4)
buffer_store_dwordx4 v[112:115], v240, s[96:99], s76 offen offset:1024 sc1
s_waitcnt lgkmcnt(2)
buffer_store_dwordx4 v[116:119], v240, s[96:99], s77 offen offset:1024 sc1
v_lshlrev_b32 v136, 16, v124
v_lshlrev_b32 v137, 16, v126
v_and_b32 v138, 0xffff0000, v124
v_and_b32 v139, 0xffff0000, v126
v_lshlrev_b32 v140, 16, v125
v_lshlrev_b32 v141, 16, v127
v_and_b32 v142, 0xffff0000, v125
v_and_b32 v143, 0xffff0000, v127
v_pk_add_f32 v[136:137], v[136:137], v[172:173]
v_pk_mul_f32 v[174:175], v[160:161], v[172:173] op_sel:[0,0] op_sel_hi:[0,1]
v_pk_fma_f32 v[174:175], v[160:161], v[172:173], v[174:175] op_sel:[1,1,0] op_sel_hi:[1,0,1] neg_lo:[1,0,0]
v_pk_add_f32 v[138:139], v[138:139], v[174:175]
v_pk_mul_f32 v[172:173], v[160:161], v[174:175] op_sel:[0,0] op_sel_hi:[0,1]
v_pk_fma_f32 v[172:173], v[160:161], v[174:175], v[172:173] op_sel:[1,1,0] op_sel_hi:[1,0,1] neg_lo:[1,0,0]
v_pk_add_f32 v[140:141], v[140:141], v[172:173]
v_pk_mul_f32 v[174:175], v[160:161], v[172:173] op_sel:[0,0] op_sel_hi:[0,1]
v_pk_fma_f32 v[174:175], v[160:161], v[172:173], v[174:175] op_sel:[1,1,0] op_sel_hi:[1,0,1] neg_lo:[1,0,0]
)ASM"
R"ASM(
v_pk_add_f32 v[142:143], v[142:143], v[174:175]
v_cvt_pk_bf16_f32 v124, v136, v138
v_cvt_pk_bf16_f32 v125, v140, v142
v_cvt_pk_bf16_f32 v126, -v137, -v139
v_cvt_pk_bf16_f32 v127, -v141, -v143
buffer_load_dwordx4 v[54:57], v13, s[96:99], s90 offen offset:256 sc1
ds_write_b64 v238, v[124:125] offset:6160
ds_write_b64 v238, v[126:127] offset:7184
ds_read_b64_tr_b16 v[120:121], v239 offset:6144
ds_read_b64_tr_b16 v[122:123], v239 offset:6272
ds_read_b64_tr_b16 v[124:125], v239 offset:7168
ds_read_b64_tr_b16 v[126:127], v239 offset:7296
s_waitcnt lgkmcnt(2)
buffer_store_dwordx4 v[120:123], v240, s[96:99], s76 offen offset:1536 sc1
s_waitcnt lgkmcnt(0)
buffer_store_dwordx4 v[124:127], v240, s[96:99], s77 offen offset:1536 sc1
s_waitcnt lgkmcnt(0)
s_nop 0
LNS_%=:
s_barrier
ds_read_b64_tr_b16 v[160:161], v196 offset:8192
ds_read_b64_tr_b16 v[162:163], v196 offset:8704
ds_read_b64_tr_b16 v[164:165], v196 offset:16384
ds_read_b64_tr_b16 v[166:167], v196 offset:16896
ds_read_b64_tr_b16 v[168:169], v197 offset:8192
ds_read_b64_tr_b16 v[170:171], v197 offset:8704
ds_read_b64_tr_b16 v[172:173], v197 offset:16384
ds_read_b64_tr_b16 v[174:175], v197 offset:16896
ds_read_b64_tr_b16 v[176:177], v198 offset:8192
ds_read_b64_tr_b16 v[178:179], v198 offset:8704
ds_read_b64_tr_b16 v[180:181], v198 offset:16384
ds_read_b64_tr_b16 v[182:183], v198 offset:16896
v_readlane_b32 s76, v0, 21
s_nop 3
s_cmp_eq_u32 s76, 2
s_cbranch_scc1 LHP_%=
s_waitcnt lgkmcnt(10)
v_mfma_f32_16x16x32_bf16 v[96:99], v[160:163], a[0:3], 0
ds_read_b64_tr_b16 v[184:185], v199 offset:8192
s_waitcnt lgkmcnt(9)
v_mfma_f32_16x16x32_bf16 v[96:99], v[164:167], a[8:11], v[96:99]
ds_read_b64_tr_b16 v[186:187], v199 offset:8704
v_mfma_f32_16x16x32_bf16 v[100:103], v[160:163], a[4:7], 0
ds_read_b64_tr_b16 v[188:189], v199 offset:16384
s_add_u32 m0, s61, 24576
v_mfma_f32_16x16x32_bf16 v[100:103], v[164:167], a[12:15], v[100:103]
ds_read_b64_tr_b16 v[190:191], v199 offset:16896
buffer_load_dwordx4 v5, s[44:47], s12 offen offset:0 lds
s_waitcnt lgkmcnt(10)
v_mfma_f32_16x16x32_bf16 v[104:107], v[168:171], a[0:3], 0
ds_read_b64_tr_b16 v[160:161], v196 offset:9216
v_and_b32 v13, 15, v1
s_waitcnt lgkmcnt(9)
v_mfma_f32_16x16x32_bf16 v[104:107], v[172:175], a[8:11], v[104:107]
ds_read_b64_tr_b16 v[162:163], v196 offset:9728
v_lshrrev_b32 v14, 4, v1
v_mfma_f32_16x16x32_bf16 v[108:111], v[168:171], a[4:7], 0
ds_read_b64_tr_b16 v[164:165], v196 offset:17408
v_lshrrev_b32 v18, 1, v13
v_mfma_f32_16x16x32_bf16 v[108:111], v[172:175], a[12:15], v[108:111]
s_waitcnt lgkmcnt(3)
ds_read_b64_tr_b16 v[166:167], v196 offset:17920
v_mfma_f32_16x16x32_bf16 v[112:115], v[176:179], a[0:3], 0
ds_read_b64_tr_b16 v[168:169], v197 offset:9216
v_xor_b32 v14, v14, v18
v_mfma_f32_16x16x32_bf16 v[112:115], v[180:183], a[8:11], v[112:115]
ds_read_b64_tr_b16 v[170:171], v197 offset:9728
v_lshlrev_b32 v13, 7, v13
v_mfma_f32_16x16x32_bf16 v[116:119], v[176:179], a[4:7], 0
ds_read_b64_tr_b16 v[172:173], v197 offset:17408
v_lshl_add_u32 v216, v14, 4, v13
v_mfma_f32_16x16x32_bf16 v[116:119], v[180:183], a[12:15], v[116:119]
ds_read_b64_tr_b16 v[174:175], v197 offset:17920
v_xor_b32 v217, 64, v216
v_mfma_f32_16x16x32_bf16 v[120:123], v[184:187], a[0:3], 0
ds_read_b64_tr_b16 v[176:177], v198 offset:9216
v_add_u32 v220, 49152, v216
v_mfma_f32_16x16x32_bf16 v[120:123], v[188:191], a[8:11], v[120:123]
ds_read_b64_tr_b16 v[178:179], v198 offset:9728
v_add_u32 v221, 49152, v217
v_mfma_f32_16x16x32_bf16 v[124:127], v[184:187], a[4:7], 0
ds_read_b64_tr_b16 v[180:181], v198 offset:17408
v_add_u32 v222, 98304, v216
v_mfma_f32_16x16x32_bf16 v[124:127], v[188:191], a[12:15], v[124:127]
s_waitcnt lgkmcnt(3)
ds_read_b64_tr_b16 v[182:183], v198 offset:17920
v_mfma_f32_16x16x32_bf16 v[128:131], v[160:163], a[0:3], 0
ds_read_b64_tr_b16 v[184:185], v199 offset:9216
v_add_u32 v223, 98304, v217
v_mfma_f32_16x16x32_bf16 v[128:131], v[164:167], a[8:11], v[128:131]
ds_read_b64_tr_b16 v[186:187], v199 offset:9728
v_lshrrev_b32 v13, 3, v1
v_mfma_f32_16x16x32_bf16 v[132:135], v[160:163], a[4:7], 0
ds_read_b64_tr_b16 v[188:189], v199 offset:17408
v_and_b32 v18, 7, v1
v_mfma_f32_16x16x32_bf16 v[132:135], v[164:167], a[12:15], v[132:135]
ds_read_b64_tr_b16 v[190:191], v199 offset:17920
v_lshrrev_b32 v14, 1, v13
v_mfma_f32_16x16x32_bf16 v[136:139], v[168:171], a[0:3], 0
ds_read_b64_tr_b16 v[160:161], v196 offset:24576
v_xor_b32 v14, v14, v18
v_mfma_f32_16x16x32_bf16 v[136:139], v[172:175], a[8:11], v[136:139]
ds_read_b64_tr_b16 v[162:163], v196 offset:25088
s_add_u32 s64, s40, s68
v_mfma_f32_16x16x32_bf16 v[140:143], v[168:171], a[4:7], 0
ds_read_b64_tr_b16 v[164:165], v196 offset:25600
s_and_b32 s64, s64, 3
v_mfma_f32_16x16x32_bf16 v[140:143], v[172:175], a[12:15], v[140:143]
s_waitcnt lgkmcnt(3)
ds_read_b64_tr_b16 v[166:167], v196 offset:26112
v_mfma_f32_16x16x32_bf16 v[144:147], v[176:179], a[0:3], 0
ds_read_b64_tr_b16 v[168:169], v197 offset:24576
s_lshl_b32 s64, s64, 4
v_mfma_f32_16x16x32_bf16 v[144:147], v[180:183], a[8:11], v[144:147]
ds_read_b64_tr_b16 v[170:171], v197 offset:25088
v_add_u32 v13, s64, v13
v_mfma_f32_16x16x32_bf16 v[148:151], v[176:179], a[4:7], 0
buffer_load_dword v17, v15, s[96:99], 0 offen sc1
ds_read_b64_tr_b16 v[172:173], v197 offset:25600
v_mfma_f32_16x16x32_bf16 v[148:151], v[180:183], a[12:15], v[148:151]
ds_read_b64_tr_b16 v[174:175], v197 offset:26112
v_mul_u32_u24 v13, 1152, v13
v_mfma_f32_16x16x32_bf16 v[152:155], v[184:187], a[0:3], 0
ds_read_b64_tr_b16 v[176:177], v198 offset:24576
v_lshl_add_u32 v13, v14, 4, v13
v_mfma_f32_16x16x32_bf16 v[152:155], v[188:191], a[8:11], v[152:155]
ds_read_b64_tr_b16 v[178:179], v198 offset:25088
v_add_u32 v23, 2048, v13
v_mfma_f32_16x16x32_bf16 v[156:159], v[184:187], a[4:7], 0
ds_read_b64_tr_b16 v[180:181], v198 offset:25600
v_xor_b32 v13, 64, v13
v_mfma_f32_16x16x32_bf16 v[156:159], v[188:191], a[12:15], v[156:159]
s_waitcnt lgkmcnt(3)
ds_read_b64_tr_b16 v[182:183], v198 offset:26112
v_mfma_f32_16x16x32_bf16 v[224:227], v[160:163], a[0:3], 0
ds_read_b64_tr_b16 v[184:185], v199 offset:24576
v_add_u32 v6, 10240, v13
v_mfma_f32_16x16x32_bf16 v[224:227], v[164:167], a[8:11], v[224:227]
ds_read_b64_tr_b16 v[186:187], v199 offset:25088
v_lshlrev_b32 v7, 4, v1
v_mfma_f32_16x16x32_bf16 v[228:231], v[160:163], a[4:7], 0
ds_read_b64_tr_b16 v[188:189], v199 offset:25600
buffer_load_dwordx4 v27, s[44:47], s13 offen offset:1024 lds
v_mfma_f32_16x16x32_bf16 v[228:231], v[164:167], a[12:15], v[228:231]
ds_read_b64_tr_b16 v[190:191], v199 offset:26112
s_add_u32 m0, s61, 32768
v_mfma_f32_16x16x32_bf16 v[232:235], v[168:171], a[0:3], 0
ds_read_b64_tr_b16 v[160:161], v196 offset:32768
v_mfma_f32_16x16x32_bf16 v[232:235], v[172:175], a[8:11], v[232:235]
ds_read_b64_tr_b16 v[162:163], v196 offset:33280
v_mfma_f32_16x16x32_bf16 v[236:239], v[168:171], a[4:7], 0
ds_read_b64_tr_b16 v[164:165], v196 offset:40960
buffer_load_dwordx4 v3, s[44:47], s14 offen offset:0 lds
v_mfma_f32_16x16x32_bf16 v[236:239], v[172:175], a[12:15], v[236:239]
s_waitcnt lgkmcnt(3)
ds_read_b64_tr_b16 v[166:167], v196 offset:41472
v_mfma_f32_16x16x32_bf16 v[240:243], v[176:179], a[0:3], 0
ds_read_b64_tr_b16 v[168:169], v197 offset:32768
v_mfma_f32_16x16x32_bf16 v[240:243], v[180:183], a[8:11], v[240:243]
ds_read_b64_tr_b16 v[170:171], v197 offset:33280
v_mfma_f32_16x16x32_bf16 v[244:247], v[176:179], a[4:7], 0
ds_read_b64_tr_b16 v[172:173], v197 offset:40960
buffer_load_dwordx4 v4, s[44:47], s14 offen offset:1024 lds
v_mfma_f32_16x16x32_bf16 v[244:247], v[180:183], a[12:15], v[244:247]
ds_read_b64_tr_b16 v[174:175], v197 offset:41472
v_mfma_f32_16x16x32_bf16 v[248:251], v[184:187], a[0:3], 0
ds_read_b64_tr_b16 v[176:177], v198 offset:32768
s_add_u32 m0, s61, 40960
v_mfma_f32_16x16x32_bf16 v[248:251], v[188:191], a[8:11], v[248:251]
ds_read_b64_tr_b16 v[178:179], v198 offset:33280
v_mfma_f32_16x16x32_bf16 v[252:255], v[184:187], a[4:7], 0
ds_read_b64_tr_b16 v[180:181], v198 offset:40960
buffer_load_dwordx4 v3, s[44:47], s15 offen offset:0 lds
v_mfma_f32_16x16x32_bf16 v[252:255], v[188:191], a[12:15], v[252:255]
s_waitcnt lgkmcnt(3)
ds_read_b64_tr_b16 v[182:183], v198 offset:41472
v_mfma_f32_16x16x32_bf16 v[96:99], v[160:163], a[16:19], v[96:99]
ds_read_b64_tr_b16 v[184:185], v199 offset:32768
v_mfma_f32_16x16x32_bf16 v[96:99], v[164:167], a[24:27], v[96:99]
ds_read_b64_tr_b16 v[186:187], v199 offset:33280
v_mfma_f32_16x16x32_bf16 v[100:103], v[160:163], a[20:23], v[100:103]
ds_read_b64_tr_b16 v[188:189], v199 offset:40960
buffer_load_dwordx4 v4, s[44:47], s15 offen offset:1024 lds
v_mfma_f32_16x16x32_bf16 v[100:103], v[164:167], a[28:31], v[100:103]
ds_read_b64_tr_b16 v[190:191], v199 offset:41472
v_mfma_f32_16x16x32_bf16 v[104:107], v[168:171], a[16:19], v[104:107]
s_waitcnt lgkmcnt(0)
s_barrier
v_mfma_f32_16x16x32_bf16 v[104:107], v[172:175], a[24:27], v[104:107]
ds_read_b64_tr_b16 v[160:161], v196 offset:33792
v_mfma_f32_16x16x32_bf16 v[108:111], v[168:171], a[20:23], v[108:111]
ds_read_b64_tr_b16 v[162:163], v196 offset:34304
s_add_u32 m0, s61, 49152
v_mfma_f32_16x16x32_bf16 v[108:111], v[172:175], a[28:31], v[108:111]
ds_read_b64_tr_b16 v[164:165], v196 offset:41984
ds_read_b64_tr_b16 v[166:167], v196 offset:42496
v_mfma_f32_16x16x32_bf16 v[112:115], v[176:179], a[16:19], v[112:115]
ds_read_b64_tr_b16 v[168:169], v197 offset:33792
buffer_load_dwordx4 v5, s[44:47], s14 offen offset:0 lds
v_mfma_f32_16x16x32_bf16 v[112:115], v[180:183], a[24:27], v[112:115]
)ASM"
R"ASM(
ds_read_b64_tr_b16 v[170:171], v197 offset:34304
v_mfma_f32_16x16x32_bf16 v[116:119], v[176:179], a[20:23], v[116:119]
ds_read_b64_tr_b16 v[172:173], v197 offset:41984
v_mfma_f32_16x16x32_bf16 v[116:119], v[180:183], a[28:31], v[116:119]
ds_read_b64_tr_b16 v[174:175], v197 offset:42496
v_mfma_f32_16x16x32_bf16 v[120:123], v[184:187], a[16:19], v[120:123]
ds_read_b64_tr_b16 v[176:177], v198 offset:33792
buffer_load_dwordx4 v27, s[44:47], s15 offen offset:1024 lds
v_mfma_f32_16x16x32_bf16 v[120:123], v[188:191], a[24:27], v[120:123]
ds_read_b64_tr_b16 v[178:179], v198 offset:34304
v_mfma_f32_16x16x32_bf16 v[124:127], v[184:187], a[20:23], v[124:127]
ds_read_b64_tr_b16 v[180:181], v198 offset:41984
v_mfma_f32_16x16x32_bf16 v[124:127], v[188:191], a[28:31], v[124:127]
s_waitcnt lgkmcnt(3)
ds_read_b64_tr_b16 v[182:183], v198 offset:42496
v_mfma_f32_16x16x32_bf16 v[128:131], v[160:163], a[16:19], v[128:131]
ds_read_b64_tr_b16 v[184:185], v199 offset:33792
s_add_u32 m0, s61, 57344
v_mfma_f32_16x16x32_bf16 v[128:131], v[164:167], a[24:27], v[128:131]
ds_read_b64_tr_b16 v[186:187], v199 offset:34304
buffer_load_dwordx4 v3, s[44:47], s16 offen offset:0 lds
v_mfma_f32_16x16x32_bf16 v[132:135], v[160:163], a[20:23], v[132:135]
ds_read_b64_tr_b16 v[188:189], v199 offset:41984
v_mfma_f32_16x16x32_bf16 v[132:135], v[164:167], a[28:31], v[132:135]
ds_read_b64_tr_b16 v[190:191], v199 offset:42496
v_mfma_f32_16x16x32_bf16 v[136:139], v[168:171], a[16:19], v[136:139]
ds_read_b64_tr_b16 v[160:161], v28 offset:0
v_mfma_f32_16x16x32_bf16 v[136:139], v[172:175], a[24:27], v[136:139]
ds_read_b64_tr_b16 v[162:163], v28 offset:512
buffer_load_dwordx4 v4, s[44:47], s16 offen offset:1024 lds
v_mfma_f32_16x16x32_bf16 v[140:143], v[168:171], a[20:23], v[140:143]
ds_read_b64_tr_b16 v[164:165], v28 offset:1024
v_mfma_f32_16x16x32_bf16 v[140:143], v[172:175], a[28:31], v[140:143]
s_waitcnt lgkmcnt(3)
ds_read_b64_tr_b16 v[166:167], v28 offset:1536
v_mfma_f32_16x16x32_bf16 v[144:147], v[176:179], a[16:19], v[144:147]
ds_read_b64_tr_b16 v[168:169], v29 offset:0
v_mfma_f32_16x16x32_bf16 v[144:147], v[180:183], a[24:27], v[144:147]
ds_read_b64_tr_b16 v[170:171], v29 offset:512
v_mfma_f32_16x16x32_bf16 v[148:151], v[176:179], a[20:23], v[148:151]
ds_read_b64_tr_b16 v[172:173], v29 offset:1024
s_add_u32 m0, s61, 65536
v_mfma_f32_16x16x32_bf16 v[148:151], v[180:183], a[28:31], v[148:151]
ds_read_b64_tr_b16 v[174:175], v29 offset:1536
buffer_load_dwordx4 v3, s[44:47], s17 offen offset:0 lds
v_mfma_f32_16x16x32_bf16 v[152:155], v[184:187], a[16:19], v[152:155]
ds_read_b64_tr_b16 v[176:177], v30 offset:0
v_mfma_f32_16x16x32_bf16 v[152:155], v[188:191], a[24:27], v[152:155]
ds_read_b64_tr_b16 v[178:179], v30 offset:512
v_mfma_f32_16x16x32_bf16 v[156:159], v[184:187], a[20:23], v[156:159]
ds_read_b64_tr_b16 v[180:181], v30 offset:1024
v_mfma_f32_16x16x32_bf16 v[156:159], v[188:191], a[28:31], v[156:159]
s_waitcnt lgkmcnt(3)
ds_read_b64_tr_b16 v[182:183], v30 offset:1536
v_mfma_f32_16x16x32_bf16 v[224:227], v[160:163], a[16:19], v[224:227]
ds_read_b64_tr_b16 v[184:185], v31 offset:0
buffer_load_dwordx4 v4, s[44:47], s17 offen offset:1024 lds
v_mfma_f32_16x16x32_bf16 v[224:227], v[164:167], a[24:27], v[224:227]
ds_read_b64_tr_b16 v[186:187], v31 offset:512
v_mfma_f32_16x16x32_bf16 v[228:231], v[160:163], a[20:23], v[228:231]
ds_read_b64_tr_b16 v[188:189], v31 offset:1024
v_mfma_f32_16x16x32_bf16 v[228:231], v[164:167], a[28:31], v[228:231]
ds_read_b64_tr_b16 v[190:191], v31 offset:1536
v_mfma_f32_16x16x32_bf16 v[232:235], v[168:171], a[16:19], v[232:235]
ds_read_b64_tr_b16 v[160:161], v28 offset:8192
v_mfma_f32_16x16x32_bf16 v[232:235], v[172:175], a[24:27], v[232:235]
ds_read_b64_tr_b16 v[162:163], v28 offset:8704
v_mfma_f32_16x16x32_bf16 v[236:239], v[168:171], a[20:23], v[236:239]
ds_read_b64_tr_b16 v[164:165], v28 offset:16384
v_mfma_f32_16x16x32_bf16 v[236:239], v[172:175], a[28:31], v[236:239]
s_waitcnt lgkmcnt(3)
ds_read_b64_tr_b16 v[166:167], v28 offset:16896
v_mfma_f32_16x16x32_bf16 v[240:243], v[176:179], a[16:19], v[240:243]
ds_read_b64_tr_b16 v[168:169], v29 offset:8192
s_add_u32 m0, s61, 73728
v_mfma_f32_16x16x32_bf16 v[240:243], v[180:183], a[24:27], v[240:243]
ds_read_b64_tr_b16 v[170:171], v29 offset:8704
buffer_load_dwordx4 v5, s[44:47], s16 offen offset:0 lds
v_mfma_f32_16x16x32_bf16 v[244:247], v[176:179], a[20:23], v[244:247]
ds_read_b64_tr_b16 v[172:173], v29 offset:16384
v_mfma_f32_16x16x32_bf16 v[244:247], v[180:183], a[28:31], v[244:247]
ds_read_b64_tr_b16 v[174:175], v29 offset:16896
v_mfma_f32_16x16x32_bf16 v[248:251], v[184:187], a[16:19], v[248:251]
ds_read_b64_tr_b16 v[176:177], v30 offset:8192
v_mfma_f32_16x16x32_bf16 v[248:251], v[188:191], a[24:27], v[248:251]
ds_read_b64_tr_b16 v[178:179], v30 offset:8704
buffer_load_dwordx4 v27, s[44:47], s17 offen offset:1024 lds
v_mfma_f32_16x16x32_bf16 v[252:255], v[184:187], a[20:23], v[252:255]
ds_read_b64_tr_b16 v[180:181], v30 offset:16384
v_mfma_f32_16x16x32_bf16 v[252:255], v[188:191], a[28:31], v[252:255]
s_waitcnt lgkmcnt(3)
ds_read_b64_tr_b16 v[182:183], v30 offset:16896
v_mfma_f32_16x16x32_bf16 v[96:99], v[160:163], a[32:35], v[96:99]
ds_read_b64_tr_b16 v[184:185], v31 offset:8192
v_mfma_f32_16x16x32_bf16 v[96:99], v[164:167], a[40:43], v[96:99]
ds_read_b64_tr_b16 v[186:187], v31 offset:8704
v_mfma_f32_16x16x32_bf16 v[100:103], v[160:163], a[36:39], v[100:103]
ds_read_b64_tr_b16 v[188:189], v31 offset:16384
v_mfma_f32_16x16x32_bf16 v[100:103], v[164:167], a[44:47], v[100:103]
ds_read_b64_tr_b16 v[190:191], v31 offset:16896
v_mfma_f32_16x16x32_bf16 v[104:107], v[168:171], a[32:35], v[104:107]
ds_read_b64_tr_b16 v[160:161], v28 offset:9216
v_mfma_f32_16x16x32_bf16 v[104:107], v[172:175], a[40:43], v[104:107]
ds_read_b64_tr_b16 v[162:163], v28 offset:9728
v_mfma_f32_16x16x32_bf16 v[108:111], v[168:171], a[36:39], v[108:111]
ds_read_b64_tr_b16 v[164:165], v28 offset:17408
v_mfma_f32_16x16x32_bf16 v[108:111], v[172:175], a[44:47], v[108:111]
s_waitcnt lgkmcnt(3)
ds_read_b64_tr_b16 v[166:167], v28 offset:17920
v_mfma_f32_16x16x32_bf16 v[112:115], v[176:179], a[32:35], v[112:115]
ds_read_b64_tr_b16 v[168:169], v29 offset:9216
v_mfma_f32_16x16x32_bf16 v[112:115], v[180:183], a[40:43], v[112:115]
ds_read_b64_tr_b16 v[170:171], v29 offset:9728
v_mfma_f32_16x16x32_bf16 v[116:119], v[176:179], a[36:39], v[116:119]
ds_read_b64_tr_b16 v[172:173], v29 offset:17408
v_mfma_f32_16x16x32_bf16 v[116:119], v[180:183], a[44:47], v[116:119]
ds_read_b64_tr_b16 v[174:175], v29 offset:17920
v_mfma_f32_16x16x32_bf16 v[120:123], v[184:187], a[32:35], v[120:123]
ds_read_b64_tr_b16 v[176:177], v30 offset:9216
v_mfma_f32_16x16x32_bf16 v[120:123], v[188:191], a[40:43], v[120:123]
ds_read_b64_tr_b16 v[178:179], v30 offset:9728
v_mfma_f32_16x16x32_bf16 v[124:127], v[184:187], a[36:39], v[124:127]
ds_read_b64_tr_b16 v[180:181], v30 offset:17408
v_mfma_f32_16x16x32_bf16 v[124:127], v[188:191], a[44:47], v[124:127]
s_waitcnt lgkmcnt(3)
ds_read_b64_tr_b16 v[182:183], v30 offset:17920
v_mfma_f32_16x16x32_bf16 v[128:131], v[160:163], a[32:35], v[128:131]
ds_read_b64_tr_b16 v[184:185], v31 offset:9216
v_mfma_f32_16x16x32_bf16 v[128:131], v[164:167], a[40:43], v[128:131]
ds_read_b64_tr_b16 v[186:187], v31 offset:9728
v_mfma_f32_16x16x32_bf16 v[132:135], v[160:163], a[36:39], v[132:135]
ds_read_b64_tr_b16 v[188:189], v31 offset:17408
v_mfma_f32_16x16x32_bf16 v[132:135], v[164:167], a[44:47], v[132:135]
ds_read_b64_tr_b16 v[190:191], v31 offset:17920
v_mfma_f32_16x16x32_bf16 v[136:139], v[168:171], a[32:35], v[136:139]
s_waitcnt vmcnt(13) lgkmcnt(0)
s_barrier
s_mov_b64 exec, 1
buffer_store_dword v219, off, s[96:99], s41 sc1
s_mov_b64 exec, -1
v_readlane_b32 s72, v0, 42
v_readlane_b32 s73, v0, 54
s_nop 2
s_lshl_b32 s72, s72, 4
s_lshl_b32 s73, s73, 5
s_add_u32 s72, s72, s73
s_add_u32 s72, s72, 49618944
s_cmp_eq_u32 s42, 0
s_cselect_b32 s72, 1073741824, s72
s_mov_b64 exec, 1
buffer_store_dword v219, off, s[96:99], s72 sc1
s_mov_b64 exec, -1
s_mov_b32 s65, 20000
LOC3_%=:
v_cmp_ne_u32 s[92:93], s43, v17
s_nop 3
s_and_b32 s92, s92, 0x3fc000
s_and_b32 s93, s93, 0x0
s_cmp_eq_u64 s[92:93], 0
s_cbranch_scc1 LOD3_%=
s_sub_u32 s65, s65, 1
s_cmp_eq_u32 s65, 0
s_cbranch_scc1 LOD3_%=
s_sleep 2
buffer_load_dword v17, v15, s[96:99], 0 offen sc1
s_waitcnt vmcnt(0)
s_branch LOC3_%=
LOD3_%=:
v_mfma_f32_16x16x32_bf16 v[136:139], v[172:175], a[40:43], v[136:139]
ds_read_b64_tr_b16 v[160:161], v28 offset:24576
s_add_u32 m0, s61, 81920
v_mfma_f32_16x16x32_bf16 v[140:143], v[168:171], a[36:39], v[140:143]
ds_read_b64_tr_b16 v[162:163], v28 offset:25088
buffer_load_dwordx4 v3, s[44:47], s18 offen offset:0 lds
v_mfma_f32_16x16x32_bf16 v[140:143], v[172:175], a[44:47], v[140:143]
ds_read_b64_tr_b16 v[164:165], v28 offset:25600
ds_read_b64_tr_b16 v[166:167], v28 offset:26112
v_mfma_f32_16x16x32_bf16 v[144:147], v[176:179], a[32:35], v[144:147]
ds_read_b64_tr_b16 v[168:169], v29 offset:24576
v_mfma_f32_16x16x32_bf16 v[144:147], v[180:183], a[40:43], v[144:147]
ds_read_b64_tr_b16 v[170:171], v29 offset:25088
v_mfma_f32_16x16x32_bf16 v[148:151], v[176:179], a[36:39], v[148:151]
ds_read_b64_tr_b16 v[172:173], v29 offset:25600
buffer_load_dwordx4 v4, s[44:47], s18 offen offset:1024 lds
v_mfma_f32_16x16x32_bf16 v[148:151], v[180:183], a[44:47], v[148:151]
ds_read_b64_tr_b16 v[174:175], v29 offset:26112
v_mfma_f32_16x16x32_bf16 v[152:155], v[184:187], a[32:35], v[152:155]
ds_read_b64_tr_b16 v[176:177], v30 offset:24576
v_mfma_f32_16x16x32_bf16 v[152:155], v[188:191], a[40:43], v[152:155]
ds_read_b64_tr_b16 v[178:179], v30 offset:25088
v_mfma_f32_16x16x32_bf16 v[156:159], v[184:187], a[36:39], v[156:159]
ds_read_b64_tr_b16 v[180:181], v30 offset:25600
v_mfma_f32_16x16x32_bf16 v[156:159], v[188:191], a[44:47], v[156:159]
s_waitcnt lgkmcnt(3)
ds_read_b64_tr_b16 v[182:183], v30 offset:26112
v_mfma_f32_16x16x32_bf16 v[224:227], v[160:163], a[32:35], v[224:227]
ds_read_b64_tr_b16 v[184:185], v31 offset:24576
s_add_u32 m0, s61, 90112
v_mfma_f32_16x16x32_bf16 v[224:227], v[164:167], a[40:43], v[224:227]
ds_read_b64_tr_b16 v[186:187], v31 offset:25088
)ASM"
R"ASM(
buffer_load_dwordx4 v3, s[44:47], s19 offen offset:0 lds
v_mfma_f32_16x16x32_bf16 v[228:231], v[160:163], a[36:39], v[228:231]
ds_read_b64_tr_b16 v[188:189], v31 offset:25600
v_mfma_f32_16x16x32_bf16 v[228:231], v[164:167], a[44:47], v[228:231]
ds_read_b64_tr_b16 v[190:191], v31 offset:26112
v_mfma_f32_16x16x32_bf16 v[232:235], v[168:171], a[32:35], v[232:235]
ds_read_b64_tr_b16 v[160:161], v28 offset:32768
v_mfma_f32_16x16x32_bf16 v[232:235], v[172:175], a[40:43], v[232:235]
ds_read_b64_tr_b16 v[162:163], v28 offset:33280
buffer_load_dwordx4 v4, s[44:47], s19 offen offset:1024 lds
v_mfma_f32_16x16x32_bf16 v[236:239], v[168:171], a[36:39], v[236:239]
ds_read_b64_tr_b16 v[164:165], v28 offset:40960
v_mfma_f32_16x16x32_bf16 v[236:239], v[172:175], a[44:47], v[236:239]
s_waitcnt lgkmcnt(3)
ds_read_b64_tr_b16 v[166:167], v28 offset:41472
v_mfma_f32_16x16x32_bf16 v[240:243], v[176:179], a[32:35], v[240:243]
ds_read_b64_tr_b16 v[168:169], v29 offset:32768
v_mfma_f32_16x16x32_bf16 v[240:243], v[180:183], a[40:43], v[240:243]
ds_read_b64_tr_b16 v[170:171], v29 offset:33280
s_add_u32 m0, s61, 98304
v_mfma_f32_16x16x32_bf16 v[244:247], v[176:179], a[36:39], v[244:247]
ds_read_b64_tr_b16 v[172:173], v29 offset:40960
buffer_load_dwordx4 v5, s[44:47], s18 offen offset:0 lds
v_mfma_f32_16x16x32_bf16 v[244:247], v[180:183], a[44:47], v[244:247]
ds_read_b64_tr_b16 v[174:175], v29 offset:41472
v_mfma_f32_16x16x32_bf16 v[248:251], v[184:187], a[32:35], v[248:251]
ds_read_b64_tr_b16 v[176:177], v30 offset:32768
v_mfma_f32_16x16x32_bf16 v[248:251], v[188:191], a[40:43], v[248:251]
ds_read_b64_tr_b16 v[178:179], v30 offset:33280
v_mfma_f32_16x16x32_bf16 v[252:255], v[184:187], a[36:39], v[252:255]
ds_read_b64_tr_b16 v[180:181], v30 offset:40960
buffer_load_dwordx4 v27, s[44:47], s19 offen offset:1024 lds
v_mfma_f32_16x16x32_bf16 v[252:255], v[188:191], a[44:47], v[252:255]
s_waitcnt lgkmcnt(3)
ds_read_b64_tr_b16 v[182:183], v30 offset:41472
v_mfma_f32_16x16x32_bf16 v[96:99], v[160:163], a[48:51], v[96:99]
ds_read_b64_tr_b16 v[184:185], v31 offset:32768
v_mfma_f32_16x16x32_bf16 v[96:99], v[164:167], a[56:59], v[96:99]
ds_read_b64_tr_b16 v[186:187], v31 offset:33280
v_mfma_f32_16x16x32_bf16 v[100:103], v[160:163], a[52:55], v[100:103]
ds_read_b64_tr_b16 v[188:189], v31 offset:40960
v_mfma_f32_16x16x32_bf16 v[100:103], v[164:167], a[60:63], v[100:103]
ds_read_b64_tr_b16 v[190:191], v31 offset:41472
s_add_u32 s62, s60, 1835008
v_mfma_f32_16x16x32_bf16 v[104:107], v[168:171], a[48:51], v[104:107]
ds_read_b64_tr_b16 v[160:161], v28 offset:33792
buffer_load_dwordx4 a[112:115], v2, s[96:99], s62 offen sc1
v_mfma_f32_16x16x32_bf16 v[104:107], v[172:175], a[56:59], v[104:107]
ds_read_b64_tr_b16 v[162:163], v28 offset:34304
v_mfma_f32_16x16x32_bf16 v[108:111], v[168:171], a[52:55], v[108:111]
ds_read_b64_tr_b16 v[164:165], v28 offset:41984
v_mfma_f32_16x16x32_bf16 v[108:111], v[172:175], a[60:63], v[108:111]
s_waitcnt lgkmcnt(3)
ds_read_b64_tr_b16 v[166:167], v28 offset:42496
v_mfma_f32_16x16x32_bf16 v[112:115], v[176:179], a[48:51], v[112:115]
ds_read_b64_tr_b16 v[168:169], v29 offset:33792
buffer_load_dwordx4 a[116:119], v2, s[96:99], s62 offen offset:512 sc1
v_mfma_f32_16x16x32_bf16 v[112:115], v[180:183], a[56:59], v[112:115]
ds_read_b64_tr_b16 v[170:171], v29 offset:34304
v_mfma_f32_16x16x32_bf16 v[116:119], v[176:179], a[52:55], v[116:119]
ds_read_b64_tr_b16 v[172:173], v29 offset:41984
v_mfma_f32_16x16x32_bf16 v[116:119], v[180:183], a[60:63], v[116:119]
ds_read_b64_tr_b16 v[174:175], v29 offset:42496
v_mfma_f32_16x16x32_bf16 v[120:123], v[184:187], a[48:51], v[120:123]
ds_read_b64_tr_b16 v[176:177], v30 offset:33792
v_mfma_f32_16x16x32_bf16 v[120:123], v[188:191], a[56:59], v[120:123]
ds_read_b64_tr_b16 v[178:179], v30 offset:34304
s_add_u32 s62, s60, 1966080
v_mfma_f32_16x16x32_bf16 v[124:127], v[184:187], a[52:55], v[124:127]
ds_read_b64_tr_b16 v[180:181], v30 offset:41984
buffer_load_dwordx4 a[120:123], v2, s[96:99], s62 offen sc1
v_mfma_f32_16x16x32_bf16 v[124:127], v[188:191], a[60:63], v[124:127]
s_waitcnt lgkmcnt(3)
ds_read_b64_tr_b16 v[182:183], v30 offset:42496
v_mfma_f32_16x16x32_bf16 v[128:131], v[160:163], a[48:51], v[128:131]
ds_read_b64_tr_b16 v[184:185], v31 offset:33792
v_mfma_f32_16x16x32_bf16 v[128:131], v[164:167], a[56:59], v[128:131]
ds_read_b64_tr_b16 v[186:187], v31 offset:34304
v_mfma_f32_16x16x32_bf16 v[132:135], v[160:163], a[52:55], v[132:135]
ds_read_b64_tr_b16 v[188:189], v31 offset:41984
buffer_load_dwordx4 a[124:127], v2, s[96:99], s62 offen offset:512 sc1
v_mfma_f32_16x16x32_bf16 v[132:135], v[164:167], a[60:63], v[132:135]
ds_read_b64_tr_b16 v[190:191], v31 offset:42496
v_mfma_f32_16x16x32_bf16 v[136:139], v[168:171], a[48:51], v[136:139]
ds_read_b64_tr_b16 v[160:161], v192 offset:0
v_mfma_f32_16x16x32_bf16 v[136:139], v[172:175], a[56:59], v[136:139]
ds_read_b64_tr_b16 v[162:163], v192 offset:512
v_mfma_f32_16x16x32_bf16 v[140:143], v[168:171], a[52:55], v[140:143]
ds_read_b64_tr_b16 v[164:165], v192 offset:1024
v_mfma_f32_16x16x32_bf16 v[140:143], v[172:175], a[60:63], v[140:143]
s_waitcnt lgkmcnt(3)
ds_read_b64_tr_b16 v[166:167], v192 offset:1536
v_mfma_f32_16x16x32_bf16 v[144:147], v[176:179], a[48:51], v[144:147]
ds_read_b64_tr_b16 v[168:169], v193 offset:0
s_add_u32 s62, s60, 2097152
v_mfma_f32_16x16x32_bf16 v[144:147], v[180:183], a[56:59], v[144:147]
ds_read_b64_tr_b16 v[170:171], v193 offset:512
buffer_load_dwordx4 a[128:131], v2, s[96:99], s62 offen sc1
v_mfma_f32_16x16x32_bf16 v[148:151], v[176:179], a[52:55], v[148:151]
ds_read_b64_tr_b16 v[172:173], v193 offset:1024
v_mfma_f32_16x16x32_bf16 v[148:151], v[180:183], a[60:63], v[148:151]
ds_read_b64_tr_b16 v[174:175], v193 offset:1536
v_mfma_f32_16x16x32_bf16 v[152:155], v[184:187], a[48:51], v[152:155]
ds_read_b64_tr_b16 v[176:177], v194 offset:0
v_mfma_f32_16x16x32_bf16 v[152:155], v[188:191], a[56:59], v[152:155]
ds_read_b64_tr_b16 v[178:179], v194 offset:512
buffer_load_dwordx4 a[132:135], v2, s[96:99], s62 offen offset:512 sc1
v_mfma_f32_16x16x32_bf16 v[156:159], v[184:187], a[52:55], v[156:159]
ds_read_b64_tr_b16 v[180:181], v194 offset:1024
v_mfma_f32_16x16x32_bf16 v[156:159], v[188:191], a[60:63], v[156:159]
s_waitcnt lgkmcnt(3)
ds_read_b64_tr_b16 v[182:183], v194 offset:1536
v_mfma_f32_16x16x32_bf16 v[224:227], v[160:163], a[48:51], v[224:227]
ds_read_b64_tr_b16 v[184:185], v195 offset:0
v_mfma_f32_16x16x32_bf16 v[224:227], v[164:167], a[56:59], v[224:227]
ds_read_b64_tr_b16 v[186:187], v195 offset:512
s_add_u32 s62, s60, 2228224
v_mfma_f32_16x16x32_bf16 v[228:231], v[160:163], a[52:55], v[228:231]
ds_read_b64_tr_b16 v[188:189], v195 offset:1024
buffer_load_dwordx4 a[136:139], v2, s[96:99], s62 offen sc1
v_mfma_f32_16x16x32_bf16 v[228:231], v[164:167], a[60:63], v[228:231]
ds_read_b64_tr_b16 v[190:191], v195 offset:1536
v_mfma_f32_16x16x32_bf16 v[232:235], v[168:171], a[48:51], v[232:235]
s_waitcnt vmcnt(23) lgkmcnt(0)
s_barrier
v_mfma_f32_16x16x32_bf16 v[232:235], v[172:175], a[56:59], v[232:235]
ds_read_b64_tr_b16 v[160:161], v192 offset:8192
v_mfma_f32_16x16x32_bf16 v[236:239], v[168:171], a[52:55], v[236:239]
ds_read_b64_tr_b16 v[162:163], v192 offset:8704
buffer_load_dwordx4 a[140:143], v2, s[96:99], s62 offen offset:512 sc1
v_mfma_f32_16x16x32_bf16 v[236:239], v[172:175], a[60:63], v[236:239]
ds_read_b64_tr_b16 v[164:165], v192 offset:16384
ds_read_b64_tr_b16 v[166:167], v192 offset:16896
v_mfma_f32_16x16x32_bf16 v[240:243], v[176:179], a[48:51], v[240:243]
ds_read_b64_tr_b16 v[168:169], v193 offset:8192
v_mfma_f32_16x16x32_bf16 v[240:243], v[180:183], a[56:59], v[240:243]
ds_read_b64_tr_b16 v[170:171], v193 offset:8704
v_mfma_f32_16x16x32_bf16 v[244:247], v[176:179], a[52:55], v[244:247]
ds_read_b64_tr_b16 v[172:173], v193 offset:16384
v_mfma_f32_16x16x32_bf16 v[244:247], v[180:183], a[60:63], v[244:247]
ds_read_b64_tr_b16 v[174:175], v193 offset:16896
s_add_u32 m0, s61, 106496
v_mfma_f32_16x16x32_bf16 v[248:251], v[184:187], a[48:51], v[248:251]
ds_read_b64_tr_b16 v[176:177], v194 offset:8192
buffer_load_dwordx4 v3, s[44:47], s20 offen offset:0 lds
v_mfma_f32_16x16x32_bf16 v[248:251], v[188:191], a[56:59], v[248:251]
ds_read_b64_tr_b16 v[178:179], v194 offset:8704
v_mfma_f32_16x16x32_bf16 v[252:255], v[184:187], a[52:55], v[252:255]
ds_read_b64_tr_b16 v[180:181], v194 offset:16384
v_mfma_f32_16x16x32_bf16 v[252:255], v[188:191], a[60:63], v[252:255]
s_waitcnt lgkmcnt(3)
ds_read_b64_tr_b16 v[182:183], v194 offset:16896
v_mfma_f32_16x16x32_bf16 v[96:99], v[160:163], a[64:67], v[96:99]
ds_read_b64_tr_b16 v[184:185], v195 offset:8192
buffer_load_dwordx4 v4, s[44:47], s20 offen offset:1024 lds
v_mfma_f32_16x16x32_bf16 v[96:99], v[164:167], a[72:75], v[96:99]
ds_read_b64_tr_b16 v[186:187], v195 offset:8704
v_mfma_f32_16x16x32_bf16 v[100:103], v[160:163], a[68:71], v[100:103]
ds_read_b64_tr_b16 v[188:189], v195 offset:16384
v_mfma_f32_16x16x32_bf16 v[100:103], v[164:167], a[76:79], v[100:103]
ds_read_b64_tr_b16 v[190:191], v195 offset:16896
v_mfma_f32_16x16x32_bf16 v[104:107], v[168:171], a[64:67], v[104:107]
ds_read_b64_tr_b16 v[160:161], v192 offset:9216
v_mfma_f32_16x16x32_bf16 v[104:107], v[172:175], a[72:75], v[104:107]
ds_read_b64_tr_b16 v[162:163], v192 offset:9728
s_add_u32 m0, s61, 114688
v_mfma_f32_16x16x32_bf16 v[108:111], v[168:171], a[68:71], v[108:111]
ds_read_b64_tr_b16 v[164:165], v192 offset:17408
buffer_load_dwordx4 v3, s[44:47], s21 offen offset:0 lds
v_mfma_f32_16x16x32_bf16 v[108:111], v[172:175], a[76:79], v[108:111]
s_waitcnt lgkmcnt(3)
ds_read_b64_tr_b16 v[166:167], v192 offset:17920
v_mfma_f32_16x16x32_bf16 v[112:115], v[176:179], a[64:67], v[112:115]
ds_read_b64_tr_b16 v[168:169], v193 offset:9216
v_mfma_f32_16x16x32_bf16 v[112:115], v[180:183], a[72:75], v[112:115]
ds_read_b64_tr_b16 v[170:171], v193 offset:9728
v_mfma_f32_16x16x32_bf16 v[116:119], v[176:179], a[68:71], v[116:119]
ds_read_b64_tr_b16 v[172:173], v193 offset:17408
buffer_load_dwordx4 v4, s[44:47], s21 offen offset:1024 lds
v_mfma_f32_16x16x32_bf16 v[116:119], v[180:183], a[76:79], v[116:119]
ds_read_b64_tr_b16 v[174:175], v193 offset:17920
v_mfma_f32_16x16x32_bf16 v[120:123], v[184:187], a[64:67], v[120:123]
ds_read_b64_tr_b16 v[176:177], v194 offset:9216
v_mfma_f32_16x16x32_bf16 v[120:123], v[188:191], a[72:75], v[120:123]
ds_read_b64_tr_b16 v[178:179], v194 offset:9728
v_mfma_f32_16x16x32_bf16 v[124:127], v[184:187], a[68:71], v[124:127]
ds_read_b64_tr_b16 v[180:181], v194 offset:17408
v_mfma_f32_16x16x32_bf16 v[124:127], v[188:191], a[76:79], v[124:127]
s_waitcnt lgkmcnt(3)
ds_read_b64_tr_b16 v[182:183], v194 offset:17920
v_mfma_f32_16x16x32_bf16 v[128:131], v[160:163], a[64:67], v[128:131]
ds_read_b64_tr_b16 v[184:185], v195 offset:9216
s_add_u32 m0, s61, 122880
v_mfma_f32_16x16x32_bf16 v[128:131], v[164:167], a[72:75], v[128:131]
ds_read_b64_tr_b16 v[186:187], v195 offset:9728
buffer_load_dwordx4 v3, s[44:47], s22 offen offset:0 lds
v_mfma_f32_16x16x32_bf16 v[132:135], v[160:163], a[68:71], v[132:135]
ds_read_b64_tr_b16 v[188:189], v195 offset:17408
v_mfma_f32_16x16x32_bf16 v[132:135], v[164:167], a[76:79], v[132:135]
ds_read_b64_tr_b16 v[190:191], v195 offset:17920
)ASM"
R"ASM(
v_mfma_f32_16x16x32_bf16 v[136:139], v[168:171], a[64:67], v[136:139]
ds_read_b64_tr_b16 v[160:161], v192 offset:24576
v_mfma_f32_16x16x32_bf16 v[136:139], v[172:175], a[72:75], v[136:139]
ds_read_b64_tr_b16 v[162:163], v192 offset:25088
buffer_load_dwordx4 v4, s[44:47], s22 offen offset:1024 lds
v_mfma_f32_16x16x32_bf16 v[140:143], v[168:171], a[68:71], v[140:143]
ds_read_b64_tr_b16 v[164:165], v192 offset:25600
v_mfma_f32_16x16x32_bf16 v[140:143], v[172:175], a[76:79], v[140:143]
s_waitcnt lgkmcnt(3)
ds_read_b64_tr_b16 v[166:167], v192 offset:26112
v_mfma_f32_16x16x32_bf16 v[144:147], v[176:179], a[64:67], v[144:147]
ds_read_b64_tr_b16 v[168:169], v193 offset:24576
v_mfma_f32_16x16x32_bf16 v[144:147], v[180:183], a[72:75], v[144:147]
ds_read_b64_tr_b16 v[170:171], v193 offset:25088
s_add_u32 m0, s61, 131072
v_mfma_f32_16x16x32_bf16 v[148:151], v[176:179], a[68:71], v[148:151]
buffer_load_dword v17, v15, s[96:99], 0 offen sc1
ds_read_b64_tr_b16 v[172:173], v193 offset:25600
v_mfma_f32_16x16x32_bf16 v[148:151], v[180:183], a[76:79], v[148:151]
ds_read_b64_tr_b16 v[174:175], v193 offset:26112
v_mfma_f32_16x16x32_bf16 v[152:155], v[184:187], a[64:67], v[152:155]
ds_read_b64_tr_b16 v[176:177], v194 offset:24576
v_mfma_f32_16x16x32_bf16 v[152:155], v[188:191], a[72:75], v[152:155]
ds_read_b64_tr_b16 v[178:179], v194 offset:25088
v_mfma_f32_16x16x32_bf16 v[156:159], v[184:187], a[68:71], v[156:159]
ds_read_b64_tr_b16 v[180:181], v194 offset:25600
buffer_load_dwordx4 v3, s[44:47], s23 offen offset:0 lds
v_mfma_f32_16x16x32_bf16 v[156:159], v[188:191], a[76:79], v[156:159]
s_waitcnt lgkmcnt(3)
ds_read_b64_tr_b16 v[182:183], v194 offset:26112
v_mfma_f32_16x16x32_bf16 v[224:227], v[160:163], a[64:67], v[224:227]
ds_read_b64_tr_b16 v[184:185], v195 offset:24576
v_mfma_f32_16x16x32_bf16 v[224:227], v[164:167], a[72:75], v[224:227]
ds_read_b64_tr_b16 v[186:187], v195 offset:25088
v_mfma_f32_16x16x32_bf16 v[228:231], v[160:163], a[68:71], v[228:231]
ds_read_b64_tr_b16 v[188:189], v195 offset:25600
buffer_load_dwordx4 v4, s[44:47], s23 offen offset:1024 lds
v_mfma_f32_16x16x32_bf16 v[228:231], v[164:167], a[76:79], v[228:231]
ds_read_b64_tr_b16 v[190:191], v195 offset:26112
s_add_u32 s62, s60, 2359296
v_mfma_f32_16x16x32_bf16 v[232:235], v[168:171], a[64:67], v[232:235]
ds_read_b64_tr_b16 v[160:161], v192 offset:32768
v_mfma_f32_16x16x32_bf16 v[232:235], v[172:175], a[72:75], v[232:235]
ds_read_b64_tr_b16 v[162:163], v192 offset:33280
v_mfma_f32_16x16x32_bf16 v[236:239], v[168:171], a[68:71], v[236:239]
ds_read_b64_tr_b16 v[164:165], v192 offset:40960
buffer_load_dwordx4 a[144:147], v2, s[96:99], s62 offen sc1
v_mfma_f32_16x16x32_bf16 v[236:239], v[172:175], a[76:79], v[236:239]
s_waitcnt lgkmcnt(3)
ds_read_b64_tr_b16 v[166:167], v192 offset:41472
v_mfma_f32_16x16x32_bf16 v[240:243], v[176:179], a[64:67], v[240:243]
ds_read_b64_tr_b16 v[168:169], v193 offset:32768
v_mfma_f32_16x16x32_bf16 v[240:243], v[180:183], a[72:75], v[240:243]
ds_read_b64_tr_b16 v[170:171], v193 offset:33280
v_mfma_f32_16x16x32_bf16 v[244:247], v[176:179], a[68:71], v[244:247]
ds_read_b64_tr_b16 v[172:173], v193 offset:40960
buffer_load_dwordx4 a[148:151], v2, s[96:99], s62 offen offset:512 sc1
v_mfma_f32_16x16x32_bf16 v[244:247], v[180:183], a[76:79], v[244:247]
ds_read_b64_tr_b16 v[174:175], v193 offset:41472
v_mfma_f32_16x16x32_bf16 v[248:251], v[184:187], a[64:67], v[248:251]
ds_read_b64_tr_b16 v[176:177], v194 offset:32768
v_mfma_f32_16x16x32_bf16 v[248:251], v[188:191], a[72:75], v[248:251]
ds_read_b64_tr_b16 v[178:179], v194 offset:33280
s_add_u32 s62, s60, 2490368
v_mfma_f32_16x16x32_bf16 v[252:255], v[184:187], a[68:71], v[252:255]
ds_read_b64_tr_b16 v[180:181], v194 offset:40960
buffer_load_dwordx4 a[152:155], v2, s[96:99], s62 offen sc1
v_mfma_f32_16x16x32_bf16 v[252:255], v[188:191], a[76:79], v[252:255]
s_waitcnt lgkmcnt(3)
ds_read_b64_tr_b16 v[182:183], v194 offset:41472
v_mfma_f32_16x16x32_bf16 v[96:99], v[160:163], a[80:83], v[96:99]
ds_read_b64_tr_b16 v[184:185], v195 offset:32768
v_mfma_f32_16x16x32_bf16 v[96:99], v[164:167], a[88:91], v[96:99]
ds_read_b64_tr_b16 v[186:187], v195 offset:33280
v_mfma_f32_16x16x32_bf16 v[100:103], v[160:163], a[84:87], v[100:103]
ds_read_b64_tr_b16 v[188:189], v195 offset:40960
buffer_load_dwordx4 a[156:159], v2, s[96:99], s62 offen offset:512 sc1
v_mfma_f32_16x16x32_bf16 v[100:103], v[164:167], a[92:95], v[100:103]
ds_read_b64_tr_b16 v[190:191], v195 offset:41472
v_mfma_f32_16x16x32_bf16 v[104:107], v[168:171], a[80:83], v[104:107]
s_waitcnt vmcnt(31) lgkmcnt(0)
s_barrier
v_mfma_f32_16x16x32_bf16 v[104:107], v[172:175], a[88:91], v[104:107]
ds_read_b64_tr_b16 v[160:161], v192 offset:33792
v_mfma_f32_16x16x32_bf16 v[108:111], v[168:171], a[84:87], v[108:111]
ds_read_b64_tr_b16 v[162:163], v192 offset:34304
v_mfma_f32_16x16x32_bf16 v[108:111], v[172:175], a[92:95], v[108:111]
ds_read_b64_tr_b16 v[164:165], v192 offset:41984
ds_read_b64_tr_b16 v[166:167], v192 offset:42496
v_mfma_f32_16x16x32_bf16 v[112:115], v[176:179], a[80:83], v[112:115]
ds_read_b64_tr_b16 v[168:169], v193 offset:33792
s_add_u32 s62, s60, 2621440
v_mfma_f32_16x16x32_bf16 v[112:115], v[180:183], a[88:91], v[112:115]
ds_read_b64_tr_b16 v[170:171], v193 offset:34304
buffer_load_dwordx4 a[160:163], v2, s[96:99], s62 offen sc1
v_mfma_f32_16x16x32_bf16 v[116:119], v[176:179], a[84:87], v[116:119]
ds_read_b64_tr_b16 v[172:173], v193 offset:41984
v_mfma_f32_16x16x32_bf16 v[116:119], v[180:183], a[92:95], v[116:119]
ds_read_b64_tr_b16 v[174:175], v193 offset:42496
v_mfma_f32_16x16x32_bf16 v[120:123], v[184:187], a[80:83], v[120:123]
ds_read_b64_tr_b16 v[176:177], v194 offset:33792
v_mfma_f32_16x16x32_bf16 v[120:123], v[188:191], a[88:91], v[120:123]
ds_read_b64_tr_b16 v[178:179], v194 offset:34304
buffer_load_dwordx4 a[164:167], v2, s[96:99], s62 offen offset:512 sc1
v_mfma_f32_16x16x32_bf16 v[124:127], v[184:187], a[84:87], v[124:127]
ds_read_b64_tr_b16 v[180:181], v194 offset:41984
v_mfma_f32_16x16x32_bf16 v[124:127], v[188:191], a[92:95], v[124:127]
s_waitcnt lgkmcnt(3)
ds_read_b64_tr_b16 v[182:183], v194 offset:42496
v_mfma_f32_16x16x32_bf16 v[128:131], v[160:163], a[80:83], v[128:131]
ds_read_b64_tr_b16 v[184:185], v195 offset:33792
v_mfma_f32_16x16x32_bf16 v[128:131], v[164:167], a[88:91], v[128:131]
ds_read_b64_tr_b16 v[186:187], v195 offset:34304
s_add_u32 s62, s60, 2752512
v_mfma_f32_16x16x32_bf16 v[132:135], v[160:163], a[84:87], v[132:135]
ds_read_b64_tr_b16 v[188:189], v195 offset:41984
buffer_load_dwordx4 a[168:171], v2, s[96:99], s62 offen sc1
v_mfma_f32_16x16x32_bf16 v[132:135], v[164:167], a[92:95], v[132:135]
ds_read_b64_tr_b16 v[190:191], v195 offset:42496
v_mfma_f32_16x16x32_bf16 v[136:139], v[168:171], a[80:83], v[136:139]
ds_read_b64_tr_b16 v[160:161], v196 offset:0
v_mfma_f32_16x16x32_bf16 v[136:139], v[172:175], a[88:91], v[136:139]
ds_read_b64_tr_b16 v[162:163], v196 offset:512
v_mfma_f32_16x16x32_bf16 v[140:143], v[168:171], a[84:87], v[140:143]
ds_read_b64_tr_b16 v[164:165], v196 offset:1024
buffer_load_dwordx4 a[172:175], v2, s[96:99], s62 offen offset:512 sc1
v_mfma_f32_16x16x32_bf16 v[140:143], v[172:175], a[92:95], v[140:143]
s_waitcnt lgkmcnt(3)
ds_read_b64_tr_b16 v[166:167], v196 offset:1536
v_mfma_f32_16x16x32_bf16 v[144:147], v[176:179], a[80:83], v[144:147]
ds_read_b64_tr_b16 v[168:169], v197 offset:0
v_mfma_f32_16x16x32_bf16 v[144:147], v[180:183], a[88:91], v[144:147]
ds_read_b64_tr_b16 v[170:171], v197 offset:512
v_mfma_f32_16x16x32_bf16 v[148:151], v[176:179], a[84:87], v[148:151]
ds_read_b64_tr_b16 v[172:173], v197 offset:1024
v_mfma_f32_16x16x32_bf16 v[148:151], v[180:183], a[92:95], v[148:151]
ds_read_b64_tr_b16 v[174:175], v197 offset:1536
s_add_u32 m0, s61, 139264
v_mfma_f32_16x16x32_bf16 v[152:155], v[184:187], a[80:83], v[152:155]
ds_read_b64_tr_b16 v[176:177], v198 offset:0
buffer_load_dwordx4 v3, s[44:47], s24 offen offset:0 lds
v_mfma_f32_16x16x32_bf16 v[152:155], v[188:191], a[88:91], v[152:155]
ds_read_b64_tr_b16 v[178:179], v198 offset:512
v_mfma_f32_16x16x32_bf16 v[156:159], v[184:187], a[84:87], v[156:159]
ds_read_b64_tr_b16 v[180:181], v198 offset:1024
v_mfma_f32_16x16x32_bf16 v[156:159], v[188:191], a[92:95], v[156:159]
s_waitcnt lgkmcnt(3)
ds_read_b64_tr_b16 v[182:183], v198 offset:1536
v_mfma_f32_16x16x32_bf16 v[224:227], v[160:163], a[80:83], v[224:227]
ds_read_b64_tr_b16 v[184:185], v199 offset:0
buffer_load_dwordx4 v4, s[44:47], s24 offen offset:1024 lds
v_mfma_f32_16x16x32_bf16 v[224:227], v[164:167], a[88:91], v[224:227]
ds_read_b64_tr_b16 v[186:187], v199 offset:512
v_mfma_f32_16x16x32_bf16 v[228:231], v[160:163], a[84:87], v[228:231]
ds_read_b64_tr_b16 v[188:189], v199 offset:1024
v_mfma_f32_16x16x32_bf16 v[228:231], v[164:167], a[92:95], v[228:231]
ds_read_b64_tr_b16 v[190:191], v199 offset:1536
v_mfma_f32_16x16x32_bf16 v[232:235], v[168:171], a[80:83], v[232:235]
ds_read_b64_tr_b16 v[160:161], v196 offset:8192
v_mfma_f32_16x16x32_bf16 v[232:235], v[172:175], a[88:91], v[232:235]
ds_read_b64_tr_b16 v[162:163], v196 offset:8704
s_add_u32 m0, s61, 0
v_mfma_f32_16x16x32_bf16 v[236:239], v[168:171], a[84:87], v[236:239]
ds_read_b64_tr_b16 v[164:165], v196 offset:16384
buffer_load_dwordx4 v3, s[44:47], s25 offen offset:0 lds
v_mfma_f32_16x16x32_bf16 v[236:239], v[172:175], a[92:95], v[236:239]
s_waitcnt lgkmcnt(3)
ds_read_b64_tr_b16 v[166:167], v196 offset:16896
v_mfma_f32_16x16x32_bf16 v[240:243], v[176:179], a[80:83], v[240:243]
ds_read_b64_tr_b16 v[168:169], v197 offset:8192
v_mfma_f32_16x16x32_bf16 v[240:243], v[180:183], a[88:91], v[240:243]
ds_read_b64_tr_b16 v[170:171], v197 offset:8704
v_mfma_f32_16x16x32_bf16 v[244:247], v[176:179], a[84:87], v[244:247]
ds_read_b64_tr_b16 v[172:173], v197 offset:16384
buffer_load_dwordx4 v4, s[44:47], s25 offen offset:1024 lds
v_mfma_f32_16x16x32_bf16 v[244:247], v[180:183], a[92:95], v[244:247]
ds_read_b64_tr_b16 v[174:175], v197 offset:16896
v_mfma_f32_16x16x32_bf16 v[248:251], v[184:187], a[80:83], v[248:251]
ds_read_b64_tr_b16 v[176:177], v198 offset:8192
v_mfma_f32_16x16x32_bf16 v[248:251], v[188:191], a[88:91], v[248:251]
ds_read_b64_tr_b16 v[178:179], v198 offset:8704
v_mfma_f32_16x16x32_bf16 v[252:255], v[184:187], a[84:87], v[252:255]
ds_read_b64_tr_b16 v[180:181], v198 offset:16384
v_mfma_f32_16x16x32_bf16 v[252:255], v[188:191], a[92:95], v[252:255]
s_waitcnt lgkmcnt(3)
ds_read_b64_tr_b16 v[182:183], v198 offset:16896
v_mfma_f32_16x16x32_bf16 v[96:99], v[160:163], a[96:99], v[96:99]
ds_read_b64_tr_b16 v[184:185], v199 offset:8192
s_add_u32 m0, s61, 8192
v_mfma_f32_16x16x32_bf16 v[96:99], v[164:167], a[104:107], v[96:99]
ds_read_b64_tr_b16 v[186:187], v199 offset:8704
buffer_load_dwordx4 v3, s[44:47], s26 offen offset:0 lds
v_mfma_f32_16x16x32_bf16 v[100:103], v[160:163], a[100:103], v[100:103]
ds_read_b64_tr_b16 v[188:189], v199 offset:16384
v_mfma_f32_16x16x32_bf16 v[100:103], v[164:167], a[108:111], v[100:103]
ds_read_b64_tr_b16 v[190:191], v199 offset:16896
v_mfma_f32_16x16x32_bf16 v[104:107], v[168:171], a[96:99], v[104:107]
ds_read_b64_tr_b16 v[160:161], v196 offset:9216
v_mfma_f32_16x16x32_bf16 v[104:107], v[172:175], a[104:107], v[104:107]
ds_read_b64_tr_b16 v[162:163], v196 offset:9728
)ASM"
R"ASM(
buffer_load_dwordx4 v4, s[44:47], s26 offen offset:1024 lds
v_mfma_f32_16x16x32_bf16 v[108:111], v[168:171], a[100:103], v[108:111]
ds_read_b64_tr_b16 v[164:165], v196 offset:17408
v_mfma_f32_16x16x32_bf16 v[108:111], v[172:175], a[108:111], v[108:111]
s_waitcnt lgkmcnt(3)
ds_read_b64_tr_b16 v[166:167], v196 offset:17920
v_mfma_f32_16x16x32_bf16 v[112:115], v[176:179], a[96:99], v[112:115]
ds_read_b64_tr_b16 v[168:169], v197 offset:9216
v_mfma_f32_16x16x32_bf16 v[112:115], v[180:183], a[104:107], v[112:115]
ds_read_b64_tr_b16 v[170:171], v197 offset:9728
s_add_u32 m0, s61, 16384
v_mfma_f32_16x16x32_bf16 v[116:119], v[176:179], a[100:103], v[116:119]
ds_read_b64_tr_b16 v[172:173], v197 offset:17408
buffer_load_dwordx4 v3, s[44:47], s27 offen offset:0 lds
v_mfma_f32_16x16x32_bf16 v[116:119], v[180:183], a[108:111], v[116:119]
ds_read_b64_tr_b16 v[174:175], v197 offset:17920
v_mfma_f32_16x16x32_bf16 v[120:123], v[184:187], a[96:99], v[120:123]
ds_read_b64_tr_b16 v[176:177], v198 offset:9216
v_mfma_f32_16x16x32_bf16 v[120:123], v[188:191], a[104:107], v[120:123]
ds_read_b64_tr_b16 v[178:179], v198 offset:9728
v_mfma_f32_16x16x32_bf16 v[124:127], v[184:187], a[100:103], v[124:127]
ds_read_b64_tr_b16 v[180:181], v198 offset:17408
buffer_load_dwordx4 v4, s[44:47], s27 offen offset:1024 lds
v_mfma_f32_16x16x32_bf16 v[124:127], v[188:191], a[108:111], v[124:127]
s_waitcnt lgkmcnt(3)
ds_read_b64_tr_b16 v[182:183], v198 offset:17920
v_mfma_f32_16x16x32_bf16 v[128:131], v[160:163], a[96:99], v[128:131]
ds_read_b64_tr_b16 v[184:185], v199 offset:9216
v_mfma_f32_16x16x32_bf16 v[128:131], v[164:167], a[104:107], v[128:131]
ds_read_b64_tr_b16 v[186:187], v199 offset:9728
v_mfma_f32_16x16x32_bf16 v[132:135], v[160:163], a[100:103], v[132:135]
ds_read_b64_tr_b16 v[188:189], v199 offset:17408
v_mfma_f32_16x16x32_bf16 v[132:135], v[164:167], a[108:111], v[132:135]
ds_read_b64_tr_b16 v[190:191], v199 offset:17920
s_add_u32 m0, s61, 24576
v_mfma_f32_16x16x32_bf16 v[136:139], v[168:171], a[96:99], v[136:139]
s_waitcnt vmcnt(29) lgkmcnt(0)
s_barrier
s_mov_b32 s65, 20000
s_waitcnt vmcnt(18)
LOC4_%=:
v_cmp_ne_u32 s[92:93], s43, v17
s_nop 3
s_and_b32 s92, s92, 0xffc00000
s_and_b32 s93, s93, 0xf
s_cmp_eq_u64 s[92:93], 0
s_cbranch_scc1 LOD4_%=
s_sub_u32 s65, s65, 1
s_cmp_eq_u32 s65, 0
s_cbranch_scc1 LOD4_%=
s_sleep 2
buffer_load_dword v17, v15, s[96:99], 0 offen sc1
s_waitcnt vmcnt(0)
s_branch LOC4_%=
LOD4_%=:
v_mfma_f32_16x16x32_bf16 v[136:139], v[172:175], a[104:107], v[136:139]
ds_read_b64_tr_b16 v[160:161], v196 offset:24576
buffer_load_dwordx4 v3, s[44:47], s28 offen offset:0 lds
v_mfma_f32_16x16x32_bf16 v[140:143], v[168:171], a[100:103], v[140:143]
ds_read_b64_tr_b16 v[162:163], v196 offset:25088
v_mfma_f32_16x16x32_bf16 v[140:143], v[172:175], a[108:111], v[140:143]
ds_read_b64_tr_b16 v[164:165], v196 offset:25600
ds_read_b64_tr_b16 v[166:167], v196 offset:26112
v_mfma_f32_16x16x32_bf16 v[144:147], v[176:179], a[96:99], v[144:147]
ds_read_b64_tr_b16 v[168:169], v197 offset:24576
v_mfma_f32_16x16x32_bf16 v[144:147], v[180:183], a[104:107], v[144:147]
ds_read_b64_tr_b16 v[170:171], v197 offset:25088
buffer_load_dwordx4 v4, s[44:47], s28 offen offset:1024 lds
v_mfma_f32_16x16x32_bf16 v[148:151], v[176:179], a[100:103], v[148:151]
ds_read_b64_tr_b16 v[172:173], v197 offset:25600
v_mfma_f32_16x16x32_bf16 v[148:151], v[180:183], a[108:111], v[148:151]
ds_read_b64_tr_b16 v[174:175], v197 offset:26112
v_mfma_f32_16x16x32_bf16 v[152:155], v[184:187], a[96:99], v[152:155]
ds_read_b64_tr_b16 v[176:177], v198 offset:24576
v_mfma_f32_16x16x32_bf16 v[152:155], v[188:191], a[104:107], v[152:155]
ds_read_b64_tr_b16 v[178:179], v198 offset:25088
s_add_u32 s62, s60, 2883584
v_mfma_f32_16x16x32_bf16 v[156:159], v[184:187], a[100:103], v[156:159]
ds_read_b64_tr_b16 v[180:181], v198 offset:25600
buffer_load_dwordx4 a[176:179], v2, s[96:99], s62 offen sc1
v_mfma_f32_16x16x32_bf16 v[156:159], v[188:191], a[108:111], v[156:159]
s_waitcnt lgkmcnt(3)
ds_read_b64_tr_b16 v[182:183], v198 offset:26112
v_mfma_f32_16x16x32_bf16 v[224:227], v[160:163], a[96:99], v[224:227]
ds_read_b64_tr_b16 v[184:185], v199 offset:24576
v_mfma_f32_16x16x32_bf16 v[224:227], v[164:167], a[104:107], v[224:227]
ds_read_b64_tr_b16 v[186:187], v199 offset:25088
v_mfma_f32_16x16x32_bf16 v[228:231], v[160:163], a[100:103], v[228:231]
ds_read_b64_tr_b16 v[188:189], v199 offset:25600
buffer_load_dwordx4 a[180:183], v2, s[96:99], s62 offen offset:512 sc1
v_mfma_f32_16x16x32_bf16 v[228:231], v[164:167], a[108:111], v[228:231]
ds_read_b64_tr_b16 v[190:191], v199 offset:26112
v_mfma_f32_16x16x32_bf16 v[232:235], v[168:171], a[96:99], v[232:235]
ds_read_b64_tr_b16 v[160:161], v196 offset:32768
v_mfma_f32_16x16x32_bf16 v[232:235], v[172:175], a[104:107], v[232:235]
ds_read_b64_tr_b16 v[162:163], v196 offset:33280
v_mfma_f32_16x16x32_bf16 v[236:239], v[168:171], a[100:103], v[236:239]
ds_read_b64_tr_b16 v[164:165], v196 offset:40960
v_mfma_f32_16x16x32_bf16 v[236:239], v[172:175], a[108:111], v[236:239]
s_waitcnt lgkmcnt(3)
ds_read_b64_tr_b16 v[166:167], v196 offset:41472
v_mfma_f32_16x16x32_bf16 v[240:243], v[176:179], a[96:99], v[240:243]
ds_read_b64_tr_b16 v[168:169], v197 offset:32768
s_add_u32 s62, s60, 3014656
v_mfma_f32_16x16x32_bf16 v[240:243], v[180:183], a[104:107], v[240:243]
ds_read_b64_tr_b16 v[170:171], v197 offset:33280
buffer_load_dwordx4 a[184:187], v2, s[96:99], s62 offen sc1
v_mfma_f32_16x16x32_bf16 v[244:247], v[176:179], a[100:103], v[244:247]
ds_read_b64_tr_b16 v[172:173], v197 offset:40960
v_mfma_f32_16x16x32_bf16 v[244:247], v[180:183], a[108:111], v[244:247]
ds_read_b64_tr_b16 v[174:175], v197 offset:41472
v_mfma_f32_16x16x32_bf16 v[248:251], v[184:187], a[96:99], v[248:251]
ds_read_b64_tr_b16 v[176:177], v198 offset:32768
v_mfma_f32_16x16x32_bf16 v[248:251], v[188:191], a[104:107], v[248:251]
ds_read_b64_tr_b16 v[178:179], v198 offset:33280
buffer_load_dwordx4 a[188:191], v2, s[96:99], s62 offen offset:512 sc1
v_mfma_f32_16x16x32_bf16 v[252:255], v[184:187], a[100:103], v[252:255]
ds_read_b64_tr_b16 v[180:181], v198 offset:40960
v_mfma_f32_16x16x32_bf16 v[252:255], v[188:191], a[108:111], v[252:255]
s_waitcnt lgkmcnt(3)
ds_read_b64_tr_b16 v[182:183], v198 offset:41472
v_mfma_f32_16x16x32_bf16 v[96:99], v[160:163], a[112:115], v[96:99]
ds_read_b64_tr_b16 v[184:185], v199 offset:32768
v_mfma_f32_16x16x32_bf16 v[96:99], v[164:167], a[120:123], v[96:99]
ds_read_b64_tr_b16 v[186:187], v199 offset:33280
s_add_u32 s62, s60, 3145728
v_mfma_f32_16x16x32_bf16 v[100:103], v[160:163], a[116:119], v[100:103]
ds_read_b64_tr_b16 v[188:189], v199 offset:40960
buffer_load_dwordx4 a[192:195], v2, s[96:99], s62 offen sc1
v_mfma_f32_16x16x32_bf16 v[100:103], v[164:167], a[124:127], v[100:103]
ds_read_b64_tr_b16 v[190:191], v199 offset:41472
v_mfma_f32_16x16x32_bf16 v[104:107], v[168:171], a[112:115], v[104:107]
ds_read_b64_tr_b16 v[160:161], v196 offset:33792
v_mfma_f32_16x16x32_bf16 v[104:107], v[172:175], a[120:123], v[104:107]
ds_read_b64_tr_b16 v[162:163], v196 offset:34304
v_mfma_f32_16x16x32_bf16 v[108:111], v[168:171], a[116:119], v[108:111]
ds_read_b64_tr_b16 v[164:165], v196 offset:41984
buffer_load_dwordx4 a[196:199], v2, s[96:99], s62 offen offset:512 sc1
v_mfma_f32_16x16x32_bf16 v[108:111], v[172:175], a[124:127], v[108:111]
s_waitcnt lgkmcnt(3)
ds_read_b64_tr_b16 v[166:167], v196 offset:42496
v_mfma_f32_16x16x32_bf16 v[112:115], v[176:179], a[112:115], v[112:115]
ds_read_b64_tr_b16 v[168:169], v197 offset:33792
v_mfma_f32_16x16x32_bf16 v[112:115], v[180:183], a[120:123], v[112:115]
ds_read_b64_tr_b16 v[170:171], v197 offset:34304
v_mfma_f32_16x16x32_bf16 v[116:119], v[176:179], a[116:119], v[116:119]
ds_read_b64_tr_b16 v[172:173], v197 offset:41984
v_mfma_f32_16x16x32_bf16 v[116:119], v[180:183], a[124:127], v[116:119]
ds_read_b64_tr_b16 v[174:175], v197 offset:42496
s_add_u32 s62, s60, 3276800
v_mfma_f32_16x16x32_bf16 v[120:123], v[184:187], a[112:115], v[120:123]
ds_read_b64_tr_b16 v[176:177], v198 offset:33792
buffer_load_dwordx4 a[200:203], v2, s[96:99], s62 offen sc1
v_mfma_f32_16x16x32_bf16 v[120:123], v[188:191], a[120:123], v[120:123]
ds_read_b64_tr_b16 v[178:179], v198 offset:34304
v_mfma_f32_16x16x32_bf16 v[124:127], v[184:187], a[116:119], v[124:127]
ds_read_b64_tr_b16 v[180:181], v198 offset:41984
v_mfma_f32_16x16x32_bf16 v[124:127], v[188:191], a[124:127], v[124:127]
s_waitcnt lgkmcnt(3)
ds_read_b64_tr_b16 v[182:183], v198 offset:42496
v_mfma_f32_16x16x32_bf16 v[128:131], v[160:163], a[112:115], v[128:131]
ds_read_b64_tr_b16 v[184:185], v199 offset:33792
buffer_load_dwordx4 a[204:207], v2, s[96:99], s62 offen offset:512 sc1
v_mfma_f32_16x16x32_bf16 v[128:131], v[164:167], a[120:123], v[128:131]
ds_read_b64_tr_b16 v[186:187], v199 offset:34304
v_mfma_f32_16x16x32_bf16 v[132:135], v[160:163], a[116:119], v[132:135]
ds_read_b64_tr_b16 v[188:189], v199 offset:41984
v_mfma_f32_16x16x32_bf16 v[132:135], v[164:167], a[124:127], v[132:135]
ds_read_b64_tr_b16 v[190:191], v199 offset:42496
v_mfma_f32_16x16x32_bf16 v[136:139], v[168:171], a[112:115], v[136:139]
ds_read_b64_tr_b16 v[160:161], v28 offset:0
v_mfma_f32_16x16x32_bf16 v[136:139], v[172:175], a[120:123], v[136:139]
ds_read_b64_tr_b16 v[162:163], v28 offset:512
s_add_u32 m0, s61, 32768
v_mfma_f32_16x16x32_bf16 v[140:143], v[168:171], a[116:119], v[140:143]
ds_read_b64_tr_b16 v[164:165], v28 offset:1024
buffer_load_dwordx4 v3, s[44:47], s29 offen offset:0 lds
v_mfma_f32_16x16x32_bf16 v[140:143], v[172:175], a[124:127], v[140:143]
s_waitcnt lgkmcnt(3)
ds_read_b64_tr_b16 v[166:167], v28 offset:1536
v_mfma_f32_16x16x32_bf16 v[144:147], v[176:179], a[112:115], v[144:147]
ds_read_b64_tr_b16 v[168:169], v29 offset:0
v_mfma_f32_16x16x32_bf16 v[144:147], v[180:183], a[120:123], v[144:147]
ds_read_b64_tr_b16 v[170:171], v29 offset:512
v_mfma_f32_16x16x32_bf16 v[148:151], v[176:179], a[116:119], v[148:151]
ds_read_b64_tr_b16 v[172:173], v29 offset:1024
buffer_load_dwordx4 v4, s[44:47], s29 offen offset:1024 lds
v_mfma_f32_16x16x32_bf16 v[148:151], v[180:183], a[124:127], v[148:151]
ds_read_b64_tr_b16 v[174:175], v29 offset:1536
v_mfma_f32_16x16x32_bf16 v[152:155], v[184:187], a[112:115], v[152:155]
ds_read_b64_tr_b16 v[176:177], v30 offset:0
v_mfma_f32_16x16x32_bf16 v[152:155], v[188:191], a[120:123], v[152:155]
ds_read_b64_tr_b16 v[178:179], v30 offset:512
v_mfma_f32_16x16x32_bf16 v[156:159], v[184:187], a[116:119], v[156:159]
ds_read_b64_tr_b16 v[180:181], v30 offset:1024
v_mfma_f32_16x16x32_bf16 v[156:159], v[188:191], a[124:127], v[156:159]
s_waitcnt lgkmcnt(3)
ds_read_b64_tr_b16 v[182:183], v30 offset:1536
v_mfma_f32_16x16x32_bf16 v[224:227], v[160:163], a[112:115], v[224:227]
ds_read_b64_tr_b16 v[184:185], v31 offset:0
)ASM"
R"ASM(
s_add_u32 m0, s61, 40960
v_mfma_f32_16x16x32_bf16 v[224:227], v[164:167], a[120:123], v[224:227]
ds_read_b64_tr_b16 v[186:187], v31 offset:512
buffer_load_dwordx4 v3, s[44:47], s30 offen offset:0 lds
v_mfma_f32_16x16x32_bf16 v[228:231], v[160:163], a[116:119], v[228:231]
ds_read_b64_tr_b16 v[188:189], v31 offset:1024
v_mfma_f32_16x16x32_bf16 v[228:231], v[164:167], a[124:127], v[228:231]
ds_read_b64_tr_b16 v[190:191], v31 offset:1536
v_mfma_f32_16x16x32_bf16 v[232:235], v[168:171], a[112:115], v[232:235]
s_waitcnt vmcnt(29) lgkmcnt(0)
s_barrier
v_mfma_f32_16x16x32_bf16 v[232:235], v[172:175], a[120:123], v[232:235]
ds_read_b64_tr_b16 v[160:161], v28 offset:8192
buffer_load_dwordx4 v4, s[44:47], s30 offen offset:1024 lds
v_mfma_f32_16x16x32_bf16 v[236:239], v[168:171], a[116:119], v[236:239]
ds_read_b64_tr_b16 v[162:163], v28 offset:8704
v_mfma_f32_16x16x32_bf16 v[236:239], v[172:175], a[124:127], v[236:239]
ds_read_b64_tr_b16 v[164:165], v28 offset:16384
ds_read_b64_tr_b16 v[166:167], v28 offset:16896
v_mfma_f32_16x16x32_bf16 v[240:243], v[176:179], a[112:115], v[240:243]
ds_read_b64_tr_b16 v[168:169], v29 offset:8192
v_mfma_f32_16x16x32_bf16 v[240:243], v[180:183], a[120:123], v[240:243]
ds_read_b64_tr_b16 v[170:171], v29 offset:8704
s_add_u32 m0, s61, 49152
v_mfma_f32_16x16x32_bf16 v[244:247], v[176:179], a[116:119], v[244:247]
ds_read_b64_tr_b16 v[172:173], v29 offset:16384
buffer_load_dwordx4 v3, s[44:47], s31 offen offset:0 lds
v_mfma_f32_16x16x32_bf16 v[244:247], v[180:183], a[124:127], v[244:247]
ds_read_b64_tr_b16 v[174:175], v29 offset:16896
v_mfma_f32_16x16x32_bf16 v[248:251], v[184:187], a[112:115], v[248:251]
ds_read_b64_tr_b16 v[176:177], v30 offset:8192
v_mfma_f32_16x16x32_bf16 v[248:251], v[188:191], a[120:123], v[248:251]
ds_read_b64_tr_b16 v[178:179], v30 offset:8704
v_mfma_f32_16x16x32_bf16 v[252:255], v[184:187], a[116:119], v[252:255]
ds_read_b64_tr_b16 v[180:181], v30 offset:16384
buffer_load_dwordx4 v4, s[44:47], s31 offen offset:1024 lds
v_mfma_f32_16x16x32_bf16 v[252:255], v[188:191], a[124:127], v[252:255]
s_waitcnt lgkmcnt(3)
ds_read_b64_tr_b16 v[182:183], v30 offset:16896
v_mfma_f32_16x16x32_bf16 v[96:99], v[160:163], a[128:131], v[96:99]
ds_read_b64_tr_b16 v[184:185], v31 offset:8192
v_mfma_f32_16x16x32_bf16 v[96:99], v[164:167], a[136:139], v[96:99]
ds_read_b64_tr_b16 v[186:187], v31 offset:8704
v_mfma_f32_16x16x32_bf16 v[100:103], v[160:163], a[132:135], v[100:103]
ds_read_b64_tr_b16 v[188:189], v31 offset:16384
v_mfma_f32_16x16x32_bf16 v[100:103], v[164:167], a[140:143], v[100:103]
ds_read_b64_tr_b16 v[190:191], v31 offset:16896
s_add_u32 s62, s60, 3407872
v_mfma_f32_16x16x32_bf16 v[104:107], v[168:171], a[128:131], v[104:107]
ds_read_b64_tr_b16 v[160:161], v28 offset:9216
buffer_load_dwordx4 a[208:211], v2, s[96:99], s62 offen sc1
v_mfma_f32_16x16x32_bf16 v[104:107], v[172:175], a[136:139], v[104:107]
ds_read_b64_tr_b16 v[162:163], v28 offset:9728
v_mfma_f32_16x16x32_bf16 v[108:111], v[168:171], a[132:135], v[108:111]
ds_read_b64_tr_b16 v[164:165], v28 offset:17408
v_mfma_f32_16x16x32_bf16 v[108:111], v[172:175], a[140:143], v[108:111]
s_waitcnt lgkmcnt(3)
ds_read_b64_tr_b16 v[166:167], v28 offset:17920
v_mfma_f32_16x16x32_bf16 v[112:115], v[176:179], a[128:131], v[112:115]
ds_read_b64_tr_b16 v[168:169], v29 offset:9216
buffer_load_dwordx4 a[212:215], v2, s[96:99], s62 offen offset:512 sc1
v_mfma_f32_16x16x32_bf16 v[112:115], v[180:183], a[136:139], v[112:115]
ds_read_b64_tr_b16 v[170:171], v29 offset:9728
v_mfma_f32_16x16x32_bf16 v[116:119], v[176:179], a[132:135], v[116:119]
ds_read_b64_tr_b16 v[172:173], v29 offset:17408
v_mfma_f32_16x16x32_bf16 v[116:119], v[180:183], a[140:143], v[116:119]
ds_read_b64_tr_b16 v[174:175], v29 offset:17920
v_mfma_f32_16x16x32_bf16 v[120:123], v[184:187], a[128:131], v[120:123]
ds_read_b64_tr_b16 v[176:177], v30 offset:9216
v_mfma_f32_16x16x32_bf16 v[120:123], v[188:191], a[136:139], v[120:123]
ds_read_b64_tr_b16 v[178:179], v30 offset:9728
s_add_u32 s62, s60, 3538944
v_mfma_f32_16x16x32_bf16 v[124:127], v[184:187], a[132:135], v[124:127]
ds_read_b64_tr_b16 v[180:181], v30 offset:17408
buffer_load_dwordx4 a[216:219], v2, s[96:99], s62 offen sc1
v_mfma_f32_16x16x32_bf16 v[124:127], v[188:191], a[140:143], v[124:127]
s_waitcnt lgkmcnt(3)
ds_read_b64_tr_b16 v[182:183], v30 offset:17920
v_mfma_f32_16x16x32_bf16 v[128:131], v[160:163], a[128:131], v[128:131]
ds_read_b64_tr_b16 v[184:185], v31 offset:9216
v_mfma_f32_16x16x32_bf16 v[128:131], v[164:167], a[136:139], v[128:131]
ds_read_b64_tr_b16 v[186:187], v31 offset:9728
v_mfma_f32_16x16x32_bf16 v[132:135], v[160:163], a[132:135], v[132:135]
ds_read_b64_tr_b16 v[188:189], v31 offset:17408
buffer_load_dwordx4 a[220:223], v2, s[96:99], s62 offen offset:512 sc1
v_mfma_f32_16x16x32_bf16 v[132:135], v[164:167], a[140:143], v[132:135]
ds_read_b64_tr_b16 v[190:191], v31 offset:17920
v_mfma_f32_16x16x32_bf16 v[136:139], v[168:171], a[128:131], v[136:139]
ds_read_b64_tr_b16 v[160:161], v28 offset:24576
v_mfma_f32_16x16x32_bf16 v[136:139], v[172:175], a[136:139], v[136:139]
ds_read_b64_tr_b16 v[162:163], v28 offset:25088
v_mfma_f32_16x16x32_bf16 v[140:143], v[168:171], a[132:135], v[140:143]
ds_read_b64_tr_b16 v[164:165], v28 offset:32768
v_mfma_f32_16x16x32_bf16 v[140:143], v[172:175], a[140:143], v[140:143]
s_waitcnt lgkmcnt(3)
ds_read_b64_tr_b16 v[166:167], v28 offset:33280
v_mfma_f32_16x16x32_bf16 v[144:147], v[176:179], a[128:131], v[144:147]
ds_read_b64_tr_b16 v[168:169], v29 offset:24576
s_add_u32 s62, s70, 0
v_mfma_f32_16x16x32_bf16 v[144:147], v[180:183], a[136:139], v[144:147]
ds_read_b64_tr_b16 v[170:171], v29 offset:25088
buffer_load_dwordx4 a[224:227], v2, s[96:99], s62 offen sc1
v_mfma_f32_16x16x32_bf16 v[148:151], v[176:179], a[132:135], v[148:151]
ds_read_b64_tr_b16 v[172:173], v29 offset:32768
v_mfma_f32_16x16x32_bf16 v[148:151], v[180:183], a[140:143], v[148:151]
ds_read_b64_tr_b16 v[174:175], v29 offset:33280
v_mfma_f32_16x16x32_bf16 v[152:155], v[184:187], a[128:131], v[152:155]
ds_read_b64_tr_b16 v[176:177], v30 offset:24576
v_mfma_f32_16x16x32_bf16 v[152:155], v[188:191], a[136:139], v[152:155]
ds_read_b64_tr_b16 v[178:179], v30 offset:25088
buffer_load_dwordx4 a[228:231], v2, s[96:99], s62 offen offset:512 sc1
v_mfma_f32_16x16x32_bf16 v[156:159], v[184:187], a[132:135], v[156:159]
ds_read_b64_tr_b16 v[180:181], v30 offset:32768
v_mfma_f32_16x16x32_bf16 v[156:159], v[188:191], a[140:143], v[156:159]
s_waitcnt lgkmcnt(3)
ds_read_b64_tr_b16 v[182:183], v30 offset:33280
s_waitcnt vmcnt(37)
v_mfma_f32_16x16x32_bf16 v[96:99], v[160:163], a[144:147], v[96:99]
ds_read_b64_tr_b16 v[184:185], v31 offset:24576
s_waitcnt vmcnt(35)
v_mfma_f32_16x16x32_bf16 v[96:99], v[164:167], a[152:155], v[96:99]
ds_read_b64_tr_b16 v[186:187], v31 offset:25088
s_add_u32 s62, s70, 131072
v_mfma_f32_16x16x32_bf16 v[100:103], v[160:163], a[148:151], v[100:103]
ds_read_b64_tr_b16 v[188:189], v31 offset:32768
buffer_load_dwordx4 a[232:235], v2, s[96:99], s62 offen sc1
s_waitcnt vmcnt(35)
v_mfma_f32_16x16x32_bf16 v[100:103], v[164:167], a[156:159], v[100:103]
ds_read_b64_tr_b16 v[190:191], v31 offset:33280
v_mfma_f32_16x16x32_bf16 v[104:107], v[168:171], a[144:147], v[104:107]
ds_read_b64_tr_b16 v[160:161], v28 offset:25600
v_mfma_f32_16x16x32_bf16 v[104:107], v[172:175], a[152:155], v[104:107]
ds_read_b64_tr_b16 v[162:163], v28 offset:26112
v_mfma_f32_16x16x32_bf16 v[108:111], v[168:171], a[148:151], v[108:111]
ds_read_b64_tr_b16 v[164:165], v28 offset:33792
buffer_load_dwordx4 a[236:239], v2, s[96:99], s62 offen offset:512 sc1
v_mfma_f32_16x16x32_bf16 v[108:111], v[172:175], a[156:159], v[108:111]
s_waitcnt lgkmcnt(3)
ds_read_b64_tr_b16 v[166:167], v28 offset:34304
v_mfma_f32_16x16x32_bf16 v[112:115], v[176:179], a[144:147], v[112:115]
ds_read_b64_tr_b16 v[168:169], v29 offset:25600
v_mfma_f32_16x16x32_bf16 v[112:115], v[180:183], a[152:155], v[112:115]
ds_read_b64_tr_b16 v[170:171], v29 offset:26112
v_mfma_f32_16x16x32_bf16 v[116:119], v[176:179], a[148:151], v[116:119]
ds_read_b64_tr_b16 v[172:173], v29 offset:33792
v_mfma_f32_16x16x32_bf16 v[116:119], v[180:183], a[156:159], v[116:119]
ds_read_b64_tr_b16 v[174:175], v29 offset:34304
s_add_u32 m0, s61, 57344
v_mfma_f32_16x16x32_bf16 v[120:123], v[184:187], a[144:147], v[120:123]
ds_read_b64_tr_b16 v[176:177], v30 offset:25600
s_add_u32 s63, s95, 0
v_mfma_f32_16x16x32_bf16 v[120:123], v[188:191], a[152:155], v[120:123]
ds_read_b64_tr_b16 v[178:179], v30 offset:26112
buffer_load_dwordx4 v23, s[48:51], s63 offen offset:0 lds
v_mfma_f32_16x16x32_bf16 v[124:127], v[184:187], a[148:151], v[124:127]
ds_read_b64_tr_b16 v[180:181], v30 offset:33792
v_mfma_f32_16x16x32_bf16 v[124:127], v[188:191], a[156:159], v[124:127]
s_waitcnt lgkmcnt(3)
ds_read_b64_tr_b16 v[182:183], v30 offset:34304
v_mfma_f32_16x16x32_bf16 v[128:131], v[160:163], a[144:147], v[128:131]
ds_read_b64_tr_b16 v[184:185], v31 offset:25600
v_mfma_f32_16x16x32_bf16 v[128:131], v[164:167], a[152:155], v[128:131]
ds_read_b64_tr_b16 v[186:187], v31 offset:26112
buffer_load_dwordx4 v6, s[48:51], s63 offen offset:1024 lds
v_mfma_f32_16x16x32_bf16 v[132:135], v[160:163], a[148:151], v[132:135]
ds_read_b64_tr_b16 v[188:189], v31 offset:33792
v_mfma_f32_16x16x32_bf16 v[132:135], v[164:167], a[156:159], v[132:135]
ds_read_b64_tr_b16 v[190:191], v31 offset:34304
v_mfma_f32_16x16x32_bf16 v[136:139], v[168:171], a[144:147], v[136:139]
s_waitcnt vmcnt(26) lgkmcnt(0)
s_barrier
v_mfma_f32_16x16x32_bf16 v[136:139], v[172:175], a[152:155], v[136:139]
ds_read_b64_tr_b16 v[160:161], v28 offset:40960
s_add_u32 m0, s61, 65536
v_mfma_f32_16x16x32_bf16 v[140:143], v[168:171], a[148:151], v[140:143]
ds_read_b64_tr_b16 v[162:163], v28 offset:41472
s_add_u32 s63, s95, 73728
v_mfma_f32_16x16x32_bf16 v[140:143], v[172:175], a[156:159], v[140:143]
ds_read_b64_tr_b16 v[164:165], v192 offset:0
ds_read_b64_tr_b16 v[166:167], v192 offset:512
v_mfma_f32_16x16x32_bf16 v[144:147], v[176:179], a[144:147], v[144:147]
ds_read_b64_tr_b16 v[168:169], v29 offset:40960
buffer_load_dwordx4 v23, s[48:51], s63 offen offset:0 lds
v_mfma_f32_16x16x32_bf16 v[144:147], v[180:183], a[152:155], v[144:147]
ds_read_b64_tr_b16 v[170:171], v29 offset:41472
v_mfma_f32_16x16x32_bf16 v[148:151], v[176:179], a[148:151], v[148:151]
ds_read_b64_tr_b16 v[172:173], v193 offset:0
v_mfma_f32_16x16x32_bf16 v[148:151], v[180:183], a[156:159], v[148:151]
ds_read_b64_tr_b16 v[174:175], v193 offset:512
v_mfma_f32_16x16x32_bf16 v[152:155], v[184:187], a[144:147], v[152:155]
ds_read_b64_tr_b16 v[176:177], v30 offset:40960
buffer_load_dwordx4 v6, s[48:51], s63 offen offset:1024 lds
v_mfma_f32_16x16x32_bf16 v[152:155], v[188:191], a[152:155], v[152:155]
ds_read_b64_tr_b16 v[178:179], v30 offset:41472
v_mfma_f32_16x16x32_bf16 v[156:159], v[184:187], a[148:151], v[156:159]
ds_read_b64_tr_b16 v[180:181], v194 offset:0
v_mfma_f32_16x16x32_bf16 v[156:159], v[188:191], a[156:159], v[156:159]
s_waitcnt lgkmcnt(3)
ds_read_b64_tr_b16 v[182:183], v194 offset:512
v_mfma_f32_16x16x32_bf16 v[96:99], v[160:163], a[160:163], v[96:99]
)ASM"
R"ASM(
ds_read_b64_tr_b16 v[184:185], v31 offset:40960
s_add_u32 m0, s61, 73728
v_mfma_f32_16x16x32_bf16 v[96:99], v[164:167], a[168:171], v[96:99]
ds_read_b64_tr_b16 v[186:187], v31 offset:41472
s_add_u32 s63, s95, 128
v_mfma_f32_16x16x32_bf16 v[100:103], v[160:163], a[164:167], v[100:103]
ds_read_b64_tr_b16 v[188:189], v195 offset:0
buffer_load_dwordx4 v23, s[48:51], s63 offen offset:0 lds
v_mfma_f32_16x16x32_bf16 v[100:103], v[164:167], a[172:175], v[100:103]
ds_read_b64_tr_b16 v[190:191], v195 offset:512
v_mfma_f32_16x16x32_bf16 v[104:107], v[168:171], a[160:163], v[104:107]
ds_read_b64_tr_b16 v[160:161], v28 offset:41984
v_mfma_f32_16x16x32_bf16 v[104:107], v[172:175], a[168:171], v[104:107]
ds_read_b64_tr_b16 v[162:163], v28 offset:42496
v_mfma_f32_16x16x32_bf16 v[108:111], v[168:171], a[164:167], v[108:111]
ds_read_b64_tr_b16 v[164:165], v192 offset:1024
buffer_load_dwordx4 v6, s[48:51], s63 offen offset:1024 lds
v_mfma_f32_16x16x32_bf16 v[108:111], v[172:175], a[172:175], v[108:111]
s_waitcnt lgkmcnt(3)
ds_read_b64_tr_b16 v[166:167], v192 offset:1536
v_mfma_f32_16x16x32_bf16 v[112:115], v[176:179], a[160:163], v[112:115]
ds_read_b64_tr_b16 v[168:169], v29 offset:41984
v_mfma_f32_16x16x32_bf16 v[112:115], v[180:183], a[168:171], v[112:115]
ds_read_b64_tr_b16 v[170:171], v29 offset:42496
s_add_u32 m0, s61, 81920
v_mfma_f32_16x16x32_bf16 v[116:119], v[176:179], a[164:167], v[116:119]
ds_read_b64_tr_b16 v[172:173], v193 offset:1024
s_add_u32 s63, s95, 73856
v_mfma_f32_16x16x32_bf16 v[116:119], v[180:183], a[172:175], v[116:119]
ds_read_b64_tr_b16 v[174:175], v193 offset:1536
buffer_load_dwordx4 v23, s[48:51], s63 offen offset:0 lds
v_mfma_f32_16x16x32_bf16 v[120:123], v[184:187], a[160:163], v[120:123]
ds_read_b64_tr_b16 v[176:177], v30 offset:41984
v_mfma_f32_16x16x32_bf16 v[120:123], v[188:191], a[168:171], v[120:123]
ds_read_b64_tr_b16 v[178:179], v30 offset:42496
v_mfma_f32_16x16x32_bf16 v[124:127], v[184:187], a[164:167], v[124:127]
ds_read_b64_tr_b16 v[180:181], v194 offset:1024
v_mfma_f32_16x16x32_bf16 v[124:127], v[188:191], a[172:175], v[124:127]
s_waitcnt lgkmcnt(3)
ds_read_b64_tr_b16 v[182:183], v194 offset:1536
v_mfma_f32_16x16x32_bf16 v[128:131], v[160:163], a[160:163], v[128:131]
ds_read_b64_tr_b16 v[184:185], v31 offset:41984
buffer_load_dwordx4 v6, s[48:51], s63 offen offset:1024 lds
v_mfma_f32_16x16x32_bf16 v[128:131], v[164:167], a[168:171], v[128:131]
ds_read_b64_tr_b16 v[186:187], v31 offset:42496
v_mfma_f32_16x16x32_bf16 v[132:135], v[160:163], a[164:167], v[132:135]
ds_read_b64_tr_b16 v[188:189], v195 offset:1024
v_mfma_f32_16x16x32_bf16 v[132:135], v[164:167], a[172:175], v[132:135]
ds_read_b64_tr_b16 v[190:191], v195 offset:1536
s_add_u32 s62, s70, 262144
v_mfma_f32_16x16x32_bf16 v[136:139], v[168:171], a[160:163], v[136:139]
ds_read_b64_tr_b16 v[160:161], v192 offset:8192
buffer_load_dwordx4 a[240:243], v2, s[96:99], s62 offen sc1
v_mfma_f32_16x16x32_bf16 v[136:139], v[172:175], a[168:171], v[136:139]
ds_read_b64_tr_b16 v[162:163], v192 offset:8704
v_mfma_f32_16x16x32_bf16 v[140:143], v[168:171], a[164:167], v[140:143]
ds_read_b64_tr_b16 v[164:165], v192 offset:16384
v_mfma_f32_16x16x32_bf16 v[140:143], v[172:175], a[172:175], v[140:143]
s_waitcnt lgkmcnt(3)
ds_read_b64_tr_b16 v[166:167], v192 offset:16896
v_mfma_f32_16x16x32_bf16 v[144:147], v[176:179], a[160:163], v[144:147]
ds_read_b64_tr_b16 v[168:169], v193 offset:8192
buffer_load_dwordx4 a[244:247], v2, s[96:99], s62 offen offset:512 sc1
v_mfma_f32_16x16x32_bf16 v[144:147], v[180:183], a[168:171], v[144:147]
ds_read_b64_tr_b16 v[170:171], v193 offset:8704
v_mfma_f32_16x16x32_bf16 v[148:151], v[176:179], a[164:167], v[148:151]
ds_read_b64_tr_b16 v[172:173], v193 offset:16384
v_mfma_f32_16x16x32_bf16 v[148:151], v[180:183], a[172:175], v[148:151]
ds_read_b64_tr_b16 v[174:175], v193 offset:16896
v_mfma_f32_16x16x32_bf16 v[152:155], v[184:187], a[160:163], v[152:155]
ds_read_b64_tr_b16 v[176:177], v194 offset:8192
v_mfma_f32_16x16x32_bf16 v[152:155], v[188:191], a[168:171], v[152:155]
ds_read_b64_tr_b16 v[178:179], v194 offset:8704
s_add_u32 s62, s70, 393216
v_mfma_f32_16x16x32_bf16 v[156:159], v[184:187], a[164:167], v[156:159]
ds_read_b64_tr_b16 v[180:181], v194 offset:16384
buffer_load_dwordx4 a[248:251], v2, s[96:99], s62 offen sc1
v_mfma_f32_16x16x32_bf16 v[156:159], v[188:191], a[172:175], v[156:159]
s_waitcnt lgkmcnt(3)
ds_read_b64_tr_b16 v[182:183], v194 offset:16896
s_waitcnt vmcnt(32)
v_mfma_f32_16x16x32_bf16 v[96:99], v[160:163], a[176:179], v[96:99]
ds_read_b64_tr_b16 v[184:185], v195 offset:8192
s_waitcnt vmcnt(30)
v_mfma_f32_16x16x32_bf16 v[96:99], v[164:167], a[184:187], v[96:99]
ds_read_b64_tr_b16 v[186:187], v195 offset:8704
v_mfma_f32_16x16x32_bf16 v[100:103], v[160:163], a[180:183], v[100:103]
ds_read_b64_tr_b16 v[188:189], v195 offset:16384
buffer_load_dwordx4 a[252:255], v2, s[96:99], s62 offen offset:512 sc1
s_waitcnt vmcnt(30)
v_mfma_f32_16x16x32_bf16 v[100:103], v[164:167], a[188:191], v[100:103]
ds_read_b64_tr_b16 v[190:191], v195 offset:16896
v_mfma_f32_16x16x32_bf16 v[104:107], v[168:171], a[176:179], v[104:107]
ds_read_b64_tr_b16 v[160:161], v192 offset:9216
v_mfma_f32_16x16x32_bf16 v[104:107], v[172:175], a[184:187], v[104:107]
ds_read_b64_tr_b16 v[162:163], v192 offset:9728
v_mfma_f32_16x16x32_bf16 v[108:111], v[168:171], a[180:183], v[108:111]
ds_read_b64_tr_b16 v[164:165], v192 offset:17408
v_mfma_f32_16x16x32_bf16 v[108:111], v[172:175], a[188:191], v[108:111]
s_waitcnt lgkmcnt(3)
ds_read_b64_tr_b16 v[166:167], v192 offset:17920
v_mfma_f32_16x16x32_bf16 v[112:115], v[176:179], a[176:179], v[112:115]
ds_read_b64_tr_b16 v[168:169], v193 offset:9216
s_add_u32 s62, s70, 524288
v_mfma_f32_16x16x32_bf16 v[112:115], v[180:183], a[184:187], v[112:115]
ds_read_b64_tr_b16 v[170:171], v193 offset:9728
buffer_load_dwordx4 a[0:3], v2, s[96:99], s62 offen sc1
v_mfma_f32_16x16x32_bf16 v[116:119], v[176:179], a[180:183], v[116:119]
ds_read_b64_tr_b16 v[172:173], v193 offset:17408
v_mfma_f32_16x16x32_bf16 v[116:119], v[180:183], a[188:191], v[116:119]
ds_read_b64_tr_b16 v[174:175], v193 offset:17920
v_mfma_f32_16x16x32_bf16 v[120:123], v[184:187], a[176:179], v[120:123]
ds_read_b64_tr_b16 v[176:177], v194 offset:9216
v_mfma_f32_16x16x32_bf16 v[120:123], v[188:191], a[184:187], v[120:123]
ds_read_b64_tr_b16 v[178:179], v194 offset:9728
buffer_load_dwordx4 a[4:7], v2, s[96:99], s62 offen offset:512 sc1
v_mfma_f32_16x16x32_bf16 v[124:127], v[184:187], a[180:183], v[124:127]
ds_read_b64_tr_b16 v[180:181], v194 offset:17408
v_mfma_f32_16x16x32_bf16 v[124:127], v[188:191], a[188:191], v[124:127]
s_waitcnt lgkmcnt(3)
ds_read_b64_tr_b16 v[182:183], v194 offset:17920
v_mfma_f32_16x16x32_bf16 v[128:131], v[160:163], a[176:179], v[128:131]
ds_read_b64_tr_b16 v[184:185], v195 offset:9216
v_mfma_f32_16x16x32_bf16 v[128:131], v[164:167], a[184:187], v[128:131]
ds_read_b64_tr_b16 v[186:187], v195 offset:9728
s_add_u32 s62, s70, 655360
v_mfma_f32_16x16x32_bf16 v[132:135], v[160:163], a[180:183], v[132:135]
ds_read_b64_tr_b16 v[188:189], v195 offset:17408
buffer_load_dwordx4 a[8:11], v2, s[96:99], s62 offen sc1
v_mfma_f32_16x16x32_bf16 v[132:135], v[164:167], a[188:191], v[132:135]
ds_read_b64_tr_b16 v[190:191], v195 offset:17920
v_mfma_f32_16x16x32_bf16 v[136:139], v[168:171], a[176:179], v[136:139]
s_waitcnt vmcnt(23) lgkmcnt(0)
s_barrier
v_mfma_f32_16x16x32_bf16 v[136:139], v[172:175], a[184:187], v[136:139]
ds_read_b64_tr_b16 v[160:161], v192 offset:24576
v_mfma_f32_16x16x32_bf16 v[140:143], v[168:171], a[180:183], v[140:143]
ds_read_b64_tr_b16 v[162:163], v192 offset:25088
buffer_load_dwordx4 a[12:15], v2, s[96:99], s62 offen offset:512 sc1
v_mfma_f32_16x16x32_bf16 v[140:143], v[172:175], a[188:191], v[140:143]
ds_read_b64_tr_b16 v[164:165], v192 offset:32768
ds_read_b64_tr_b16 v[166:167], v192 offset:33280
v_mfma_f32_16x16x32_bf16 v[144:147], v[176:179], a[176:179], v[144:147]
ds_read_b64_tr_b16 v[168:169], v193 offset:24576
v_mfma_f32_16x16x32_bf16 v[144:147], v[180:183], a[184:187], v[144:147]
ds_read_b64_tr_b16 v[170:171], v193 offset:25088
v_mfma_f32_16x16x32_bf16 v[148:151], v[176:179], a[180:183], v[148:151]
ds_read_b64_tr_b16 v[172:173], v193 offset:32768
v_mfma_f32_16x16x32_bf16 v[148:151], v[180:183], a[188:191], v[148:151]
ds_read_b64_tr_b16 v[174:175], v193 offset:33280
s_add_u32 m0, s61, 90112
v_mfma_f32_16x16x32_bf16 v[152:155], v[184:187], a[176:179], v[152:155]
ds_read_b64_tr_b16 v[176:177], v194 offset:24576
s_add_u32 s63, s95, 256
v_mfma_f32_16x16x32_bf16 v[152:155], v[188:191], a[184:187], v[152:155]
ds_read_b64_tr_b16 v[178:179], v194 offset:25088
buffer_load_dwordx4 v23, s[48:51], s63 offen offset:0 lds
v_mfma_f32_16x16x32_bf16 v[156:159], v[184:187], a[180:183], v[156:159]
ds_read_b64_tr_b16 v[180:181], v194 offset:32768
v_mfma_f32_16x16x32_bf16 v[156:159], v[188:191], a[188:191], v[156:159]
s_waitcnt lgkmcnt(3)
ds_read_b64_tr_b16 v[182:183], v194 offset:33280
v_mfma_f32_16x16x32_bf16 v[96:99], v[160:163], a[192:195], v[96:99]
ds_read_b64_tr_b16 v[184:185], v195 offset:24576
v_mfma_f32_16x16x32_bf16 v[96:99], v[164:167], a[200:203], v[96:99]
ds_read_b64_tr_b16 v[186:187], v195 offset:25088
buffer_load_dwordx4 v6, s[48:51], s63 offen offset:1024 lds
v_mfma_f32_16x16x32_bf16 v[100:103], v[160:163], a[196:199], v[100:103]
ds_read_b64_tr_b16 v[188:189], v195 offset:32768
v_mfma_f32_16x16x32_bf16 v[100:103], v[164:167], a[204:207], v[100:103]
ds_read_b64_tr_b16 v[190:191], v195 offset:33280
v_mfma_f32_16x16x32_bf16 v[104:107], v[168:171], a[192:195], v[104:107]
ds_read_b64_tr_b16 v[160:161], v192 offset:25600
v_mfma_f32_16x16x32_bf16 v[104:107], v[172:175], a[200:203], v[104:107]
ds_read_b64_tr_b16 v[162:163], v192 offset:26112
s_add_u32 m0, s61, 98304
v_mfma_f32_16x16x32_bf16 v[108:111], v[168:171], a[196:199], v[108:111]
ds_read_b64_tr_b16 v[164:165], v192 offset:33792
s_add_u32 s63, s95, 73984
v_mfma_f32_16x16x32_bf16 v[108:111], v[172:175], a[204:207], v[108:111]
s_waitcnt lgkmcnt(3)
ds_read_b64_tr_b16 v[166:167], v192 offset:34304
v_mfma_f32_16x16x32_bf16 v[112:115], v[176:179], a[192:195], v[112:115]
ds_read_b64_tr_b16 v[168:169], v193 offset:25600
buffer_load_dwordx4 v23, s[48:51], s63 offen offset:0 lds
v_mfma_f32_16x16x32_bf16 v[112:115], v[180:183], a[200:203], v[112:115]
ds_read_b64_tr_b16 v[170:171], v193 offset:26112
v_mfma_f32_16x16x32_bf16 v[116:119], v[176:179], a[196:199], v[116:119]
ds_read_b64_tr_b16 v[172:173], v193 offset:33792
v_mfma_f32_16x16x32_bf16 v[116:119], v[180:183], a[204:207], v[116:119]
ds_read_b64_tr_b16 v[174:175], v193 offset:34304
v_mfma_f32_16x16x32_bf16 v[120:123], v[184:187], a[192:195], v[120:123]
ds_read_b64_tr_b16 v[176:177], v194 offset:25600
buffer_load_dwordx4 v6, s[48:51], s63 offen offset:1024 lds
v_mfma_f32_16x16x32_bf16 v[120:123], v[188:191], a[200:203], v[120:123]
ds_read_b64_tr_b16 v[178:179], v194 offset:26112
v_mfma_f32_16x16x32_bf16 v[124:127], v[184:187], a[196:199], v[124:127]
ds_read_b64_tr_b16 v[180:181], v194 offset:33792
v_mfma_f32_16x16x32_bf16 v[124:127], v[188:191], a[204:207], v[124:127]
s_waitcnt lgkmcnt(3)
)ASM"
R"ASM(
ds_read_b64_tr_b16 v[182:183], v194 offset:34304
v_mfma_f32_16x16x32_bf16 v[128:131], v[160:163], a[192:195], v[128:131]
ds_read_b64_tr_b16 v[184:185], v195 offset:25600
s_add_u32 m0, s61, 106496
v_mfma_f32_16x16x32_bf16 v[128:131], v[164:167], a[200:203], v[128:131]
ds_read_b64_tr_b16 v[186:187], v195 offset:26112
s_add_u32 s63, s95, 384
v_mfma_f32_16x16x32_bf16 v[132:135], v[160:163], a[196:199], v[132:135]
ds_read_b64_tr_b16 v[188:189], v195 offset:33792
buffer_load_dwordx4 v23, s[48:51], s63 offen offset:0 lds
v_mfma_f32_16x16x32_bf16 v[132:135], v[164:167], a[204:207], v[132:135]
ds_read_b64_tr_b16 v[190:191], v195 offset:34304
v_mfma_f32_16x16x32_bf16 v[136:139], v[168:171], a[192:195], v[136:139]
ds_read_b64_tr_b16 v[160:161], v192 offset:40960
v_mfma_f32_16x16x32_bf16 v[136:139], v[172:175], a[200:203], v[136:139]
ds_read_b64_tr_b16 v[162:163], v192 offset:41472
v_mfma_f32_16x16x32_bf16 v[140:143], v[168:171], a[196:199], v[140:143]
ds_read_b64_tr_b16 v[164:165], v196 offset:0
buffer_load_dwordx4 v6, s[48:51], s63 offen offset:1024 lds
v_mfma_f32_16x16x32_bf16 v[140:143], v[172:175], a[204:207], v[140:143]
s_waitcnt lgkmcnt(3)
ds_read_b64_tr_b16 v[166:167], v196 offset:512
v_mfma_f32_16x16x32_bf16 v[144:147], v[176:179], a[192:195], v[144:147]
ds_read_b64_tr_b16 v[168:169], v193 offset:40960
v_mfma_f32_16x16x32_bf16 v[144:147], v[180:183], a[200:203], v[144:147]
ds_read_b64_tr_b16 v[170:171], v193 offset:41472
s_add_u32 m0, s61, 114688
v_mfma_f32_16x16x32_bf16 v[148:151], v[176:179], a[196:199], v[148:151]
ds_read_b64_tr_b16 v[172:173], v197 offset:0
s_add_u32 s63, s95, 74112
v_mfma_f32_16x16x32_bf16 v[148:151], v[180:183], a[204:207], v[148:151]
ds_read_b64_tr_b16 v[174:175], v197 offset:512
buffer_load_dwordx4 v23, s[48:51], s63 offen offset:0 lds
v_mfma_f32_16x16x32_bf16 v[152:155], v[184:187], a[192:195], v[152:155]
ds_read_b64_tr_b16 v[176:177], v194 offset:40960
v_mfma_f32_16x16x32_bf16 v[152:155], v[188:191], a[200:203], v[152:155]
ds_read_b64_tr_b16 v[178:179], v194 offset:41472
v_mfma_f32_16x16x32_bf16 v[156:159], v[184:187], a[196:199], v[156:159]
ds_read_b64_tr_b16 v[180:181], v198 offset:0
v_mfma_f32_16x16x32_bf16 v[156:159], v[188:191], a[204:207], v[156:159]
s_waitcnt lgkmcnt(3)
ds_read_b64_tr_b16 v[182:183], v198 offset:512
s_waitcnt vmcnt(30)
v_mfma_f32_16x16x32_bf16 v[96:99], v[160:163], a[208:211], v[96:99]
ds_read_b64_tr_b16 v[184:185], v195 offset:40960
buffer_load_dwordx4 v6, s[48:51], s63 offen offset:1024 lds
s_waitcnt vmcnt(29)
v_mfma_f32_16x16x32_bf16 v[96:99], v[164:167], a[216:219], v[96:99]
ds_read_b64_tr_b16 v[186:187], v195 offset:41472
v_mfma_f32_16x16x32_bf16 v[100:103], v[160:163], a[212:215], v[100:103]
ds_read_b64_tr_b16 v[188:189], v199 offset:0
s_waitcnt vmcnt(28)
v_mfma_f32_16x16x32_bf16 v[100:103], v[164:167], a[220:223], v[100:103]
ds_read_b64_tr_b16 v[190:191], v199 offset:512
s_add_u32 s62, s70, 786432
v_mfma_f32_16x16x32_bf16 v[104:107], v[168:171], a[208:211], v[104:107]
ds_read_b64_tr_b16 v[160:161], v192 offset:41984
buffer_load_dwordx4 a[16:19], v2, s[96:99], s62 offen sc1
v_mfma_f32_16x16x32_bf16 v[104:107], v[172:175], a[216:219], v[104:107]
ds_read_b64_tr_b16 v[162:163], v192 offset:42496
v_mfma_f32_16x16x32_bf16 v[108:111], v[168:171], a[212:215], v[108:111]
ds_read_b64_tr_b16 v[164:165], v196 offset:1024
v_mfma_f32_16x16x32_bf16 v[108:111], v[172:175], a[220:223], v[108:111]
s_waitcnt lgkmcnt(3)
ds_read_b64_tr_b16 v[166:167], v196 offset:1536
v_mfma_f32_16x16x32_bf16 v[112:115], v[176:179], a[208:211], v[112:115]
ds_read_b64_tr_b16 v[168:169], v193 offset:41984
buffer_load_dwordx4 a[20:23], v2, s[96:99], s62 offen offset:512 sc1
v_mfma_f32_16x16x32_bf16 v[112:115], v[180:183], a[216:219], v[112:115]
ds_read_b64_tr_b16 v[170:171], v193 offset:42496
v_mfma_f32_16x16x32_bf16 v[116:119], v[176:179], a[212:215], v[116:119]
ds_read_b64_tr_b16 v[172:173], v197 offset:1024
v_mfma_f32_16x16x32_bf16 v[116:119], v[180:183], a[220:223], v[116:119]
ds_read_b64_tr_b16 v[174:175], v197 offset:1536
v_mfma_f32_16x16x32_bf16 v[120:123], v[184:187], a[208:211], v[120:123]
ds_read_b64_tr_b16 v[176:177], v194 offset:41984
v_mfma_f32_16x16x32_bf16 v[120:123], v[188:191], a[216:219], v[120:123]
ds_read_b64_tr_b16 v[178:179], v194 offset:42496
s_add_u32 s62, s70, 917504
v_mfma_f32_16x16x32_bf16 v[124:127], v[184:187], a[212:215], v[124:127]
ds_read_b64_tr_b16 v[180:181], v198 offset:1024
buffer_load_dwordx4 a[24:27], v2, s[96:99], s62 offen sc1
v_mfma_f32_16x16x32_bf16 v[124:127], v[188:191], a[220:223], v[124:127]
s_waitcnt lgkmcnt(3)
ds_read_b64_tr_b16 v[182:183], v198 offset:1536
v_mfma_f32_16x16x32_bf16 v[128:131], v[160:163], a[208:211], v[128:131]
ds_read_b64_tr_b16 v[184:185], v195 offset:41984
v_mfma_f32_16x16x32_bf16 v[128:131], v[164:167], a[216:219], v[128:131]
ds_read_b64_tr_b16 v[186:187], v195 offset:42496
v_mfma_f32_16x16x32_bf16 v[132:135], v[160:163], a[212:215], v[132:135]
ds_read_b64_tr_b16 v[188:189], v199 offset:1024
buffer_load_dwordx4 a[28:31], v2, s[96:99], s62 offen offset:512 sc1
v_mfma_f32_16x16x32_bf16 v[132:135], v[164:167], a[220:223], v[132:135]
ds_read_b64_tr_b16 v[190:191], v199 offset:1536
v_mfma_f32_16x16x32_bf16 v[136:139], v[168:171], a[208:211], v[136:139]
s_waitcnt vmcnt(20) lgkmcnt(0)
s_barrier
v_mfma_f32_16x16x32_bf16 v[136:139], v[172:175], a[216:219], v[136:139]
ds_read_b128 v[160:163], v220 offset:8192
v_mfma_f32_16x16x32_bf16 v[140:143], v[168:171], a[212:215], v[140:143]
v_mfma_f32_16x16x32_bf16 v[140:143], v[172:175], a[220:223], v[140:143]
ds_read_b128 v[164:167], v221 offset:8192
s_add_u32 m0, s61, 122880
v_mfma_f32_16x16x32_bf16 v[144:147], v[176:179], a[208:211], v[144:147]
ds_read_b128 v[168:171], v220 offset:10240
buffer_load_dwordx4 v5, s[44:47], s20 offen offset:0 lds
v_mfma_f32_16x16x32_bf16 v[144:147], v[180:183], a[216:219], v[144:147]
v_mfma_f32_16x16x32_bf16 v[148:151], v[176:179], a[212:215], v[148:151]
ds_read_b128 v[172:175], v221 offset:10240
v_mfma_f32_16x16x32_bf16 v[148:151], v[180:183], a[220:223], v[148:151]
v_mfma_f32_16x16x32_bf16 v[152:155], v[184:187], a[208:211], v[152:155]
ds_read_b128 v[176:179], v220 offset:12288
buffer_load_dwordx4 v27, s[44:47], s21 offen offset:1024 lds
v_mfma_f32_16x16x32_bf16 v[152:155], v[188:191], a[216:219], v[152:155]
v_mfma_f32_16x16x32_bf16 v[156:159], v[184:187], a[212:215], v[156:159]
ds_read_b128 v[180:183], v221 offset:12288
v_mfma_f32_16x16x32_bf16 v[156:159], v[188:191], a[220:223], v[156:159]
s_waitcnt lgkmcnt(2)
v_mfma_f32_16x16x32_bf16 v[96:99], v[160:163], a[224:227], v[96:99]
ds_read_b128 v[184:187], v220 offset:14336
v_mfma_f32_16x16x32_bf16 v[96:99], v[164:167], a[232:235], v[96:99]
s_add_u32 m0, s61, 131072
s_nop 0
buffer_load_dwordx4 v5, s[44:47], s22 offen offset:0 lds
v_mfma_f32_16x16x32_bf16 v[100:103], v[160:163], a[228:231], v[100:103]
ds_read_b128 v[188:191], v221 offset:14336
v_mfma_f32_16x16x32_bf16 v[100:103], v[164:167], a[236:239], v[100:103]
v_mfma_f32_16x16x32_bf16 v[104:107], v[168:171], a[224:227], v[104:107]
ds_read_b128 v[160:163], v220 offset:16384
v_mfma_f32_16x16x32_bf16 v[104:107], v[172:175], a[232:235], v[104:107]
buffer_load_dwordx4 v27, s[44:47], s23 offen offset:1024 lds
v_mfma_f32_16x16x32_bf16 v[108:111], v[168:171], a[228:231], v[108:111]
ds_read_b128 v[164:167], v221 offset:16384
v_mfma_f32_16x16x32_bf16 v[108:111], v[172:175], a[236:239], v[108:111]
s_waitcnt lgkmcnt(2)
v_mfma_f32_16x16x32_bf16 v[112:115], v[176:179], a[224:227], v[112:115]
ds_read_b128 v[168:171], v220 offset:18432
v_mfma_f32_16x16x32_bf16 v[112:115], v[180:183], a[232:235], v[112:115]
v_mfma_f32_16x16x32_bf16 v[116:119], v[176:179], a[228:231], v[116:119]
ds_read_b128 v[172:175], v221 offset:18432
v_mfma_f32_16x16x32_bf16 v[116:119], v[180:183], a[236:239], v[116:119]
s_add_u32 m0, s61, 139264
s_nop 0
buffer_load_dwordx4 v5, s[44:47], s24 offen offset:0 lds
v_mfma_f32_16x16x32_bf16 v[120:123], v[184:187], a[224:227], v[120:123]
ds_read_b128 v[176:179], v220 offset:20480
v_mfma_f32_16x16x32_bf16 v[120:123], v[188:191], a[232:235], v[120:123]
v_mfma_f32_16x16x32_bf16 v[124:127], v[184:187], a[228:231], v[124:127]
ds_read_b128 v[180:183], v221 offset:20480
v_mfma_f32_16x16x32_bf16 v[124:127], v[188:191], a[236:239], v[124:127]
s_waitcnt lgkmcnt(2)
buffer_load_dwordx4 v27, s[44:47], s25 offen offset:1024 lds
v_mfma_f32_16x16x32_bf16 v[128:131], v[160:163], a[224:227], v[128:131]
ds_read_b128 v[184:187], v220 offset:22528
v_mfma_f32_16x16x32_bf16 v[128:131], v[164:167], a[232:235], v[128:131]
v_mfma_f32_16x16x32_bf16 v[132:135], v[160:163], a[228:231], v[132:135]
ds_read_b128 v[188:191], v221 offset:22528
v_mfma_f32_16x16x32_bf16 v[132:135], v[164:167], a[236:239], v[132:135]
v_mfma_f32_16x16x32_bf16 v[136:139], v[168:171], a[224:227], v[136:139]
ds_read_b128 v[160:163], v220 offset:24576
v_mfma_f32_16x16x32_bf16 v[136:139], v[172:175], a[232:235], v[136:139]
s_add_u32 m0, s61, 0
s_nop 0
buffer_load_dwordx4 v5, s[44:47], s26 offen offset:0 lds
v_mfma_f32_16x16x32_bf16 v[140:143], v[168:171], a[228:231], v[140:143]
ds_read_b128 v[164:167], v221 offset:24576
v_mfma_f32_16x16x32_bf16 v[140:143], v[172:175], a[236:239], v[140:143]
s_waitcnt lgkmcnt(2)
v_mfma_f32_16x16x32_bf16 v[144:147], v[176:179], a[224:227], v[144:147]
ds_read_b128 v[168:171], v220 offset:26624
v_mfma_f32_16x16x32_bf16 v[144:147], v[180:183], a[232:235], v[144:147]
buffer_load_dwordx4 v27, s[44:47], s27 offen offset:1024 lds
v_mfma_f32_16x16x32_bf16 v[148:151], v[176:179], a[228:231], v[148:151]
ds_read_b128 v[172:175], v221 offset:26624
v_mfma_f32_16x16x32_bf16 v[148:151], v[180:183], a[236:239], v[148:151]
v_mfma_f32_16x16x32_bf16 v[152:155], v[184:187], a[224:227], v[152:155]
ds_read_b128 v[176:179], v220 offset:28672
v_mfma_f32_16x16x32_bf16 v[152:155], v[188:191], a[232:235], v[152:155]
v_mfma_f32_16x16x32_bf16 v[156:159], v[184:187], a[228:231], v[156:159]
ds_read_b128 v[180:183], v221 offset:28672
v_mfma_f32_16x16x32_bf16 v[156:159], v[188:191], a[236:239], v[156:159]
s_waitcnt lgkmcnt(2)
s_add_u32 m0, s61, 8192
s_waitcnt vmcnt(27)
v_mfma_f32_16x16x32_bf16 v[96:99], v[160:163], a[240:243], v[96:99]
ds_read_b128 v[184:187], v220 offset:30720
buffer_load_dwordx4 v5, s[44:47], s28 offen offset:0 lds
s_waitcnt vmcnt(26)
v_mfma_f32_16x16x32_bf16 v[96:99], v[164:167], a[248:251], v[96:99]
v_mfma_f32_16x16x32_bf16 v[100:103], v[160:163], a[244:247], v[100:103]
ds_read_b128 v[188:191], v221 offset:30720
s_waitcnt vmcnt(25)
v_mfma_f32_16x16x32_bf16 v[100:103], v[164:167], a[252:255], v[100:103]
v_mfma_f32_16x16x32_bf16 v[104:107], v[168:171], a[240:243], v[104:107]
ds_read_b128 v[160:163], v220 offset:32768
buffer_load_dwordx4 v27, s[44:47], s29 offen offset:1024 lds
v_mfma_f32_16x16x32_bf16 v[104:107], v[172:175], a[248:251], v[104:107]
v_mfma_f32_16x16x32_bf16 v[108:111], v[168:171], a[244:247], v[108:111]
ds_read_b128 v[164:167], v221 offset:32768
v_mfma_f32_16x16x32_bf16 v[108:111], v[172:175], a[252:255], v[108:111]
)ASM"
R"ASM(
s_waitcnt lgkmcnt(2)
v_mfma_f32_16x16x32_bf16 v[112:115], v[176:179], a[240:243], v[112:115]
ds_read_b128 v[168:171], v220 offset:34816
v_mfma_f32_16x16x32_bf16 v[112:115], v[180:183], a[248:251], v[112:115]
s_add_u32 m0, s61, 16384
s_nop 0
buffer_load_dwordx4 v5, s[44:47], s30 offen offset:0 lds
v_mfma_f32_16x16x32_bf16 v[116:119], v[176:179], a[244:247], v[116:119]
ds_read_b128 v[172:175], v221 offset:34816
v_mfma_f32_16x16x32_bf16 v[116:119], v[180:183], a[252:255], v[116:119]
v_mfma_f32_16x16x32_bf16 v[120:123], v[184:187], a[240:243], v[120:123]
ds_read_b128 v[176:179], v220 offset:36864
v_mfma_f32_16x16x32_bf16 v[120:123], v[188:191], a[248:251], v[120:123]
buffer_load_dwordx4 v27, s[44:47], s31 offen offset:1024 lds
v_mfma_f32_16x16x32_bf16 v[124:127], v[184:187], a[244:247], v[124:127]
ds_read_b128 v[180:183], v221 offset:36864
v_mfma_f32_16x16x32_bf16 v[124:127], v[188:191], a[252:255], v[124:127]
s_waitcnt lgkmcnt(2)
v_mfma_f32_16x16x32_bf16 v[128:131], v[160:163], a[240:243], v[128:131]
ds_read_b128 v[184:187], v220 offset:38912
v_mfma_f32_16x16x32_bf16 v[128:131], v[164:167], a[248:251], v[128:131]
v_mfma_f32_16x16x32_bf16 v[132:135], v[160:163], a[244:247], v[132:135]
ds_read_b128 v[188:191], v221 offset:38912
v_mfma_f32_16x16x32_bf16 v[132:135], v[164:167], a[252:255], v[132:135]
s_add_u32 m0, s61, 24576
s_add_u32 s63, s95, 147456
v_mfma_f32_16x16x32_bf16 v[136:139], v[168:171], a[240:243], v[136:139]
s_waitcnt vmcnt(16) lgkmcnt(0)
s_barrier
v_mfma_f32_16x16x32_bf16 v[136:139], v[172:175], a[248:251], v[136:139]
ds_read_b128 v[160:163], v220 offset:40960
buffer_load_dwordx4 v23, s[48:51], s63 offen offset:0 lds
v_mfma_f32_16x16x32_bf16 v[140:143], v[168:171], a[244:247], v[140:143]
v_mfma_f32_16x16x32_bf16 v[140:143], v[172:175], a[252:255], v[140:143]
ds_read_b128 v[164:167], v221 offset:40960
v_mfma_f32_16x16x32_bf16 v[144:147], v[176:179], a[240:243], v[144:147]
ds_read_b128 v[168:171], v220 offset:43008
v_mfma_f32_16x16x32_bf16 v[144:147], v[180:183], a[248:251], v[144:147]
buffer_load_dwordx4 v6, s[48:51], s63 offen offset:1024 lds
v_mfma_f32_16x16x32_bf16 v[148:151], v[176:179], a[244:247], v[148:151]
ds_read_b128 v[172:175], v221 offset:43008
v_mfma_f32_16x16x32_bf16 v[148:151], v[180:183], a[252:255], v[148:151]
v_mfma_f32_16x16x32_bf16 v[152:155], v[184:187], a[240:243], v[152:155]
ds_read_b128 v[176:179], v220 offset:45056
v_mfma_f32_16x16x32_bf16 v[152:155], v[188:191], a[248:251], v[152:155]
s_add_u32 m0, s61, 32768
s_add_u32 s63, s95, 147584
v_mfma_f32_16x16x32_bf16 v[156:159], v[184:187], a[244:247], v[156:159]
ds_read_b128 v[180:183], v221 offset:45056
buffer_load_dwordx4 v23, s[48:51], s63 offen offset:0 lds
v_mfma_f32_16x16x32_bf16 v[156:159], v[188:191], a[252:255], v[156:159]
s_waitcnt lgkmcnt(2)
v_mfma_f32_16x16x32_bf16 v[96:99], v[160:163], a[0:3], v[96:99]
ds_read_b128 v[184:187], v220 offset:47104
v_mfma_f32_16x16x32_bf16 v[96:99], v[164:167], a[8:11], v[96:99]
v_mfma_f32_16x16x32_bf16 v[100:103], v[160:163], a[4:7], v[100:103]
ds_read_b128 v[188:191], v221 offset:47104
buffer_load_dwordx4 v6, s[48:51], s63 offen offset:1024 lds
v_mfma_f32_16x16x32_bf16 v[100:103], v[164:167], a[12:15], v[100:103]
v_mfma_f32_16x16x32_bf16 v[104:107], v[168:171], a[0:3], v[104:107]
ds_read_b128 v[160:163], v222 offset:0
v_mfma_f32_16x16x32_bf16 v[104:107], v[172:175], a[8:11], v[104:107]
v_mfma_f32_16x16x32_bf16 v[108:111], v[168:171], a[4:7], v[108:111]
ds_read_b128 v[164:167], v223 offset:0
v_mfma_f32_16x16x32_bf16 v[108:111], v[172:175], a[12:15], v[108:111]
s_waitcnt lgkmcnt(2)
s_add_u32 m0, s61, 40960
v_mfma_f32_16x16x32_bf16 v[112:115], v[176:179], a[0:3], v[112:115]
ds_read_b128 v[168:171], v222 offset:2048
s_add_u32 s63, s95, 147712
v_mfma_f32_16x16x32_bf16 v[112:115], v[180:183], a[8:11], v[112:115]
buffer_load_dwordx4 v23, s[48:51], s63 offen offset:0 lds
v_mfma_f32_16x16x32_bf16 v[116:119], v[176:179], a[4:7], v[116:119]
ds_read_b128 v[172:175], v223 offset:2048
v_mfma_f32_16x16x32_bf16 v[116:119], v[180:183], a[12:15], v[116:119]
v_mfma_f32_16x16x32_bf16 v[120:123], v[184:187], a[0:3], v[120:123]
ds_read_b128 v[176:179], v222 offset:4096
v_mfma_f32_16x16x32_bf16 v[120:123], v[188:191], a[8:11], v[120:123]
buffer_load_dwordx4 v6, s[48:51], s63 offen offset:1024 lds
v_mfma_f32_16x16x32_bf16 v[124:127], v[184:187], a[4:7], v[124:127]
ds_read_b128 v[180:183], v223 offset:4096
v_mfma_f32_16x16x32_bf16 v[124:127], v[188:191], a[12:15], v[124:127]
s_waitcnt lgkmcnt(2)
v_mfma_f32_16x16x32_bf16 v[128:131], v[160:163], a[0:3], v[128:131]
ds_read_b128 v[184:187], v222 offset:6144
v_mfma_f32_16x16x32_bf16 v[128:131], v[164:167], a[8:11], v[128:131]
s_add_u32 m0, s61, 49152
s_add_u32 s63, s95, 147840
v_mfma_f32_16x16x32_bf16 v[132:135], v[160:163], a[4:7], v[132:135]
ds_read_b128 v[188:191], v223 offset:6144
buffer_load_dwordx4 v23, s[48:51], s63 offen offset:0 lds
v_mfma_f32_16x16x32_bf16 v[132:135], v[164:167], a[12:15], v[132:135]
v_mfma_f32_16x16x32_bf16 v[136:139], v[168:171], a[0:3], v[136:139]
ds_read_b128 v[160:163], v222 offset:8192
v_mfma_f32_16x16x32_bf16 v[136:139], v[172:175], a[8:11], v[136:139]
v_mfma_f32_16x16x32_bf16 v[140:143], v[168:171], a[4:7], v[140:143]
ds_read_b128 v[164:167], v223 offset:8192
buffer_load_dwordx4 v6, s[48:51], s63 offen offset:1024 lds
v_mfma_f32_16x16x32_bf16 v[140:143], v[172:175], a[12:15], v[140:143]
s_waitcnt lgkmcnt(2)
v_mfma_f32_16x16x32_bf16 v[144:147], v[176:179], a[0:3], v[144:147]
ds_read_b128 v[168:171], v222 offset:10240
v_mfma_f32_16x16x32_bf16 v[144:147], v[180:183], a[8:11], v[144:147]
v_mfma_f32_16x16x32_bf16 v[148:151], v[176:179], a[4:7], v[148:151]
ds_read_b128 v[172:175], v223 offset:10240
v_mfma_f32_16x16x32_bf16 v[148:151], v[180:183], a[12:15], v[148:151]
v_mfma_f32_16x16x32_bf16 v[152:155], v[184:187], a[0:3], v[152:155]
ds_read_b128 v[176:179], v222 offset:12288
v_mfma_f32_16x16x32_bf16 v[152:155], v[188:191], a[8:11], v[152:155]
v_mfma_f32_16x16x32_bf16 v[156:159], v[184:187], a[4:7], v[156:159]
ds_read_b128 v[180:183], v223 offset:12288
v_mfma_f32_16x16x32_bf16 v[156:159], v[188:191], a[12:15], v[156:159]
s_waitcnt lgkmcnt(2)
s_waitcnt vmcnt(23)
v_mfma_f32_16x16x32_bf16 v[96:99], v[160:163], a[16:19], v[96:99]
ds_read_b128 v[184:187], v222 offset:14336
s_waitcnt vmcnt(21)
v_mfma_f32_16x16x32_bf16 v[96:99], v[164:167], a[24:27], v[96:99]
v_mfma_f32_16x16x32_bf16 v[100:103], v[160:163], a[20:23], v[100:103]
ds_read_b128 v[188:191], v223 offset:14336
s_waitcnt vmcnt(20)
v_mfma_f32_16x16x32_bf16 v[100:103], v[164:167], a[28:31], v[100:103]
v_mfma_f32_16x16x32_bf16 v[104:107], v[168:171], a[16:19], v[104:107]
ds_read_b128 v[160:163], v222 offset:16384
v_mfma_f32_16x16x32_bf16 v[104:107], v[172:175], a[24:27], v[104:107]
v_mfma_f32_16x16x32_bf16 v[108:111], v[168:171], a[20:23], v[108:111]
ds_read_b128 v[164:167], v223 offset:16384
v_mfma_f32_16x16x32_bf16 v[108:111], v[172:175], a[28:31], v[108:111]
s_waitcnt lgkmcnt(2)
v_mfma_f32_16x16x32_bf16 v[112:115], v[176:179], a[16:19], v[112:115]
ds_read_b128 v[168:171], v222 offset:18432
v_mfma_f32_16x16x32_bf16 v[112:115], v[180:183], a[24:27], v[112:115]
v_mfma_f32_16x16x32_bf16 v[116:119], v[176:179], a[20:23], v[116:119]
ds_read_b128 v[172:175], v223 offset:18432
v_mfma_f32_16x16x32_bf16 v[116:119], v[180:183], a[28:31], v[116:119]
v_mfma_f32_16x16x32_bf16 v[120:123], v[184:187], a[16:19], v[120:123]
ds_read_b128 v[176:179], v222 offset:20480
v_mfma_f32_16x16x32_bf16 v[120:123], v[188:191], a[24:27], v[120:123]
v_mfma_f32_16x16x32_bf16 v[124:127], v[184:187], a[20:23], v[124:127]
ds_read_b128 v[180:183], v223 offset:20480
v_mfma_f32_16x16x32_bf16 v[124:127], v[188:191], a[28:31], v[124:127]
s_waitcnt lgkmcnt(2)
v_mfma_f32_16x16x32_bf16 v[128:131], v[160:163], a[16:19], v[128:131]
ds_read_b128 v[184:187], v222 offset:22528
v_mfma_f32_16x16x32_bf16 v[128:131], v[164:167], a[24:27], v[128:131]
v_mfma_f32_16x16x32_bf16 v[132:135], v[160:163], a[20:23], v[132:135]
ds_read_b128 v[188:191], v223 offset:22528
v_mfma_f32_16x16x32_bf16 v[132:135], v[164:167], a[28:31], v[132:135]
v_mfma_f32_16x16x32_bf16 v[136:139], v[168:171], a[16:19], v[136:139]
s_waitcnt vmcnt(8) lgkmcnt(0)
s_barrier
v_mfma_f32_16x16x32_bf16 v[136:139], v[172:175], a[24:27], v[136:139]
ds_read_b64_tr_b16 v[160:161], v29 offset:24576
v_mfma_f32_16x16x32_bf16 v[140:143], v[168:171], a[20:23], v[140:143]
ds_read_b64_tr_b16 v[162:163], v29 offset:25088
v_mfma_f32_16x16x32_bf16 v[140:143], v[172:175], a[28:31], v[140:143]
ds_read_b64_tr_b16 v[164:165], v29 offset:25600
ds_read_b64_tr_b16 v[166:167], v29 offset:26112
v_mfma_f32_16x16x32_bf16 v[144:147], v[176:179], a[16:19], v[144:147]
ds_read_b64_tr_b16 v[168:169], v30 offset:24576
v_mfma_f32_16x16x32_bf16 v[144:147], v[180:183], a[24:27], v[144:147]
ds_read_b64_tr_b16 v[170:171], v30 offset:25088
v_mfma_f32_16x16x32_bf16 v[148:151], v[176:179], a[20:23], v[148:151]
ds_read_b64_tr_b16 v[172:173], v30 offset:25600
v_mfma_f32_16x16x32_bf16 v[148:151], v[180:183], a[28:31], v[148:151]
ds_read_b64_tr_b16 v[174:175], v30 offset:26112
v_mfma_f32_16x16x32_bf16 v[152:155], v[184:187], a[16:19], v[152:155]
ds_read_b64_tr_b16 v[176:177], v31 offset:24576
v_mfma_f32_16x16x32_bf16 v[152:155], v[188:191], a[24:27], v[152:155]
ds_read_b64_tr_b16 v[178:179], v31 offset:25088
v_mfma_f32_16x16x32_bf16 v[156:159], v[184:187], a[20:23], v[156:159]
ds_read_b64_tr_b16 v[180:181], v31 offset:25600
v_mfma_f32_16x16x32_bf16 v[156:159], v[188:191], a[28:31], v[156:159]
s_waitcnt lgkmcnt(3)
ds_read_b64_tr_b16 v[182:183], v31 offset:26112
v_mfma_f32_16x16x32_bf16 v[232:235], v[160:163], a[128:131], v[232:235]
ds_read_b64_tr_b16 v[184:185], v29 offset:32768
v_mfma_f32_16x16x32_bf16 v[232:235], v[164:167], a[136:139], v[232:235]
ds_read_b64_tr_b16 v[186:187], v29 offset:33280
v_mfma_f32_16x16x32_bf16 v[236:239], v[160:163], a[132:135], v[236:239]
ds_read_b64_tr_b16 v[188:189], v29 offset:33792
v_mfma_f32_16x16x32_bf16 v[236:239], v[164:167], a[140:143], v[236:239]
ds_read_b64_tr_b16 v[190:191], v29 offset:34304
v_mfma_f32_16x16x32_bf16 v[240:243], v[168:171], a[128:131], v[240:243]
ds_read_b64_tr_b16 v[160:161], v30 offset:32768
v_cvt_pk_bf16_f32 v72, v104, v105
v_mfma_f32_16x16x32_bf16 v[240:243], v[172:175], a[136:139], v[240:243]
ds_read_b64_tr_b16 v[162:163], v30 offset:33280
v_cvt_pk_bf16_f32 v73, v106, v107
v_mfma_f32_16x16x32_bf16 v[244:247], v[168:171], a[132:135], v[244:247]
ds_read_b64_tr_b16 v[164:165], v30 offset:33792
v_cvt_pk_bf16_f32 v74, v108, v109
v_mfma_f32_16x16x32_bf16 v[244:247], v[172:175], a[140:143], v[244:247]
s_waitcnt lgkmcnt(3)
ds_read_b64_tr_b16 v[166:167], v30 offset:34304
v_mfma_f32_16x16x32_bf16 v[248:251], v[176:179], a[128:131], v[248:251]
ds_read_b64_tr_b16 v[168:169], v31 offset:32768
v_cvt_pk_bf16_f32 v75, v110, v111
v_mfma_f32_16x16x32_bf16 v[248:251], v[180:183], a[136:139], v[248:251]
ds_read_b64_tr_b16 v[170:171], v31 offset:33280
)ASM"
R"ASM(
v_cvt_pk_bf16_f32 v76, v136, v137
v_mfma_f32_16x16x32_bf16 v[252:255], v[176:179], a[132:135], v[252:255]
ds_read_b64_tr_b16 v[172:173], v31 offset:33792
v_cvt_pk_bf16_f32 v77, v138, v139
v_mfma_f32_16x16x32_bf16 v[252:255], v[180:183], a[140:143], v[252:255]
ds_read_b64_tr_b16 v[174:175], v31 offset:34304
v_cvt_pk_bf16_f32 v78, v140, v141
v_mfma_f32_16x16x32_bf16 v[232:235], v[184:187], a[144:147], v[232:235]
ds_read_b64_tr_b16 v[176:177], v29 offset:40960
v_cvt_pk_bf16_f32 v79, v142, v143
v_mfma_f32_16x16x32_bf16 v[232:235], v[188:191], a[152:155], v[232:235]
ds_read_b64_tr_b16 v[178:179], v29 offset:41472
s_add_u32 s77, s68, 1
v_mfma_f32_16x16x32_bf16 v[236:239], v[184:187], a[148:151], v[236:239]
ds_read_b64_tr_b16 v[180:181], v29 offset:41984
s_and_b32 s77, s77, 3
v_mfma_f32_16x16x32_bf16 v[236:239], v[188:191], a[156:159], v[236:239]
s_waitcnt lgkmcnt(3)
ds_read_b64_tr_b16 v[182:183], v29 offset:42496
v_mfma_f32_16x16x32_bf16 v[240:243], v[160:163], a[144:147], v[240:243]
ds_read_b64_tr_b16 v[184:185], v30 offset:40960
s_mul_i32 s77, s77, 84480
v_mfma_f32_16x16x32_bf16 v[240:243], v[164:167], a[152:155], v[240:243]
ds_read_b64_tr_b16 v[186:187], v30 offset:41472
s_add_u32 s77, s77, s66
v_mfma_f32_16x16x32_bf16 v[244:247], v[160:163], a[148:151], v[244:247]
ds_read_b64_tr_b16 v[188:189], v30 offset:41984
buffer_store_dwordx4 v[72:75], v7, s[96:99], s77 offen offset:0 sc1
v_mfma_f32_16x16x32_bf16 v[244:247], v[164:167], a[156:159], v[244:247]
ds_read_b64_tr_b16 v[190:191], v30 offset:42496
buffer_store_dwordx4 v[76:79], v7, s[96:99], s77 offen offset:1024 sc1
v_mfma_f32_16x16x32_bf16 v[248:251], v[168:171], a[144:147], v[248:251]
ds_read_b64_tr_b16 v[160:161], v31 offset:40960
v_cvt_pk_bf16_f32 v64, v112, v113
v_mfma_f32_16x16x32_bf16 v[248:251], v[172:175], a[152:155], v[248:251]
ds_read_b64_tr_b16 v[162:163], v31 offset:41472
v_cvt_pk_bf16_f32 v65, v114, v115
v_mfma_f32_16x16x32_bf16 v[252:255], v[168:171], a[148:151], v[252:255]
ds_read_b64_tr_b16 v[164:165], v31 offset:41984
v_cvt_pk_bf16_f32 v66, v116, v117
v_mfma_f32_16x16x32_bf16 v[252:255], v[172:175], a[156:159], v[252:255]
s_waitcnt lgkmcnt(3)
ds_read_b64_tr_b16 v[166:167], v31 offset:42496
v_mfma_f32_16x16x32_bf16 v[232:235], v[176:179], a[160:163], v[232:235]
ds_read_b64_tr_b16 v[168:169], v193 offset:0
v_cvt_pk_bf16_f32 v67, v118, v119
v_mfma_f32_16x16x32_bf16 v[232:235], v[180:183], a[168:171], v[232:235]
ds_read_b64_tr_b16 v[170:171], v193 offset:512
v_cvt_pk_bf16_f32 v68, v144, v145
v_mfma_f32_16x16x32_bf16 v[236:239], v[176:179], a[164:167], v[236:239]
ds_read_b64_tr_b16 v[172:173], v193 offset:1024
v_cvt_pk_bf16_f32 v69, v146, v147
v_mfma_f32_16x16x32_bf16 v[236:239], v[180:183], a[172:175], v[236:239]
ds_read_b64_tr_b16 v[174:175], v193 offset:1536
v_cvt_pk_bf16_f32 v70, v148, v149
v_mfma_f32_16x16x32_bf16 v[240:243], v[184:187], a[160:163], v[240:243]
ds_read_b64_tr_b16 v[176:177], v194 offset:0
v_cvt_pk_bf16_f32 v71, v150, v151
v_mfma_f32_16x16x32_bf16 v[240:243], v[188:191], a[168:171], v[240:243]
ds_read_b64_tr_b16 v[178:179], v194 offset:512
s_add_u32 s78, s68, 2
v_mfma_f32_16x16x32_bf16 v[244:247], v[184:187], a[164:167], v[244:247]
ds_read_b64_tr_b16 v[180:181], v194 offset:1024
s_and_b32 s78, s78, 3
v_mfma_f32_16x16x32_bf16 v[244:247], v[188:191], a[172:175], v[244:247]
s_waitcnt lgkmcnt(3)
ds_read_b64_tr_b16 v[182:183], v194 offset:1536
v_mfma_f32_16x16x32_bf16 v[248:251], v[160:163], a[160:163], v[248:251]
ds_read_b64_tr_b16 v[184:185], v195 offset:0
s_mul_i32 s78, s78, 84480
v_mfma_f32_16x16x32_bf16 v[248:251], v[164:167], a[168:171], v[248:251]
ds_read_b64_tr_b16 v[186:187], v195 offset:512
s_add_u32 s78, s78, s66
v_mfma_f32_16x16x32_bf16 v[252:255], v[160:163], a[164:167], v[252:255]
ds_read_b64_tr_b16 v[188:189], v195 offset:1024
buffer_store_dwordx4 v[64:67], v7, s[96:99], s78 offen offset:0 sc1
v_mfma_f32_16x16x32_bf16 v[252:255], v[164:167], a[172:175], v[252:255]
ds_read_b64_tr_b16 v[190:191], v195 offset:1536
buffer_store_dwordx4 v[68:71], v7, s[96:99], s78 offen offset:1024 sc1
v_mfma_f32_16x16x32_bf16 v[232:235], v[168:171], a[176:179], v[232:235]
ds_read_b64_tr_b16 v[160:161], v193 offset:8192
v_cvt_pk_bf16_f32 v72, v120, v121
v_mfma_f32_16x16x32_bf16 v[232:235], v[172:175], a[184:187], v[232:235]
ds_read_b64_tr_b16 v[162:163], v193 offset:8704
v_cvt_pk_bf16_f32 v73, v122, v123
v_mfma_f32_16x16x32_bf16 v[236:239], v[168:171], a[180:183], v[236:239]
ds_read_b64_tr_b16 v[164:165], v193 offset:9216
v_cvt_pk_bf16_f32 v74, v124, v125
v_mfma_f32_16x16x32_bf16 v[236:239], v[172:175], a[188:191], v[236:239]
s_waitcnt lgkmcnt(3)
ds_read_b64_tr_b16 v[166:167], v193 offset:9728
v_mfma_f32_16x16x32_bf16 v[240:243], v[176:179], a[176:179], v[240:243]
ds_read_b64_tr_b16 v[168:169], v194 offset:8192
v_cvt_pk_bf16_f32 v75, v126, v127
v_mfma_f32_16x16x32_bf16 v[240:243], v[180:183], a[184:187], v[240:243]
ds_read_b64_tr_b16 v[170:171], v194 offset:8704
v_cvt_pk_bf16_f32 v76, v152, v153
v_mfma_f32_16x16x32_bf16 v[244:247], v[176:179], a[180:183], v[244:247]
ds_read_b64_tr_b16 v[172:173], v194 offset:9216
v_cvt_pk_bf16_f32 v77, v154, v155
v_mfma_f32_16x16x32_bf16 v[244:247], v[180:183], a[188:191], v[244:247]
ds_read_b64_tr_b16 v[174:175], v194 offset:9728
v_cvt_pk_bf16_f32 v78, v156, v157
v_mfma_f32_16x16x32_bf16 v[248:251], v[184:187], a[176:179], v[248:251]
ds_read_b64_tr_b16 v[176:177], v195 offset:8192
v_cvt_pk_bf16_f32 v79, v158, v159
v_mfma_f32_16x16x32_bf16 v[248:251], v[188:191], a[184:187], v[248:251]
ds_read_b64_tr_b16 v[178:179], v195 offset:8704
s_add_u32 s79, s68, 3
v_mfma_f32_16x16x32_bf16 v[252:255], v[184:187], a[180:183], v[252:255]
ds_read_b64_tr_b16 v[180:181], v195 offset:9216
s_and_b32 s79, s79, 3
v_mfma_f32_16x16x32_bf16 v[252:255], v[188:191], a[188:191], v[252:255]
s_waitcnt lgkmcnt(3)
ds_read_b64_tr_b16 v[182:183], v195 offset:9728
v_mfma_f32_16x16x32_bf16 v[232:235], v[160:163], a[192:195], v[232:235]
ds_read_b64_tr_b16 v[184:185], v193 offset:16384
s_mul_i32 s79, s79, 84480
v_mfma_f32_16x16x32_bf16 v[232:235], v[164:167], a[200:203], v[232:235]
ds_read_b64_tr_b16 v[186:187], v193 offset:16896
s_add_u32 s79, s79, s66
v_mfma_f32_16x16x32_bf16 v[236:239], v[160:163], a[196:199], v[236:239]
ds_read_b64_tr_b16 v[188:189], v193 offset:17408
buffer_store_dwordx4 v[72:75], v7, s[96:99], s79 offen offset:0 sc1
v_mfma_f32_16x16x32_bf16 v[236:239], v[164:167], a[204:207], v[236:239]
ds_read_b64_tr_b16 v[190:191], v193 offset:17920
buffer_store_dwordx4 v[76:79], v7, s[96:99], s79 offen offset:1024 sc1
v_mfma_f32_16x16x32_bf16 v[240:243], v[168:171], a[192:195], v[240:243]
s_waitcnt vmcnt(6) lgkmcnt(0)
s_barrier
v_mfma_f32_16x16x32_bf16 v[240:243], v[172:175], a[200:203], v[240:243]
ds_read_b64_tr_b16 v[160:161], v194 offset:16384
v_mov_b32 v73, v219
v_mfma_f32_16x16x32_bf16 v[244:247], v[168:171], a[196:199], v[244:247]
ds_read_b64_tr_b16 v[162:163], v194 offset:16896
v_mov_b32 v75, v219
v_mfma_f32_16x16x32_bf16 v[244:247], v[172:175], a[204:207], v[244:247]
ds_read_b64_tr_b16 v[164:165], v194 offset:17408
ds_read_b64_tr_b16 v[166:167], v194 offset:17920
v_mfma_f32_16x16x32_bf16 v[248:251], v[176:179], a[192:195], v[248:251]
ds_read_b64_tr_b16 v[168:169], v195 offset:16384
v_mov_b32 v77, v219
v_mfma_f32_16x16x32_bf16 v[248:251], v[180:183], a[200:203], v[248:251]
ds_read_b64_tr_b16 v[170:171], v195 offset:16896
v_mov_b32 v79, v219
v_mfma_f32_16x16x32_bf16 v[252:255], v[176:179], a[196:199], v[252:255]
ds_read_b64_tr_b16 v[172:173], v195 offset:17408
v_mov_b32 v81, v219
v_mfma_f32_16x16x32_bf16 v[252:255], v[180:183], a[204:207], v[252:255]
ds_read_b64_tr_b16 v[174:175], v195 offset:17920
v_mov_b32 v83, v219
v_mfma_f32_16x16x32_bf16 v[232:235], v[184:187], a[208:211], v[232:235]
ds_read_b128 v[176:179], v216 offset:26624
v_mov_b32 v85, v219
v_mfma_f32_16x16x32_bf16 v[232:235], v[188:191], a[216:219], v[232:235]
v_mov_b32 v87, v219
v_mov_b32 v89, v219
v_mfma_f32_16x16x32_bf16 v[236:239], v[184:187], a[212:215], v[236:239]
ds_read_b128 v[180:183], v217 offset:26624
v_mov_b32 v91, v219
v_mfma_f32_16x16x32_bf16 v[236:239], v[188:191], a[220:223], v[236:239]
s_waitcnt lgkmcnt(2)
v_mov_b32 v93, v219
v_mfma_f32_16x16x32_bf16 v[240:243], v[160:163], a[208:211], v[240:243]
ds_read_b128 v[184:187], v216 offset:28672
v_mov_b32 v95, v219
v_mfma_f32_16x16x32_bf16 v[240:243], v[164:167], a[216:219], v[240:243]
s_add_u32 s88, s68, 1
s_and_b32 s88, s88, 3
v_mfma_f32_16x16x32_bf16 v[244:247], v[160:163], a[212:215], v[244:247]
ds_read_b128 v[188:191], v217 offset:28672
s_mul_i32 s88, s88, 4224
v_mfma_f32_16x16x32_bf16 v[244:247], v[164:167], a[220:223], v[244:247]
s_add_u32 s88, s88, s67
s_add_u32 s89, s68, 2
v_mfma_f32_16x16x32_bf16 v[248:251], v[168:171], a[208:211], v[248:251]
ds_read_b128 v[160:163], v216 offset:30720
s_and_b32 s89, s89, 3
v_mfma_f32_16x16x32_bf16 v[248:251], v[172:175], a[216:219], v[248:251]
s_mul_i32 s89, s89, 4224
s_add_u32 s89, s89, s67
v_mfma_f32_16x16x32_bf16 v[252:255], v[168:171], a[212:215], v[252:255]
ds_read_b128 v[164:167], v217 offset:30720
s_add_u32 s90, s68, 3
v_mfma_f32_16x16x32_bf16 v[252:255], v[172:175], a[220:223], v[252:255]
s_waitcnt lgkmcnt(2)
s_and_b32 s90, s90, 3
v_mfma_f32_16x16x32_bf16 v[232:235], v[176:179], a[224:227], v[232:235]
ds_read_b128 v[168:171], v216 offset:34816
s_mul_i32 s90, s90, 4224
v_mfma_f32_16x16x32_bf16 v[232:235], v[180:183], a[232:235], v[232:235]
s_add_u32 s90, s90, s67
s_add_u32 s91, s67, 16896
v_mfma_f32_16x16x32_bf16 v[236:239], v[176:179], a[228:231], v[236:239]
ds_read_b128 v[172:175], v217 offset:34816
v_and_b32 v19, 7, v1
v_mfma_f32_16x16x32_bf16 v[236:239], v[180:183], a[236:239], v[236:239]
v_mul_u32_u24 v19, 4224, v19
v_add_u32 v19, 4096, v19
v_mfma_f32_16x16x32_bf16 v[240:243], v[184:187], a[224:227], v[240:243]
)ASM"
R"ASM(
ds_read_b128 v[176:179], v216 offset:36864
v_lshlrev_b32 v11, 1, v8
v_mfma_f32_16x16x32_bf16 v[240:243], v[188:191], a[232:235], v[240:243]
v_readlane_b32 s72, v0, 18
s_nop 1
v_mfma_f32_16x16x32_bf16 v[244:247], v[184:187], a[228:231], v[244:247]
ds_read_b128 v[180:183], v217 offset:36864
s_nop 0
s_lshl_b32 s72, s72, 1
v_mfma_f32_16x16x32_bf16 v[244:247], v[188:191], a[236:239], v[244:247]
s_waitcnt lgkmcnt(2)
s_add_u32 s74, s72, 36864
v_mfma_f32_16x16x32_bf16 v[248:251], v[160:163], a[224:227], v[248:251]
ds_read_b128 v[184:187], v216 offset:38912
v_mfma_f32_16x16x32_bf16 v[248:251], v[164:167], a[232:235], v[248:251]
v_mfma_f32_16x16x32_bf16 v[252:255], v[160:163], a[228:231], v[252:255]
ds_read_b128 v[188:191], v217 offset:38912
v_mfma_f32_16x16x32_bf16 v[252:255], v[164:167], a[236:239], v[252:255]
v_mfma_f32_16x16x32_bf16 v[232:235], v[168:171], a[240:243], v[232:235]
ds_read_b128 v[160:163], v216 offset:43008
v_mfma_f32_16x16x32_bf16 v[232:235], v[172:175], a[248:251], v[232:235]
v_mfma_f32_16x16x32_bf16 v[236:239], v[168:171], a[244:247], v[236:239]
ds_read_b128 v[164:167], v217 offset:43008
v_mfma_f32_16x16x32_bf16 v[236:239], v[172:175], a[252:255], v[236:239]
s_waitcnt lgkmcnt(2)
v_mfma_f32_16x16x32_bf16 v[240:243], v[176:179], a[240:243], v[240:243]
ds_read_b128 v[168:171], v216 offset:45056
v_mfma_f32_16x16x32_bf16 v[240:243], v[180:183], a[248:251], v[240:243]
v_mfma_f32_16x16x32_bf16 v[244:247], v[176:179], a[244:247], v[244:247]
ds_read_b128 v[172:175], v217 offset:45056
v_mfma_f32_16x16x32_bf16 v[244:247], v[180:183], a[252:255], v[244:247]
v_mfma_f32_16x16x32_bf16 v[248:251], v[184:187], a[240:243], v[248:251]
ds_read_b128 v[176:179], v216 offset:47104
v_mfma_f32_16x16x32_bf16 v[248:251], v[188:191], a[248:251], v[248:251]
v_mfma_f32_16x16x32_bf16 v[252:255], v[184:187], a[244:247], v[252:255]
ds_read_b128 v[180:183], v217 offset:47104
v_mfma_f32_16x16x32_bf16 v[252:255], v[188:191], a[252:255], v[252:255]
s_waitcnt lgkmcnt(2)
v_mfma_f32_16x16x32_bf16 v[232:235], v[160:163], a[0:3], v[232:235]
ds_read_b128 v[184:187], v220 offset:2048
v_mfma_f32_16x16x32_bf16 v[232:235], v[164:167], a[8:11], v[232:235]
v_mfma_f32_16x16x32_bf16 v[236:239], v[160:163], a[4:7], v[236:239]
ds_read_b128 v[188:191], v221 offset:2048
v_mfma_f32_16x16x32_bf16 v[236:239], v[164:167], a[12:15], v[236:239]
v_mfma_f32_16x16x32_bf16 v[240:243], v[168:171], a[0:3], v[240:243]
ds_read_b128 v[160:163], v220 offset:4096
v_mfma_f32_16x16x32_bf16 v[240:243], v[172:175], a[8:11], v[240:243]
v_mfma_f32_16x16x32_bf16 v[244:247], v[168:171], a[4:7], v[244:247]
ds_read_b128 v[164:167], v221 offset:4096
v_mfma_f32_16x16x32_bf16 v[244:247], v[172:175], a[12:15], v[244:247]
s_waitcnt lgkmcnt(2)
v_mfma_f32_16x16x32_bf16 v[248:251], v[176:179], a[0:3], v[248:251]
ds_read_b128 v[168:171], v220 offset:6144
v_mfma_f32_16x16x32_bf16 v[248:251], v[180:183], a[8:11], v[248:251]
v_mfma_f32_16x16x32_bf16 v[252:255], v[176:179], a[4:7], v[252:255]
ds_read_b128 v[172:175], v221 offset:6144
v_mfma_f32_16x16x32_bf16 v[252:255], v[180:183], a[12:15], v[252:255]
v_mfma_f32_16x16x32_bf16 v[232:235], v[184:187], a[16:19], v[232:235]
ds_read_b64_tr_b16 v[176:177], v28 offset:24576
v_mfma_f32_16x16x32_bf16 v[232:235], v[188:191], a[24:27], v[232:235]
ds_read_b64_tr_b16 v[178:179], v28 offset:25088
v_mfma_f32_16x16x32_bf16 v[236:239], v[184:187], a[20:23], v[236:239]
ds_read_b64_tr_b16 v[180:181], v28 offset:25600
v_mfma_f32_16x16x32_bf16 v[236:239], v[188:191], a[28:31], v[236:239]
s_waitcnt lgkmcnt(3)
ds_read_b64_tr_b16 v[182:183], v28 offset:26112
v_mfma_f32_16x16x32_bf16 v[240:243], v[160:163], a[16:19], v[240:243]
ds_read_b64_tr_b16 v[184:185], v28 offset:32768
s_waitcnt vmcnt(0)
v_mfma_f32_16x16x32_bf16 v[240:243], v[164:167], a[24:27], v[240:243]
ds_read_b64_tr_b16 v[186:187], v28 offset:33280
v_cmp_gt_u32 vcc, 4, v1
v_mfma_f32_16x16x32_bf16 v[244:247], v[160:163], a[20:23], v[244:247]
ds_read_b64_tr_b16 v[188:189], v28 offset:33792
v_mul_u32_u24 v9, 84480, v1
v_mfma_f32_16x16x32_bf16 v[244:247], v[164:167], a[28:31], v[244:247]
ds_read_b64_tr_b16 v[190:191], v28 offset:34304
v_mov_b32 v10, 0x7f000000
v_mfma_f32_16x16x32_bf16 v[248:251], v[168:171], a[16:19], v[248:251]
ds_read_b64_tr_b16 v[160:161], v28 offset:40960
v_cndmask_b32 v9, v10, v9, vcc
v_mfma_f32_16x16x32_bf16 v[248:251], v[172:175], a[24:27], v[248:251]
ds_read_b64_tr_b16 v[162:163], v28 offset:41472
s_add_u32 s64, s66, 4096
v_mfma_f32_16x16x32_bf16 v[252:255], v[168:171], a[20:23], v[252:255]
ds_read_b64_tr_b16 v[164:165], v28 offset:41984
buffer_store_dword v219, v9, s[96:99], s64 offen sc1
v_mfma_f32_16x16x32_bf16 v[252:255], v[172:175], a[28:31], v[252:255]
s_waitcnt lgkmcnt(3)
ds_read_b64_tr_b16 v[166:167], v28 offset:42496
v_mfma_f32_16x16x32_bf16 v[224:227], v[176:179], a[128:131], v[224:227]
ds_read_b64_tr_b16 v[168:169], v192 offset:0
v_mfma_f32_16x16x32_bf16 v[224:227], v[180:183], a[136:139], v[224:227]
ds_read_b64_tr_b16 v[170:171], v192 offset:512
v_mfma_f32_16x16x32_bf16 v[228:231], v[176:179], a[132:135], v[228:231]
ds_read_b64_tr_b16 v[172:173], v192 offset:1024
v_mfma_f32_16x16x32_bf16 v[228:231], v[180:183], a[140:143], v[228:231]
ds_read_b64_tr_b16 v[174:175], v192 offset:1536
v_cvt_pk_bf16_f32 v72, v232, v233
v_mfma_f32_16x16x32_bf16 v[224:227], v[184:187], a[144:147], v[224:227]
ds_read_b64_tr_b16 v[176:177], v192 offset:8192
v_cvt_pk_bf16_f32 v74, v234, v235
v_mfma_f32_16x16x32_bf16 v[224:227], v[188:191], a[152:155], v[224:227]
ds_read_b64_tr_b16 v[178:179], v192 offset:8704
v_cvt_pk_bf16_f32 v76, v236, v237
v_mfma_f32_16x16x32_bf16 v[228:231], v[184:187], a[148:151], v[228:231]
ds_read_b64_tr_b16 v[180:181], v192 offset:9216
v_cvt_pk_bf16_f32 v78, v238, v239
v_mfma_f32_16x16x32_bf16 v[228:231], v[188:191], a[156:159], v[228:231]
s_waitcnt lgkmcnt(3)
ds_read_b64_tr_b16 v[182:183], v192 offset:9728
v_mfma_f32_16x16x32_bf16 v[224:227], v[160:163], a[160:163], v[224:227]
ds_read_b64_tr_b16 v[184:185], v192 offset:16384
buffer_store_dwordx4 v[72:75], v7, s[96:99], s77 offen offset:2048 sc1
v_mfma_f32_16x16x32_bf16 v[224:227], v[164:167], a[168:171], v[224:227]
ds_read_b64_tr_b16 v[186:187], v192 offset:16896
buffer_store_dwordx4 v[76:79], v7, s[96:99], s77 offen offset:3072 sc1
v_mfma_f32_16x16x32_bf16 v[228:231], v[160:163], a[164:167], v[228:231]
ds_read_b64_tr_b16 v[188:189], v192 offset:17408
v_cvt_pk_bf16_f32 v80, v240, v241
v_mfma_f32_16x16x32_bf16 v[228:231], v[164:167], a[172:175], v[228:231]
ds_read_b64_tr_b16 v[190:191], v192 offset:17920
v_cvt_pk_bf16_f32 v82, v242, v243
v_mfma_f32_16x16x32_bf16 v[224:227], v[168:171], a[176:179], v[224:227]
ds_read_b128 v[160:163], v216 offset:24576
v_cvt_pk_bf16_f32 v84, v244, v245
v_mfma_f32_16x16x32_bf16 v[224:227], v[172:175], a[184:187], v[224:227]
v_cvt_pk_bf16_f32 v86, v246, v247
buffer_store_dwordx4 v[80:83], v7, s[96:99], s78 offen offset:2048 sc1
v_mfma_f32_16x16x32_bf16 v[228:231], v[168:171], a[180:183], v[228:231]
ds_read_b128 v[164:167], v217 offset:24576
buffer_store_dwordx4 v[84:87], v7, s[96:99], s78 offen offset:3072 sc1
v_mfma_f32_16x16x32_bf16 v[228:231], v[172:175], a[188:191], v[228:231]
s_waitcnt lgkmcnt(2)
v_cvt_pk_bf16_f32 v88, v248, v249
v_mfma_f32_16x16x32_bf16 v[224:227], v[176:179], a[192:195], v[224:227]
ds_read_b128 v[168:171], v216 offset:32768
v_cvt_pk_bf16_f32 v90, v250, v251
v_mfma_f32_16x16x32_bf16 v[224:227], v[180:183], a[200:203], v[224:227]
v_cvt_pk_bf16_f32 v92, v252, v253
v_cvt_pk_bf16_f32 v94, v254, v255
v_mfma_f32_16x16x32_bf16 v[228:231], v[176:179], a[196:199], v[228:231]
ds_read_b128 v[172:175], v217 offset:32768
buffer_store_dwordx4 v[88:91], v7, s[96:99], s79 offen offset:2048 sc1
v_mfma_f32_16x16x32_bf16 v[228:231], v[180:183], a[204:207], v[228:231]
buffer_store_dwordx4 v[92:95], v7, s[96:99], s79 offen offset:3072 sc1
v_mfma_f32_16x16x32_bf16 v[224:227], v[184:187], a[208:211], v[224:227]
ds_read_b128 v[176:179], v216 offset:40960
v_mfma_f32_16x16x32_bf16 v[224:227], v[188:191], a[216:219], v[224:227]
v_mfma_f32_16x16x32_bf16 v[228:231], v[184:187], a[212:215], v[228:231]
ds_read_b128 v[180:183], v217 offset:40960
v_mfma_f32_16x16x32_bf16 v[228:231], v[188:191], a[220:223], v[228:231]
s_waitcnt lgkmcnt(2)
v_mfma_f32_16x16x32_bf16 v[224:227], v[160:163], a[224:227], v[224:227]
ds_read_b128 v[184:187], v220 offset:0
v_mfma_f32_16x16x32_bf16 v[224:227], v[164:167], a[232:235], v[224:227]
v_mfma_f32_16x16x32_bf16 v[228:231], v[160:163], a[228:231], v[228:231]
ds_read_b128 v[188:191], v221 offset:0
buffer_load_dword v16, v19, s[96:99], s67 offen sc1
v_mfma_f32_16x16x32_bf16 v[228:231], v[164:167], a[236:239], v[228:231]
v_mfma_f32_16x16x32_bf16 v[224:227], v[168:171], a[240:243], v[224:227]
v_mfma_f32_16x16x32_bf16 v[224:227], v[172:175], a[248:251], v[224:227]
v_mfma_f32_16x16x32_bf16 v[228:231], v[168:171], a[244:247], v[228:231]
v_mfma_f32_16x16x32_bf16 v[228:231], v[172:175], a[252:255], v[228:231]
s_waitcnt lgkmcnt(3)
v_mfma_f32_16x16x32_bf16 v[224:227], v[176:179], a[0:3], v[224:227]
s_waitcnt lgkmcnt(2)
v_mfma_f32_16x16x32_bf16 v[224:227], v[180:183], a[8:11], v[224:227]
v_mfma_f32_16x16x32_bf16 v[228:231], v[176:179], a[4:7], v[228:231]
v_mfma_f32_16x16x32_bf16 v[228:231], v[180:183], a[12:15], v[228:231]
s_waitcnt lgkmcnt(1)
v_mfma_f32_16x16x32_bf16 v[224:227], v[184:187], a[16:19], v[224:227]
s_waitcnt lgkmcnt(0)
v_mfma_f32_16x16x32_bf16 v[224:227], v[188:191], a[24:27], v[224:227]
v_mfma_f32_16x16x32_bf16 v[228:231], v[184:187], a[20:23], v[228:231]
v_mfma_f32_16x16x32_bf16 v[228:231], v[188:191], a[28:31], v[228:231]
v_rsq_f32 v51, v52
v_rsq_f32 v55, v56
v_lshlrev_b32 v60, 16, v32
v_lshlrev_b32 v58, 16, v44
v_add_f32 v59, v60, v50
v_mul_f32 v59, v59, v51
v_fmac_f32 v60, v59, v58
v_add_f32 v96, v96, v60
v_and_b32 v60, 0xffff0000, v32
v_and_b32 v58, 0xffff0000, v44
v_add_f32 v59, v60, v50
v_mul_f32 v59, v59, v51
v_fmac_f32 v60, v59, v58
v_add_f32 v97, v97, v60
v_lshlrev_b32 v60, 16, v33
v_lshlrev_b32 v58, 16, v45
v_add_f32 v59, v60, v50
v_mul_f32 v59, v59, v51
v_fmac_f32 v60, v59, v58
v_add_f32 v98, v98, v60
v_and_b32 v60, 0xffff0000, v33
v_and_b32 v58, 0xffff0000, v45
v_add_f32 v59, v60, v50
v_mul_f32 v59, v59, v51
)ASM"
R"ASM(
v_fmac_f32 v60, v59, v58
v_add_f32 v99, v99, v60
v_lshlrev_b32 v60, 16, v34
v_lshlrev_b32 v58, 16, v44
v_add_f32 v59, v60, v54
v_mul_f32 v59, v59, v55
v_fmac_f32 v60, v59, v58
v_add_f32 v100, v100, v60
v_and_b32 v60, 0xffff0000, v34
v_and_b32 v58, 0xffff0000, v44
v_add_f32 v59, v60, v54
v_mul_f32 v59, v59, v55
v_fmac_f32 v60, v59, v58
v_add_f32 v101, v101, v60
v_lshlrev_b32 v60, 16, v35
v_lshlrev_b32 v58, 16, v45
v_add_f32 v59, v60, v54
v_mul_f32 v59, v59, v55
v_fmac_f32 v60, v59, v58
v_add_f32 v102, v102, v60
v_and_b32 v60, 0xffff0000, v35
v_and_b32 v58, 0xffff0000, v45
v_add_f32 v59, v60, v54
v_mul_f32 v59, v59, v55
v_fmac_f32 v60, v59, v58
v_add_f32 v103, v103, v60
v_lshlrev_b32 v60, 16, v36
v_lshlrev_b32 v58, 16, v46
v_add_f32 v59, v60, v50
v_mul_f32 v59, v59, v51
v_fmac_f32 v60, v59, v58
v_add_f32 v128, v128, v60
v_and_b32 v60, 0xffff0000, v36
v_and_b32 v58, 0xffff0000, v46
v_add_f32 v59, v60, v50
v_mul_f32 v59, v59, v51
v_fmac_f32 v60, v59, v58
v_add_f32 v129, v129, v60
v_lshlrev_b32 v60, 16, v37
v_lshlrev_b32 v58, 16, v47
v_add_f32 v59, v60, v50
v_mul_f32 v59, v59, v51
v_fmac_f32 v60, v59, v58
v_add_f32 v130, v130, v60
v_and_b32 v60, 0xffff0000, v37
v_and_b32 v58, 0xffff0000, v47
v_add_f32 v59, v60, v50
v_mul_f32 v59, v59, v51
v_fmac_f32 v60, v59, v58
v_add_f32 v131, v131, v60
v_lshlrev_b32 v60, 16, v38
v_lshlrev_b32 v58, 16, v46
v_add_f32 v59, v60, v54
v_mul_f32 v59, v59, v55
v_fmac_f32 v60, v59, v58
v_add_f32 v132, v132, v60
v_and_b32 v60, 0xffff0000, v38
v_and_b32 v58, 0xffff0000, v46
v_add_f32 v59, v60, v54
v_mul_f32 v59, v59, v55
v_fmac_f32 v60, v59, v58
v_add_f32 v133, v133, v60
v_lshlrev_b32 v60, 16, v39
v_lshlrev_b32 v58, 16, v47
v_add_f32 v59, v60, v54
v_mul_f32 v59, v59, v55
v_fmac_f32 v60, v59, v58
v_add_f32 v134, v134, v60
v_and_b32 v60, 0xffff0000, v39
v_and_b32 v58, 0xffff0000, v47
v_add_f32 v59, v60, v54
v_mul_f32 v59, v59, v55
v_fmac_f32 v60, v59, v58
v_add_f32 v135, v135, v60
v_lshlrev_b32 v60, 16, v40
v_lshlrev_b32 v58, 16, v48
v_add_f32 v59, v60, v50
v_mul_f32 v59, v59, v51
v_fmac_f32 v60, v59, v58
v_add_f32 v224, v224, v60
v_and_b32 v60, 0xffff0000, v40
v_and_b32 v58, 0xffff0000, v48
v_add_f32 v59, v60, v50
v_mul_f32 v59, v59, v51
v_fmac_f32 v60, v59, v58
v_add_f32 v225, v225, v60
v_lshlrev_b32 v60, 16, v41
v_lshlrev_b32 v58, 16, v49
v_add_f32 v59, v60, v50
v_mul_f32 v59, v59, v51
v_fmac_f32 v60, v59, v58
v_add_f32 v226, v226, v60
v_and_b32 v60, 0xffff0000, v41
v_and_b32 v58, 0xffff0000, v49
v_add_f32 v59, v60, v50
v_mul_f32 v59, v59, v51
v_fmac_f32 v60, v59, v58
v_add_f32 v227, v227, v60
v_lshlrev_b32 v60, 16, v42
v_lshlrev_b32 v58, 16, v48
v_add_f32 v59, v60, v54
v_mul_f32 v59, v59, v55
v_fmac_f32 v60, v59, v58
v_add_f32 v228, v228, v60
v_and_b32 v60, 0xffff0000, v42
v_and_b32 v58, 0xffff0000, v48
v_add_f32 v59, v60, v54
v_mul_f32 v59, v59, v55
v_fmac_f32 v60, v59, v58
v_add_f32 v229, v229, v60
v_lshlrev_b32 v60, 16, v43
v_lshlrev_b32 v58, 16, v49
v_add_f32 v59, v60, v54
v_mul_f32 v59, v59, v55
v_fmac_f32 v60, v59, v58
v_add_f32 v230, v230, v60
v_and_b32 v60, 0xffff0000, v43
v_and_b32 v58, 0xffff0000, v49
v_add_f32 v59, v60, v54
v_mul_f32 v59, v59, v55
v_fmac_f32 v60, v59, v58
v_add_f32 v231, v231, v60
s_lshl_b32 s65, 1, s68
s_andn2_b32 s65, 0x1f, s65
s_mov_b32 s63, 20000
s_waitcnt vmcnt(0)
LOeA_%=:
v_cmp_ne_u32 vcc, s43, v16
s_nop 3
s_and_b32 vcc_lo, vcc_lo, s65
s_cmp_eq_u32 vcc_lo, 0
s_cbranch_scc1 LOeB_%=
s_sub_u32 s63, s63, 1
s_cmp_eq_u32 s63, 0
s_cbranch_scc1 LOeB_%=
s_sleep 2
buffer_load_dword v16, v19, s[96:99], s67 offen sc1
s_waitcnt vmcnt(0)
s_branch LOeA_%=
LOeB_%=:
buffer_load_dwordx4 v[104:107], v7, s[96:99], s88 offen offset:0 sc1
buffer_load_dwordx4 v[108:111], v7, s[96:99], s88 offen offset:1024 sc1
buffer_load_dwordx4 v[112:115], v7, s[96:99], s89 offen offset:0 sc1
buffer_load_dwordx4 v[116:119], v7, s[96:99], s89 offen offset:1024 sc1
buffer_load_dwordx4 v[120:123], v7, s[96:99], s90 offen offset:0 sc1
buffer_load_dwordx4 v[124:127], v7, s[96:99], s90 offen offset:1024 sc1
buffer_load_dwordx4 v[136:139], v7, s[96:99], s91 offen offset:0 sc1
buffer_load_dwordx4 v[140:143], v7, s[96:99], s91 offen offset:1024 sc1
buffer_load_dwordx4 v[160:163], v7, s[96:99], s88 offen offset:2048 sc1
buffer_load_dwordx4 v[164:167], v7, s[96:99], s88 offen offset:3072 sc1
buffer_load_dwordx4 v[168:171], v7, s[96:99], s89 offen offset:2048 sc1
buffer_load_dwordx4 v[172:175], v7, s[96:99], s89 offen offset:3072 sc1
buffer_load_dwordx4 v[176:179], v7, s[96:99], s90 offen offset:2048 sc1
buffer_load_dwordx4 v[180:183], v7, s[96:99], s90 offen offset:3072 sc1
buffer_load_dwordx4 v[184:187], v7, s[96:99], s91 offen offset:2048 sc1
buffer_load_dwordx4 v[188:191], v7, s[96:99], s91 offen offset:3072 sc1
s_waitcnt vmcnt(15)
v_lshlrev_b32 v64, 16, v104
v_and_b32 v65, 0xffff0000, v104
v_lshlrev_b32 v66, 16, v105
v_and_b32 v67, 0xffff0000, v105
v_pk_add_f32 v[96:97], v[96:97], v[64:65]
v_pk_add_f32 v[98:99], v[98:99], v[66:67]
v_lshlrev_b32 v64, 16, v106
v_and_b32 v65, 0xffff0000, v106
v_lshlrev_b32 v66, 16, v107
v_and_b32 v67, 0xffff0000, v107
v_pk_add_f32 v[100:101], v[100:101], v[64:65]
v_pk_add_f32 v[102:103], v[102:103], v[66:67]
s_waitcnt vmcnt(14)
v_lshlrev_b32 v64, 16, v108
v_and_b32 v65, 0xffff0000, v108
v_lshlrev_b32 v66, 16, v109
v_and_b32 v67, 0xffff0000, v109
v_pk_add_f32 v[128:129], v[128:129], v[64:65]
v_pk_add_f32 v[130:131], v[130:131], v[66:67]
v_lshlrev_b32 v64, 16, v110
v_and_b32 v65, 0xffff0000, v110
v_lshlrev_b32 v66, 16, v111
v_and_b32 v67, 0xffff0000, v111
v_pk_add_f32 v[132:133], v[132:133], v[64:65]
v_pk_add_f32 v[134:135], v[134:135], v[66:67]
s_waitcnt vmcnt(13)
v_lshlrev_b32 v64, 16, v112
v_and_b32 v65, 0xffff0000, v112
v_lshlrev_b32 v66, 16, v113
v_and_b32 v67, 0xffff0000, v113
v_pk_add_f32 v[96:97], v[96:97], v[64:65]
v_pk_add_f32 v[98:99], v[98:99], v[66:67]
v_lshlrev_b32 v64, 16, v114
v_and_b32 v65, 0xffff0000, v114
v_lshlrev_b32 v66, 16, v115
v_and_b32 v67, 0xffff0000, v115
v_pk_add_f32 v[100:101], v[100:101], v[64:65]
v_pk_add_f32 v[102:103], v[102:103], v[66:67]
s_waitcnt vmcnt(12)
v_lshlrev_b32 v64, 16, v116
v_and_b32 v65, 0xffff0000, v116
v_lshlrev_b32 v66, 16, v117
v_and_b32 v67, 0xffff0000, v117
)ASM"
R"ASM(
v_pk_add_f32 v[128:129], v[128:129], v[64:65]
v_pk_add_f32 v[130:131], v[130:131], v[66:67]
v_lshlrev_b32 v64, 16, v118
v_and_b32 v65, 0xffff0000, v118
v_lshlrev_b32 v66, 16, v119
v_and_b32 v67, 0xffff0000, v119
v_pk_add_f32 v[132:133], v[132:133], v[64:65]
v_pk_add_f32 v[134:135], v[134:135], v[66:67]
s_waitcnt vmcnt(11)
v_lshlrev_b32 v64, 16, v120
v_and_b32 v65, 0xffff0000, v120
v_lshlrev_b32 v66, 16, v121
v_and_b32 v67, 0xffff0000, v121
v_pk_add_f32 v[96:97], v[96:97], v[64:65]
v_pk_add_f32 v[98:99], v[98:99], v[66:67]
v_lshlrev_b32 v64, 16, v122
v_and_b32 v65, 0xffff0000, v122
v_lshlrev_b32 v66, 16, v123
v_and_b32 v67, 0xffff0000, v123
v_pk_add_f32 v[100:101], v[100:101], v[64:65]
v_pk_add_f32 v[102:103], v[102:103], v[66:67]
s_waitcnt vmcnt(10)
v_lshlrev_b32 v64, 16, v124
v_and_b32 v65, 0xffff0000, v124
v_lshlrev_b32 v66, 16, v125
v_and_b32 v67, 0xffff0000, v125
v_pk_add_f32 v[128:129], v[128:129], v[64:65]
v_pk_add_f32 v[130:131], v[130:131], v[66:67]
v_lshlrev_b32 v64, 16, v126
v_and_b32 v65, 0xffff0000, v126
v_lshlrev_b32 v66, 16, v127
v_and_b32 v67, 0xffff0000, v127
v_pk_add_f32 v[132:133], v[132:133], v[64:65]
v_pk_add_f32 v[134:135], v[134:135], v[66:67]
s_waitcnt vmcnt(9)
v_lshlrev_b32 v64, 16, v136
v_and_b32 v65, 0xffff0000, v136
v_lshlrev_b32 v66, 16, v137
v_and_b32 v67, 0xffff0000, v137
v_pk_add_f32 v[96:97], v[96:97], v[64:65]
v_pk_add_f32 v[98:99], v[98:99], v[66:67]
v_lshlrev_b32 v64, 16, v138
v_and_b32 v65, 0xffff0000, v138
v_lshlrev_b32 v66, 16, v139
v_and_b32 v67, 0xffff0000, v139
v_pk_add_f32 v[100:101], v[100:101], v[64:65]
v_pk_add_f32 v[102:103], v[102:103], v[66:67]
s_waitcnt vmcnt(8)
v_lshlrev_b32 v64, 16, v140
v_and_b32 v65, 0xffff0000, v140
v_lshlrev_b32 v66, 16, v141
v_and_b32 v67, 0xffff0000, v141
v_pk_add_f32 v[128:129], v[128:129], v[64:65]
v_pk_add_f32 v[130:131], v[130:131], v[66:67]
v_lshlrev_b32 v64, 16, v142
v_and_b32 v65, 0xffff0000, v142
v_lshlrev_b32 v66, 16, v143
v_and_b32 v67, 0xffff0000, v143
v_pk_add_f32 v[132:133], v[132:133], v[64:65]
v_pk_add_f32 v[134:135], v[134:135], v[66:67]
buffer_store_dwordx4 v[96:99], v11, s[56:59], s72 offen offset:0 sc1
buffer_store_dwordx4 v[100:103], v11, s[56:59], s74 offen offset:0 sc1
buffer_store_dwordx4 v[128:131], v11, s[56:59], s72 offen offset:256 sc1
buffer_store_dwordx4 v[132:135], v11, s[56:59], s74 offen offset:256 sc1
s_mov_b32 s62, 20000
s_waitcnt vmcnt(4)
s_branch LOK_%=
LOL_%=:
s_sleep 8
buffer_load_dwordx4 v[160:163], v7, s[96:99], s88 offen offset:2048 sc1
buffer_load_dwordx4 v[164:167], v7, s[96:99], s88 offen offset:3072 sc1
buffer_load_dwordx4 v[168:171], v7, s[96:99], s89 offen offset:2048 sc1
buffer_load_dwordx4 v[172:175], v7, s[96:99], s89 offen offset:3072 sc1
buffer_load_dwordx4 v[176:179], v7, s[96:99], s90 offen offset:2048 sc1
buffer_load_dwordx4 v[180:183], v7, s[96:99], s90 offen offset:3072 sc1
buffer_load_dwordx4 v[184:187], v7, s[96:99], s91 offen offset:2048 sc1
buffer_load_dwordx4 v[188:191], v7, s[96:99], s91 offen offset:3072 sc1
s_waitcnt vmcnt(0)
LOK_%=:
v_xor_b32 v64, v161, v219
v_xor_b32 v65, v163, v219
v_or_b32 v14, v64, v65
v_xor_b32 v64, v165, v219
v_xor_b32 v65, v167, v219
v_or3_b32 v14, v14, v64, v65
v_xor_b32 v64, v169, v219
v_xor_b32 v65, v171, v219
v_or3_b32 v14, v14, v64, v65
v_xor_b32 v64, v173, v219
v_xor_b32 v65, v175, v219
v_or3_b32 v14, v14, v64, v65
v_xor_b32 v64, v177, v219
v_xor_b32 v65, v179, v219
v_or3_b32 v14, v14, v64, v65
v_xor_b32 v64, v181, v219
v_xor_b32 v65, v183, v219
v_or3_b32 v14, v14, v64, v65
v_xor_b32 v64, v185, v219
v_xor_b32 v65, v187, v219
v_or3_b32 v14, v14, v64, v65
v_xor_b32 v64, v189, v219
v_xor_b32 v65, v191, v219
v_or3_b32 v14, v14, v64, v65
v_cmp_ne_u32 vcc, 0, v14
s_sub_u32 s62, s62, 1
s_cmp_eq_u32 s62, 0
s_cbranch_scc1 LOM_%=
s_nop 0
s_cmp_lg_u64 vcc, 0
s_cbranch_scc1 LOL_%=
LOM_%=:
v_lshlrev_b32 v64, 16, v160
v_and_b32 v65, 0xffff0000, v160
v_lshlrev_b32 v66, 16, v162
v_and_b32 v67, 0xffff0000, v162
v_pk_add_f32 v[224:225], v[224:225], v[64:65]
v_pk_add_f32 v[226:227], v[226:227], v[66:67]
v_lshlrev_b32 v64, 16, v164
v_and_b32 v65, 0xffff0000, v164
v_lshlrev_b32 v66, 16, v166
v_and_b32 v67, 0xffff0000, v166
v_pk_add_f32 v[228:229], v[228:229], v[64:65]
v_pk_add_f32 v[230:231], v[230:231], v[66:67]
v_lshlrev_b32 v64, 16, v168
v_and_b32 v65, 0xffff0000, v168
v_lshlrev_b32 v66, 16, v170
v_and_b32 v67, 0xffff0000, v170
v_pk_add_f32 v[224:225], v[224:225], v[64:65]
v_pk_add_f32 v[226:227], v[226:227], v[66:67]
v_lshlrev_b32 v64, 16, v172
v_and_b32 v65, 0xffff0000, v172
v_lshlrev_b32 v66, 16, v174
v_and_b32 v67, 0xffff0000, v174
v_pk_add_f32 v[228:229], v[228:229], v[64:65]
v_pk_add_f32 v[230:231], v[230:231], v[66:67]
v_lshlrev_b32 v64, 16, v176
v_and_b32 v65, 0xffff0000, v176
v_lshlrev_b32 v66, 16, v178
v_and_b32 v67, 0xffff0000, v178
v_pk_add_f32 v[224:225], v[224:225], v[64:65]
v_pk_add_f32 v[226:227], v[226:227], v[66:67]
v_lshlrev_b32 v64, 16, v180
v_and_b32 v65, 0xffff0000, v180
v_lshlrev_b32 v66, 16, v182
v_and_b32 v67, 0xffff0000, v182
v_pk_add_f32 v[228:229], v[228:229], v[64:65]
v_pk_add_f32 v[230:231], v[230:231], v[66:67]
v_lshlrev_b32 v64, 16, v184
v_and_b32 v65, 0xffff0000, v184
v_lshlrev_b32 v66, 16, v186
v_and_b32 v67, 0xffff0000, v186
v_pk_add_f32 v[224:225], v[224:225], v[64:65]
v_pk_add_f32 v[226:227], v[226:227], v[66:67]
v_lshlrev_b32 v64, 16, v188
v_and_b32 v65, 0xffff0000, v188
v_lshlrev_b32 v66, 16, v190
v_and_b32 v67, 0xffff0000, v190
v_pk_add_f32 v[228:229], v[228:229], v[64:65]
v_pk_add_f32 v[230:231], v[230:231], v[66:67]
buffer_store_dwordx4 v[224:227], v11, s[56:59], s72 offen offset:512 sc1
buffer_store_dwordx4 v[228:231], v11, s[56:59], s74 offen offset:512 sc1
s_branch LEND_%=
LHP_%=:
s_waitcnt lgkmcnt(10)
v_mfma_f32_16x16x32_bf16 v[96:99], v[160:163], a[0:3], 0
ds_read_b64_tr_b16 v[184:185], v199 offset:8192
s_waitcnt lgkmcnt(9)
v_mfma_f32_16x16x32_bf16 v[96:99], v[164:167], a[8:11], v[96:99]
ds_read_b64_tr_b16 v[186:187], v199 offset:8704
v_mfma_f32_16x16x32_bf16 v[100:103], v[160:163], a[4:7], 0
ds_read_b64_tr_b16 v[188:189], v199 offset:16384
s_add_u32 m0, s61, 24576
v_mfma_f32_16x16x32_bf16 v[100:103], v[164:167], a[12:15], v[100:103]
ds_read_b64_tr_b16 v[190:191], v199 offset:16896
buffer_load_dwordx4 v5, s[44:47], s12 offen offset:0 lds
s_waitcnt lgkmcnt(10)
v_mfma_f32_16x16x32_bf16 v[104:107], v[168:171], a[0:3], 0
ds_read_b64_tr_b16 v[160:161], v196 offset:9216
v_and_b32 v13, 15, v1
s_waitcnt lgkmcnt(9)
v_mfma_f32_16x16x32_bf16 v[104:107], v[172:175], a[8:11], v[104:107]
ds_read_b64_tr_b16 v[162:163], v196 offset:9728
v_lshrrev_b32 v14, 4, v1
v_mfma_f32_16x16x32_bf16 v[108:111], v[168:171], a[4:7], 0
ds_read_b64_tr_b16 v[164:165], v196 offset:17408
v_lshrrev_b32 v18, 1, v13
v_mfma_f32_16x16x32_bf16 v[108:111], v[172:175], a[12:15], v[108:111]
s_waitcnt lgkmcnt(3)
ds_read_b64_tr_b16 v[166:167], v196 offset:17920
v_mfma_f32_16x16x32_bf16 v[112:115], v[176:179], a[0:3], 0
ds_read_b64_tr_b16 v[168:169], v197 offset:9216
v_xor_b32 v14, v14, v18
v_mfma_f32_16x16x32_bf16 v[112:115], v[180:183], a[8:11], v[112:115]
ds_read_b64_tr_b16 v[170:171], v197 offset:9728
v_lshlrev_b32 v13, 7, v13
v_mfma_f32_16x16x32_bf16 v[116:119], v[176:179], a[4:7], 0
ds_read_b64_tr_b16 v[172:173], v197 offset:17408
v_lshl_add_u32 v216, v14, 4, v13
v_mfma_f32_16x16x32_bf16 v[116:119], v[180:183], a[12:15], v[116:119]
ds_read_b64_tr_b16 v[174:175], v197 offset:17920
)ASM"
R"ASM(
v_xor_b32 v217, 64, v216
v_mfma_f32_16x16x32_bf16 v[120:123], v[184:187], a[0:3], 0
ds_read_b64_tr_b16 v[176:177], v198 offset:9216
v_add_u32 v220, 49152, v216
v_mfma_f32_16x16x32_bf16 v[120:123], v[188:191], a[8:11], v[120:123]
ds_read_b64_tr_b16 v[178:179], v198 offset:9728
v_add_u32 v221, 49152, v217
v_mfma_f32_16x16x32_bf16 v[124:127], v[184:187], a[4:7], 0
ds_read_b64_tr_b16 v[180:181], v198 offset:17408
v_add_u32 v222, 98304, v216
v_mfma_f32_16x16x32_bf16 v[124:127], v[188:191], a[12:15], v[124:127]
s_waitcnt lgkmcnt(3)
ds_read_b64_tr_b16 v[182:183], v198 offset:17920
v_mfma_f32_16x16x32_bf16 v[128:131], v[160:163], a[0:3], 0
ds_read_b64_tr_b16 v[184:185], v199 offset:9216
v_add_u32 v223, 98304, v217
v_mfma_f32_16x16x32_bf16 v[128:131], v[164:167], a[8:11], v[128:131]
ds_read_b64_tr_b16 v[186:187], v199 offset:9728
v_lshrrev_b32 v13, 3, v1
v_mfma_f32_16x16x32_bf16 v[132:135], v[160:163], a[4:7], 0
ds_read_b64_tr_b16 v[188:189], v199 offset:17408
v_and_b32 v18, 7, v1
v_mfma_f32_16x16x32_bf16 v[132:135], v[164:167], a[12:15], v[132:135]
ds_read_b64_tr_b16 v[190:191], v199 offset:17920
v_lshrrev_b32 v14, 1, v13
v_mfma_f32_16x16x32_bf16 v[136:139], v[168:171], a[0:3], 0
ds_read_b64_tr_b16 v[160:161], v196 offset:24576
v_xor_b32 v14, v14, v18
v_mfma_f32_16x16x32_bf16 v[136:139], v[172:175], a[8:11], v[136:139]
ds_read_b64_tr_b16 v[162:163], v196 offset:25088
s_add_u32 s64, s40, s68
v_mfma_f32_16x16x32_bf16 v[140:143], v[168:171], a[4:7], 0
ds_read_b64_tr_b16 v[164:165], v196 offset:25600
s_and_b32 s64, s64, 3
v_mfma_f32_16x16x32_bf16 v[140:143], v[172:175], a[12:15], v[140:143]
s_waitcnt lgkmcnt(3)
ds_read_b64_tr_b16 v[166:167], v196 offset:26112
v_mfma_f32_16x16x32_bf16 v[144:147], v[176:179], a[0:3], 0
ds_read_b64_tr_b16 v[168:169], v197 offset:24576
s_lshl_b32 s64, s64, 4
v_mfma_f32_16x16x32_bf16 v[144:147], v[180:183], a[8:11], v[144:147]
ds_read_b64_tr_b16 v[170:171], v197 offset:25088
v_add_u32 v13, s64, v13
v_mfma_f32_16x16x32_bf16 v[148:151], v[176:179], a[4:7], 0
buffer_load_dword v17, v15, s[96:99], 0 offen sc1
ds_read_b64_tr_b16 v[172:173], v197 offset:25600
v_mfma_f32_16x16x32_bf16 v[148:151], v[180:183], a[12:15], v[148:151]
ds_read_b64_tr_b16 v[174:175], v197 offset:26112
v_mul_u32_u24 v13, 1152, v13
v_mfma_f32_16x16x32_bf16 v[152:155], v[184:187], a[0:3], 0
ds_read_b64_tr_b16 v[176:177], v198 offset:24576
v_lshl_add_u32 v13, v14, 4, v13
v_mfma_f32_16x16x32_bf16 v[152:155], v[188:191], a[8:11], v[152:155]
ds_read_b64_tr_b16 v[178:179], v198 offset:25088
v_add_u32 v23, 2048, v13
v_mfma_f32_16x16x32_bf16 v[156:159], v[184:187], a[4:7], 0
ds_read_b64_tr_b16 v[180:181], v198 offset:25600
v_xor_b32 v13, 64, v13
v_mfma_f32_16x16x32_bf16 v[156:159], v[188:191], a[12:15], v[156:159]
s_waitcnt lgkmcnt(3)
ds_read_b64_tr_b16 v[182:183], v198 offset:26112
v_mfma_f32_16x16x32_bf16 v[224:227], v[160:163], a[0:3], 0
ds_read_b64_tr_b16 v[184:185], v199 offset:24576
v_add_u32 v6, 10240, v13
v_mfma_f32_16x16x32_bf16 v[224:227], v[164:167], a[8:11], v[224:227]
ds_read_b64_tr_b16 v[186:187], v199 offset:25088
v_lshlrev_b32 v7, 4, v1
v_mfma_f32_16x16x32_bf16 v[228:231], v[160:163], a[4:7], 0
ds_read_b64_tr_b16 v[188:189], v199 offset:25600
buffer_load_dwordx4 v27, s[44:47], s13 offen offset:1024 lds
v_mfma_f32_16x16x32_bf16 v[228:231], v[164:167], a[12:15], v[228:231]
ds_read_b64_tr_b16 v[190:191], v199 offset:26112
s_add_u32 m0, s61, 32768
v_mfma_f32_16x16x32_bf16 v[232:235], v[168:171], a[0:3], 0
ds_read_b64_tr_b16 v[160:161], v196 offset:32768
v_mfma_f32_16x16x32_bf16 v[232:235], v[172:175], a[8:11], v[232:235]
ds_read_b64_tr_b16 v[162:163], v196 offset:33280
v_mfma_f32_16x16x32_bf16 v[236:239], v[168:171], a[4:7], 0
ds_read_b64_tr_b16 v[164:165], v196 offset:40960
buffer_load_dwordx4 v3, s[44:47], s14 offen offset:0 lds
v_mfma_f32_16x16x32_bf16 v[236:239], v[172:175], a[12:15], v[236:239]
s_waitcnt lgkmcnt(3)
ds_read_b64_tr_b16 v[166:167], v196 offset:41472
v_mfma_f32_16x16x32_bf16 v[240:243], v[176:179], a[0:3], 0
ds_read_b64_tr_b16 v[168:169], v197 offset:32768
v_mfma_f32_16x16x32_bf16 v[240:243], v[180:183], a[8:11], v[240:243]
ds_read_b64_tr_b16 v[170:171], v197 offset:33280
v_mfma_f32_16x16x32_bf16 v[244:247], v[176:179], a[4:7], 0
ds_read_b64_tr_b16 v[172:173], v197 offset:40960
buffer_load_dwordx4 v4, s[44:47], s14 offen offset:1024 lds
v_mfma_f32_16x16x32_bf16 v[244:247], v[180:183], a[12:15], v[244:247]
ds_read_b64_tr_b16 v[174:175], v197 offset:41472
v_mfma_f32_16x16x32_bf16 v[248:251], v[184:187], a[0:3], 0
ds_read_b64_tr_b16 v[176:177], v198 offset:32768
s_add_u32 m0, s61, 40960
v_mfma_f32_16x16x32_bf16 v[248:251], v[188:191], a[8:11], v[248:251]
ds_read_b64_tr_b16 v[178:179], v198 offset:33280
v_mfma_f32_16x16x32_bf16 v[252:255], v[184:187], a[4:7], 0
ds_read_b64_tr_b16 v[180:181], v198 offset:40960
buffer_load_dwordx4 v3, s[44:47], s15 offen offset:0 lds
v_mfma_f32_16x16x32_bf16 v[252:255], v[188:191], a[12:15], v[252:255]
s_waitcnt lgkmcnt(3)
ds_read_b64_tr_b16 v[182:183], v198 offset:41472
v_mfma_f32_16x16x32_bf16 v[96:99], v[160:163], a[16:19], v[96:99]
ds_read_b64_tr_b16 v[184:185], v199 offset:32768
v_mfma_f32_16x16x32_bf16 v[96:99], v[164:167], a[24:27], v[96:99]
ds_read_b64_tr_b16 v[186:187], v199 offset:33280
v_mfma_f32_16x16x32_bf16 v[100:103], v[160:163], a[20:23], v[100:103]
ds_read_b64_tr_b16 v[188:189], v199 offset:40960
buffer_load_dwordx4 v4, s[44:47], s15 offen offset:1024 lds
v_mfma_f32_16x16x32_bf16 v[100:103], v[164:167], a[28:31], v[100:103]
ds_read_b64_tr_b16 v[190:191], v199 offset:41472
v_mfma_f32_16x16x32_bf16 v[104:107], v[168:171], a[16:19], v[104:107]
s_waitcnt lgkmcnt(0)
s_barrier
v_mfma_f32_16x16x32_bf16 v[104:107], v[172:175], a[24:27], v[104:107]
ds_read_b64_tr_b16 v[160:161], v196 offset:33792
v_mfma_f32_16x16x32_bf16 v[108:111], v[168:171], a[20:23], v[108:111]
ds_read_b64_tr_b16 v[162:163], v196 offset:34304
s_add_u32 m0, s61, 49152
v_mfma_f32_16x16x32_bf16 v[108:111], v[172:175], a[28:31], v[108:111]
ds_read_b64_tr_b16 v[164:165], v196 offset:41984
ds_read_b64_tr_b16 v[166:167], v196 offset:42496
v_mfma_f32_16x16x32_bf16 v[112:115], v[176:179], a[16:19], v[112:115]
ds_read_b64_tr_b16 v[168:169], v197 offset:33792
buffer_load_dwordx4 v5, s[44:47], s14 offen offset:0 lds
v_mfma_f32_16x16x32_bf16 v[112:115], v[180:183], a[24:27], v[112:115]
ds_read_b64_tr_b16 v[170:171], v197 offset:34304
v_mfma_f32_16x16x32_bf16 v[116:119], v[176:179], a[20:23], v[116:119]
ds_read_b64_tr_b16 v[172:173], v197 offset:41984
v_mfma_f32_16x16x32_bf16 v[116:119], v[180:183], a[28:31], v[116:119]
ds_read_b64_tr_b16 v[174:175], v197 offset:42496
v_mfma_f32_16x16x32_bf16 v[120:123], v[184:187], a[16:19], v[120:123]
ds_read_b64_tr_b16 v[176:177], v198 offset:33792
buffer_load_dwordx4 v27, s[44:47], s15 offen offset:1024 lds
v_mfma_f32_16x16x32_bf16 v[120:123], v[188:191], a[24:27], v[120:123]
ds_read_b64_tr_b16 v[178:179], v198 offset:34304
v_mfma_f32_16x16x32_bf16 v[124:127], v[184:187], a[20:23], v[124:127]
ds_read_b64_tr_b16 v[180:181], v198 offset:41984
v_mfma_f32_16x16x32_bf16 v[124:127], v[188:191], a[28:31], v[124:127]
s_waitcnt lgkmcnt(3)
ds_read_b64_tr_b16 v[182:183], v198 offset:42496
v_mfma_f32_16x16x32_bf16 v[128:131], v[160:163], a[16:19], v[128:131]
ds_read_b64_tr_b16 v[184:185], v199 offset:33792
s_add_u32 m0, s61, 57344
v_mfma_f32_16x16x32_bf16 v[128:131], v[164:167], a[24:27], v[128:131]
ds_read_b64_tr_b16 v[186:187], v199 offset:34304
buffer_load_dwordx4 v3, s[44:47], s16 offen offset:0 lds
v_mfma_f32_16x16x32_bf16 v[132:135], v[160:163], a[20:23], v[132:135]
ds_read_b64_tr_b16 v[188:189], v199 offset:41984
v_mfma_f32_16x16x32_bf16 v[132:135], v[164:167], a[28:31], v[132:135]
ds_read_b64_tr_b16 v[190:191], v199 offset:42496
v_mfma_f32_16x16x32_bf16 v[136:139], v[168:171], a[16:19], v[136:139]
ds_read_b64_tr_b16 v[160:161], v28 offset:0
v_mfma_f32_16x16x32_bf16 v[136:139], v[172:175], a[24:27], v[136:139]
ds_read_b64_tr_b16 v[162:163], v28 offset:512
buffer_load_dwordx4 v4, s[44:47], s16 offen offset:1024 lds
v_mfma_f32_16x16x32_bf16 v[140:143], v[168:171], a[20:23], v[140:143]
ds_read_b64_tr_b16 v[164:165], v28 offset:1024
v_mfma_f32_16x16x32_bf16 v[140:143], v[172:175], a[28:31], v[140:143]
s_waitcnt lgkmcnt(3)
ds_read_b64_tr_b16 v[166:167], v28 offset:1536
v_mfma_f32_16x16x32_bf16 v[144:147], v[176:179], a[16:19], v[144:147]
ds_read_b64_tr_b16 v[168:169], v29 offset:0
v_mfma_f32_16x16x32_bf16 v[144:147], v[180:183], a[24:27], v[144:147]
ds_read_b64_tr_b16 v[170:171], v29 offset:512
v_mfma_f32_16x16x32_bf16 v[148:151], v[176:179], a[20:23], v[148:151]
ds_read_b64_tr_b16 v[172:173], v29 offset:1024
s_add_u32 m0, s61, 65536
v_mfma_f32_16x16x32_bf16 v[148:151], v[180:183], a[28:31], v[148:151]
ds_read_b64_tr_b16 v[174:175], v29 offset:1536
buffer_load_dwordx4 v3, s[44:47], s17 offen offset:0 lds
v_mfma_f32_16x16x32_bf16 v[152:155], v[184:187], a[16:19], v[152:155]
ds_read_b64_tr_b16 v[176:177], v30 offset:0
v_mfma_f32_16x16x32_bf16 v[152:155], v[188:191], a[24:27], v[152:155]
ds_read_b64_tr_b16 v[178:179], v30 offset:512
v_mfma_f32_16x16x32_bf16 v[156:159], v[184:187], a[20:23], v[156:159]
ds_read_b64_tr_b16 v[180:181], v30 offset:1024
v_mfma_f32_16x16x32_bf16 v[156:159], v[188:191], a[28:31], v[156:159]
s_waitcnt lgkmcnt(3)
ds_read_b64_tr_b16 v[182:183], v30 offset:1536
v_mfma_f32_16x16x32_bf16 v[224:227], v[160:163], a[16:19], v[224:227]
ds_read_b64_tr_b16 v[184:185], v31 offset:0
buffer_load_dwordx4 v4, s[44:47], s17 offen offset:1024 lds
v_mfma_f32_16x16x32_bf16 v[224:227], v[164:167], a[24:27], v[224:227]
ds_read_b64_tr_b16 v[186:187], v31 offset:512
v_mfma_f32_16x16x32_bf16 v[228:231], v[160:163], a[20:23], v[228:231]
ds_read_b64_tr_b16 v[188:189], v31 offset:1024
v_mfma_f32_16x16x32_bf16 v[228:231], v[164:167], a[28:31], v[228:231]
ds_read_b64_tr_b16 v[190:191], v31 offset:1536
v_mfma_f32_16x16x32_bf16 v[232:235], v[168:171], a[16:19], v[232:235]
ds_read_b64_tr_b16 v[160:161], v28 offset:8192
v_mfma_f32_16x16x32_bf16 v[232:235], v[172:175], a[24:27], v[232:235]
ds_read_b64_tr_b16 v[162:163], v28 offset:8704
v_mfma_f32_16x16x32_bf16 v[236:239], v[168:171], a[20:23], v[236:239]
ds_read_b64_tr_b16 v[164:165], v28 offset:16384
v_mfma_f32_16x16x32_bf16 v[236:239], v[172:175], a[28:31], v[236:239]
s_waitcnt lgkmcnt(3)
ds_read_b64_tr_b16 v[166:167], v28 offset:16896
v_mfma_f32_16x16x32_bf16 v[240:243], v[176:179], a[16:19], v[240:243]
)ASM"
R"ASM(
ds_read_b64_tr_b16 v[168:169], v29 offset:8192
s_add_u32 m0, s61, 73728
v_mfma_f32_16x16x32_bf16 v[240:243], v[180:183], a[24:27], v[240:243]
ds_read_b64_tr_b16 v[170:171], v29 offset:8704
buffer_load_dwordx4 v5, s[44:47], s16 offen offset:0 lds
v_mfma_f32_16x16x32_bf16 v[244:247], v[176:179], a[20:23], v[244:247]
ds_read_b64_tr_b16 v[172:173], v29 offset:16384
v_mfma_f32_16x16x32_bf16 v[244:247], v[180:183], a[28:31], v[244:247]
ds_read_b64_tr_b16 v[174:175], v29 offset:16896
v_mfma_f32_16x16x32_bf16 v[248:251], v[184:187], a[16:19], v[248:251]
ds_read_b64_tr_b16 v[176:177], v30 offset:8192
v_mfma_f32_16x16x32_bf16 v[248:251], v[188:191], a[24:27], v[248:251]
ds_read_b64_tr_b16 v[178:179], v30 offset:8704
buffer_load_dwordx4 v27, s[44:47], s17 offen offset:1024 lds
v_mfma_f32_16x16x32_bf16 v[252:255], v[184:187], a[20:23], v[252:255]
ds_read_b64_tr_b16 v[180:181], v30 offset:16384
v_mfma_f32_16x16x32_bf16 v[252:255], v[188:191], a[28:31], v[252:255]
s_waitcnt lgkmcnt(3)
ds_read_b64_tr_b16 v[182:183], v30 offset:16896
v_mfma_f32_16x16x32_bf16 v[96:99], v[160:163], a[32:35], v[96:99]
ds_read_b64_tr_b16 v[184:185], v31 offset:8192
v_mfma_f32_16x16x32_bf16 v[96:99], v[164:167], a[40:43], v[96:99]
ds_read_b64_tr_b16 v[186:187], v31 offset:8704
v_mfma_f32_16x16x32_bf16 v[100:103], v[160:163], a[36:39], v[100:103]
ds_read_b64_tr_b16 v[188:189], v31 offset:16384
v_mfma_f32_16x16x32_bf16 v[100:103], v[164:167], a[44:47], v[100:103]
ds_read_b64_tr_b16 v[190:191], v31 offset:16896
v_mfma_f32_16x16x32_bf16 v[104:107], v[168:171], a[32:35], v[104:107]
ds_read_b64_tr_b16 v[160:161], v28 offset:9216
v_mfma_f32_16x16x32_bf16 v[104:107], v[172:175], a[40:43], v[104:107]
ds_read_b64_tr_b16 v[162:163], v28 offset:9728
v_mfma_f32_16x16x32_bf16 v[108:111], v[168:171], a[36:39], v[108:111]
ds_read_b64_tr_b16 v[164:165], v28 offset:17408
v_mfma_f32_16x16x32_bf16 v[108:111], v[172:175], a[44:47], v[108:111]
s_waitcnt lgkmcnt(3)
ds_read_b64_tr_b16 v[166:167], v28 offset:17920
v_mfma_f32_16x16x32_bf16 v[112:115], v[176:179], a[32:35], v[112:115]
ds_read_b64_tr_b16 v[168:169], v29 offset:9216
v_mfma_f32_16x16x32_bf16 v[112:115], v[180:183], a[40:43], v[112:115]
ds_read_b64_tr_b16 v[170:171], v29 offset:9728
v_mfma_f32_16x16x32_bf16 v[116:119], v[176:179], a[36:39], v[116:119]
ds_read_b64_tr_b16 v[172:173], v29 offset:17408
v_mfma_f32_16x16x32_bf16 v[116:119], v[180:183], a[44:47], v[116:119]
ds_read_b64_tr_b16 v[174:175], v29 offset:17920
v_mfma_f32_16x16x32_bf16 v[120:123], v[184:187], a[32:35], v[120:123]
ds_read_b64_tr_b16 v[176:177], v30 offset:9216
v_mfma_f32_16x16x32_bf16 v[120:123], v[188:191], a[40:43], v[120:123]
ds_read_b64_tr_b16 v[178:179], v30 offset:9728
v_mfma_f32_16x16x32_bf16 v[124:127], v[184:187], a[36:39], v[124:127]
ds_read_b64_tr_b16 v[180:181], v30 offset:17408
v_mfma_f32_16x16x32_bf16 v[124:127], v[188:191], a[44:47], v[124:127]
s_waitcnt lgkmcnt(3)
ds_read_b64_tr_b16 v[182:183], v30 offset:17920
v_mfma_f32_16x16x32_bf16 v[128:131], v[160:163], a[32:35], v[128:131]
ds_read_b64_tr_b16 v[184:185], v31 offset:9216
v_mfma_f32_16x16x32_bf16 v[128:131], v[164:167], a[40:43], v[128:131]
ds_read_b64_tr_b16 v[186:187], v31 offset:9728
v_mfma_f32_16x16x32_bf16 v[132:135], v[160:163], a[36:39], v[132:135]
ds_read_b64_tr_b16 v[188:189], v31 offset:17408
v_mfma_f32_16x16x32_bf16 v[132:135], v[164:167], a[44:47], v[132:135]
ds_read_b64_tr_b16 v[190:191], v31 offset:17920
v_mfma_f32_16x16x32_bf16 v[136:139], v[168:171], a[32:35], v[136:139]
s_waitcnt vmcnt(13) lgkmcnt(0)
s_barrier
s_mov_b64 exec, 1
buffer_store_dword v219, off, s[96:99], s41 sc1
s_mov_b64 exec, -1
v_readlane_b32 s72, v0, 42
v_readlane_b32 s73, v0, 54
s_nop 2
s_lshl_b32 s72, s72, 4
s_lshl_b32 s73, s73, 5
s_add_u32 s72, s72, s73
s_add_u32 s72, s72, 49618944
s_cmp_eq_u32 s42, 0
s_cselect_b32 s72, 1073741824, s72
s_mov_b64 exec, 1
buffer_store_dword v219, off, s[96:99], s72 sc1
s_mov_b64 exec, -1
s_mov_b32 s65, 20000
LHC5_%=:
v_cmp_ne_u32 s[92:93], s43, v17
s_nop 3
s_and_b32 s92, s92, 0x3fc000
s_and_b32 s93, s93, 0x0
s_cmp_eq_u64 s[92:93], 0
s_cbranch_scc1 LHD5_%=
s_sub_u32 s65, s65, 1
s_cmp_eq_u32 s65, 0
s_cbranch_scc1 LHD5_%=
s_sleep 2
buffer_load_dword v17, v15, s[96:99], 0 offen sc1
s_waitcnt vmcnt(0)
s_branch LHC5_%=
LHD5_%=:
v_mfma_f32_16x16x32_bf16 v[136:139], v[172:175], a[40:43], v[136:139]
ds_read_b64_tr_b16 v[160:161], v28 offset:24576
s_add_u32 m0, s61, 81920
v_mfma_f32_16x16x32_bf16 v[140:143], v[168:171], a[36:39], v[140:143]
ds_read_b64_tr_b16 v[162:163], v28 offset:25088
buffer_load_dwordx4 v3, s[44:47], s18 offen offset:0 lds
v_mfma_f32_16x16x32_bf16 v[140:143], v[172:175], a[44:47], v[140:143]
ds_read_b64_tr_b16 v[164:165], v28 offset:25600
ds_read_b64_tr_b16 v[166:167], v28 offset:26112
v_mfma_f32_16x16x32_bf16 v[144:147], v[176:179], a[32:35], v[144:147]
ds_read_b64_tr_b16 v[168:169], v29 offset:24576
v_mfma_f32_16x16x32_bf16 v[144:147], v[180:183], a[40:43], v[144:147]
ds_read_b64_tr_b16 v[170:171], v29 offset:25088
v_mfma_f32_16x16x32_bf16 v[148:151], v[176:179], a[36:39], v[148:151]
ds_read_b64_tr_b16 v[172:173], v29 offset:25600
buffer_load_dwordx4 v4, s[44:47], s18 offen offset:1024 lds
v_mfma_f32_16x16x32_bf16 v[148:151], v[180:183], a[44:47], v[148:151]
ds_read_b64_tr_b16 v[174:175], v29 offset:26112
v_mfma_f32_16x16x32_bf16 v[152:155], v[184:187], a[32:35], v[152:155]
ds_read_b64_tr_b16 v[176:177], v30 offset:24576
v_mfma_f32_16x16x32_bf16 v[152:155], v[188:191], a[40:43], v[152:155]
ds_read_b64_tr_b16 v[178:179], v30 offset:25088
v_mfma_f32_16x16x32_bf16 v[156:159], v[184:187], a[36:39], v[156:159]
ds_read_b64_tr_b16 v[180:181], v30 offset:25600
v_mfma_f32_16x16x32_bf16 v[156:159], v[188:191], a[44:47], v[156:159]
s_waitcnt lgkmcnt(3)
ds_read_b64_tr_b16 v[182:183], v30 offset:26112
v_mfma_f32_16x16x32_bf16 v[224:227], v[160:163], a[32:35], v[224:227]
ds_read_b64_tr_b16 v[184:185], v31 offset:24576
s_add_u32 m0, s61, 90112
v_mfma_f32_16x16x32_bf16 v[224:227], v[164:167], a[40:43], v[224:227]
ds_read_b64_tr_b16 v[186:187], v31 offset:25088
buffer_load_dwordx4 v3, s[44:47], s19 offen offset:0 lds
v_mfma_f32_16x16x32_bf16 v[228:231], v[160:163], a[36:39], v[228:231]
ds_read_b64_tr_b16 v[188:189], v31 offset:25600
v_mfma_f32_16x16x32_bf16 v[228:231], v[164:167], a[44:47], v[228:231]
ds_read_b64_tr_b16 v[190:191], v31 offset:26112
v_mfma_f32_16x16x32_bf16 v[232:235], v[168:171], a[32:35], v[232:235]
ds_read_b64_tr_b16 v[160:161], v28 offset:32768
v_mfma_f32_16x16x32_bf16 v[232:235], v[172:175], a[40:43], v[232:235]
ds_read_b64_tr_b16 v[162:163], v28 offset:33280
buffer_load_dwordx4 v4, s[44:47], s19 offen offset:1024 lds
v_mfma_f32_16x16x32_bf16 v[236:239], v[168:171], a[36:39], v[236:239]
ds_read_b64_tr_b16 v[164:165], v28 offset:40960
v_mfma_f32_16x16x32_bf16 v[236:239], v[172:175], a[44:47], v[236:239]
s_waitcnt lgkmcnt(3)
ds_read_b64_tr_b16 v[166:167], v28 offset:41472
v_mfma_f32_16x16x32_bf16 v[240:243], v[176:179], a[32:35], v[240:243]
ds_read_b64_tr_b16 v[168:169], v29 offset:32768
v_mfma_f32_16x16x32_bf16 v[240:243], v[180:183], a[40:43], v[240:243]
ds_read_b64_tr_b16 v[170:171], v29 offset:33280
s_add_u32 m0, s61, 98304
v_mfma_f32_16x16x32_bf16 v[244:247], v[176:179], a[36:39], v[244:247]
ds_read_b64_tr_b16 v[172:173], v29 offset:40960
buffer_load_dwordx4 v5, s[44:47], s18 offen offset:0 lds
v_mfma_f32_16x16x32_bf16 v[244:247], v[180:183], a[44:47], v[244:247]
ds_read_b64_tr_b16 v[174:175], v29 offset:41472
v_mfma_f32_16x16x32_bf16 v[248:251], v[184:187], a[32:35], v[248:251]
ds_read_b64_tr_b16 v[176:177], v30 offset:32768
v_mfma_f32_16x16x32_bf16 v[248:251], v[188:191], a[40:43], v[248:251]
ds_read_b64_tr_b16 v[178:179], v30 offset:33280
v_mfma_f32_16x16x32_bf16 v[252:255], v[184:187], a[36:39], v[252:255]
ds_read_b64_tr_b16 v[180:181], v30 offset:40960
buffer_load_dwordx4 v27, s[44:47], s19 offen offset:1024 lds
v_mfma_f32_16x16x32_bf16 v[252:255], v[188:191], a[44:47], v[252:255]
s_waitcnt lgkmcnt(3)
ds_read_b64_tr_b16 v[182:183], v30 offset:41472
v_mfma_f32_16x16x32_bf16 v[96:99], v[160:163], a[48:51], v[96:99]
ds_read_b64_tr_b16 v[184:185], v31 offset:32768
v_mfma_f32_16x16x32_bf16 v[96:99], v[164:167], a[56:59], v[96:99]
ds_read_b64_tr_b16 v[186:187], v31 offset:33280
v_mfma_f32_16x16x32_bf16 v[100:103], v[160:163], a[52:55], v[100:103]
ds_read_b64_tr_b16 v[188:189], v31 offset:40960
v_mfma_f32_16x16x32_bf16 v[100:103], v[164:167], a[60:63], v[100:103]
ds_read_b64_tr_b16 v[190:191], v31 offset:41472
s_add_u32 s62, s60, 1835008
v_mfma_f32_16x16x32_bf16 v[104:107], v[168:171], a[48:51], v[104:107]
ds_read_b64_tr_b16 v[160:161], v28 offset:33792
buffer_load_dwordx4 a[112:115], v2, s[96:99], s62 offen sc1
v_mfma_f32_16x16x32_bf16 v[104:107], v[172:175], a[56:59], v[104:107]
ds_read_b64_tr_b16 v[162:163], v28 offset:34304
v_mfma_f32_16x16x32_bf16 v[108:111], v[168:171], a[52:55], v[108:111]
ds_read_b64_tr_b16 v[164:165], v28 offset:41984
v_mfma_f32_16x16x32_bf16 v[108:111], v[172:175], a[60:63], v[108:111]
s_waitcnt lgkmcnt(3)
ds_read_b64_tr_b16 v[166:167], v28 offset:42496
v_mfma_f32_16x16x32_bf16 v[112:115], v[176:179], a[48:51], v[112:115]
ds_read_b64_tr_b16 v[168:169], v29 offset:33792
buffer_load_dwordx4 a[116:119], v2, s[96:99], s62 offen offset:512 sc1
v_mfma_f32_16x16x32_bf16 v[112:115], v[180:183], a[56:59], v[112:115]
ds_read_b64_tr_b16 v[170:171], v29 offset:34304
v_mfma_f32_16x16x32_bf16 v[116:119], v[176:179], a[52:55], v[116:119]
ds_read_b64_tr_b16 v[172:173], v29 offset:41984
v_mfma_f32_16x16x32_bf16 v[116:119], v[180:183], a[60:63], v[116:119]
ds_read_b64_tr_b16 v[174:175], v29 offset:42496
v_mfma_f32_16x16x32_bf16 v[120:123], v[184:187], a[48:51], v[120:123]
ds_read_b64_tr_b16 v[176:177], v30 offset:33792
v_mfma_f32_16x16x32_bf16 v[120:123], v[188:191], a[56:59], v[120:123]
ds_read_b64_tr_b16 v[178:179], v30 offset:34304
s_add_u32 s62, s60, 1966080
v_mfma_f32_16x16x32_bf16 v[124:127], v[184:187], a[52:55], v[124:127]
ds_read_b64_tr_b16 v[180:181], v30 offset:41984
buffer_load_dwordx4 a[120:123], v2, s[96:99], s62 offen sc1
v_mfma_f32_16x16x32_bf16 v[124:127], v[188:191], a[60:63], v[124:127]
s_waitcnt lgkmcnt(3)
)ASM"
R"ASM(
ds_read_b64_tr_b16 v[182:183], v30 offset:42496
v_mfma_f32_16x16x32_bf16 v[128:131], v[160:163], a[48:51], v[128:131]
ds_read_b64_tr_b16 v[184:185], v31 offset:33792
v_mfma_f32_16x16x32_bf16 v[128:131], v[164:167], a[56:59], v[128:131]
ds_read_b64_tr_b16 v[186:187], v31 offset:34304
v_mfma_f32_16x16x32_bf16 v[132:135], v[160:163], a[52:55], v[132:135]
ds_read_b64_tr_b16 v[188:189], v31 offset:41984
buffer_load_dwordx4 a[124:127], v2, s[96:99], s62 offen offset:512 sc1
v_mfma_f32_16x16x32_bf16 v[132:135], v[164:167], a[60:63], v[132:135]
ds_read_b64_tr_b16 v[190:191], v31 offset:42496
v_mfma_f32_16x16x32_bf16 v[136:139], v[168:171], a[48:51], v[136:139]
ds_read_b64_tr_b16 v[160:161], v192 offset:0
v_mfma_f32_16x16x32_bf16 v[136:139], v[172:175], a[56:59], v[136:139]
ds_read_b64_tr_b16 v[162:163], v192 offset:512
v_mfma_f32_16x16x32_bf16 v[140:143], v[168:171], a[52:55], v[140:143]
ds_read_b64_tr_b16 v[164:165], v192 offset:1024
v_mfma_f32_16x16x32_bf16 v[140:143], v[172:175], a[60:63], v[140:143]
s_waitcnt lgkmcnt(3)
ds_read_b64_tr_b16 v[166:167], v192 offset:1536
v_mfma_f32_16x16x32_bf16 v[144:147], v[176:179], a[48:51], v[144:147]
ds_read_b64_tr_b16 v[168:169], v193 offset:0
s_add_u32 s62, s60, 2097152
v_mfma_f32_16x16x32_bf16 v[144:147], v[180:183], a[56:59], v[144:147]
ds_read_b64_tr_b16 v[170:171], v193 offset:512
buffer_load_dwordx4 a[128:131], v2, s[96:99], s62 offen sc1
v_mfma_f32_16x16x32_bf16 v[148:151], v[176:179], a[52:55], v[148:151]
ds_read_b64_tr_b16 v[172:173], v193 offset:1024
v_mfma_f32_16x16x32_bf16 v[148:151], v[180:183], a[60:63], v[148:151]
ds_read_b64_tr_b16 v[174:175], v193 offset:1536
v_mfma_f32_16x16x32_bf16 v[152:155], v[184:187], a[48:51], v[152:155]
ds_read_b64_tr_b16 v[176:177], v194 offset:0
v_mfma_f32_16x16x32_bf16 v[152:155], v[188:191], a[56:59], v[152:155]
ds_read_b64_tr_b16 v[178:179], v194 offset:512
buffer_load_dwordx4 a[132:135], v2, s[96:99], s62 offen offset:512 sc1
v_mfma_f32_16x16x32_bf16 v[156:159], v[184:187], a[52:55], v[156:159]
ds_read_b64_tr_b16 v[180:181], v194 offset:1024
v_mfma_f32_16x16x32_bf16 v[156:159], v[188:191], a[60:63], v[156:159]
s_waitcnt lgkmcnt(3)
ds_read_b64_tr_b16 v[182:183], v194 offset:1536
v_mfma_f32_16x16x32_bf16 v[224:227], v[160:163], a[48:51], v[224:227]
ds_read_b64_tr_b16 v[184:185], v195 offset:0
v_mfma_f32_16x16x32_bf16 v[224:227], v[164:167], a[56:59], v[224:227]
ds_read_b64_tr_b16 v[186:187], v195 offset:512
s_add_u32 s62, s60, 2228224
v_mfma_f32_16x16x32_bf16 v[228:231], v[160:163], a[52:55], v[228:231]
ds_read_b64_tr_b16 v[188:189], v195 offset:1024
buffer_load_dwordx4 a[136:139], v2, s[96:99], s62 offen sc1
v_mfma_f32_16x16x32_bf16 v[228:231], v[164:167], a[60:63], v[228:231]
ds_read_b64_tr_b16 v[190:191], v195 offset:1536
v_mfma_f32_16x16x32_bf16 v[232:235], v[168:171], a[48:51], v[232:235]
s_waitcnt vmcnt(23) lgkmcnt(0)
s_barrier
v_mfma_f32_16x16x32_bf16 v[232:235], v[172:175], a[56:59], v[232:235]
ds_read_b64_tr_b16 v[160:161], v192 offset:8192
v_mfma_f32_16x16x32_bf16 v[236:239], v[168:171], a[52:55], v[236:239]
ds_read_b64_tr_b16 v[162:163], v192 offset:8704
buffer_load_dwordx4 a[140:143], v2, s[96:99], s62 offen offset:512 sc1
v_mfma_f32_16x16x32_bf16 v[236:239], v[172:175], a[60:63], v[236:239]
ds_read_b64_tr_b16 v[164:165], v192 offset:16384
ds_read_b64_tr_b16 v[166:167], v192 offset:16896
v_mfma_f32_16x16x32_bf16 v[240:243], v[176:179], a[48:51], v[240:243]
ds_read_b64_tr_b16 v[168:169], v193 offset:8192
v_mfma_f32_16x16x32_bf16 v[240:243], v[180:183], a[56:59], v[240:243]
ds_read_b64_tr_b16 v[170:171], v193 offset:8704
v_mfma_f32_16x16x32_bf16 v[244:247], v[176:179], a[52:55], v[244:247]
ds_read_b64_tr_b16 v[172:173], v193 offset:16384
v_mfma_f32_16x16x32_bf16 v[244:247], v[180:183], a[60:63], v[244:247]
ds_read_b64_tr_b16 v[174:175], v193 offset:16896
s_add_u32 m0, s61, 106496
v_mfma_f32_16x16x32_bf16 v[248:251], v[184:187], a[48:51], v[248:251]
ds_read_b64_tr_b16 v[176:177], v194 offset:8192
buffer_load_dwordx4 v3, s[44:47], s20 offen offset:0 lds
v_mfma_f32_16x16x32_bf16 v[248:251], v[188:191], a[56:59], v[248:251]
ds_read_b64_tr_b16 v[178:179], v194 offset:8704
v_mfma_f32_16x16x32_bf16 v[252:255], v[184:187], a[52:55], v[252:255]
ds_read_b64_tr_b16 v[180:181], v194 offset:16384
v_mfma_f32_16x16x32_bf16 v[252:255], v[188:191], a[60:63], v[252:255]
s_waitcnt lgkmcnt(3)
ds_read_b64_tr_b16 v[182:183], v194 offset:16896
v_mfma_f32_16x16x32_bf16 v[96:99], v[160:163], a[64:67], v[96:99]
ds_read_b64_tr_b16 v[184:185], v195 offset:8192
buffer_load_dwordx4 v4, s[44:47], s20 offen offset:1024 lds
v_mfma_f32_16x16x32_bf16 v[96:99], v[164:167], a[72:75], v[96:99]
ds_read_b64_tr_b16 v[186:187], v195 offset:8704
v_mfma_f32_16x16x32_bf16 v[100:103], v[160:163], a[68:71], v[100:103]
ds_read_b64_tr_b16 v[188:189], v195 offset:16384
v_mfma_f32_16x16x32_bf16 v[100:103], v[164:167], a[76:79], v[100:103]
ds_read_b64_tr_b16 v[190:191], v195 offset:16896
v_mfma_f32_16x16x32_bf16 v[104:107], v[168:171], a[64:67], v[104:107]
ds_read_b64_tr_b16 v[160:161], v192 offset:9216
v_mfma_f32_16x16x32_bf16 v[104:107], v[172:175], a[72:75], v[104:107]
ds_read_b64_tr_b16 v[162:163], v192 offset:9728
s_add_u32 m0, s61, 114688
v_mfma_f32_16x16x32_bf16 v[108:111], v[168:171], a[68:71], v[108:111]
ds_read_b64_tr_b16 v[164:165], v192 offset:17408
buffer_load_dwordx4 v3, s[44:47], s21 offen offset:0 lds
v_mfma_f32_16x16x32_bf16 v[108:111], v[172:175], a[76:79], v[108:111]
s_waitcnt lgkmcnt(3)
ds_read_b64_tr_b16 v[166:167], v192 offset:17920
v_mfma_f32_16x16x32_bf16 v[112:115], v[176:179], a[64:67], v[112:115]
ds_read_b64_tr_b16 v[168:169], v193 offset:9216
v_mfma_f32_16x16x32_bf16 v[112:115], v[180:183], a[72:75], v[112:115]
ds_read_b64_tr_b16 v[170:171], v193 offset:9728
v_mfma_f32_16x16x32_bf16 v[116:119], v[176:179], a[68:71], v[116:119]
ds_read_b64_tr_b16 v[172:173], v193 offset:17408
buffer_load_dwordx4 v4, s[44:47], s21 offen offset:1024 lds
v_mfma_f32_16x16x32_bf16 v[116:119], v[180:183], a[76:79], v[116:119]
ds_read_b64_tr_b16 v[174:175], v193 offset:17920
v_mfma_f32_16x16x32_bf16 v[120:123], v[184:187], a[64:67], v[120:123]
ds_read_b64_tr_b16 v[176:177], v194 offset:9216
v_mfma_f32_16x16x32_bf16 v[120:123], v[188:191], a[72:75], v[120:123]
ds_read_b64_tr_b16 v[178:179], v194 offset:9728
v_mfma_f32_16x16x32_bf16 v[124:127], v[184:187], a[68:71], v[124:127]
ds_read_b64_tr_b16 v[180:181], v194 offset:17408
v_mfma_f32_16x16x32_bf16 v[124:127], v[188:191], a[76:79], v[124:127]
s_waitcnt lgkmcnt(3)
ds_read_b64_tr_b16 v[182:183], v194 offset:17920
v_mfma_f32_16x16x32_bf16 v[128:131], v[160:163], a[64:67], v[128:131]
ds_read_b64_tr_b16 v[184:185], v195 offset:9216
s_add_u32 m0, s61, 122880
v_mfma_f32_16x16x32_bf16 v[128:131], v[164:167], a[72:75], v[128:131]
ds_read_b64_tr_b16 v[186:187], v195 offset:9728
buffer_load_dwordx4 v3, s[44:47], s22 offen offset:0 lds
v_mfma_f32_16x16x32_bf16 v[132:135], v[160:163], a[68:71], v[132:135]
ds_read_b64_tr_b16 v[188:189], v195 offset:17408
v_mfma_f32_16x16x32_bf16 v[132:135], v[164:167], a[76:79], v[132:135]
ds_read_b64_tr_b16 v[190:191], v195 offset:17920
v_mfma_f32_16x16x32_bf16 v[136:139], v[168:171], a[64:67], v[136:139]
ds_read_b64_tr_b16 v[160:161], v192 offset:24576
v_mfma_f32_16x16x32_bf16 v[136:139], v[172:175], a[72:75], v[136:139]
ds_read_b64_tr_b16 v[162:163], v192 offset:25088
buffer_load_dwordx4 v4, s[44:47], s22 offen offset:1024 lds
v_mfma_f32_16x16x32_bf16 v[140:143], v[168:171], a[68:71], v[140:143]
ds_read_b64_tr_b16 v[164:165], v192 offset:25600
v_mfma_f32_16x16x32_bf16 v[140:143], v[172:175], a[76:79], v[140:143]
s_waitcnt lgkmcnt(3)
ds_read_b64_tr_b16 v[166:167], v192 offset:26112
v_mfma_f32_16x16x32_bf16 v[144:147], v[176:179], a[64:67], v[144:147]
ds_read_b64_tr_b16 v[168:169], v193 offset:24576
v_mfma_f32_16x16x32_bf16 v[144:147], v[180:183], a[72:75], v[144:147]
ds_read_b64_tr_b16 v[170:171], v193 offset:25088
s_add_u32 m0, s61, 131072
v_mfma_f32_16x16x32_bf16 v[148:151], v[176:179], a[68:71], v[148:151]
buffer_load_dword v17, v15, s[96:99], 0 offen sc1
ds_read_b64_tr_b16 v[172:173], v193 offset:25600
v_mfma_f32_16x16x32_bf16 v[148:151], v[180:183], a[76:79], v[148:151]
ds_read_b64_tr_b16 v[174:175], v193 offset:26112
v_mfma_f32_16x16x32_bf16 v[152:155], v[184:187], a[64:67], v[152:155]
ds_read_b64_tr_b16 v[176:177], v194 offset:24576
v_mfma_f32_16x16x32_bf16 v[152:155], v[188:191], a[72:75], v[152:155]
ds_read_b64_tr_b16 v[178:179], v194 offset:25088
v_mfma_f32_16x16x32_bf16 v[156:159], v[184:187], a[68:71], v[156:159]
ds_read_b64_tr_b16 v[180:181], v194 offset:25600
buffer_load_dwordx4 v3, s[44:47], s23 offen offset:0 lds
v_mfma_f32_16x16x32_bf16 v[156:159], v[188:191], a[76:79], v[156:159]
s_waitcnt lgkmcnt(3)
ds_read_b64_tr_b16 v[182:183], v194 offset:26112
v_mfma_f32_16x16x32_bf16 v[224:227], v[160:163], a[64:67], v[224:227]
ds_read_b64_tr_b16 v[184:185], v195 offset:24576
v_mfma_f32_16x16x32_bf16 v[224:227], v[164:167], a[72:75], v[224:227]
ds_read_b64_tr_b16 v[186:187], v195 offset:25088
v_mfma_f32_16x16x32_bf16 v[228:231], v[160:163], a[68:71], v[228:231]
ds_read_b64_tr_b16 v[188:189], v195 offset:25600
buffer_load_dwordx4 v4, s[44:47], s23 offen offset:1024 lds
v_mfma_f32_16x16x32_bf16 v[228:231], v[164:167], a[76:79], v[228:231]
ds_read_b64_tr_b16 v[190:191], v195 offset:26112
s_add_u32 s62, s60, 2359296
v_mfma_f32_16x16x32_bf16 v[232:235], v[168:171], a[64:67], v[232:235]
ds_read_b64_tr_b16 v[160:161], v192 offset:32768
v_mfma_f32_16x16x32_bf16 v[232:235], v[172:175], a[72:75], v[232:235]
ds_read_b64_tr_b16 v[162:163], v192 offset:33280
v_mfma_f32_16x16x32_bf16 v[236:239], v[168:171], a[68:71], v[236:239]
ds_read_b64_tr_b16 v[164:165], v192 offset:40960
buffer_load_dwordx4 a[144:147], v2, s[96:99], s62 offen sc1
v_mfma_f32_16x16x32_bf16 v[236:239], v[172:175], a[76:79], v[236:239]
s_waitcnt lgkmcnt(3)
ds_read_b64_tr_b16 v[166:167], v192 offset:41472
v_mfma_f32_16x16x32_bf16 v[240:243], v[176:179], a[64:67], v[240:243]
ds_read_b64_tr_b16 v[168:169], v193 offset:32768
v_mfma_f32_16x16x32_bf16 v[240:243], v[180:183], a[72:75], v[240:243]
ds_read_b64_tr_b16 v[170:171], v193 offset:33280
v_mfma_f32_16x16x32_bf16 v[244:247], v[176:179], a[68:71], v[244:247]
ds_read_b64_tr_b16 v[172:173], v193 offset:40960
buffer_load_dwordx4 a[148:151], v2, s[96:99], s62 offen offset:512 sc1
v_mfma_f32_16x16x32_bf16 v[244:247], v[180:183], a[76:79], v[244:247]
ds_read_b64_tr_b16 v[174:175], v193 offset:41472
v_mfma_f32_16x16x32_bf16 v[248:251], v[184:187], a[64:67], v[248:251]
ds_read_b64_tr_b16 v[176:177], v194 offset:32768
v_mfma_f32_16x16x32_bf16 v[248:251], v[188:191], a[72:75], v[248:251]
ds_read_b64_tr_b16 v[178:179], v194 offset:33280
s_add_u32 s62, s60, 2490368
v_mfma_f32_16x16x32_bf16 v[252:255], v[184:187], a[68:71], v[252:255]
ds_read_b64_tr_b16 v[180:181], v194 offset:40960
buffer_load_dwordx4 a[152:155], v2, s[96:99], s62 offen sc1
v_mfma_f32_16x16x32_bf16 v[252:255], v[188:191], a[76:79], v[252:255]
s_waitcnt lgkmcnt(3)
ds_read_b64_tr_b16 v[182:183], v194 offset:41472
v_mfma_f32_16x16x32_bf16 v[96:99], v[160:163], a[80:83], v[96:99]
ds_read_b64_tr_b16 v[184:185], v195 offset:32768
v_mfma_f32_16x16x32_bf16 v[96:99], v[164:167], a[88:91], v[96:99]
)ASM"
R"ASM(
ds_read_b64_tr_b16 v[186:187], v195 offset:33280
v_mfma_f32_16x16x32_bf16 v[100:103], v[160:163], a[84:87], v[100:103]
ds_read_b64_tr_b16 v[188:189], v195 offset:40960
buffer_load_dwordx4 a[156:159], v2, s[96:99], s62 offen offset:512 sc1
v_mfma_f32_16x16x32_bf16 v[100:103], v[164:167], a[92:95], v[100:103]
ds_read_b64_tr_b16 v[190:191], v195 offset:41472
v_mfma_f32_16x16x32_bf16 v[104:107], v[168:171], a[80:83], v[104:107]
s_waitcnt vmcnt(31) lgkmcnt(0)
s_barrier
v_mfma_f32_16x16x32_bf16 v[104:107], v[172:175], a[88:91], v[104:107]
ds_read_b64_tr_b16 v[160:161], v192 offset:33792
v_mfma_f32_16x16x32_bf16 v[108:111], v[168:171], a[84:87], v[108:111]
ds_read_b64_tr_b16 v[162:163], v192 offset:34304
v_mfma_f32_16x16x32_bf16 v[108:111], v[172:175], a[92:95], v[108:111]
ds_read_b64_tr_b16 v[164:165], v192 offset:41984
ds_read_b64_tr_b16 v[166:167], v192 offset:42496
v_mfma_f32_16x16x32_bf16 v[112:115], v[176:179], a[80:83], v[112:115]
ds_read_b64_tr_b16 v[168:169], v193 offset:33792
s_add_u32 s62, s60, 2621440
v_mfma_f32_16x16x32_bf16 v[112:115], v[180:183], a[88:91], v[112:115]
ds_read_b64_tr_b16 v[170:171], v193 offset:34304
buffer_load_dwordx4 a[160:163], v2, s[96:99], s62 offen sc1
v_mfma_f32_16x16x32_bf16 v[116:119], v[176:179], a[84:87], v[116:119]
ds_read_b64_tr_b16 v[172:173], v193 offset:41984
v_mfma_f32_16x16x32_bf16 v[116:119], v[180:183], a[92:95], v[116:119]
ds_read_b64_tr_b16 v[174:175], v193 offset:42496
v_mfma_f32_16x16x32_bf16 v[120:123], v[184:187], a[80:83], v[120:123]
ds_read_b64_tr_b16 v[176:177], v194 offset:33792
v_mfma_f32_16x16x32_bf16 v[120:123], v[188:191], a[88:91], v[120:123]
ds_read_b64_tr_b16 v[178:179], v194 offset:34304
buffer_load_dwordx4 a[164:167], v2, s[96:99], s62 offen offset:512 sc1
v_mfma_f32_16x16x32_bf16 v[124:127], v[184:187], a[84:87], v[124:127]
ds_read_b64_tr_b16 v[180:181], v194 offset:41984
v_mfma_f32_16x16x32_bf16 v[124:127], v[188:191], a[92:95], v[124:127]
s_waitcnt lgkmcnt(3)
ds_read_b64_tr_b16 v[182:183], v194 offset:42496
v_mfma_f32_16x16x32_bf16 v[128:131], v[160:163], a[80:83], v[128:131]
ds_read_b64_tr_b16 v[184:185], v195 offset:33792
v_mfma_f32_16x16x32_bf16 v[128:131], v[164:167], a[88:91], v[128:131]
ds_read_b64_tr_b16 v[186:187], v195 offset:34304
s_add_u32 s62, s60, 2752512
v_mfma_f32_16x16x32_bf16 v[132:135], v[160:163], a[84:87], v[132:135]
ds_read_b64_tr_b16 v[188:189], v195 offset:41984
buffer_load_dwordx4 a[168:171], v2, s[96:99], s62 offen sc1
v_mfma_f32_16x16x32_bf16 v[132:135], v[164:167], a[92:95], v[132:135]
ds_read_b64_tr_b16 v[190:191], v195 offset:42496
v_mfma_f32_16x16x32_bf16 v[136:139], v[168:171], a[80:83], v[136:139]
ds_read_b64_tr_b16 v[160:161], v196 offset:0
v_mfma_f32_16x16x32_bf16 v[136:139], v[172:175], a[88:91], v[136:139]
ds_read_b64_tr_b16 v[162:163], v196 offset:512
v_mfma_f32_16x16x32_bf16 v[140:143], v[168:171], a[84:87], v[140:143]
ds_read_b64_tr_b16 v[164:165], v196 offset:1024
buffer_load_dwordx4 a[172:175], v2, s[96:99], s62 offen offset:512 sc1
v_mfma_f32_16x16x32_bf16 v[140:143], v[172:175], a[92:95], v[140:143]
s_waitcnt lgkmcnt(3)
ds_read_b64_tr_b16 v[166:167], v196 offset:1536
v_mfma_f32_16x16x32_bf16 v[144:147], v[176:179], a[80:83], v[144:147]
ds_read_b64_tr_b16 v[168:169], v197 offset:0
v_mfma_f32_16x16x32_bf16 v[144:147], v[180:183], a[88:91], v[144:147]
ds_read_b64_tr_b16 v[170:171], v197 offset:512
v_mfma_f32_16x16x32_bf16 v[148:151], v[176:179], a[84:87], v[148:151]
ds_read_b64_tr_b16 v[172:173], v197 offset:1024
v_mfma_f32_16x16x32_bf16 v[148:151], v[180:183], a[92:95], v[148:151]
ds_read_b64_tr_b16 v[174:175], v197 offset:1536
s_add_u32 m0, s61, 139264
v_mfma_f32_16x16x32_bf16 v[152:155], v[184:187], a[80:83], v[152:155]
ds_read_b64_tr_b16 v[176:177], v198 offset:0
buffer_load_dwordx4 v3, s[44:47], s24 offen offset:0 lds
v_mfma_f32_16x16x32_bf16 v[152:155], v[188:191], a[88:91], v[152:155]
ds_read_b64_tr_b16 v[178:179], v198 offset:512
v_mfma_f32_16x16x32_bf16 v[156:159], v[184:187], a[84:87], v[156:159]
ds_read_b64_tr_b16 v[180:181], v198 offset:1024
v_mfma_f32_16x16x32_bf16 v[156:159], v[188:191], a[92:95], v[156:159]
s_waitcnt lgkmcnt(3)
ds_read_b64_tr_b16 v[182:183], v198 offset:1536
v_mfma_f32_16x16x32_bf16 v[224:227], v[160:163], a[80:83], v[224:227]
ds_read_b64_tr_b16 v[184:185], v199 offset:0
buffer_load_dwordx4 v4, s[44:47], s24 offen offset:1024 lds
v_mfma_f32_16x16x32_bf16 v[224:227], v[164:167], a[88:91], v[224:227]
ds_read_b64_tr_b16 v[186:187], v199 offset:512
v_mfma_f32_16x16x32_bf16 v[228:231], v[160:163], a[84:87], v[228:231]
ds_read_b64_tr_b16 v[188:189], v199 offset:1024
v_mfma_f32_16x16x32_bf16 v[228:231], v[164:167], a[92:95], v[228:231]
ds_read_b64_tr_b16 v[190:191], v199 offset:1536
v_mfma_f32_16x16x32_bf16 v[232:235], v[168:171], a[80:83], v[232:235]
ds_read_b64_tr_b16 v[160:161], v196 offset:8192
v_mfma_f32_16x16x32_bf16 v[232:235], v[172:175], a[88:91], v[232:235]
ds_read_b64_tr_b16 v[162:163], v196 offset:8704
s_add_u32 m0, s61, 0
v_mfma_f32_16x16x32_bf16 v[236:239], v[168:171], a[84:87], v[236:239]
ds_read_b64_tr_b16 v[164:165], v196 offset:16384
buffer_load_dwordx4 v3, s[44:47], s25 offen offset:0 lds
v_mfma_f32_16x16x32_bf16 v[236:239], v[172:175], a[92:95], v[236:239]
s_waitcnt lgkmcnt(3)
ds_read_b64_tr_b16 v[166:167], v196 offset:16896
v_mfma_f32_16x16x32_bf16 v[240:243], v[176:179], a[80:83], v[240:243]
ds_read_b64_tr_b16 v[168:169], v197 offset:8192
v_mfma_f32_16x16x32_bf16 v[240:243], v[180:183], a[88:91], v[240:243]
ds_read_b64_tr_b16 v[170:171], v197 offset:8704
v_mfma_f32_16x16x32_bf16 v[244:247], v[176:179], a[84:87], v[244:247]
ds_read_b64_tr_b16 v[172:173], v197 offset:16384
buffer_load_dwordx4 v4, s[44:47], s25 offen offset:1024 lds
v_mfma_f32_16x16x32_bf16 v[244:247], v[180:183], a[92:95], v[244:247]
ds_read_b64_tr_b16 v[174:175], v197 offset:16896
v_mfma_f32_16x16x32_bf16 v[248:251], v[184:187], a[80:83], v[248:251]
ds_read_b64_tr_b16 v[176:177], v198 offset:8192
v_mfma_f32_16x16x32_bf16 v[248:251], v[188:191], a[88:91], v[248:251]
ds_read_b64_tr_b16 v[178:179], v198 offset:8704
v_mfma_f32_16x16x32_bf16 v[252:255], v[184:187], a[84:87], v[252:255]
ds_read_b64_tr_b16 v[180:181], v198 offset:16384
v_mfma_f32_16x16x32_bf16 v[252:255], v[188:191], a[92:95], v[252:255]
s_waitcnt lgkmcnt(3)
ds_read_b64_tr_b16 v[182:183], v198 offset:16896
v_mfma_f32_16x16x32_bf16 v[96:99], v[160:163], a[96:99], v[96:99]
ds_read_b64_tr_b16 v[184:185], v199 offset:8192
s_add_u32 m0, s61, 8192
v_mfma_f32_16x16x32_bf16 v[96:99], v[164:167], a[104:107], v[96:99]
ds_read_b64_tr_b16 v[186:187], v199 offset:8704
buffer_load_dwordx4 v3, s[44:47], s26 offen offset:0 lds
v_mfma_f32_16x16x32_bf16 v[100:103], v[160:163], a[100:103], v[100:103]
ds_read_b64_tr_b16 v[188:189], v199 offset:16384
v_mfma_f32_16x16x32_bf16 v[100:103], v[164:167], a[108:111], v[100:103]
ds_read_b64_tr_b16 v[190:191], v199 offset:16896
v_mfma_f32_16x16x32_bf16 v[104:107], v[168:171], a[96:99], v[104:107]
ds_read_b64_tr_b16 v[160:161], v196 offset:9216
v_mfma_f32_16x16x32_bf16 v[104:107], v[172:175], a[104:107], v[104:107]
ds_read_b64_tr_b16 v[162:163], v196 offset:9728
buffer_load_dwordx4 v4, s[44:47], s26 offen offset:1024 lds
v_mfma_f32_16x16x32_bf16 v[108:111], v[168:171], a[100:103], v[108:111]
ds_read_b64_tr_b16 v[164:165], v196 offset:17408
v_mfma_f32_16x16x32_bf16 v[108:111], v[172:175], a[108:111], v[108:111]
s_waitcnt lgkmcnt(3)
ds_read_b64_tr_b16 v[166:167], v196 offset:17920
v_mfma_f32_16x16x32_bf16 v[112:115], v[176:179], a[96:99], v[112:115]
ds_read_b64_tr_b16 v[168:169], v197 offset:9216
v_mfma_f32_16x16x32_bf16 v[112:115], v[180:183], a[104:107], v[112:115]
ds_read_b64_tr_b16 v[170:171], v197 offset:9728
s_add_u32 m0, s61, 16384
v_mfma_f32_16x16x32_bf16 v[116:119], v[176:179], a[100:103], v[116:119]
ds_read_b64_tr_b16 v[172:173], v197 offset:17408
buffer_load_dwordx4 v3, s[44:47], s27 offen offset:0 lds
v_mfma_f32_16x16x32_bf16 v[116:119], v[180:183], a[108:111], v[116:119]
ds_read_b64_tr_b16 v[174:175], v197 offset:17920
v_mfma_f32_16x16x32_bf16 v[120:123], v[184:187], a[96:99], v[120:123]
ds_read_b64_tr_b16 v[176:177], v198 offset:9216
v_mfma_f32_16x16x32_bf16 v[120:123], v[188:191], a[104:107], v[120:123]
ds_read_b64_tr_b16 v[178:179], v198 offset:9728
v_mfma_f32_16x16x32_bf16 v[124:127], v[184:187], a[100:103], v[124:127]
ds_read_b64_tr_b16 v[180:181], v198 offset:17408
buffer_load_dwordx4 v4, s[44:47], s27 offen offset:1024 lds
v_mfma_f32_16x16x32_bf16 v[124:127], v[188:191], a[108:111], v[124:127]
s_waitcnt lgkmcnt(3)
ds_read_b64_tr_b16 v[182:183], v198 offset:17920
v_mfma_f32_16x16x32_bf16 v[128:131], v[160:163], a[96:99], v[128:131]
ds_read_b64_tr_b16 v[184:185], v199 offset:9216
v_mfma_f32_16x16x32_bf16 v[128:131], v[164:167], a[104:107], v[128:131]
ds_read_b64_tr_b16 v[186:187], v199 offset:9728
v_mfma_f32_16x16x32_bf16 v[132:135], v[160:163], a[100:103], v[132:135]
ds_read_b64_tr_b16 v[188:189], v199 offset:17408
v_mfma_f32_16x16x32_bf16 v[132:135], v[164:167], a[108:111], v[132:135]
ds_read_b64_tr_b16 v[190:191], v199 offset:17920
s_add_u32 m0, s61, 24576
v_mfma_f32_16x16x32_bf16 v[136:139], v[168:171], a[96:99], v[136:139]
s_waitcnt vmcnt(29) lgkmcnt(0)
s_barrier
s_mov_b32 s65, 20000
s_waitcnt vmcnt(18)
LHC6_%=:
v_cmp_ne_u32 s[92:93], s43, v17
s_nop 3
s_and_b32 s92, s92, 0xffc00000
s_and_b32 s93, s93, 0x3
s_cmp_eq_u64 s[92:93], 0
s_cbranch_scc1 LHD6_%=
s_sub_u32 s65, s65, 1
s_cmp_eq_u32 s65, 0
s_cbranch_scc1 LHD6_%=
s_sleep 2
buffer_load_dword v17, v15, s[96:99], 0 offen sc1
s_waitcnt vmcnt(0)
s_branch LHC6_%=
LHD6_%=:
v_mfma_f32_16x16x32_bf16 v[136:139], v[172:175], a[104:107], v[136:139]
ds_read_b64_tr_b16 v[160:161], v196 offset:24576
buffer_load_dwordx4 v3, s[44:47], s28 offen offset:0 lds
v_mfma_f32_16x16x32_bf16 v[140:143], v[168:171], a[100:103], v[140:143]
ds_read_b64_tr_b16 v[162:163], v196 offset:25088
v_mfma_f32_16x16x32_bf16 v[140:143], v[172:175], a[108:111], v[140:143]
ds_read_b64_tr_b16 v[164:165], v196 offset:25600
ds_read_b64_tr_b16 v[166:167], v196 offset:26112
v_mfma_f32_16x16x32_bf16 v[144:147], v[176:179], a[96:99], v[144:147]
ds_read_b64_tr_b16 v[168:169], v197 offset:24576
v_mfma_f32_16x16x32_bf16 v[144:147], v[180:183], a[104:107], v[144:147]
ds_read_b64_tr_b16 v[170:171], v197 offset:25088
buffer_load_dwordx4 v4, s[44:47], s28 offen offset:1024 lds
v_mfma_f32_16x16x32_bf16 v[148:151], v[176:179], a[100:103], v[148:151]
ds_read_b64_tr_b16 v[172:173], v197 offset:25600
v_mfma_f32_16x16x32_bf16 v[148:151], v[180:183], a[108:111], v[148:151]
ds_read_b64_tr_b16 v[174:175], v197 offset:26112
v_mfma_f32_16x16x32_bf16 v[152:155], v[184:187], a[96:99], v[152:155]
)ASM"
R"ASM(
ds_read_b64_tr_b16 v[176:177], v198 offset:24576
v_mfma_f32_16x16x32_bf16 v[152:155], v[188:191], a[104:107], v[152:155]
ds_read_b64_tr_b16 v[178:179], v198 offset:25088
s_add_u32 s62, s60, 2883584
v_mfma_f32_16x16x32_bf16 v[156:159], v[184:187], a[100:103], v[156:159]
ds_read_b64_tr_b16 v[180:181], v198 offset:25600
buffer_load_dwordx4 a[176:179], v2, s[96:99], s62 offen sc1
v_mfma_f32_16x16x32_bf16 v[156:159], v[188:191], a[108:111], v[156:159]
s_waitcnt lgkmcnt(3)
ds_read_b64_tr_b16 v[182:183], v198 offset:26112
v_mfma_f32_16x16x32_bf16 v[224:227], v[160:163], a[96:99], v[224:227]
ds_read_b64_tr_b16 v[184:185], v199 offset:24576
v_mfma_f32_16x16x32_bf16 v[224:227], v[164:167], a[104:107], v[224:227]
ds_read_b64_tr_b16 v[186:187], v199 offset:25088
v_mfma_f32_16x16x32_bf16 v[228:231], v[160:163], a[100:103], v[228:231]
ds_read_b64_tr_b16 v[188:189], v199 offset:25600
buffer_load_dwordx4 a[180:183], v2, s[96:99], s62 offen offset:512 sc1
v_mfma_f32_16x16x32_bf16 v[228:231], v[164:167], a[108:111], v[228:231]
ds_read_b64_tr_b16 v[190:191], v199 offset:26112
v_mfma_f32_16x16x32_bf16 v[232:235], v[168:171], a[96:99], v[232:235]
ds_read_b64_tr_b16 v[160:161], v196 offset:32768
v_mfma_f32_16x16x32_bf16 v[232:235], v[172:175], a[104:107], v[232:235]
ds_read_b64_tr_b16 v[162:163], v196 offset:33280
v_mfma_f32_16x16x32_bf16 v[236:239], v[168:171], a[100:103], v[236:239]
ds_read_b64_tr_b16 v[164:165], v196 offset:40960
v_mfma_f32_16x16x32_bf16 v[236:239], v[172:175], a[108:111], v[236:239]
s_waitcnt lgkmcnt(3)
ds_read_b64_tr_b16 v[166:167], v196 offset:41472
v_mfma_f32_16x16x32_bf16 v[240:243], v[176:179], a[96:99], v[240:243]
ds_read_b64_tr_b16 v[168:169], v197 offset:32768
s_add_u32 s62, s60, 3014656
v_mfma_f32_16x16x32_bf16 v[240:243], v[180:183], a[104:107], v[240:243]
ds_read_b64_tr_b16 v[170:171], v197 offset:33280
buffer_load_dwordx4 a[184:187], v2, s[96:99], s62 offen sc1
v_mfma_f32_16x16x32_bf16 v[244:247], v[176:179], a[100:103], v[244:247]
ds_read_b64_tr_b16 v[172:173], v197 offset:40960
v_mfma_f32_16x16x32_bf16 v[244:247], v[180:183], a[108:111], v[244:247]
ds_read_b64_tr_b16 v[174:175], v197 offset:41472
v_mfma_f32_16x16x32_bf16 v[248:251], v[184:187], a[96:99], v[248:251]
ds_read_b64_tr_b16 v[176:177], v198 offset:32768
v_mfma_f32_16x16x32_bf16 v[248:251], v[188:191], a[104:107], v[248:251]
ds_read_b64_tr_b16 v[178:179], v198 offset:33280
buffer_load_dwordx4 a[188:191], v2, s[96:99], s62 offen offset:512 sc1
v_mfma_f32_16x16x32_bf16 v[252:255], v[184:187], a[100:103], v[252:255]
ds_read_b64_tr_b16 v[180:181], v198 offset:40960
v_mfma_f32_16x16x32_bf16 v[252:255], v[188:191], a[108:111], v[252:255]
s_waitcnt lgkmcnt(3)
ds_read_b64_tr_b16 v[182:183], v198 offset:41472
v_mfma_f32_16x16x32_bf16 v[96:99], v[160:163], a[112:115], v[96:99]
ds_read_b64_tr_b16 v[184:185], v199 offset:32768
v_mfma_f32_16x16x32_bf16 v[96:99], v[164:167], a[120:123], v[96:99]
ds_read_b64_tr_b16 v[186:187], v199 offset:33280
s_add_u32 s62, s60, 3145728
v_mfma_f32_16x16x32_bf16 v[100:103], v[160:163], a[116:119], v[100:103]
ds_read_b64_tr_b16 v[188:189], v199 offset:40960
buffer_load_dwordx4 a[192:195], v2, s[96:99], s62 offen sc1
v_mfma_f32_16x16x32_bf16 v[100:103], v[164:167], a[124:127], v[100:103]
ds_read_b64_tr_b16 v[190:191], v199 offset:41472
v_mfma_f32_16x16x32_bf16 v[104:107], v[168:171], a[112:115], v[104:107]
ds_read_b64_tr_b16 v[160:161], v196 offset:33792
v_mfma_f32_16x16x32_bf16 v[104:107], v[172:175], a[120:123], v[104:107]
ds_read_b64_tr_b16 v[162:163], v196 offset:34304
v_mfma_f32_16x16x32_bf16 v[108:111], v[168:171], a[116:119], v[108:111]
ds_read_b64_tr_b16 v[164:165], v196 offset:41984
buffer_load_dwordx4 a[196:199], v2, s[96:99], s62 offen offset:512 sc1
v_mfma_f32_16x16x32_bf16 v[108:111], v[172:175], a[124:127], v[108:111]
s_waitcnt lgkmcnt(3)
ds_read_b64_tr_b16 v[166:167], v196 offset:42496
v_mfma_f32_16x16x32_bf16 v[112:115], v[176:179], a[112:115], v[112:115]
ds_read_b64_tr_b16 v[168:169], v197 offset:33792
v_mfma_f32_16x16x32_bf16 v[112:115], v[180:183], a[120:123], v[112:115]
ds_read_b64_tr_b16 v[170:171], v197 offset:34304
v_mfma_f32_16x16x32_bf16 v[116:119], v[176:179], a[116:119], v[116:119]
ds_read_b64_tr_b16 v[172:173], v197 offset:41984
v_mfma_f32_16x16x32_bf16 v[116:119], v[180:183], a[124:127], v[116:119]
ds_read_b64_tr_b16 v[174:175], v197 offset:42496
s_add_u32 s62, s60, 3276800
v_mfma_f32_16x16x32_bf16 v[120:123], v[184:187], a[112:115], v[120:123]
ds_read_b64_tr_b16 v[176:177], v198 offset:33792
buffer_load_dwordx4 a[200:203], v2, s[96:99], s62 offen sc1
v_mfma_f32_16x16x32_bf16 v[120:123], v[188:191], a[120:123], v[120:123]
ds_read_b64_tr_b16 v[178:179], v198 offset:34304
v_mfma_f32_16x16x32_bf16 v[124:127], v[184:187], a[116:119], v[124:127]
ds_read_b64_tr_b16 v[180:181], v198 offset:41984
v_mfma_f32_16x16x32_bf16 v[124:127], v[188:191], a[124:127], v[124:127]
s_waitcnt lgkmcnt(3)
ds_read_b64_tr_b16 v[182:183], v198 offset:42496
v_mfma_f32_16x16x32_bf16 v[128:131], v[160:163], a[112:115], v[128:131]
ds_read_b64_tr_b16 v[184:185], v199 offset:33792
buffer_load_dwordx4 a[204:207], v2, s[96:99], s62 offen offset:512 sc1
v_mfma_f32_16x16x32_bf16 v[128:131], v[164:167], a[120:123], v[128:131]
ds_read_b64_tr_b16 v[186:187], v199 offset:34304
v_mfma_f32_16x16x32_bf16 v[132:135], v[160:163], a[116:119], v[132:135]
ds_read_b64_tr_b16 v[188:189], v199 offset:41984
v_mfma_f32_16x16x32_bf16 v[132:135], v[164:167], a[124:127], v[132:135]
ds_read_b64_tr_b16 v[190:191], v199 offset:42496
v_mfma_f32_16x16x32_bf16 v[136:139], v[168:171], a[112:115], v[136:139]
ds_read_b64_tr_b16 v[160:161], v28 offset:0
v_mfma_f32_16x16x32_bf16 v[136:139], v[172:175], a[120:123], v[136:139]
ds_read_b64_tr_b16 v[162:163], v28 offset:512
s_add_u32 m0, s61, 32768
v_mfma_f32_16x16x32_bf16 v[140:143], v[168:171], a[116:119], v[140:143]
ds_read_b64_tr_b16 v[164:165], v28 offset:1024
buffer_load_dwordx4 v3, s[44:47], s29 offen offset:0 lds
v_mfma_f32_16x16x32_bf16 v[140:143], v[172:175], a[124:127], v[140:143]
s_waitcnt lgkmcnt(3)
ds_read_b64_tr_b16 v[166:167], v28 offset:1536
v_mfma_f32_16x16x32_bf16 v[144:147], v[176:179], a[112:115], v[144:147]
ds_read_b64_tr_b16 v[168:169], v29 offset:0
v_mfma_f32_16x16x32_bf16 v[144:147], v[180:183], a[120:123], v[144:147]
ds_read_b64_tr_b16 v[170:171], v29 offset:512
v_mfma_f32_16x16x32_bf16 v[148:151], v[176:179], a[116:119], v[148:151]
ds_read_b64_tr_b16 v[172:173], v29 offset:1024
buffer_load_dwordx4 v4, s[44:47], s29 offen offset:1024 lds
v_mfma_f32_16x16x32_bf16 v[148:151], v[180:183], a[124:127], v[148:151]
ds_read_b64_tr_b16 v[174:175], v29 offset:1536
v_mfma_f32_16x16x32_bf16 v[152:155], v[184:187], a[112:115], v[152:155]
ds_read_b64_tr_b16 v[176:177], v30 offset:0
v_mfma_f32_16x16x32_bf16 v[152:155], v[188:191], a[120:123], v[152:155]
ds_read_b64_tr_b16 v[178:179], v30 offset:512
v_mfma_f32_16x16x32_bf16 v[156:159], v[184:187], a[116:119], v[156:159]
ds_read_b64_tr_b16 v[180:181], v30 offset:1024
v_mfma_f32_16x16x32_bf16 v[156:159], v[188:191], a[124:127], v[156:159]
s_waitcnt lgkmcnt(3)
ds_read_b64_tr_b16 v[182:183], v30 offset:1536
v_mfma_f32_16x16x32_bf16 v[224:227], v[160:163], a[112:115], v[224:227]
ds_read_b64_tr_b16 v[184:185], v31 offset:0
s_add_u32 m0, s61, 40960
v_mfma_f32_16x16x32_bf16 v[224:227], v[164:167], a[120:123], v[224:227]
ds_read_b64_tr_b16 v[186:187], v31 offset:512
buffer_load_dwordx4 v3, s[44:47], s30 offen offset:0 lds
v_mfma_f32_16x16x32_bf16 v[228:231], v[160:163], a[116:119], v[228:231]
ds_read_b64_tr_b16 v[188:189], v31 offset:1024
v_mfma_f32_16x16x32_bf16 v[228:231], v[164:167], a[124:127], v[228:231]
ds_read_b64_tr_b16 v[190:191], v31 offset:1536
v_mfma_f32_16x16x32_bf16 v[232:235], v[168:171], a[112:115], v[232:235]
s_waitcnt vmcnt(29) lgkmcnt(0)
s_barrier
v_mfma_f32_16x16x32_bf16 v[232:235], v[172:175], a[120:123], v[232:235]
ds_read_b64_tr_b16 v[160:161], v28 offset:8192
buffer_load_dwordx4 v4, s[44:47], s30 offen offset:1024 lds
v_mfma_f32_16x16x32_bf16 v[236:239], v[168:171], a[116:119], v[236:239]
ds_read_b64_tr_b16 v[162:163], v28 offset:8704
v_mfma_f32_16x16x32_bf16 v[236:239], v[172:175], a[124:127], v[236:239]
ds_read_b64_tr_b16 v[164:165], v28 offset:16384
ds_read_b64_tr_b16 v[166:167], v28 offset:16896
v_mfma_f32_16x16x32_bf16 v[240:243], v[176:179], a[112:115], v[240:243]
ds_read_b64_tr_b16 v[168:169], v29 offset:8192
v_mfma_f32_16x16x32_bf16 v[240:243], v[180:183], a[120:123], v[240:243]
ds_read_b64_tr_b16 v[170:171], v29 offset:8704
s_add_u32 m0, s61, 49152
v_mfma_f32_16x16x32_bf16 v[244:247], v[176:179], a[116:119], v[244:247]
ds_read_b64_tr_b16 v[172:173], v29 offset:16384
buffer_load_dwordx4 v3, s[44:47], s31 offen offset:0 lds
v_mfma_f32_16x16x32_bf16 v[244:247], v[180:183], a[124:127], v[244:247]
ds_read_b64_tr_b16 v[174:175], v29 offset:16896
v_mfma_f32_16x16x32_bf16 v[248:251], v[184:187], a[112:115], v[248:251]
ds_read_b64_tr_b16 v[176:177], v30 offset:8192
v_mfma_f32_16x16x32_bf16 v[248:251], v[188:191], a[120:123], v[248:251]
ds_read_b64_tr_b16 v[178:179], v30 offset:8704
v_mfma_f32_16x16x32_bf16 v[252:255], v[184:187], a[116:119], v[252:255]
ds_read_b64_tr_b16 v[180:181], v30 offset:16384
buffer_load_dwordx4 v4, s[44:47], s31 offen offset:1024 lds
v_mfma_f32_16x16x32_bf16 v[252:255], v[188:191], a[124:127], v[252:255]
s_waitcnt lgkmcnt(3)
ds_read_b64_tr_b16 v[182:183], v30 offset:16896
v_mfma_f32_16x16x32_bf16 v[96:99], v[160:163], a[128:131], v[96:99]
ds_read_b64_tr_b16 v[184:185], v31 offset:8192
v_mfma_f32_16x16x32_bf16 v[96:99], v[164:167], a[136:139], v[96:99]
ds_read_b64_tr_b16 v[186:187], v31 offset:8704
v_mfma_f32_16x16x32_bf16 v[100:103], v[160:163], a[132:135], v[100:103]
ds_read_b64_tr_b16 v[188:189], v31 offset:16384
v_mfma_f32_16x16x32_bf16 v[100:103], v[164:167], a[140:143], v[100:103]
ds_read_b64_tr_b16 v[190:191], v31 offset:16896
s_add_u32 s62, s60, 3407872
v_mfma_f32_16x16x32_bf16 v[104:107], v[168:171], a[128:131], v[104:107]
ds_read_b64_tr_b16 v[160:161], v28 offset:9216
buffer_load_dwordx4 a[208:211], v2, s[96:99], s62 offen sc1
v_mfma_f32_16x16x32_bf16 v[104:107], v[172:175], a[136:139], v[104:107]
ds_read_b64_tr_b16 v[162:163], v28 offset:9728
v_mfma_f32_16x16x32_bf16 v[108:111], v[168:171], a[132:135], v[108:111]
ds_read_b64_tr_b16 v[164:165], v28 offset:17408
v_mfma_f32_16x16x32_bf16 v[108:111], v[172:175], a[140:143], v[108:111]
s_waitcnt lgkmcnt(3)
ds_read_b64_tr_b16 v[166:167], v28 offset:17920
v_mfma_f32_16x16x32_bf16 v[112:115], v[176:179], a[128:131], v[112:115]
ds_read_b64_tr_b16 v[168:169], v29 offset:9216
buffer_load_dwordx4 a[212:215], v2, s[96:99], s62 offen offset:512 sc1
v_mfma_f32_16x16x32_bf16 v[112:115], v[180:183], a[136:139], v[112:115]
ds_read_b64_tr_b16 v[170:171], v29 offset:9728
v_mfma_f32_16x16x32_bf16 v[116:119], v[176:179], a[132:135], v[116:119]
ds_read_b64_tr_b16 v[172:173], v29 offset:17408
v_mfma_f32_16x16x32_bf16 v[116:119], v[180:183], a[140:143], v[116:119]
ds_read_b64_tr_b16 v[174:175], v29 offset:17920
v_mfma_f32_16x16x32_bf16 v[120:123], v[184:187], a[128:131], v[120:123]
ds_read_b64_tr_b16 v[176:177], v30 offset:9216
v_mfma_f32_16x16x32_bf16 v[120:123], v[188:191], a[136:139], v[120:123]
ds_read_b64_tr_b16 v[178:179], v30 offset:9728
s_add_u32 s62, s60, 3538944
v_mfma_f32_16x16x32_bf16 v[124:127], v[184:187], a[132:135], v[124:127]
)ASM"
R"ASM(
ds_read_b64_tr_b16 v[180:181], v30 offset:17408
buffer_load_dwordx4 a[216:219], v2, s[96:99], s62 offen sc1
v_mfma_f32_16x16x32_bf16 v[124:127], v[188:191], a[140:143], v[124:127]
s_waitcnt lgkmcnt(3)
ds_read_b64_tr_b16 v[182:183], v30 offset:17920
v_mfma_f32_16x16x32_bf16 v[128:131], v[160:163], a[128:131], v[128:131]
ds_read_b64_tr_b16 v[184:185], v31 offset:9216
v_mfma_f32_16x16x32_bf16 v[128:131], v[164:167], a[136:139], v[128:131]
ds_read_b64_tr_b16 v[186:187], v31 offset:9728
v_mfma_f32_16x16x32_bf16 v[132:135], v[160:163], a[132:135], v[132:135]
ds_read_b64_tr_b16 v[188:189], v31 offset:17408
buffer_load_dwordx4 a[220:223], v2, s[96:99], s62 offen offset:512 sc1
v_mfma_f32_16x16x32_bf16 v[132:135], v[164:167], a[140:143], v[132:135]
ds_read_b64_tr_b16 v[190:191], v31 offset:17920
v_mfma_f32_16x16x32_bf16 v[136:139], v[168:171], a[128:131], v[136:139]
ds_read_b64_tr_b16 v[160:161], v28 offset:24576
v_mfma_f32_16x16x32_bf16 v[136:139], v[172:175], a[136:139], v[136:139]
ds_read_b64_tr_b16 v[162:163], v28 offset:25088
v_mfma_f32_16x16x32_bf16 v[140:143], v[168:171], a[132:135], v[140:143]
ds_read_b64_tr_b16 v[164:165], v28 offset:32768
v_mfma_f32_16x16x32_bf16 v[140:143], v[172:175], a[140:143], v[140:143]
s_waitcnt lgkmcnt(3)
ds_read_b64_tr_b16 v[166:167], v28 offset:33280
v_mfma_f32_16x16x32_bf16 v[144:147], v[176:179], a[128:131], v[144:147]
ds_read_b64_tr_b16 v[168:169], v29 offset:24576
s_add_u32 s62, s60, 3670016
v_mfma_f32_16x16x32_bf16 v[144:147], v[180:183], a[136:139], v[144:147]
ds_read_b64_tr_b16 v[170:171], v29 offset:25088
v_mfma_f32_16x16x32_bf16 v[148:151], v[176:179], a[132:135], v[148:151]
buffer_load_dword v17, v15, s[96:99], 0 offen sc1
ds_read_b64_tr_b16 v[172:173], v29 offset:32768
v_mfma_f32_16x16x32_bf16 v[148:151], v[180:183], a[140:143], v[148:151]
ds_read_b64_tr_b16 v[174:175], v29 offset:33280
v_mfma_f32_16x16x32_bf16 v[152:155], v[184:187], a[128:131], v[152:155]
ds_read_b64_tr_b16 v[176:177], v30 offset:24576
v_mfma_f32_16x16x32_bf16 v[152:155], v[188:191], a[136:139], v[152:155]
ds_read_b64_tr_b16 v[178:179], v30 offset:25088
v_mfma_f32_16x16x32_bf16 v[156:159], v[184:187], a[132:135], v[156:159]
ds_read_b64_tr_b16 v[180:181], v30 offset:32768
buffer_load_dwordx4 a[224:227], v2, s[96:99], s62 offen sc1
v_mfma_f32_16x16x32_bf16 v[156:159], v[188:191], a[140:143], v[156:159]
s_waitcnt lgkmcnt(3)
ds_read_b64_tr_b16 v[182:183], v30 offset:33280
s_waitcnt vmcnt(37)
v_mfma_f32_16x16x32_bf16 v[96:99], v[160:163], a[144:147], v[96:99]
ds_read_b64_tr_b16 v[184:185], v31 offset:24576
s_waitcnt vmcnt(35)
v_mfma_f32_16x16x32_bf16 v[96:99], v[164:167], a[152:155], v[96:99]
ds_read_b64_tr_b16 v[186:187], v31 offset:25088
v_mfma_f32_16x16x32_bf16 v[100:103], v[160:163], a[148:151], v[100:103]
ds_read_b64_tr_b16 v[188:189], v31 offset:32768
buffer_load_dwordx4 a[228:231], v2, s[96:99], s62 offen offset:512 sc1
s_waitcnt vmcnt(35)
v_mfma_f32_16x16x32_bf16 v[100:103], v[164:167], a[156:159], v[100:103]
ds_read_b64_tr_b16 v[190:191], v31 offset:33280
s_add_u32 s62, s60, 3801088
v_mfma_f32_16x16x32_bf16 v[104:107], v[168:171], a[144:147], v[104:107]
ds_read_b64_tr_b16 v[160:161], v28 offset:25600
v_mfma_f32_16x16x32_bf16 v[104:107], v[172:175], a[152:155], v[104:107]
ds_read_b64_tr_b16 v[162:163], v28 offset:26112
v_mfma_f32_16x16x32_bf16 v[108:111], v[168:171], a[148:151], v[108:111]
ds_read_b64_tr_b16 v[164:165], v28 offset:33792
buffer_load_dwordx4 a[232:235], v2, s[96:99], s62 offen sc1
v_mfma_f32_16x16x32_bf16 v[108:111], v[172:175], a[156:159], v[108:111]
s_waitcnt lgkmcnt(3)
ds_read_b64_tr_b16 v[166:167], v28 offset:34304
v_mfma_f32_16x16x32_bf16 v[112:115], v[176:179], a[144:147], v[112:115]
ds_read_b64_tr_b16 v[168:169], v29 offset:25600
v_mfma_f32_16x16x32_bf16 v[112:115], v[180:183], a[152:155], v[112:115]
ds_read_b64_tr_b16 v[170:171], v29 offset:26112
v_mfma_f32_16x16x32_bf16 v[116:119], v[176:179], a[148:151], v[116:119]
ds_read_b64_tr_b16 v[172:173], v29 offset:33792
buffer_load_dwordx4 a[236:239], v2, s[96:99], s62 offen offset:512 sc1
v_mfma_f32_16x16x32_bf16 v[116:119], v[180:183], a[156:159], v[116:119]
ds_read_b64_tr_b16 v[174:175], v29 offset:34304
v_mfma_f32_16x16x32_bf16 v[120:123], v[184:187], a[144:147], v[120:123]
ds_read_b64_tr_b16 v[176:177], v30 offset:25600
s_add_u32 m0, s61, 57344
v_mfma_f32_16x16x32_bf16 v[120:123], v[188:191], a[152:155], v[120:123]
ds_read_b64_tr_b16 v[178:179], v30 offset:26112
v_mfma_f32_16x16x32_bf16 v[124:127], v[184:187], a[148:151], v[124:127]
ds_read_b64_tr_b16 v[180:181], v30 offset:33792
buffer_load_dwordx4 v3, s[44:47], s32 offen offset:0 lds
v_mfma_f32_16x16x32_bf16 v[124:127], v[188:191], a[156:159], v[124:127]
s_waitcnt lgkmcnt(3)
ds_read_b64_tr_b16 v[182:183], v30 offset:34304
v_mfma_f32_16x16x32_bf16 v[128:131], v[160:163], a[144:147], v[128:131]
ds_read_b64_tr_b16 v[184:185], v31 offset:25600
v_mfma_f32_16x16x32_bf16 v[128:131], v[164:167], a[152:155], v[128:131]
ds_read_b64_tr_b16 v[186:187], v31 offset:26112
v_mfma_f32_16x16x32_bf16 v[132:135], v[160:163], a[148:151], v[132:135]
ds_read_b64_tr_b16 v[188:189], v31 offset:33792
buffer_load_dwordx4 v4, s[44:47], s32 offen offset:1024 lds
v_mfma_f32_16x16x32_bf16 v[132:135], v[164:167], a[156:159], v[132:135]
ds_read_b64_tr_b16 v[190:191], v31 offset:34304
v_mfma_f32_16x16x32_bf16 v[136:139], v[168:171], a[144:147], v[136:139]
s_waitcnt vmcnt(27) lgkmcnt(0)
s_barrier
v_mfma_f32_16x16x32_bf16 v[136:139], v[172:175], a[152:155], v[136:139]
ds_read_b64_tr_b16 v[160:161], v28 offset:40960
v_mfma_f32_16x16x32_bf16 v[140:143], v[168:171], a[148:151], v[140:143]
ds_read_b64_tr_b16 v[162:163], v28 offset:41472
s_add_u32 m0, s61, 65536
v_mfma_f32_16x16x32_bf16 v[140:143], v[172:175], a[156:159], v[140:143]
ds_read_b64_tr_b16 v[164:165], v192 offset:0
ds_read_b64_tr_b16 v[166:167], v192 offset:512
v_mfma_f32_16x16x32_bf16 v[144:147], v[176:179], a[144:147], v[144:147]
ds_read_b64_tr_b16 v[168:169], v29 offset:40960
buffer_load_dwordx4 v3, s[44:47], s33 offen offset:0 lds
v_mfma_f32_16x16x32_bf16 v[144:147], v[180:183], a[152:155], v[144:147]
ds_read_b64_tr_b16 v[170:171], v29 offset:41472
v_mfma_f32_16x16x32_bf16 v[148:151], v[176:179], a[148:151], v[148:151]
ds_read_b64_tr_b16 v[172:173], v193 offset:0
v_mfma_f32_16x16x32_bf16 v[148:151], v[180:183], a[156:159], v[148:151]
ds_read_b64_tr_b16 v[174:175], v193 offset:512
v_mfma_f32_16x16x32_bf16 v[152:155], v[184:187], a[144:147], v[152:155]
ds_read_b64_tr_b16 v[176:177], v30 offset:40960
buffer_load_dwordx4 v4, s[44:47], s33 offen offset:1024 lds
v_mfma_f32_16x16x32_bf16 v[152:155], v[188:191], a[152:155], v[152:155]
ds_read_b64_tr_b16 v[178:179], v30 offset:41472
v_mfma_f32_16x16x32_bf16 v[156:159], v[184:187], a[148:151], v[156:159]
ds_read_b64_tr_b16 v[180:181], v194 offset:0
v_mfma_f32_16x16x32_bf16 v[156:159], v[188:191], a[156:159], v[156:159]
s_waitcnt lgkmcnt(3)
ds_read_b64_tr_b16 v[182:183], v194 offset:512
v_mfma_f32_16x16x32_bf16 v[96:99], v[160:163], a[160:163], v[96:99]
ds_read_b64_tr_b16 v[184:185], v31 offset:40960
s_add_u32 m0, s61, 73728
v_mfma_f32_16x16x32_bf16 v[96:99], v[164:167], a[168:171], v[96:99]
ds_read_b64_tr_b16 v[186:187], v31 offset:41472
s_add_u32 s63, s95, 0
v_mfma_f32_16x16x32_bf16 v[100:103], v[160:163], a[164:167], v[100:103]
ds_read_b64_tr_b16 v[188:189], v195 offset:0
buffer_load_dwordx4 v23, s[48:51], s63 offen offset:0 lds
v_mfma_f32_16x16x32_bf16 v[100:103], v[164:167], a[172:175], v[100:103]
ds_read_b64_tr_b16 v[190:191], v195 offset:512
v_mfma_f32_16x16x32_bf16 v[104:107], v[168:171], a[160:163], v[104:107]
ds_read_b64_tr_b16 v[160:161], v28 offset:41984
v_mfma_f32_16x16x32_bf16 v[104:107], v[172:175], a[168:171], v[104:107]
ds_read_b64_tr_b16 v[162:163], v28 offset:42496
v_mfma_f32_16x16x32_bf16 v[108:111], v[168:171], a[164:167], v[108:111]
ds_read_b64_tr_b16 v[164:165], v192 offset:1024
buffer_load_dwordx4 v6, s[48:51], s63 offen offset:1024 lds
v_mfma_f32_16x16x32_bf16 v[108:111], v[172:175], a[172:175], v[108:111]
s_waitcnt lgkmcnt(3)
ds_read_b64_tr_b16 v[166:167], v192 offset:1536
v_mfma_f32_16x16x32_bf16 v[112:115], v[176:179], a[160:163], v[112:115]
ds_read_b64_tr_b16 v[168:169], v29 offset:41984
v_mfma_f32_16x16x32_bf16 v[112:115], v[180:183], a[168:171], v[112:115]
ds_read_b64_tr_b16 v[170:171], v29 offset:42496
v_mfma_f32_16x16x32_bf16 v[116:119], v[176:179], a[164:167], v[116:119]
ds_read_b64_tr_b16 v[172:173], v193 offset:1024
s_add_u32 m0, s61, 81920
v_mfma_f32_16x16x32_bf16 v[116:119], v[180:183], a[172:175], v[116:119]
ds_read_b64_tr_b16 v[174:175], v193 offset:1536
s_add_u32 s63, s95, 73728
v_mfma_f32_16x16x32_bf16 v[120:123], v[184:187], a[160:163], v[120:123]
ds_read_b64_tr_b16 v[176:177], v30 offset:41984
buffer_load_dwordx4 v23, s[48:51], s63 offen offset:0 lds
v_mfma_f32_16x16x32_bf16 v[120:123], v[188:191], a[168:171], v[120:123]
ds_read_b64_tr_b16 v[178:179], v30 offset:42496
v_mfma_f32_16x16x32_bf16 v[124:127], v[184:187], a[164:167], v[124:127]
ds_read_b64_tr_b16 v[180:181], v194 offset:1024
v_mfma_f32_16x16x32_bf16 v[124:127], v[188:191], a[172:175], v[124:127]
s_waitcnt lgkmcnt(3)
ds_read_b64_tr_b16 v[182:183], v194 offset:1536
v_mfma_f32_16x16x32_bf16 v[128:131], v[160:163], a[160:163], v[128:131]
ds_read_b64_tr_b16 v[184:185], v31 offset:41984
buffer_load_dwordx4 v6, s[48:51], s63 offen offset:1024 lds
v_mfma_f32_16x16x32_bf16 v[128:131], v[164:167], a[168:171], v[128:131]
ds_read_b64_tr_b16 v[186:187], v31 offset:42496
v_mfma_f32_16x16x32_bf16 v[132:135], v[160:163], a[164:167], v[132:135]
ds_read_b64_tr_b16 v[188:189], v195 offset:1024
v_mfma_f32_16x16x32_bf16 v[132:135], v[164:167], a[172:175], v[132:135]
ds_read_b64_tr_b16 v[190:191], v195 offset:1536
v_mfma_f32_16x16x32_bf16 v[136:139], v[168:171], a[160:163], v[136:139]
ds_read_b64_tr_b16 v[160:161], v192 offset:8192
s_add_u32 s62, s70, 2097152
v_mfma_f32_16x16x32_bf16 v[136:139], v[172:175], a[168:171], v[136:139]
ds_read_b64_tr_b16 v[162:163], v192 offset:8704
buffer_load_dwordx4 a[240:243], v2, s[96:99], s62 offen sc1
v_mfma_f32_16x16x32_bf16 v[140:143], v[168:171], a[164:167], v[140:143]
ds_read_b64_tr_b16 v[164:165], v192 offset:16384
v_mfma_f32_16x16x32_bf16 v[140:143], v[172:175], a[172:175], v[140:143]
s_waitcnt lgkmcnt(3)
ds_read_b64_tr_b16 v[166:167], v192 offset:16896
v_mfma_f32_16x16x32_bf16 v[144:147], v[176:179], a[160:163], v[144:147]
ds_read_b64_tr_b16 v[168:169], v193 offset:8192
v_mfma_f32_16x16x32_bf16 v[144:147], v[180:183], a[168:171], v[144:147]
ds_read_b64_tr_b16 v[170:171], v193 offset:8704
buffer_load_dwordx4 a[244:247], v2, s[96:99], s62 offen offset:512 sc1
v_mfma_f32_16x16x32_bf16 v[148:151], v[176:179], a[164:167], v[148:151]
ds_read_b64_tr_b16 v[172:173], v193 offset:16384
v_mfma_f32_16x16x32_bf16 v[148:151], v[180:183], a[172:175], v[148:151]
ds_read_b64_tr_b16 v[174:175], v193 offset:16896
v_mfma_f32_16x16x32_bf16 v[152:155], v[184:187], a[160:163], v[152:155]
ds_read_b64_tr_b16 v[176:177], v194 offset:8192
v_mfma_f32_16x16x32_bf16 v[152:155], v[188:191], a[168:171], v[152:155]
ds_read_b64_tr_b16 v[178:179], v194 offset:8704
v_mfma_f32_16x16x32_bf16 v[156:159], v[184:187], a[164:167], v[156:159]
)ASM"
R"ASM(
ds_read_b64_tr_b16 v[180:181], v194 offset:16384
s_add_u32 s62, s70, 2228224
v_mfma_f32_16x16x32_bf16 v[156:159], v[188:191], a[172:175], v[156:159]
s_waitcnt lgkmcnt(3)
ds_read_b64_tr_b16 v[182:183], v194 offset:16896
s_waitcnt vmcnt(32)
v_mfma_f32_16x16x32_bf16 v[96:99], v[160:163], a[176:179], v[96:99]
ds_read_b64_tr_b16 v[184:185], v195 offset:8192
buffer_load_dwordx4 a[248:251], v2, s[96:99], s62 offen sc1
s_waitcnt vmcnt(31)
v_mfma_f32_16x16x32_bf16 v[96:99], v[164:167], a[184:187], v[96:99]
ds_read_b64_tr_b16 v[186:187], v195 offset:8704
v_mfma_f32_16x16x32_bf16 v[100:103], v[160:163], a[180:183], v[100:103]
ds_read_b64_tr_b16 v[188:189], v195 offset:16384
s_waitcnt vmcnt(30)
v_mfma_f32_16x16x32_bf16 v[100:103], v[164:167], a[188:191], v[100:103]
ds_read_b64_tr_b16 v[190:191], v195 offset:16896
v_mfma_f32_16x16x32_bf16 v[104:107], v[168:171], a[176:179], v[104:107]
ds_read_b64_tr_b16 v[160:161], v192 offset:9216
buffer_load_dwordx4 a[252:255], v2, s[96:99], s62 offen offset:512 sc1
v_mfma_f32_16x16x32_bf16 v[104:107], v[172:175], a[184:187], v[104:107]
ds_read_b64_tr_b16 v[162:163], v192 offset:9728
v_mfma_f32_16x16x32_bf16 v[108:111], v[168:171], a[180:183], v[108:111]
ds_read_b64_tr_b16 v[164:165], v192 offset:17408
v_mfma_f32_16x16x32_bf16 v[108:111], v[172:175], a[188:191], v[108:111]
s_waitcnt lgkmcnt(3)
ds_read_b64_tr_b16 v[166:167], v192 offset:17920
v_mfma_f32_16x16x32_bf16 v[112:115], v[176:179], a[176:179], v[112:115]
ds_read_b64_tr_b16 v[168:169], v193 offset:9216
s_add_u32 s62, s70, 4456448
v_mfma_f32_16x16x32_bf16 v[112:115], v[180:183], a[184:187], v[112:115]
ds_read_b64_tr_b16 v[170:171], v193 offset:9728
buffer_load_dwordx4 a[0:3], v2, s[96:99], s62 offen sc1
v_mfma_f32_16x16x32_bf16 v[116:119], v[176:179], a[180:183], v[116:119]
ds_read_b64_tr_b16 v[172:173], v193 offset:17408
v_mfma_f32_16x16x32_bf16 v[116:119], v[180:183], a[188:191], v[116:119]
ds_read_b64_tr_b16 v[174:175], v193 offset:17920
v_mfma_f32_16x16x32_bf16 v[120:123], v[184:187], a[176:179], v[120:123]
ds_read_b64_tr_b16 v[176:177], v194 offset:9216
v_mfma_f32_16x16x32_bf16 v[120:123], v[188:191], a[184:187], v[120:123]
ds_read_b64_tr_b16 v[178:179], v194 offset:9728
buffer_load_dwordx4 a[4:7], v2, s[96:99], s62 offen offset:512 sc1
v_mfma_f32_16x16x32_bf16 v[124:127], v[184:187], a[180:183], v[124:127]
ds_read_b64_tr_b16 v[180:181], v194 offset:17408
v_mfma_f32_16x16x32_bf16 v[124:127], v[188:191], a[188:191], v[124:127]
s_waitcnt lgkmcnt(3)
ds_read_b64_tr_b16 v[182:183], v194 offset:17920
v_mfma_f32_16x16x32_bf16 v[128:131], v[160:163], a[176:179], v[128:131]
ds_read_b64_tr_b16 v[184:185], v195 offset:9216
v_mfma_f32_16x16x32_bf16 v[128:131], v[164:167], a[184:187], v[128:131]
ds_read_b64_tr_b16 v[186:187], v195 offset:9728
v_mfma_f32_16x16x32_bf16 v[132:135], v[160:163], a[180:183], v[132:135]
ds_read_b64_tr_b16 v[188:189], v195 offset:17408
s_add_u32 s62, s70, 4587520
v_mfma_f32_16x16x32_bf16 v[132:135], v[164:167], a[188:191], v[132:135]
ds_read_b64_tr_b16 v[190:191], v195 offset:17920
buffer_load_dwordx4 a[8:11], v2, s[96:99], s62 offen sc1
v_mfma_f32_16x16x32_bf16 v[136:139], v[168:171], a[176:179], v[136:139]
s_waitcnt vmcnt(24) lgkmcnt(0)
s_barrier
s_mov_b32 s65, 20000
s_waitcnt vmcnt(19)
LHC7_%=:
v_cmp_ne_u32 s[92:93], s43, v17
s_nop 3
s_and_b32 s92, s92, 0x0
s_and_b32 s93, s93, 0xc
s_cmp_eq_u64 s[92:93], 0
s_cbranch_scc1 LHD7_%=
s_sub_u32 s65, s65, 1
s_cmp_eq_u32 s65, 0
s_cbranch_scc1 LHD7_%=
s_sleep 2
buffer_load_dword v17, v15, s[96:99], 0 offen sc1
s_waitcnt vmcnt(0)
s_branch LHC7_%=
LHD7_%=:
v_mfma_f32_16x16x32_bf16 v[136:139], v[172:175], a[184:187], v[136:139]
ds_read_b64_tr_b16 v[160:161], v192 offset:24576
v_mfma_f32_16x16x32_bf16 v[140:143], v[168:171], a[180:183], v[140:143]
ds_read_b64_tr_b16 v[162:163], v192 offset:25088
v_mfma_f32_16x16x32_bf16 v[140:143], v[172:175], a[188:191], v[140:143]
ds_read_b64_tr_b16 v[164:165], v192 offset:32768
ds_read_b64_tr_b16 v[166:167], v192 offset:33280
v_mfma_f32_16x16x32_bf16 v[144:147], v[176:179], a[176:179], v[144:147]
ds_read_b64_tr_b16 v[168:169], v193 offset:24576
buffer_load_dwordx4 a[12:15], v2, s[96:99], s62 offen offset:512 sc1
v_mfma_f32_16x16x32_bf16 v[144:147], v[180:183], a[184:187], v[144:147]
ds_read_b64_tr_b16 v[170:171], v193 offset:25088
v_mfma_f32_16x16x32_bf16 v[148:151], v[176:179], a[180:183], v[148:151]
ds_read_b64_tr_b16 v[172:173], v193 offset:32768
v_mfma_f32_16x16x32_bf16 v[148:151], v[180:183], a[188:191], v[148:151]
ds_read_b64_tr_b16 v[174:175], v193 offset:33280
v_mfma_f32_16x16x32_bf16 v[152:155], v[184:187], a[176:179], v[152:155]
ds_read_b64_tr_b16 v[176:177], v194 offset:24576
s_add_u32 m0, s61, 90112
v_mfma_f32_16x16x32_bf16 v[152:155], v[188:191], a[184:187], v[152:155]
ds_read_b64_tr_b16 v[178:179], v194 offset:25088
s_add_u32 s63, s95, 0
v_mfma_f32_16x16x32_bf16 v[156:159], v[184:187], a[180:183], v[156:159]
ds_read_b64_tr_b16 v[180:181], v194 offset:32768
buffer_load_dwordx4 v23, s[84:87], s63 offen offset:0 lds
v_mfma_f32_16x16x32_bf16 v[156:159], v[188:191], a[188:191], v[156:159]
s_waitcnt lgkmcnt(3)
ds_read_b64_tr_b16 v[182:183], v194 offset:33280
v_mfma_f32_16x16x32_bf16 v[96:99], v[160:163], a[192:195], v[96:99]
ds_read_b64_tr_b16 v[184:185], v195 offset:24576
v_mfma_f32_16x16x32_bf16 v[96:99], v[164:167], a[200:203], v[96:99]
ds_read_b64_tr_b16 v[186:187], v195 offset:25088
v_mfma_f32_16x16x32_bf16 v[100:103], v[160:163], a[196:199], v[100:103]
ds_read_b64_tr_b16 v[188:189], v195 offset:32768
buffer_load_dwordx4 v6, s[84:87], s63 offen offset:1024 lds
v_mfma_f32_16x16x32_bf16 v[100:103], v[164:167], a[204:207], v[100:103]
ds_read_b64_tr_b16 v[190:191], v195 offset:33280
v_mfma_f32_16x16x32_bf16 v[104:107], v[168:171], a[192:195], v[104:107]
ds_read_b64_tr_b16 v[160:161], v192 offset:25600
v_mfma_f32_16x16x32_bf16 v[104:107], v[172:175], a[200:203], v[104:107]
ds_read_b64_tr_b16 v[162:163], v192 offset:26112
v_mfma_f32_16x16x32_bf16 v[108:111], v[168:171], a[196:199], v[108:111]
ds_read_b64_tr_b16 v[164:165], v192 offset:33792
s_add_u32 m0, s61, 98304
v_mfma_f32_16x16x32_bf16 v[108:111], v[172:175], a[204:207], v[108:111]
s_waitcnt lgkmcnt(3)
ds_read_b64_tr_b16 v[166:167], v192 offset:34304
v_mfma_f32_16x16x32_bf16 v[112:115], v[176:179], a[192:195], v[112:115]
ds_read_b64_tr_b16 v[168:169], v193 offset:25600
s_add_u32 s63, s95, 73728
v_mfma_f32_16x16x32_bf16 v[112:115], v[180:183], a[200:203], v[112:115]
ds_read_b64_tr_b16 v[170:171], v193 offset:26112
buffer_load_dwordx4 v23, s[84:87], s63 offen offset:0 lds
v_mfma_f32_16x16x32_bf16 v[116:119], v[176:179], a[196:199], v[116:119]
ds_read_b64_tr_b16 v[172:173], v193 offset:33792
v_mfma_f32_16x16x32_bf16 v[116:119], v[180:183], a[204:207], v[116:119]
ds_read_b64_tr_b16 v[174:175], v193 offset:34304
v_mfma_f32_16x16x32_bf16 v[120:123], v[184:187], a[192:195], v[120:123]
ds_read_b64_tr_b16 v[176:177], v194 offset:25600
v_mfma_f32_16x16x32_bf16 v[120:123], v[188:191], a[200:203], v[120:123]
ds_read_b64_tr_b16 v[178:179], v194 offset:26112
buffer_load_dwordx4 v6, s[84:87], s63 offen offset:1024 lds
v_mfma_f32_16x16x32_bf16 v[124:127], v[184:187], a[196:199], v[124:127]
ds_read_b64_tr_b16 v[180:181], v194 offset:33792
v_mfma_f32_16x16x32_bf16 v[124:127], v[188:191], a[204:207], v[124:127]
s_waitcnt lgkmcnt(3)
ds_read_b64_tr_b16 v[182:183], v194 offset:34304
v_mfma_f32_16x16x32_bf16 v[128:131], v[160:163], a[192:195], v[128:131]
ds_read_b64_tr_b16 v[184:185], v195 offset:25600
s_add_u32 m0, s61, 106496
v_mfma_f32_16x16x32_bf16 v[128:131], v[164:167], a[200:203], v[128:131]
ds_read_b64_tr_b16 v[186:187], v195 offset:26112
buffer_load_dwordx4 v3, s[44:47], s38 offen offset:0 lds
v_mfma_f32_16x16x32_bf16 v[132:135], v[160:163], a[196:199], v[132:135]
ds_read_b64_tr_b16 v[188:189], v195 offset:33792
v_mfma_f32_16x16x32_bf16 v[132:135], v[164:167], a[204:207], v[132:135]
ds_read_b64_tr_b16 v[190:191], v195 offset:34304
v_mfma_f32_16x16x32_bf16 v[136:139], v[168:171], a[192:195], v[136:139]
ds_read_b64_tr_b16 v[160:161], v192 offset:40960
v_mfma_f32_16x16x32_bf16 v[136:139], v[172:175], a[200:203], v[136:139]
ds_read_b64_tr_b16 v[162:163], v192 offset:41472
buffer_load_dwordx4 v4, s[44:47], s38 offen offset:1024 lds
v_mfma_f32_16x16x32_bf16 v[140:143], v[168:171], a[196:199], v[140:143]
ds_read_b64_tr_b16 v[164:165], v196 offset:0
v_mfma_f32_16x16x32_bf16 v[140:143], v[172:175], a[204:207], v[140:143]
s_waitcnt lgkmcnt(3)
ds_read_b64_tr_b16 v[166:167], v196 offset:512
v_mfma_f32_16x16x32_bf16 v[144:147], v[176:179], a[192:195], v[144:147]
ds_read_b64_tr_b16 v[168:169], v193 offset:40960
v_mfma_f32_16x16x32_bf16 v[144:147], v[180:183], a[200:203], v[144:147]
ds_read_b64_tr_b16 v[170:171], v193 offset:41472
v_mfma_f32_16x16x32_bf16 v[148:151], v[176:179], a[196:199], v[148:151]
ds_read_b64_tr_b16 v[172:173], v197 offset:0
s_add_u32 m0, s61, 114688
v_mfma_f32_16x16x32_bf16 v[148:151], v[180:183], a[204:207], v[148:151]
ds_read_b64_tr_b16 v[174:175], v197 offset:512
buffer_load_dwordx4 v3, s[44:47], s39 offen offset:0 lds
v_mfma_f32_16x16x32_bf16 v[152:155], v[184:187], a[192:195], v[152:155]
ds_read_b64_tr_b16 v[176:177], v194 offset:40960
v_mfma_f32_16x16x32_bf16 v[152:155], v[188:191], a[200:203], v[152:155]
ds_read_b64_tr_b16 v[178:179], v194 offset:41472
v_mfma_f32_16x16x32_bf16 v[156:159], v[184:187], a[196:199], v[156:159]
ds_read_b64_tr_b16 v[180:181], v198 offset:0
v_mfma_f32_16x16x32_bf16 v[156:159], v[188:191], a[204:207], v[156:159]
s_waitcnt lgkmcnt(3)
ds_read_b64_tr_b16 v[182:183], v198 offset:512
v_mfma_f32_16x16x32_bf16 v[96:99], v[160:163], a[208:211], v[96:99]
ds_read_b64_tr_b16 v[184:185], v195 offset:40960
buffer_load_dwordx4 v4, s[44:47], s39 offen offset:1024 lds
v_mfma_f32_16x16x32_bf16 v[96:99], v[164:167], a[216:219], v[96:99]
ds_read_b64_tr_b16 v[186:187], v195 offset:41472
v_mfma_f32_16x16x32_bf16 v[100:103], v[160:163], a[212:215], v[100:103]
ds_read_b64_tr_b16 v[188:189], v199 offset:0
v_mfma_f32_16x16x32_bf16 v[100:103], v[164:167], a[220:223], v[100:103]
ds_read_b64_tr_b16 v[190:191], v199 offset:512
v_mfma_f32_16x16x32_bf16 v[104:107], v[168:171], a[208:211], v[104:107]
ds_read_b64_tr_b16 v[160:161], v192 offset:41984
s_add_u32 s62, s60, 3932160
v_mfma_f32_16x16x32_bf16 v[104:107], v[172:175], a[216:219], v[104:107]
ds_read_b64_tr_b16 v[162:163], v192 offset:42496
buffer_load_dwordx4 a[16:19], v2, s[96:99], s62 offen sc1
v_mfma_f32_16x16x32_bf16 v[108:111], v[168:171], a[212:215], v[108:111]
ds_read_b64_tr_b16 v[164:165], v196 offset:1024
)ASM"
R"ASM(
v_mfma_f32_16x16x32_bf16 v[108:111], v[172:175], a[220:223], v[108:111]
s_waitcnt lgkmcnt(3)
ds_read_b64_tr_b16 v[166:167], v196 offset:1536
v_mfma_f32_16x16x32_bf16 v[112:115], v[176:179], a[208:211], v[112:115]
ds_read_b64_tr_b16 v[168:169], v193 offset:41984
v_mfma_f32_16x16x32_bf16 v[112:115], v[180:183], a[216:219], v[112:115]
ds_read_b64_tr_b16 v[170:171], v193 offset:42496
buffer_load_dwordx4 a[20:23], v2, s[96:99], s62 offen offset:512 sc1
v_mfma_f32_16x16x32_bf16 v[116:119], v[176:179], a[212:215], v[116:119]
ds_read_b64_tr_b16 v[172:173], v197 offset:1024
v_mfma_f32_16x16x32_bf16 v[116:119], v[180:183], a[220:223], v[116:119]
ds_read_b64_tr_b16 v[174:175], v197 offset:1536
v_mfma_f32_16x16x32_bf16 v[120:123], v[184:187], a[208:211], v[120:123]
ds_read_b64_tr_b16 v[176:177], v194 offset:41984
v_mfma_f32_16x16x32_bf16 v[120:123], v[188:191], a[216:219], v[120:123]
ds_read_b64_tr_b16 v[178:179], v194 offset:42496
v_mfma_f32_16x16x32_bf16 v[124:127], v[184:187], a[212:215], v[124:127]
ds_read_b64_tr_b16 v[180:181], v198 offset:1024
s_add_u32 s62, s60, 4063232
v_mfma_f32_16x16x32_bf16 v[124:127], v[188:191], a[220:223], v[124:127]
s_waitcnt lgkmcnt(3)
ds_read_b64_tr_b16 v[182:183], v198 offset:1536
v_mfma_f32_16x16x32_bf16 v[128:131], v[160:163], a[208:211], v[128:131]
ds_read_b64_tr_b16 v[184:185], v195 offset:41984
buffer_load_dwordx4 a[24:27], v2, s[96:99], s62 offen sc1
v_mfma_f32_16x16x32_bf16 v[128:131], v[164:167], a[216:219], v[128:131]
ds_read_b64_tr_b16 v[186:187], v195 offset:42496
v_mfma_f32_16x16x32_bf16 v[132:135], v[160:163], a[212:215], v[132:135]
ds_read_b64_tr_b16 v[188:189], v199 offset:1024
v_mfma_f32_16x16x32_bf16 v[132:135], v[164:167], a[220:223], v[132:135]
ds_read_b64_tr_b16 v[190:191], v199 offset:1536
v_mfma_f32_16x16x32_bf16 v[136:139], v[168:171], a[208:211], v[136:139]
s_waitcnt vmcnt(19) lgkmcnt(0)
s_barrier
v_mfma_f32_16x16x32_bf16 v[136:139], v[172:175], a[216:219], v[136:139]
ds_read_b64_tr_b16 v[160:161], v196 offset:8192
buffer_load_dwordx4 a[28:31], v2, s[96:99], s62 offen offset:512 sc1
v_mfma_f32_16x16x32_bf16 v[140:143], v[168:171], a[212:215], v[140:143]
ds_read_b64_tr_b16 v[162:163], v196 offset:8704
v_mfma_f32_16x16x32_bf16 v[140:143], v[172:175], a[220:223], v[140:143]
ds_read_b64_tr_b16 v[164:165], v196 offset:16384
ds_read_b64_tr_b16 v[166:167], v196 offset:16896
v_mfma_f32_16x16x32_bf16 v[144:147], v[176:179], a[208:211], v[144:147]
ds_read_b64_tr_b16 v[168:169], v197 offset:8192
s_add_u32 m0, s61, 122880
v_mfma_f32_16x16x32_bf16 v[144:147], v[180:183], a[216:219], v[144:147]
ds_read_b64_tr_b16 v[170:171], v197 offset:8704
buffer_load_dwordx4 v5, s[44:47], s20 offen offset:0 lds
v_mfma_f32_16x16x32_bf16 v[148:151], v[176:179], a[212:215], v[148:151]
ds_read_b64_tr_b16 v[172:173], v197 offset:16384
v_mfma_f32_16x16x32_bf16 v[148:151], v[180:183], a[220:223], v[148:151]
ds_read_b64_tr_b16 v[174:175], v197 offset:16896
v_mfma_f32_16x16x32_bf16 v[152:155], v[184:187], a[208:211], v[152:155]
ds_read_b64_tr_b16 v[176:177], v198 offset:8192
v_mfma_f32_16x16x32_bf16 v[152:155], v[188:191], a[216:219], v[152:155]
ds_read_b64_tr_b16 v[178:179], v198 offset:8704
buffer_load_dwordx4 v27, s[44:47], s21 offen offset:1024 lds
v_mfma_f32_16x16x32_bf16 v[156:159], v[184:187], a[212:215], v[156:159]
ds_read_b64_tr_b16 v[180:181], v198 offset:16384
v_mfma_f32_16x16x32_bf16 v[156:159], v[188:191], a[220:223], v[156:159]
s_waitcnt lgkmcnt(3)
ds_read_b64_tr_b16 v[182:183], v198 offset:16896
v_mfma_f32_16x16x32_bf16 v[96:99], v[160:163], a[224:227], v[96:99]
ds_read_b64_tr_b16 v[184:185], v199 offset:8192
v_mfma_f32_16x16x32_bf16 v[96:99], v[164:167], a[232:235], v[96:99]
ds_read_b64_tr_b16 v[186:187], v199 offset:8704
v_mfma_f32_16x16x32_bf16 v[100:103], v[160:163], a[228:231], v[100:103]
ds_read_b64_tr_b16 v[188:189], v199 offset:16384
s_add_u32 m0, s61, 131072
v_mfma_f32_16x16x32_bf16 v[100:103], v[164:167], a[236:239], v[100:103]
ds_read_b64_tr_b16 v[190:191], v199 offset:16896
buffer_load_dwordx4 v5, s[44:47], s22 offen offset:0 lds
v_mfma_f32_16x16x32_bf16 v[104:107], v[168:171], a[224:227], v[104:107]
ds_read_b64_tr_b16 v[160:161], v196 offset:9216
v_mfma_f32_16x16x32_bf16 v[104:107], v[172:175], a[232:235], v[104:107]
ds_read_b64_tr_b16 v[162:163], v196 offset:9728
v_mfma_f32_16x16x32_bf16 v[108:111], v[168:171], a[228:231], v[108:111]
ds_read_b64_tr_b16 v[164:165], v196 offset:17408
v_mfma_f32_16x16x32_bf16 v[108:111], v[172:175], a[236:239], v[108:111]
s_waitcnt lgkmcnt(3)
ds_read_b64_tr_b16 v[166:167], v196 offset:17920
v_mfma_f32_16x16x32_bf16 v[112:115], v[176:179], a[224:227], v[112:115]
ds_read_b64_tr_b16 v[168:169], v197 offset:9216
buffer_load_dwordx4 v27, s[44:47], s23 offen offset:1024 lds
v_mfma_f32_16x16x32_bf16 v[112:115], v[180:183], a[232:235], v[112:115]
ds_read_b64_tr_b16 v[170:171], v197 offset:9728
v_mfma_f32_16x16x32_bf16 v[116:119], v[176:179], a[228:231], v[116:119]
ds_read_b64_tr_b16 v[172:173], v197 offset:17408
v_mfma_f32_16x16x32_bf16 v[116:119], v[180:183], a[236:239], v[116:119]
ds_read_b64_tr_b16 v[174:175], v197 offset:17920
v_mfma_f32_16x16x32_bf16 v[120:123], v[184:187], a[224:227], v[120:123]
ds_read_b64_tr_b16 v[176:177], v198 offset:9216
s_add_u32 m0, s61, 139264
v_mfma_f32_16x16x32_bf16 v[120:123], v[188:191], a[232:235], v[120:123]
ds_read_b64_tr_b16 v[178:179], v198 offset:9728
buffer_load_dwordx4 v5, s[44:47], s24 offen offset:0 lds
v_mfma_f32_16x16x32_bf16 v[124:127], v[184:187], a[228:231], v[124:127]
ds_read_b64_tr_b16 v[180:181], v198 offset:17408
v_mfma_f32_16x16x32_bf16 v[124:127], v[188:191], a[236:239], v[124:127]
s_waitcnt lgkmcnt(3)
ds_read_b64_tr_b16 v[182:183], v198 offset:17920
v_mfma_f32_16x16x32_bf16 v[128:131], v[160:163], a[224:227], v[128:131]
ds_read_b64_tr_b16 v[184:185], v199 offset:9216
v_mfma_f32_16x16x32_bf16 v[128:131], v[164:167], a[232:235], v[128:131]
ds_read_b64_tr_b16 v[186:187], v199 offset:9728
buffer_load_dwordx4 v27, s[44:47], s25 offen offset:1024 lds
v_mfma_f32_16x16x32_bf16 v[132:135], v[160:163], a[228:231], v[132:135]
ds_read_b64_tr_b16 v[188:189], v199 offset:17408
v_mfma_f32_16x16x32_bf16 v[132:135], v[164:167], a[236:239], v[132:135]
ds_read_b64_tr_b16 v[190:191], v199 offset:17920
v_mfma_f32_16x16x32_bf16 v[136:139], v[168:171], a[224:227], v[136:139]
ds_read_b128 v[160:163], v220 offset:24576
v_mfma_f32_16x16x32_bf16 v[136:139], v[172:175], a[232:235], v[136:139]
v_mfma_f32_16x16x32_bf16 v[140:143], v[168:171], a[228:231], v[140:143]
ds_read_b128 v[164:167], v221 offset:24576
s_add_u32 m0, s61, 0
v_mfma_f32_16x16x32_bf16 v[140:143], v[172:175], a[236:239], v[140:143]
s_waitcnt lgkmcnt(2)
buffer_load_dwordx4 v5, s[44:47], s26 offen offset:0 lds
v_mfma_f32_16x16x32_bf16 v[144:147], v[176:179], a[224:227], v[144:147]
ds_read_b128 v[168:171], v220 offset:26624
v_mfma_f32_16x16x32_bf16 v[144:147], v[180:183], a[232:235], v[144:147]
v_mfma_f32_16x16x32_bf16 v[148:151], v[176:179], a[228:231], v[148:151]
ds_read_b128 v[172:175], v221 offset:26624
v_mfma_f32_16x16x32_bf16 v[148:151], v[180:183], a[236:239], v[148:151]
buffer_load_dwordx4 v27, s[44:47], s27 offen offset:1024 lds
v_mfma_f32_16x16x32_bf16 v[152:155], v[184:187], a[224:227], v[152:155]
ds_read_b128 v[176:179], v220 offset:28672
v_mfma_f32_16x16x32_bf16 v[152:155], v[188:191], a[232:235], v[152:155]
v_mfma_f32_16x16x32_bf16 v[156:159], v[184:187], a[228:231], v[156:159]
ds_read_b128 v[180:183], v221 offset:28672
v_mfma_f32_16x16x32_bf16 v[156:159], v[188:191], a[236:239], v[156:159]
s_waitcnt lgkmcnt(2)
s_waitcnt vmcnt(27)
v_mfma_f32_16x16x32_bf16 v[96:99], v[160:163], a[240:243], v[96:99]
ds_read_b128 v[184:187], v220 offset:30720
s_add_u32 m0, s61, 8192
s_waitcnt vmcnt(25)
v_mfma_f32_16x16x32_bf16 v[96:99], v[164:167], a[248:251], v[96:99]
buffer_load_dwordx4 v5, s[44:47], s28 offen offset:0 lds
v_mfma_f32_16x16x32_bf16 v[100:103], v[160:163], a[244:247], v[100:103]
ds_read_b128 v[188:191], v221 offset:30720
s_waitcnt vmcnt(25)
v_mfma_f32_16x16x32_bf16 v[100:103], v[164:167], a[252:255], v[100:103]
v_mfma_f32_16x16x32_bf16 v[104:107], v[168:171], a[240:243], v[104:107]
ds_read_b128 v[160:163], v220 offset:32768
v_mfma_f32_16x16x32_bf16 v[104:107], v[172:175], a[248:251], v[104:107]
buffer_load_dwordx4 v27, s[44:47], s29 offen offset:1024 lds
v_mfma_f32_16x16x32_bf16 v[108:111], v[168:171], a[244:247], v[108:111]
ds_read_b128 v[164:167], v221 offset:32768
v_mfma_f32_16x16x32_bf16 v[108:111], v[172:175], a[252:255], v[108:111]
s_waitcnt lgkmcnt(2)
v_mfma_f32_16x16x32_bf16 v[112:115], v[176:179], a[240:243], v[112:115]
ds_read_b128 v[168:171], v220 offset:34816
v_mfma_f32_16x16x32_bf16 v[112:115], v[180:183], a[248:251], v[112:115]
v_mfma_f32_16x16x32_bf16 v[116:119], v[176:179], a[244:247], v[116:119]
ds_read_b128 v[172:175], v221 offset:34816
s_add_u32 m0, s61, 16384
v_mfma_f32_16x16x32_bf16 v[116:119], v[180:183], a[252:255], v[116:119]
buffer_load_dwordx4 v5, s[44:47], s30 offen offset:0 lds
v_mfma_f32_16x16x32_bf16 v[120:123], v[184:187], a[240:243], v[120:123]
ds_read_b128 v[176:179], v220 offset:36864
v_mfma_f32_16x16x32_bf16 v[120:123], v[188:191], a[248:251], v[120:123]
v_mfma_f32_16x16x32_bf16 v[124:127], v[184:187], a[244:247], v[124:127]
ds_read_b128 v[180:183], v221 offset:36864
v_mfma_f32_16x16x32_bf16 v[124:127], v[188:191], a[252:255], v[124:127]
s_waitcnt lgkmcnt(2)
buffer_load_dwordx4 v27, s[44:47], s31 offen offset:1024 lds
v_mfma_f32_16x16x32_bf16 v[128:131], v[160:163], a[240:243], v[128:131]
ds_read_b128 v[184:187], v220 offset:38912
v_mfma_f32_16x16x32_bf16 v[128:131], v[164:167], a[248:251], v[128:131]
v_mfma_f32_16x16x32_bf16 v[132:135], v[160:163], a[244:247], v[132:135]
ds_read_b128 v[188:191], v221 offset:38912
v_mfma_f32_16x16x32_bf16 v[132:135], v[164:167], a[252:255], v[132:135]
v_mfma_f32_16x16x32_bf16 v[136:139], v[168:171], a[240:243], v[136:139]
s_waitcnt vmcnt(16) lgkmcnt(0)
s_barrier
v_mfma_f32_16x16x32_bf16 v[136:139], v[172:175], a[248:251], v[136:139]
ds_read_b128 v[160:163], v220 offset:40960
s_add_u32 m0, s61, 24576
v_mfma_f32_16x16x32_bf16 v[140:143], v[168:171], a[244:247], v[140:143]
buffer_load_dwordx4 v5, s[44:47], s32 offen offset:0 lds
v_mfma_f32_16x16x32_bf16 v[140:143], v[172:175], a[252:255], v[140:143]
ds_read_b128 v[164:167], v221 offset:40960
v_mfma_f32_16x16x32_bf16 v[144:147], v[176:179], a[240:243], v[144:147]
ds_read_b128 v[168:171], v220 offset:43008
v_mfma_f32_16x16x32_bf16 v[144:147], v[180:183], a[248:251], v[144:147]
v_mfma_f32_16x16x32_bf16 v[148:151], v[176:179], a[244:247], v[148:151]
ds_read_b128 v[172:175], v221 offset:43008
buffer_load_dwordx4 v27, s[44:47], s33 offen offset:1024 lds
v_mfma_f32_16x16x32_bf16 v[148:151], v[180:183], a[252:255], v[148:151]
v_mfma_f32_16x16x32_bf16 v[152:155], v[184:187], a[240:243], v[152:155]
ds_read_b128 v[176:179], v220 offset:45056
v_mfma_f32_16x16x32_bf16 v[152:155], v[188:191], a[248:251], v[152:155]
v_mfma_f32_16x16x32_bf16 v[156:159], v[184:187], a[244:247], v[156:159]
ds_read_b128 v[180:183], v221 offset:45056
s_add_u32 m0, s61, 32768
v_mfma_f32_16x16x32_bf16 v[156:159], v[188:191], a[252:255], v[156:159]
s_waitcnt lgkmcnt(2)
s_add_u32 s63, s95, 147456
)ASM"
R"ASM(
v_mfma_f32_16x16x32_bf16 v[96:99], v[160:163], a[0:3], v[96:99]
ds_read_b128 v[184:187], v220 offset:47104
buffer_load_dwordx4 v23, s[48:51], s63 offen offset:0 lds
v_mfma_f32_16x16x32_bf16 v[96:99], v[164:167], a[8:11], v[96:99]
v_mfma_f32_16x16x32_bf16 v[100:103], v[160:163], a[4:7], v[100:103]
ds_read_b128 v[188:191], v221 offset:47104
v_mfma_f32_16x16x32_bf16 v[100:103], v[164:167], a[12:15], v[100:103]
v_mfma_f32_16x16x32_bf16 v[104:107], v[168:171], a[0:3], v[104:107]
ds_read_b128 v[160:163], v222 offset:0
buffer_load_dwordx4 v6, s[48:51], s63 offen offset:1024 lds
v_mfma_f32_16x16x32_bf16 v[104:107], v[172:175], a[8:11], v[104:107]
v_mfma_f32_16x16x32_bf16 v[108:111], v[168:171], a[4:7], v[108:111]
ds_read_b128 v[164:167], v223 offset:0
v_mfma_f32_16x16x32_bf16 v[108:111], v[172:175], a[12:15], v[108:111]
s_waitcnt lgkmcnt(2)
v_mfma_f32_16x16x32_bf16 v[112:115], v[176:179], a[0:3], v[112:115]
ds_read_b128 v[168:171], v222 offset:2048
s_add_u32 m0, s61, 40960
v_mfma_f32_16x16x32_bf16 v[112:115], v[180:183], a[8:11], v[112:115]
s_add_u32 s63, s95, 147456
buffer_load_dwordx4 v23, s[84:87], s63 offen offset:0 lds
v_mfma_f32_16x16x32_bf16 v[116:119], v[176:179], a[4:7], v[116:119]
ds_read_b128 v[172:175], v223 offset:2048
v_mfma_f32_16x16x32_bf16 v[116:119], v[180:183], a[12:15], v[116:119]
v_mfma_f32_16x16x32_bf16 v[120:123], v[184:187], a[0:3], v[120:123]
ds_read_b128 v[176:179], v222 offset:4096
v_mfma_f32_16x16x32_bf16 v[120:123], v[188:191], a[8:11], v[120:123]
buffer_load_dwordx4 v6, s[84:87], s63 offen offset:1024 lds
v_mfma_f32_16x16x32_bf16 v[124:127], v[184:187], a[4:7], v[124:127]
ds_read_b128 v[180:183], v223 offset:4096
v_mfma_f32_16x16x32_bf16 v[124:127], v[188:191], a[12:15], v[124:127]
s_waitcnt lgkmcnt(2)
v_mfma_f32_16x16x32_bf16 v[128:131], v[160:163], a[0:3], v[128:131]
ds_read_b128 v[184:187], v222 offset:6144
v_mfma_f32_16x16x32_bf16 v[128:131], v[164:167], a[8:11], v[128:131]
v_mfma_f32_16x16x32_bf16 v[132:135], v[160:163], a[4:7], v[132:135]
ds_read_b128 v[188:191], v223 offset:6144
s_add_u32 m0, s61, 49152
v_mfma_f32_16x16x32_bf16 v[132:135], v[164:167], a[12:15], v[132:135]
buffer_load_dwordx4 v5, s[44:47], s38 offen offset:0 lds
v_mfma_f32_16x16x32_bf16 v[136:139], v[168:171], a[0:3], v[136:139]
ds_read_b64_tr_b16 v[160:161], v28 offset:8192
v_mfma_f32_16x16x32_bf16 v[136:139], v[172:175], a[8:11], v[136:139]
ds_read_b64_tr_b16 v[162:163], v28 offset:8704
v_mfma_f32_16x16x32_bf16 v[140:143], v[168:171], a[4:7], v[140:143]
ds_read_b64_tr_b16 v[164:165], v28 offset:16384
v_mfma_f32_16x16x32_bf16 v[140:143], v[172:175], a[12:15], v[140:143]
s_waitcnt lgkmcnt(3)
ds_read_b64_tr_b16 v[166:167], v28 offset:16896
v_mfma_f32_16x16x32_bf16 v[144:147], v[176:179], a[0:3], v[144:147]
ds_read_b64_tr_b16 v[168:169], v29 offset:8192
buffer_load_dwordx4 v27, s[44:47], s39 offen offset:1024 lds
v_mfma_f32_16x16x32_bf16 v[144:147], v[180:183], a[8:11], v[144:147]
ds_read_b64_tr_b16 v[170:171], v29 offset:8704
v_mfma_f32_16x16x32_bf16 v[148:151], v[176:179], a[4:7], v[148:151]
ds_read_b64_tr_b16 v[172:173], v29 offset:16384
v_mfma_f32_16x16x32_bf16 v[148:151], v[180:183], a[12:15], v[148:151]
ds_read_b64_tr_b16 v[174:175], v29 offset:16896
v_mfma_f32_16x16x32_bf16 v[152:155], v[184:187], a[0:3], v[152:155]
ds_read_b64_tr_b16 v[176:177], v30 offset:8192
v_mfma_f32_16x16x32_bf16 v[152:155], v[188:191], a[8:11], v[152:155]
ds_read_b64_tr_b16 v[178:179], v30 offset:8704
v_mfma_f32_16x16x32_bf16 v[156:159], v[184:187], a[4:7], v[156:159]
ds_read_b64_tr_b16 v[180:181], v30 offset:16384
v_mfma_f32_16x16x32_bf16 v[156:159], v[188:191], a[12:15], v[156:159]
s_waitcnt lgkmcnt(3)
ds_read_b64_tr_b16 v[182:183], v30 offset:16896
s_waitcnt vmcnt(23)
v_mfma_f32_16x16x32_bf16 v[96:99], v[160:163], a[16:19], v[96:99]
ds_read_b64_tr_b16 v[184:185], v31 offset:8192
s_waitcnt vmcnt(21)
v_mfma_f32_16x16x32_bf16 v[96:99], v[164:167], a[24:27], v[96:99]
ds_read_b64_tr_b16 v[186:187], v31 offset:8704
v_mfma_f32_16x16x32_bf16 v[100:103], v[160:163], a[20:23], v[100:103]
ds_read_b64_tr_b16 v[188:189], v31 offset:16384
s_waitcnt vmcnt(20)
v_mfma_f32_16x16x32_bf16 v[100:103], v[164:167], a[28:31], v[100:103]
ds_read_b64_tr_b16 v[190:191], v31 offset:16896
v_mfma_f32_16x16x32_bf16 v[104:107], v[168:171], a[16:19], v[104:107]
ds_read_b64_tr_b16 v[160:161], v28 offset:9216
v_mfma_f32_16x16x32_bf16 v[104:107], v[172:175], a[24:27], v[104:107]
ds_read_b64_tr_b16 v[162:163], v28 offset:9728
v_mfma_f32_16x16x32_bf16 v[108:111], v[168:171], a[20:23], v[108:111]
ds_read_b64_tr_b16 v[164:165], v28 offset:17408
v_mfma_f32_16x16x32_bf16 v[108:111], v[172:175], a[28:31], v[108:111]
s_waitcnt lgkmcnt(3)
ds_read_b64_tr_b16 v[166:167], v28 offset:17920
v_mfma_f32_16x16x32_bf16 v[112:115], v[176:179], a[16:19], v[112:115]
ds_read_b64_tr_b16 v[168:169], v29 offset:9216
v_mfma_f32_16x16x32_bf16 v[112:115], v[180:183], a[24:27], v[112:115]
ds_read_b64_tr_b16 v[170:171], v29 offset:9728
v_mfma_f32_16x16x32_bf16 v[116:119], v[176:179], a[20:23], v[116:119]
ds_read_b64_tr_b16 v[172:173], v29 offset:17408
v_mfma_f32_16x16x32_bf16 v[116:119], v[180:183], a[28:31], v[116:119]
ds_read_b64_tr_b16 v[174:175], v29 offset:17920
v_mfma_f32_16x16x32_bf16 v[120:123], v[184:187], a[16:19], v[120:123]
ds_read_b64_tr_b16 v[176:177], v30 offset:9216
v_mfma_f32_16x16x32_bf16 v[120:123], v[188:191], a[24:27], v[120:123]
ds_read_b64_tr_b16 v[178:179], v30 offset:9728
v_mfma_f32_16x16x32_bf16 v[124:127], v[184:187], a[20:23], v[124:127]
ds_read_b64_tr_b16 v[180:181], v30 offset:17408
v_mfma_f32_16x16x32_bf16 v[124:127], v[188:191], a[28:31], v[124:127]
s_waitcnt lgkmcnt(3)
ds_read_b64_tr_b16 v[182:183], v30 offset:17920
v_mfma_f32_16x16x32_bf16 v[128:131], v[160:163], a[16:19], v[128:131]
ds_read_b64_tr_b16 v[184:185], v31 offset:9216
v_mfma_f32_16x16x32_bf16 v[128:131], v[164:167], a[24:27], v[128:131]
ds_read_b64_tr_b16 v[186:187], v31 offset:9728
v_mfma_f32_16x16x32_bf16 v[132:135], v[160:163], a[20:23], v[132:135]
ds_read_b64_tr_b16 v[188:189], v31 offset:17408
v_mfma_f32_16x16x32_bf16 v[132:135], v[164:167], a[28:31], v[132:135]
ds_read_b64_tr_b16 v[190:191], v31 offset:17920
v_mfma_f32_16x16x32_bf16 v[136:139], v[168:171], a[16:19], v[136:139]
s_waitcnt vmcnt(8) lgkmcnt(0)
s_barrier
v_mfma_f32_16x16x32_bf16 v[136:139], v[172:175], a[24:27], v[136:139]
ds_read_b64_tr_b16 v[160:161], v29 offset:24576
v_mfma_f32_16x16x32_bf16 v[140:143], v[168:171], a[20:23], v[140:143]
ds_read_b64_tr_b16 v[162:163], v29 offset:25088
v_mfma_f32_16x16x32_bf16 v[140:143], v[172:175], a[28:31], v[140:143]
ds_read_b64_tr_b16 v[164:165], v29 offset:25600
ds_read_b64_tr_b16 v[166:167], v29 offset:26112
v_mfma_f32_16x16x32_bf16 v[144:147], v[176:179], a[16:19], v[144:147]
ds_read_b64_tr_b16 v[168:169], v30 offset:24576
v_mfma_f32_16x16x32_bf16 v[144:147], v[180:183], a[24:27], v[144:147]
ds_read_b64_tr_b16 v[170:171], v30 offset:25088
v_mfma_f32_16x16x32_bf16 v[148:151], v[176:179], a[20:23], v[148:151]
ds_read_b64_tr_b16 v[172:173], v30 offset:25600
v_mfma_f32_16x16x32_bf16 v[148:151], v[180:183], a[28:31], v[148:151]
ds_read_b64_tr_b16 v[174:175], v30 offset:26112
v_mfma_f32_16x16x32_bf16 v[152:155], v[184:187], a[16:19], v[152:155]
ds_read_b64_tr_b16 v[176:177], v31 offset:24576
v_mfma_f32_16x16x32_bf16 v[152:155], v[188:191], a[24:27], v[152:155]
ds_read_b64_tr_b16 v[178:179], v31 offset:25088
v_mfma_f32_16x16x32_bf16 v[156:159], v[184:187], a[20:23], v[156:159]
ds_read_b64_tr_b16 v[180:181], v31 offset:25600
v_mfma_f32_16x16x32_bf16 v[156:159], v[188:191], a[28:31], v[156:159]
s_waitcnt lgkmcnt(3)
ds_read_b64_tr_b16 v[182:183], v31 offset:26112
v_mfma_f32_16x16x32_bf16 v[232:235], v[160:163], a[128:131], v[232:235]
ds_read_b64_tr_b16 v[184:185], v29 offset:32768
v_mfma_f32_16x16x32_bf16 v[232:235], v[164:167], a[136:139], v[232:235]
ds_read_b64_tr_b16 v[186:187], v29 offset:33280
v_mfma_f32_16x16x32_bf16 v[236:239], v[160:163], a[132:135], v[236:239]
ds_read_b64_tr_b16 v[188:189], v29 offset:33792
v_mfma_f32_16x16x32_bf16 v[236:239], v[164:167], a[140:143], v[236:239]
ds_read_b64_tr_b16 v[190:191], v29 offset:34304
v_mfma_f32_16x16x32_bf16 v[240:243], v[168:171], a[128:131], v[240:243]
ds_read_b64_tr_b16 v[160:161], v30 offset:32768
v_cvt_pk_bf16_f32 v64, v96, v97
v_mfma_f32_16x16x32_bf16 v[240:243], v[172:175], a[136:139], v[240:243]
ds_read_b64_tr_b16 v[162:163], v30 offset:33280
v_cvt_pk_bf16_f32 v65, v98, v99
v_mfma_f32_16x16x32_bf16 v[244:247], v[168:171], a[132:135], v[244:247]
ds_read_b64_tr_b16 v[164:165], v30 offset:33792
v_cvt_pk_bf16_f32 v66, v100, v101
v_mfma_f32_16x16x32_bf16 v[244:247], v[172:175], a[140:143], v[244:247]
s_waitcnt lgkmcnt(3)
ds_read_b64_tr_b16 v[166:167], v30 offset:34304
v_mfma_f32_16x16x32_bf16 v[248:251], v[176:179], a[128:131], v[248:251]
ds_read_b64_tr_b16 v[168:169], v31 offset:32768
v_cvt_pk_bf16_f32 v67, v102, v103
v_mfma_f32_16x16x32_bf16 v[248:251], v[180:183], a[136:139], v[248:251]
ds_read_b64_tr_b16 v[170:171], v31 offset:33280
v_cvt_pk_bf16_f32 v68, v128, v129
v_mfma_f32_16x16x32_bf16 v[252:255], v[176:179], a[132:135], v[252:255]
ds_read_b64_tr_b16 v[172:173], v31 offset:33792
v_cvt_pk_bf16_f32 v69, v130, v131
v_mfma_f32_16x16x32_bf16 v[252:255], v[180:183], a[140:143], v[252:255]
ds_read_b64_tr_b16 v[174:175], v31 offset:34304
v_cvt_pk_bf16_f32 v70, v132, v133
v_mfma_f32_16x16x32_bf16 v[232:235], v[184:187], a[144:147], v[232:235]
ds_read_b64_tr_b16 v[176:177], v29 offset:40960
v_cvt_pk_bf16_f32 v71, v134, v135
v_mfma_f32_16x16x32_bf16 v[232:235], v[188:191], a[152:155], v[232:235]
ds_read_b64_tr_b16 v[178:179], v29 offset:41472
s_add_u32 s76, s68, 0
v_mfma_f32_16x16x32_bf16 v[236:239], v[184:187], a[148:151], v[236:239]
ds_read_b64_tr_b16 v[180:181], v29 offset:41984
s_and_b32 s76, s76, 3
v_mfma_f32_16x16x32_bf16 v[236:239], v[188:191], a[156:159], v[236:239]
s_waitcnt lgkmcnt(3)
ds_read_b64_tr_b16 v[182:183], v29 offset:42496
v_mfma_f32_16x16x32_bf16 v[240:243], v[160:163], a[144:147], v[240:243]
ds_read_b64_tr_b16 v[184:185], v30 offset:40960
s_mul_i32 s76, s76, 84480
v_mfma_f32_16x16x32_bf16 v[240:243], v[164:167], a[152:155], v[240:243]
ds_read_b64_tr_b16 v[186:187], v30 offset:41472
s_add_u32 s76, s76, s66
v_mfma_f32_16x16x32_bf16 v[244:247], v[160:163], a[148:151], v[244:247]
ds_read_b64_tr_b16 v[188:189], v30 offset:41984
buffer_store_dwordx4 v[64:67], v7, s[96:99], s76 offen offset:0 sc1
v_mfma_f32_16x16x32_bf16 v[244:247], v[164:167], a[156:159], v[244:247]
ds_read_b64_tr_b16 v[190:191], v30 offset:42496
buffer_store_dwordx4 v[68:71], v7, s[96:99], s76 offen offset:1024 sc1
v_mfma_f32_16x16x32_bf16 v[248:251], v[168:171], a[144:147], v[248:251]
ds_read_b64_tr_b16 v[160:161], v31 offset:40960
v_cvt_pk_bf16_f32 v72, v104, v105
v_mfma_f32_16x16x32_bf16 v[248:251], v[172:175], a[152:155], v[248:251]
ds_read_b64_tr_b16 v[162:163], v31 offset:41472
)ASM"
R"ASM(
v_cvt_pk_bf16_f32 v73, v106, v107
v_mfma_f32_16x16x32_bf16 v[252:255], v[168:171], a[148:151], v[252:255]
ds_read_b64_tr_b16 v[164:165], v31 offset:41984
v_cvt_pk_bf16_f32 v74, v108, v109
v_mfma_f32_16x16x32_bf16 v[252:255], v[172:175], a[156:159], v[252:255]
s_waitcnt lgkmcnt(3)
ds_read_b64_tr_b16 v[166:167], v31 offset:42496
v_mfma_f32_16x16x32_bf16 v[232:235], v[176:179], a[160:163], v[232:235]
ds_read_b64_tr_b16 v[168:169], v193 offset:0
v_cvt_pk_bf16_f32 v75, v110, v111
v_mfma_f32_16x16x32_bf16 v[232:235], v[180:183], a[168:171], v[232:235]
ds_read_b64_tr_b16 v[170:171], v193 offset:512
v_cvt_pk_bf16_f32 v76, v136, v137
v_mfma_f32_16x16x32_bf16 v[236:239], v[176:179], a[164:167], v[236:239]
ds_read_b64_tr_b16 v[172:173], v193 offset:1024
v_cvt_pk_bf16_f32 v77, v138, v139
v_mfma_f32_16x16x32_bf16 v[236:239], v[180:183], a[172:175], v[236:239]
ds_read_b64_tr_b16 v[174:175], v193 offset:1536
v_cvt_pk_bf16_f32 v78, v140, v141
v_mfma_f32_16x16x32_bf16 v[240:243], v[184:187], a[160:163], v[240:243]
ds_read_b64_tr_b16 v[176:177], v194 offset:0
v_cvt_pk_bf16_f32 v79, v142, v143
v_mfma_f32_16x16x32_bf16 v[240:243], v[188:191], a[168:171], v[240:243]
ds_read_b64_tr_b16 v[178:179], v194 offset:512
s_add_u32 s77, s68, 1
v_mfma_f32_16x16x32_bf16 v[244:247], v[184:187], a[164:167], v[244:247]
ds_read_b64_tr_b16 v[180:181], v194 offset:1024
s_and_b32 s77, s77, 3
v_mfma_f32_16x16x32_bf16 v[244:247], v[188:191], a[172:175], v[244:247]
s_waitcnt lgkmcnt(3)
ds_read_b64_tr_b16 v[182:183], v194 offset:1536
v_mfma_f32_16x16x32_bf16 v[248:251], v[160:163], a[160:163], v[248:251]
ds_read_b64_tr_b16 v[184:185], v195 offset:0
s_mul_i32 s77, s77, 84480
v_mfma_f32_16x16x32_bf16 v[248:251], v[164:167], a[168:171], v[248:251]
ds_read_b64_tr_b16 v[186:187], v195 offset:512
s_add_u32 s77, s77, s66
v_mfma_f32_16x16x32_bf16 v[252:255], v[160:163], a[164:167], v[252:255]
ds_read_b64_tr_b16 v[188:189], v195 offset:1024
buffer_store_dwordx4 v[72:75], v7, s[96:99], s77 offen offset:0 sc1
v_mfma_f32_16x16x32_bf16 v[252:255], v[164:167], a[172:175], v[252:255]
ds_read_b64_tr_b16 v[190:191], v195 offset:1536
buffer_store_dwordx4 v[76:79], v7, s[96:99], s77 offen offset:1024 sc1
v_mfma_f32_16x16x32_bf16 v[232:235], v[168:171], a[176:179], v[232:235]
ds_read_b64_tr_b16 v[160:161], v193 offset:8192
v_cvt_pk_bf16_f32 v64, v112, v113
v_mfma_f32_16x16x32_bf16 v[232:235], v[172:175], a[184:187], v[232:235]
ds_read_b64_tr_b16 v[162:163], v193 offset:8704
v_cvt_pk_bf16_f32 v65, v114, v115
v_mfma_f32_16x16x32_bf16 v[236:239], v[168:171], a[180:183], v[236:239]
ds_read_b64_tr_b16 v[164:165], v193 offset:9216
v_cvt_pk_bf16_f32 v66, v116, v117
v_mfma_f32_16x16x32_bf16 v[236:239], v[172:175], a[188:191], v[236:239]
s_waitcnt lgkmcnt(3)
ds_read_b64_tr_b16 v[166:167], v193 offset:9728
v_mfma_f32_16x16x32_bf16 v[240:243], v[176:179], a[176:179], v[240:243]
ds_read_b64_tr_b16 v[168:169], v194 offset:8192
v_cvt_pk_bf16_f32 v67, v118, v119
v_mfma_f32_16x16x32_bf16 v[240:243], v[180:183], a[184:187], v[240:243]
ds_read_b64_tr_b16 v[170:171], v194 offset:8704
v_cvt_pk_bf16_f32 v68, v144, v145
v_mfma_f32_16x16x32_bf16 v[244:247], v[176:179], a[180:183], v[244:247]
ds_read_b64_tr_b16 v[172:173], v194 offset:9216
v_cvt_pk_bf16_f32 v69, v146, v147
v_mfma_f32_16x16x32_bf16 v[244:247], v[180:183], a[188:191], v[244:247]
ds_read_b64_tr_b16 v[174:175], v194 offset:9728
v_cvt_pk_bf16_f32 v70, v148, v149
v_mfma_f32_16x16x32_bf16 v[248:251], v[184:187], a[176:179], v[248:251]
ds_read_b64_tr_b16 v[176:177], v195 offset:8192
v_cvt_pk_bf16_f32 v71, v150, v151
v_mfma_f32_16x16x32_bf16 v[248:251], v[188:191], a[184:187], v[248:251]
ds_read_b64_tr_b16 v[178:179], v195 offset:8704
s_add_u32 s78, s68, 2
v_mfma_f32_16x16x32_bf16 v[252:255], v[184:187], a[180:183], v[252:255]
ds_read_b64_tr_b16 v[180:181], v195 offset:9216
s_and_b32 s78, s78, 3
v_mfma_f32_16x16x32_bf16 v[252:255], v[188:191], a[188:191], v[252:255]
s_waitcnt lgkmcnt(3)
ds_read_b64_tr_b16 v[182:183], v195 offset:9728
v_mfma_f32_16x16x32_bf16 v[232:235], v[160:163], a[192:195], v[232:235]
ds_read_b64_tr_b16 v[184:185], v193 offset:16384
s_mul_i32 s78, s78, 84480
v_mfma_f32_16x16x32_bf16 v[232:235], v[164:167], a[200:203], v[232:235]
ds_read_b64_tr_b16 v[186:187], v193 offset:16896
s_add_u32 s78, s78, s66
v_mfma_f32_16x16x32_bf16 v[236:239], v[160:163], a[196:199], v[236:239]
ds_read_b64_tr_b16 v[188:189], v193 offset:17408
buffer_store_dwordx4 v[64:67], v7, s[96:99], s78 offen offset:0 sc1
v_mfma_f32_16x16x32_bf16 v[236:239], v[164:167], a[204:207], v[236:239]
ds_read_b64_tr_b16 v[190:191], v193 offset:17920
buffer_store_dwordx4 v[68:71], v7, s[96:99], s78 offen offset:1024 sc1
v_mfma_f32_16x16x32_bf16 v[240:243], v[168:171], a[192:195], v[240:243]
s_waitcnt vmcnt(6) lgkmcnt(0)
s_barrier
v_mfma_f32_16x16x32_bf16 v[240:243], v[172:175], a[200:203], v[240:243]
ds_read_b64_tr_b16 v[160:161], v194 offset:16384
v_cvt_pk_bf16_f32 v72, v120, v121
v_mfma_f32_16x16x32_bf16 v[244:247], v[168:171], a[196:199], v[244:247]
ds_read_b64_tr_b16 v[162:163], v194 offset:16896
v_cvt_pk_bf16_f32 v73, v122, v123
v_mfma_f32_16x16x32_bf16 v[244:247], v[172:175], a[204:207], v[244:247]
ds_read_b64_tr_b16 v[164:165], v194 offset:17408
ds_read_b64_tr_b16 v[166:167], v194 offset:17920
v_mfma_f32_16x16x32_bf16 v[248:251], v[176:179], a[192:195], v[248:251]
ds_read_b64_tr_b16 v[168:169], v195 offset:16384
v_cvt_pk_bf16_f32 v74, v124, v125
v_mfma_f32_16x16x32_bf16 v[248:251], v[180:183], a[200:203], v[248:251]
ds_read_b64_tr_b16 v[170:171], v195 offset:16896
v_cvt_pk_bf16_f32 v75, v126, v127
v_mfma_f32_16x16x32_bf16 v[252:255], v[176:179], a[196:199], v[252:255]
ds_read_b64_tr_b16 v[172:173], v195 offset:17408
v_cvt_pk_bf16_f32 v76, v152, v153
v_mfma_f32_16x16x32_bf16 v[252:255], v[180:183], a[204:207], v[252:255]
ds_read_b64_tr_b16 v[174:175], v195 offset:17920
v_cvt_pk_bf16_f32 v77, v154, v155
v_mfma_f32_16x16x32_bf16 v[232:235], v[184:187], a[208:211], v[232:235]
ds_read_b64_tr_b16 v[176:177], v193 offset:24576
v_cvt_pk_bf16_f32 v78, v156, v157
v_mfma_f32_16x16x32_bf16 v[232:235], v[188:191], a[216:219], v[232:235]
ds_read_b64_tr_b16 v[178:179], v193 offset:25088
v_cvt_pk_bf16_f32 v79, v158, v159
v_mfma_f32_16x16x32_bf16 v[236:239], v[184:187], a[212:215], v[236:239]
ds_read_b64_tr_b16 v[180:181], v193 offset:25600
s_add_u32 s79, s68, 3
v_mfma_f32_16x16x32_bf16 v[236:239], v[188:191], a[220:223], v[236:239]
s_waitcnt lgkmcnt(3)
ds_read_b64_tr_b16 v[182:183], v193 offset:26112
v_mfma_f32_16x16x32_bf16 v[240:243], v[160:163], a[208:211], v[240:243]
ds_read_b64_tr_b16 v[184:185], v194 offset:24576
s_and_b32 s79, s79, 3
v_mfma_f32_16x16x32_bf16 v[240:243], v[164:167], a[216:219], v[240:243]
ds_read_b64_tr_b16 v[186:187], v194 offset:25088
s_mul_i32 s79, s79, 84480
v_mfma_f32_16x16x32_bf16 v[244:247], v[160:163], a[212:215], v[244:247]
ds_read_b64_tr_b16 v[188:189], v194 offset:25600
s_add_u32 s79, s79, s66
v_mfma_f32_16x16x32_bf16 v[244:247], v[164:167], a[220:223], v[244:247]
ds_read_b64_tr_b16 v[190:191], v194 offset:26112
buffer_store_dwordx4 v[72:75], v7, s[96:99], s79 offen offset:0 sc1
v_mfma_f32_16x16x32_bf16 v[248:251], v[168:171], a[208:211], v[248:251]
ds_read_b64_tr_b16 v[160:161], v195 offset:24576
buffer_store_dwordx4 v[76:79], v7, s[96:99], s79 offen offset:1024 sc1
v_mfma_f32_16x16x32_bf16 v[248:251], v[172:175], a[216:219], v[248:251]
ds_read_b64_tr_b16 v[162:163], v195 offset:25088
v_mov_b32 v65, v219
v_mfma_f32_16x16x32_bf16 v[252:255], v[168:171], a[212:215], v[252:255]
ds_read_b64_tr_b16 v[164:165], v195 offset:25600
v_mov_b32 v67, v219
v_mfma_f32_16x16x32_bf16 v[252:255], v[172:175], a[220:223], v[252:255]
s_waitcnt lgkmcnt(3)
ds_read_b64_tr_b16 v[166:167], v195 offset:26112
v_mfma_f32_16x16x32_bf16 v[232:235], v[176:179], a[224:227], v[232:235]
ds_read_b128 v[168:171], v216 offset:34816
v_mov_b32 v69, v219
v_mfma_f32_16x16x32_bf16 v[232:235], v[180:183], a[232:235], v[232:235]
v_mov_b32 v71, v219
v_mov_b32 v73, v219
v_mfma_f32_16x16x32_bf16 v[236:239], v[176:179], a[228:231], v[236:239]
ds_read_b128 v[172:175], v217 offset:34816
v_mov_b32 v75, v219
v_mfma_f32_16x16x32_bf16 v[236:239], v[180:183], a[236:239], v[236:239]
v_mov_b32 v77, v219
v_mov_b32 v79, v219
v_mfma_f32_16x16x32_bf16 v[240:243], v[184:187], a[224:227], v[240:243]
ds_read_b128 v[176:179], v216 offset:36864
v_mov_b32 v81, v219
v_mfma_f32_16x16x32_bf16 v[240:243], v[188:191], a[232:235], v[240:243]
v_mov_b32 v83, v219
v_mov_b32 v85, v219
v_mfma_f32_16x16x32_bf16 v[244:247], v[184:187], a[228:231], v[244:247]
ds_read_b128 v[180:183], v217 offset:36864
v_mov_b32 v87, v219
v_mfma_f32_16x16x32_bf16 v[244:247], v[188:191], a[236:239], v[244:247]
s_waitcnt lgkmcnt(2)
v_mov_b32 v89, v219
v_mfma_f32_16x16x32_bf16 v[248:251], v[160:163], a[224:227], v[248:251]
ds_read_b128 v[184:187], v216 offset:38912
v_mov_b32 v91, v219
v_mfma_f32_16x16x32_bf16 v[248:251], v[164:167], a[232:235], v[248:251]
v_mov_b32 v93, v219
v_mov_b32 v95, v219
v_mfma_f32_16x16x32_bf16 v[252:255], v[160:163], a[228:231], v[252:255]
ds_read_b128 v[188:191], v217 offset:38912
v_mfma_f32_16x16x32_bf16 v[252:255], v[164:167], a[236:239], v[252:255]
v_mfma_f32_16x16x32_bf16 v[232:235], v[168:171], a[240:243], v[232:235]
ds_read_b128 v[160:163], v216 offset:43008
v_mfma_f32_16x16x32_bf16 v[232:235], v[172:175], a[248:251], v[232:235]
v_mfma_f32_16x16x32_bf16 v[236:239], v[168:171], a[244:247], v[236:239]
ds_read_b128 v[164:167], v217 offset:43008
v_mfma_f32_16x16x32_bf16 v[236:239], v[172:175], a[252:255], v[236:239]
s_waitcnt lgkmcnt(2)
v_mfma_f32_16x16x32_bf16 v[240:243], v[176:179], a[240:243], v[240:243]
ds_read_b128 v[168:171], v216 offset:45056
v_mfma_f32_16x16x32_bf16 v[240:243], v[180:183], a[248:251], v[240:243]
v_mfma_f32_16x16x32_bf16 v[244:247], v[176:179], a[244:247], v[244:247]
ds_read_b128 v[172:175], v217 offset:45056
v_mfma_f32_16x16x32_bf16 v[244:247], v[180:183], a[252:255], v[244:247]
v_mfma_f32_16x16x32_bf16 v[248:251], v[184:187], a[240:243], v[248:251]
ds_read_b128 v[176:179], v216 offset:47104
v_mfma_f32_16x16x32_bf16 v[248:251], v[188:191], a[248:251], v[248:251]
)ASM"
R"ASM(
v_mfma_f32_16x16x32_bf16 v[252:255], v[184:187], a[244:247], v[252:255]
ds_read_b128 v[180:183], v217 offset:47104
v_mfma_f32_16x16x32_bf16 v[252:255], v[188:191], a[252:255], v[252:255]
s_waitcnt lgkmcnt(2)
v_mfma_f32_16x16x32_bf16 v[232:235], v[160:163], a[0:3], v[232:235]
ds_read_b64_tr_b16 v[184:185], v197 offset:0
v_mfma_f32_16x16x32_bf16 v[232:235], v[164:167], a[8:11], v[232:235]
ds_read_b64_tr_b16 v[186:187], v197 offset:512
v_mfma_f32_16x16x32_bf16 v[236:239], v[160:163], a[4:7], v[236:239]
ds_read_b64_tr_b16 v[188:189], v197 offset:1024
v_mfma_f32_16x16x32_bf16 v[236:239], v[164:167], a[12:15], v[236:239]
ds_read_b64_tr_b16 v[190:191], v197 offset:1536
v_mfma_f32_16x16x32_bf16 v[240:243], v[168:171], a[0:3], v[240:243]
ds_read_b64_tr_b16 v[160:161], v198 offset:0
v_mfma_f32_16x16x32_bf16 v[240:243], v[172:175], a[8:11], v[240:243]
ds_read_b64_tr_b16 v[162:163], v198 offset:512
v_mfma_f32_16x16x32_bf16 v[244:247], v[168:171], a[4:7], v[244:247]
ds_read_b64_tr_b16 v[164:165], v198 offset:1024
v_mfma_f32_16x16x32_bf16 v[244:247], v[172:175], a[12:15], v[244:247]
s_waitcnt lgkmcnt(3)
ds_read_b64_tr_b16 v[166:167], v198 offset:1536
v_mfma_f32_16x16x32_bf16 v[248:251], v[176:179], a[0:3], v[248:251]
ds_read_b64_tr_b16 v[168:169], v199 offset:0
v_mfma_f32_16x16x32_bf16 v[248:251], v[180:183], a[8:11], v[248:251]
ds_read_b64_tr_b16 v[170:171], v199 offset:512
v_mfma_f32_16x16x32_bf16 v[252:255], v[176:179], a[4:7], v[252:255]
ds_read_b64_tr_b16 v[172:173], v199 offset:1024
v_mfma_f32_16x16x32_bf16 v[252:255], v[180:183], a[12:15], v[252:255]
ds_read_b64_tr_b16 v[174:175], v199 offset:1536
v_mfma_f32_16x16x32_bf16 v[232:235], v[184:187], a[16:19], v[232:235]
ds_read_b64_tr_b16 v[176:177], v28 offset:24576
v_mfma_f32_16x16x32_bf16 v[232:235], v[188:191], a[24:27], v[232:235]
ds_read_b64_tr_b16 v[178:179], v28 offset:25088
v_mfma_f32_16x16x32_bf16 v[236:239], v[184:187], a[20:23], v[236:239]
ds_read_b64_tr_b16 v[180:181], v28 offset:25600
v_mfma_f32_16x16x32_bf16 v[236:239], v[188:191], a[28:31], v[236:239]
s_waitcnt lgkmcnt(3)
ds_read_b64_tr_b16 v[182:183], v28 offset:26112
v_mfma_f32_16x16x32_bf16 v[240:243], v[160:163], a[16:19], v[240:243]
ds_read_b64_tr_b16 v[184:185], v28 offset:32768
s_waitcnt vmcnt(0)
v_mfma_f32_16x16x32_bf16 v[240:243], v[164:167], a[24:27], v[240:243]
ds_read_b64_tr_b16 v[186:187], v28 offset:33280
v_cmp_gt_u32 vcc, 4, v1
v_mfma_f32_16x16x32_bf16 v[244:247], v[160:163], a[20:23], v[244:247]
ds_read_b64_tr_b16 v[188:189], v28 offset:33792
v_mul_u32_u24 v9, 84480, v1
v_mfma_f32_16x16x32_bf16 v[244:247], v[164:167], a[28:31], v[244:247]
ds_read_b64_tr_b16 v[190:191], v28 offset:34304
v_mov_b32 v10, 0x7f000000
v_mfma_f32_16x16x32_bf16 v[248:251], v[168:171], a[16:19], v[248:251]
ds_read_b64_tr_b16 v[160:161], v28 offset:40960
v_cndmask_b32 v9, v10, v9, vcc
v_mfma_f32_16x16x32_bf16 v[248:251], v[172:175], a[24:27], v[248:251]
ds_read_b64_tr_b16 v[162:163], v28 offset:41472
s_add_u32 s64, s66, 4096
v_mfma_f32_16x16x32_bf16 v[252:255], v[168:171], a[20:23], v[252:255]
ds_read_b64_tr_b16 v[164:165], v28 offset:41984
buffer_store_dword v219, v9, s[96:99], s64 offen sc1
v_mfma_f32_16x16x32_bf16 v[252:255], v[172:175], a[28:31], v[252:255]
s_waitcnt lgkmcnt(3)
ds_read_b64_tr_b16 v[166:167], v28 offset:42496
v_mfma_f32_16x16x32_bf16 v[224:227], v[176:179], a[128:131], v[224:227]
ds_read_b64_tr_b16 v[168:169], v192 offset:0
v_mfma_f32_16x16x32_bf16 v[224:227], v[180:183], a[136:139], v[224:227]
ds_read_b64_tr_b16 v[170:171], v192 offset:512
v_mfma_f32_16x16x32_bf16 v[228:231], v[176:179], a[132:135], v[228:231]
ds_read_b64_tr_b16 v[172:173], v192 offset:1024
v_mfma_f32_16x16x32_bf16 v[228:231], v[180:183], a[140:143], v[228:231]
ds_read_b64_tr_b16 v[174:175], v192 offset:1536
v_cvt_pk_bf16_f32 v72, v232, v233
v_mfma_f32_16x16x32_bf16 v[224:227], v[184:187], a[144:147], v[224:227]
ds_read_b64_tr_b16 v[176:177], v192 offset:8192
v_cvt_pk_bf16_f32 v74, v234, v235
v_mfma_f32_16x16x32_bf16 v[224:227], v[188:191], a[152:155], v[224:227]
ds_read_b64_tr_b16 v[178:179], v192 offset:8704
v_cvt_pk_bf16_f32 v76, v236, v237
v_mfma_f32_16x16x32_bf16 v[228:231], v[184:187], a[148:151], v[228:231]
ds_read_b64_tr_b16 v[180:181], v192 offset:9216
v_cvt_pk_bf16_f32 v78, v238, v239
v_mfma_f32_16x16x32_bf16 v[228:231], v[188:191], a[156:159], v[228:231]
s_waitcnt lgkmcnt(3)
ds_read_b64_tr_b16 v[182:183], v192 offset:9728
v_mfma_f32_16x16x32_bf16 v[224:227], v[160:163], a[160:163], v[224:227]
ds_read_b64_tr_b16 v[184:185], v192 offset:16384
buffer_store_dwordx4 v[72:75], v7, s[96:99], s77 offen offset:2048 sc1
v_mfma_f32_16x16x32_bf16 v[224:227], v[164:167], a[168:171], v[224:227]
ds_read_b64_tr_b16 v[186:187], v192 offset:16896
buffer_store_dwordx4 v[76:79], v7, s[96:99], s77 offen offset:3072 sc1
v_mfma_f32_16x16x32_bf16 v[228:231], v[160:163], a[164:167], v[228:231]
ds_read_b64_tr_b16 v[188:189], v192 offset:17408
v_cvt_pk_bf16_f32 v80, v240, v241
v_mfma_f32_16x16x32_bf16 v[228:231], v[164:167], a[172:175], v[228:231]
ds_read_b64_tr_b16 v[190:191], v192 offset:17920
v_cvt_pk_bf16_f32 v82, v242, v243
v_mfma_f32_16x16x32_bf16 v[224:227], v[168:171], a[176:179], v[224:227]
ds_read_b64_tr_b16 v[160:161], v192 offset:24576
v_cvt_pk_bf16_f32 v84, v244, v245
v_mfma_f32_16x16x32_bf16 v[224:227], v[172:175], a[184:187], v[224:227]
ds_read_b64_tr_b16 v[162:163], v192 offset:25088
v_cvt_pk_bf16_f32 v86, v246, v247
v_mfma_f32_16x16x32_bf16 v[228:231], v[168:171], a[180:183], v[228:231]
ds_read_b64_tr_b16 v[164:165], v192 offset:25600
buffer_store_dwordx4 v[80:83], v7, s[96:99], s78 offen offset:2048 sc1
v_mfma_f32_16x16x32_bf16 v[228:231], v[172:175], a[188:191], v[228:231]
s_waitcnt lgkmcnt(3)
ds_read_b64_tr_b16 v[166:167], v192 offset:26112
v_mfma_f32_16x16x32_bf16 v[224:227], v[176:179], a[192:195], v[224:227]
ds_read_b128 v[168:171], v216 offset:32768
buffer_store_dwordx4 v[84:87], v7, s[96:99], s78 offen offset:3072 sc1
v_mfma_f32_16x16x32_bf16 v[224:227], v[180:183], a[200:203], v[224:227]
v_cvt_pk_bf16_f32 v88, v248, v249
v_cvt_pk_bf16_f32 v90, v250, v251
v_mfma_f32_16x16x32_bf16 v[228:231], v[176:179], a[196:199], v[228:231]
ds_read_b128 v[172:175], v217 offset:32768
v_cvt_pk_bf16_f32 v92, v252, v253
v_mfma_f32_16x16x32_bf16 v[228:231], v[180:183], a[204:207], v[228:231]
v_cvt_pk_bf16_f32 v94, v254, v255
buffer_store_dwordx4 v[88:91], v7, s[96:99], s79 offen offset:2048 sc1
v_mfma_f32_16x16x32_bf16 v[224:227], v[184:187], a[208:211], v[224:227]
ds_read_b128 v[176:179], v216 offset:40960
buffer_store_dwordx4 v[92:95], v7, s[96:99], s79 offen offset:3072 sc1
v_mfma_f32_16x16x32_bf16 v[224:227], v[188:191], a[216:219], v[224:227]
v_mfma_f32_16x16x32_bf16 v[228:231], v[184:187], a[212:215], v[228:231]
ds_read_b128 v[180:183], v217 offset:40960
v_mfma_f32_16x16x32_bf16 v[228:231], v[188:191], a[220:223], v[228:231]
s_waitcnt lgkmcnt(2)
v_mfma_f32_16x16x32_bf16 v[224:227], v[160:163], a[224:227], v[224:227]
ds_read_b64_tr_b16 v[184:185], v196 offset:0
v_mfma_f32_16x16x32_bf16 v[224:227], v[164:167], a[232:235], v[224:227]
ds_read_b64_tr_b16 v[186:187], v196 offset:512
v_mfma_f32_16x16x32_bf16 v[228:231], v[160:163], a[228:231], v[228:231]
ds_read_b64_tr_b16 v[188:189], v196 offset:1024
v_mfma_f32_16x16x32_bf16 v[228:231], v[164:167], a[236:239], v[228:231]
ds_read_b64_tr_b16 v[190:191], v196 offset:1536
v_mfma_f32_16x16x32_bf16 v[224:227], v[168:171], a[240:243], v[224:227]
v_mfma_f32_16x16x32_bf16 v[224:227], v[172:175], a[248:251], v[224:227]
v_mfma_f32_16x16x32_bf16 v[228:231], v[168:171], a[244:247], v[228:231]
v_mfma_f32_16x16x32_bf16 v[228:231], v[172:175], a[252:255], v[228:231]
s_waitcnt lgkmcnt(5)
v_mfma_f32_16x16x32_bf16 v[224:227], v[176:179], a[0:3], v[224:227]
s_waitcnt lgkmcnt(4)
v_mfma_f32_16x16x32_bf16 v[224:227], v[180:183], a[8:11], v[224:227]
v_mfma_f32_16x16x32_bf16 v[228:231], v[176:179], a[4:7], v[228:231]
v_mfma_f32_16x16x32_bf16 v[228:231], v[180:183], a[12:15], v[228:231]
s_waitcnt lgkmcnt(2)
v_mfma_f32_16x16x32_bf16 v[224:227], v[184:187], a[16:19], v[224:227]
s_waitcnt lgkmcnt(0)
v_mfma_f32_16x16x32_bf16 v[224:227], v[188:191], a[24:27], v[224:227]
v_mfma_f32_16x16x32_bf16 v[228:231], v[184:187], a[20:23], v[228:231]
v_mfma_f32_16x16x32_bf16 v[228:231], v[188:191], a[28:31], v[228:231]
s_nop 5
v_cvt_pk_bf16_f32 v64, v224, v225
v_cvt_pk_bf16_f32 v66, v226, v227
v_cvt_pk_bf16_f32 v68, v228, v229
v_cvt_pk_bf16_f32 v70, v230, v231
buffer_store_dwordx4 v[64:67], v7, s[96:99], s76 offen offset:2048 sc1
buffer_store_dwordx4 v[68:71], v7, s[96:99], s76 offen offset:3072 sc1
LEND_%=:
s_waitcnt vmcnt(0) lgkmcnt(0)
)ASM"
  : "+v"(tid) : [ka]"s"(ka), [b]"s"(blockIdx.x)
  : "v1","v2","v3","v4","v5","v6","v7","v8","v9","v10","v11","v12","v13","v14","v15","v16","v17","v18","v19","v20","v21","v22","v23","v24","v25","v26","v27","v28","v29","v30","v31","v32","v33","v34","v35","v36","v37","v38","v39","v40","v41","v42","v43","v44","v45","v46","v47","v48","v49","v50","v51","v52","v53","v54","v55","v56","v57","v58","v59","v60","v61","v62","v63","v64","v65","v66","v67","v68","v69","v70","v71","v72","v73","v74","v75","v76","v77","v78","v79","v80","v81","v82","v83","v84","v85","v86","v87","v88","v89","v90","v91","v92","v93","v94","v95","v96","v97","v98","v99","v100","v101","v102","v103","v104","v105","v106","v107","v108","v109","v110","v111","v112","v113","v114","v115","v116","v117","v118","v119","v120","v121","v122","v123","v124","v125","v126","v127","v128","v129","v130","v131","v132","v133","v134","v135","v136","v137","v138","v139","v140","v141","v142","v143","v144","v145","v146","v147","v148","v149","v150","v151","v152","v153","v154","v155","v156","v157","v158","v159","v160","v161","v162","v163","v164","v165","v166","v167","v168","v169","v170","v171","v172","v173","v174","v175","v176","v177","v178","v179","v180","v181","v182","v183","v184","v185","v186","v187","v188","v189","v190","v191","v192","v193","v194","v195","v196","v197","v198","v199","v200","v201","v202","v203","v204","v205","v206","v207","v208","v209","v210","v211","v212","v213","v214","v215","v216","v217","v218","v219","v220","v221","v222","v223","v224","v225","v226","v227","v228","v229","v230","v231","v232","v233","v234","v235","v236","v237","v238","v239","v240","v241","v242","v243","v244","v245","v246","v247","v248","v249","v250","v251","v252","v253","v254","v255","a0","a1","a2","a3","a4","a5","a6","a7","a8","a9","a10","a11","a12","a13","a14","a15","a16","a17","a18","a19","a20","a21","a22","a23","a24","a25","a26","a27","a28","a29","a30","a31","a32","a33","a34","a35","a36","a37","a38","a39","a40","a41","a42","a43","a44","a45","a46","a47","a48","a49","a50","a51","a52","a53","a54","a55","a56","a57","a58","a59","a60","a61","a62","a63","a64","a65","a66","a67","a68","a69","a70","a71","a72","a73","a74","a75","a76","a77","a78","a79","a80","a81","a82","a83","a84","a85","a86","a87","a88","a89","a90","a91","a92","a93","a94","a95","a96","a97","a98","a99","a100","a101","a102","a103","a104","a105","a106","a107","a108","a109","a110","a111","a112","a113","a114","a115","a116","a117","a118","a119","a120","a121","a122","a123","a124","a125","a126","a127","a128","a129","a130","a131","a132","a133","a134","a135","a136","a137","a138","a139","a140","a141","a142","a143","a144","a145","a146","a147","a148","a149","a150","a151","a152","a153","a154","a155","a156","a157","a158","a159","a160","a161","a162","a163","a164","a165","a166","a167","a168","a169","a170","a171","a172","a173","a174","a175","a176","a177","a178","a179","a180","a181","a182","a183","a184","a185","a186","a187","a188","a189","a190","a191","a192","a193","a194","a195","a196","a197","a198","a199","a200","a201","a202","a203","a204","a205","a206","a207","a208","a209","a210","a211","a212","a213","a214","a215","a216","a217","a218","a219","a220","a221","a222","a223","a224","a225","a226","a227","a228","a229","a230","a231","a232","a233","a234","a235","a236","a237","a238","a239","a240","a241","a242","a243","a244","a245","a246","a247","a248","a249","a250","a251","a252","a253","a254","a255","s4","s5","s6","s7","s8","s9","s10","s11","s12","s13","s14","s15","s16","s17","s18","s19","s20","s21","s22","s23","s24","s25","s26","s27","s28","s29","s30","s31","s32","s33","s34","s35","s36","s37","s38","s39","s40","s41","s42","s43","s44","s45","s46","s47","s48","s49","s50","s51","s52","s53","s54","s55","s56","s57","s58","s59","s60","s61","s62","s63","s64","s65","s66","s67","s68","s69","s70","s71","s72","s73","s74","s75","s76","s77","s78","s79","s80","s81","s82","s83","s84","s85","s86","s87","s88","s89","s90","s91","s92","s93","s94","s95","s96","s97","s98","s99","vcc","scc","memory");
}

extern "C" void kernel_launch(void* const* d_in, const int* in_sizes, int n_in, void* d_out, int out_size, void* d_ws, size_t ws_size, hipStream_t stream){
  Tab T;
  T.r[0][0]=769u;  T.r[0][1]=0u;  T.r[0][2]=128u;  T.r[0][3]=192u;  T.r[0][4]=0u;  T.r[0][5]=196720u;  T.r[0][6]=983039u;  T.r[0][7]=4294901782u;
  T.r[1][0]=131841u;  T.r[1][1]=0u;  T.r[1][2]=384u;  T.r[1][3]=448u;  T.r[1][4]=0u;  T.r[1][5]=262257u;  T.r[1][6]=1048575u;  T.r[1][7]=4294901783u;
  T.r[2][0]=262913u;  T.r[2][1]=0u;  T.r[2][2]=640u;  T.r[2][3]=704u;  T.r[2][4]=0u;  T.r[2][5]=327794u;  T.r[2][6]=1114111u;  T.r[2][7]=4294901784u;
  T.r[3][0]=393985u;  T.r[3][1]=0u;  T.r[3][2]=896u;  T.r[3][3]=960u;  T.r[3][4]=0u;  T.r[3][5]=393331u;  T.r[3][6]=1179647u;  T.r[3][7]=4294901785u;
  T.r[4][0]=525057u;  T.r[4][1]=0u;  T.r[4][2]=1152u;  T.r[4][3]=1216u;  T.r[4][4]=0u;  T.r[4][5]=458868u;  T.r[4][6]=1245183u;  T.r[4][7]=4294901786u;
  T.r[5][0]=656128u;  T.r[5][1]=1408u;  T.r[5][2]=1472u;  T.r[5][3]=1792u;  T.r[5][4]=0u;  T.r[5][5]=524288u;  T.r[5][6]=1245195u;  T.r[5][7]=4294901787u;
  T.r[6][0]=787200u;  T.r[6][1]=1664u;  T.r[6][2]=1728u;  T.r[6][3]=1856u;  T.r[6][4]=0u;  T.r[6][5]=589825u;  T.r[6][6]=1310732u;  T.r[6][7]=4294901894u;
  T.r[7][0]=984064u;  T.r[7][1]=2048u;  T.r[7][2]=2112u;  T.r[7][3]=2176u;  T.r[7][4]=2240u;  T.r[7][5]=655362u;  T.r[7][6]=1376269u;  T.r[7][7]=9306247u;
  T.r[8][0]=1180417u;  T.r[8][1]=0u;  T.r[8][2]=128u;  T.r[8][3]=192u;  T.r[8][4]=0u;  T.r[8][5]=1966197u;  T.r[8][6]=2818047u;  T.r[8][7]=4294901810u;
  T.r[9][0]=1311489u;  T.r[9][1]=0u;  T.r[9][2]=384u;  T.r[9][3]=448u;  T.r[9][4]=0u;  T.r[9][5]=2031734u;  T.r[9][6]=2883583u;  T.r[9][7]=4294901811u;
  T.r[10][0]=1442561u;  T.r[10][1]=0u;  T.r[10][2]=640u;  T.r[10][3]=704u;  T.r[10][4]=0u;  T.r[10][5]=2097271u;  T.r[10][6]=2949119u;  T.r[10][7]=4294901812u;
  T.r[11][0]=1573633u;  T.r[11][1]=0u;  T.r[11][2]=896u;  T.r[11][3]=960u;  T.r[11][4]=0u;  T.r[11][5]=2162808u;  T.r[11][6]=3014655u;  T.r[11][7]=4294901813u;
  T.r[12][0]=1704704u;  T.r[12][1]=1152u;  T.r[12][2]=1216u;  T.r[12][3]=1536u;  T.r[12][4]=0u;  T.r[12][5]=2228345u;  T.r[12][6]=3014694u;  T.r[12][7]=4294901814u;
  T.r[13][0]=1835776u;  T.r[13][1]=1408u;  T.r[13][2]=1472u;  T.r[13][3]=1600u;  T.r[13][4]=0u;  T.r[13][5]=2293882u;  T.r[13][6]=3080231u;  T.r[13][7]=4294901815u;
  T.r[14][0]=2032384u;  T.r[14][1]=1792u;  T.r[14][2]=1856u;  T.r[14][3]=2176u;  T.r[14][4]=0u;  T.r[14][5]=2359324u;  T.r[14][6]=3145768u;  T.r[14][7]=4294901896u;
  T.r[15][0]=2163456u;  T.r[15][1]=2048u;  T.r[15][2]=2112u;  T.r[15][3]=2240u;  T.r[15][4]=0u;  T.r[15][5]=2424861u;  T.r[15][6]=3211305u;  T.r[15][7]=4294901897u;
  T.r[16][0]=2360065u;  T.r[16][1]=0u;  T.r[16][2]=128u;  T.r[16][3]=192u;  T.r[16][4]=0u;  T.r[16][5]=3866747u;  T.r[16][6]=4653055u;  T.r[16][7]=4294901838u;
  T.r[17][0]=2491137u;  T.r[17][1]=0u;  T.r[17][2]=384u;  T.r[17][3]=448u;  T.r[17][4]=0u;  T.r[17][5]=3932284u;  T.r[17][6]=4718591u;  T.r[17][7]=4294901839u;
  T.r[18][0]=2622209u;  T.r[18][1]=0u;  T.r[18][2]=640u;  T.r[18][3]=704u;  T.r[18][4]=0u;  T.r[18][5]=3997821u;  T.r[18][6]=4784127u;  T.r[18][7]=4294901840u;
  T.r[19][0]=2753281u;  T.r[19][1]=0u;  T.r[19][2]=896u;  T.r[19][3]=960u;  T.r[19][4]=0u;  T.r[19][5]=4063358u;  T.r[19][6]=4849663u;  T.r[19][7]=4294901841u;
  T.r[20][0]=2884353u;  T.r[20][1]=0u;  T.r[20][2]=1152u;  T.r[20][3]=1216u;  T.r[20][4]=0u;  T.r[20][5]=4128895u;  T.r[20][6]=4915199u;  T.r[20][7]=4294901842u;
  T.r[21][0]=3015424u;  T.r[21][1]=1408u;  T.r[21][2]=1472u;  T.r[21][3]=1792u;  T.r[21][4]=0u;  T.r[21][5]=4194360u;  T.r[21][6]=4915267u;  T.r[21][7]=4294901843u;
  T.r[22][0]=3146496u;  T.r[22][1]=1664u;  T.r[22][2]=1728u;  T.r[22][3]=1856u;  T.r[22][4]=0u;  T.r[22][5]=4259897u;  T.r[22][6]=4980804u;  T.r[22][7]=4294901898u;
  T.r[23][0]=3343360u;  T.r[23][1]=2048u;  T.r[23][2]=2112u;  T.r[23][3]=2176u;  T.r[23][4]=2240u;  T.r[23][5]=4325434u;  T.r[23][6]=5046341u;  T.r[23][7]=9371787u;
  T.r[24][0]=3539713u;  T.r[24][1]=0u;  T.r[24][2]=128u;  T.r[24][3]=192u;  T.r[24][4]=0u;  T.r[24][5]=5636224u;  T.r[24][6]=6488063u;  T.r[24][7]=4294901866u;
  T.r[25][0]=3670785u;  T.r[25][1]=0u;  T.r[25][2]=384u;  T.r[25][3]=448u;  T.r[25][4]=0u;  T.r[25][5]=5701761u;  T.r[25][6]=6553599u;  T.r[25][7]=4294901867u;
  T.r[26][0]=3801857u;  T.r[26][1]=0u;  T.r[26][2]=640u;  T.r[26][3]=704u;  T.r[26][4]=0u;  T.r[26][5]=5767298u;  T.r[26][6]=6619135u;  T.r[26][7]=4294901868u;
  T.r[27][0]=3932929u;  T.r[27][1]=0u;  T.r[27][2]=896u;  T.r[27][3]=960u;  T.r[27][4]=0u;  T.r[27][5]=5832835u;  T.r[27][6]=6684671u;  T.r[27][7]=4294901869u;
  T.r[28][0]=4064000u;  T.r[28][1]=1152u;  T.r[28][2]=1216u;  T.r[28][3]=1536u;  T.r[28][4]=0u;  T.r[28][5]=5898372u;  T.r[28][6]=6684766u;  T.r[28][7]=4294901870u;
  T.r[29][0]=4195072u;  T.r[29][1]=1408u;  T.r[29][2]=1472u;  T.r[29][3]=1600u;  T.r[29][4]=0u;  T.r[29][5]=5963909u;  T.r[29][6]=6750303u;  T.r[29][7]=4294901871u;
  T.r[30][0]=4391680u;  T.r[30][1]=1792u;  T.r[30][2]=1856u;  T.r[30][3]=2176u;  T.r[30][4]=0u;  T.r[30][5]=6029396u;  T.r[30][6]=6815840u;  T.r[30][7]=4294901900u;
  T.r[31][0]=4522752u;  T.r[31][1]=2048u;  T.r[31][2]=2112u;  T.r[31][3]=2240u;  T.r[31][4]=0u;  T.r[31][5]=6094933u;  T.r[31][6]=6881377u;  T.r[31][7]=4294901901u;
  T.tab[0][0]=737280u;  T.tab[0][1]=884736u;  T.tab[0][2]=1105920u;  T.tab[0][3]=36864u;  T.tab[0][4]=184320u;  T.tab[0][5]=331776u;  T.tab[0][6]=479232u;  T.tab[0][7]=626688u;  T.tab[0][8]=774144u;  T.tab[0][9]=921600u;  T.tab[0][10]=1142784u;  T.tab[0][11]=811008u;  T.tab[0][12]=958464u;  T.tab[0][13]=1179648u;  T.tab[0][14]=73728u;  T.tab[0][15]=221184u;  T.tab[0][16]=368640u;  T.tab[0][17]=516096u;  T.tab[0][18]=663552u;  T.tab[0][19]=847872u;  T.tab[0][20]=995328u;  T.tab[0][21]=1216512u;  T.tab[0][22]=110592u;  T.tab[0][23]=258048u;  T.tab[0][24]=405504u;  T.tab[0][25]=552960u;  T.tab[0][26]=700416u;  T.tab[0][27]=1032192u;  T.tab[0][28]=0u;  T.tab[0][29]=0u;  T.tab[0][30]=0u;  T.tab[0][31]=0u;  T.tab[0][32]=0u;  T.tab[0][33]=0u;  T.tab[0][34]=0u;  T.tab[0][35]=0u;
  T.tab[1][0]=2285568u;  T.tab[1][1]=2433024u;  T.tab[1][2]=1363968u;  T.tab[1][3]=1511424u;  T.tab[1][4]=1658880u;  T.tab[1][5]=1806336u;  T.tab[1][6]=1953792u;  T.tab[1][7]=2101248u;  T.tab[1][8]=2322432u;  T.tab[1][9]=2469888u;  T.tab[1][10]=1990656u;  T.tab[1][11]=2138112u;  T.tab[1][12]=2359296u;  T.tab[1][13]=2506752u;  T.tab[1][14]=1400832u;  T.tab[1][15]=1548288u;  T.tab[1][16]=1695744u;  T.tab[1][17]=1843200u;  T.tab[1][18]=2027520u;  T.tab[1][19]=2174976u;  T.tab[1][20]=2396160u;  T.tab[1][21]=2543616u;  T.tab[1][22]=1437696u;  T.tab[1][23]=1585152u;  T.tab[1][24]=1732608u;  T.tab[1][25]=1880064u;  T.tab[1][26]=2211840u;  T.tab[1][27]=2248704u;  T.tab[1][28]=0u;  T.tab[1][29]=0u;  T.tab[1][30]=0u;  T.tab[1][31]=0u;  T.tab[1][32]=0u;  T.tab[1][33]=0u;  T.tab[1][34]=0u;  T.tab[1][35]=0u;
  T.tab[2][0]=3391488u;  T.tab[2][1]=3538944u;  T.tab[2][2]=3760128u;  T.tab[2][3]=2691072u;  T.tab[2][4]=2838528u;  T.tab[2][5]=2985984u;  T.tab[2][6]=3133440u;  T.tab[2][7]=3280896u;  T.tab[2][8]=3428352u;  T.tab[2][9]=3575808u;  T.tab[2][10]=3796992u;  T.tab[2][11]=3465216u;  T.tab[2][12]=3612672u;  T.tab[2][13]=3833856u;  T.tab[2][14]=2727936u;  T.tab[2][15]=2875392u;  T.tab[2][16]=3022848u;  T.tab[2][17]=3170304u;  T.tab[2][18]=3317760u;  T.tab[2][19]=3502080u;  T.tab[2][20]=3649536u;  T.tab[2][21]=3870720u;  T.tab[2][22]=2764800u;  T.tab[2][23]=2912256u;  T.tab[2][24]=3059712u;  T.tab[2][25]=3207168u;  T.tab[2][26]=3354624u;  T.tab[2][27]=3686400u;  T.tab[2][28]=0u;  T.tab[2][29]=0u;  T.tab[2][30]=0u;  T.tab[2][31]=0u;  T.tab[2][32]=0u;  T.tab[2][33]=0u;  T.tab[2][34]=0u;  T.tab[2][35]=0u;
  T.tab[3][0]=4939776u;  T.tab[3][1]=5087232u;  T.tab[3][2]=4018176u;  T.tab[3][3]=4165632u;  T.tab[3][4]=4313088u;  T.tab[3][5]=4460544u;  T.tab[3][6]=4608000u;  T.tab[3][7]=4755456u;  T.tab[3][8]=4976640u;  T.tab[3][9]=5124096u;  T.tab[3][10]=4644864u;  T.tab[3][11]=4792320u;  T.tab[3][12]=5013504u;  T.tab[3][13]=5160960u;  T.tab[3][14]=4055040u;  T.tab[3][15]=4202496u;  T.tab[3][16]=4349952u;  T.tab[3][17]=4497408u;  T.tab[3][18]=4681728u;  T.tab[3][19]=4829184u;  T.tab[3][20]=5050368u;  T.tab[3][21]=5197824u;  T.tab[3][22]=4091904u;  T.tab[3][23]=4239360u;  T.tab[3][24]=4386816u;  T.tab[3][25]=4534272u;  T.tab[3][26]=4866048u;  T.tab[3][27]=4902912u;  T.tab[3][28]=0u;  T.tab[3][29]=0u;  T.tab[3][30]=0u;  T.tab[3][31]=0u;  T.tab[3][32]=0u;  T.tab[3][33]=0u;  T.tab[3][34]=0u;  T.tab[3][35]=0u;
  T.tab[4][0]=0u;  T.tab[4][1]=147456u;  T.tab[4][2]=294912u;  T.tab[4][3]=442368u;  T.tab[4][4]=589824u;  T.tab[4][5]=1327104u;  T.tab[4][6]=1474560u;  T.tab[4][7]=1622016u;  T.tab[4][8]=1769472u;  T.tab[4][9]=1916928u;  T.tab[4][10]=2064384u;  T.tab[4][11]=2654208u;  T.tab[4][12]=2801664u;  T.tab[4][13]=2949120u;  T.tab[4][14]=3096576u;  T.tab[4][15]=3244032u;  T.tab[4][16]=3981312u;  T.tab[4][17]=4128768u;  T.tab[4][18]=4276224u;  T.tab[4][19]=4423680u;  T.tab[4][20]=4571136u;  T.tab[4][21]=4718592u;  T.tab[4][22]=1069056u;  T.tab[4][23]=1253376u;  T.tab[4][24]=2580480u;  T.tab[4][25]=2617344u;  T.tab[4][26]=3723264u;  T.tab[4][27]=3907584u;  T.tab[4][28]=5234688u;  T.tab[4][29]=5271552u;  T.tab[4][30]=0u;  T.tab[4][31]=0u;  T.tab[4][32]=0u;  T.tab[4][33]=0u;  T.tab[4][34]=1290240u;  T.tab[4][35]=3944448u;

  hipFuncSetAttribute((const void*)fused_kernel, hipFuncAttributeMaxDynamicSharedMemorySize, 160*1024);
  fused_kernel<<<256,256,160*1024,stream>>>((const u16*)d_in[0],(const u16*)d_in[1],(const u16*)d_in[2],(const u16*)d_in[3],(const u16*)d_in[4],(const u16*)d_in[5],(const u16*)d_in[6],
     (const u16*)d_in[7],(const u16*)d_in[8],(const u16*)d_in[10],(const u16*)d_in[11],(const u16*)d_in[12],(float*)d_out,(char*)d_ws,T);
}
```
